```python
import torch

def _softmax(x, axis=-1):
    return torch.softmax(x, dim=axis)
def _gelu(x):
    return torch.nn.functional.gelu(x, approximate="tanh")
def _silu(x):
    return torch.nn.functional.silu(x)
def _sigmoid(x):
    return torch.sigmoid(x)
def _layernorm(x, axis=-1, eps=1e-5):
    m = x.mean(dim=axis, keepdim=True)
    v = x.var(dim=axis, keepdim=True, unbiased=False)
    return (x - m) / torch.sqrt(v + eps)
def _scatter_add(tgt, idx, src, axis=0):
    return tgt.clone().index_add_(axis, idx, src)
def _segment_sum(src, seg, n):
    out = torch.zeros((n,) + src.shape[1:], dtype=src.dtype)
    return out.index_add_(0, seg, src)
def _pad(x, pw):
    flat = tuple(v for pair in reversed(pw) for v in pair)
    return torch.nn.functional.pad(x, flat)

kernel_name = 'sequential_preLN__compressed_kv_cla__dense_activation_squared_relu__prompt'

def setup_inputs():

    _w = (lambda a: (lambda t: t.to(torch.bfloat16) if t.is_floating_point() else t)(torch.as_tensor(a)))
    def _A_setup():
        B, H, Sq, Sk, D, M = 1, 32, 2048, 2048, 128, 576
        Dc = 4 * D
        return {
            "x": _w((lambda *s: torch.randn(*s, dtype=torch.bfloat16) * 0.05)(B, Sq, M)),
            "c_kv": _w((lambda *s: torch.randn(*s, dtype=torch.bfloat16) * 0.4)(B, Sk, Dc)),
            "w_q": _w((lambda *s: torch.randn(*s, dtype=torch.bfloat16) * 3.2)(M, H, D) * (M ** -0.5)),
            "w_uk": _w((lambda *s: torch.randn(*s, dtype=torch.bfloat16) * 3.2)(Dc, H, D) * (Dc ** -0.5)),
            "w_uv": _w((lambda *s: torch.randn(*s, dtype=torch.bfloat16) * 0.4)(Dc, H, D) * (Dc ** -0.5)),
            "w_o": _w((lambda *s: torch.randn(*s, dtype=torch.bfloat16) * 0.4)(H, D, M) * ((H * D) ** -0.5)),
        }
    _A_in = _A_setup()

    def _B_setup():
        B, S, D, F = 1, 2048, 576, 4608
        return {
            "x": _w((lambda *s: torch.randn(*s, dtype=torch.bfloat16) * 0.05)(B, S, D)),
            "w1": _w((lambda *s: torch.randn(*s, dtype=torch.bfloat16) * 0.05)(D, F)),
            "b1": _w((lambda *s: torch.randn(*s, dtype=torch.bfloat16) * 0.4)(F)),
            "w2": _w((lambda *s: torch.randn(*s, dtype=torch.bfloat16) * 0.05)(F, D)),
            "b2": _w((lambda *s: torch.randn(*s, dtype=torch.bfloat16) * 3.2)(D)),
        }
    _B_in = _B_setup()

    return {
        "A_x": _A_in["x"],
        "A_c_kv": _A_in["c_kv"],
        "A_w_q": _A_in["w_q"],
        "A_w_uk": _A_in["w_uk"],
        "A_w_uv": _A_in["w_uv"],
        "A_w_o": _A_in["w_o"],
        "B_x": _B_in["x"],
        "B_w1": _B_in["w1"],
        "B_b1": _B_in["b1"],
        "B_w2": _B_in["w2"],
        "B_b2": _B_in["b2"],
    }

def reference(A_x, A_c_kv, A_w_q, A_w_uk, A_w_uv, A_w_o, B_x, B_w1, B_b1, B_w2, B_b2):
    def _A_ref(x, c_kv, w_q, w_uk, w_uv, w_o):
        B, Sq, M = x.shape[0], x.shape[1], x.shape[2]
        Sk = c_kv.shape[1]
        H, D = w_q.shape[1], w_q.shape[2]
        q = torch.einsum("bqm,mhd->bhqd", x, w_q)
        k = torch.einsum("bkc,chd->bhkd", c_kv, w_uk)
        v = torch.einsum("bkc,chd->bhkd", c_kv, w_uv)
        scores = torch.einsum("bhqd,bhkd->bhqk", q, k) * (D ** -0.5)
        off = Sk - Sq
        qi = torch.arange(Sq)[:, None]
        kj = torch.arange(Sk)[None, :]
        cmask = kj <= (qi + off)
        scores = torch.where(cmask[None, None], scores, -1e9)
        p = _softmax(scores, axis=-1)
        ctx = torch.einsum("bhqk,bhkd->bhqd", p, v)
        return torch.einsum("bhqd,hdm->bqm", ctx, w_o)

    def _B_ref(x, w1, b1, w2, b2):
        h = torch.einsum("bsd,df->bsf", x, w1) + b1
        zero = torch.zeros_like(h)
        r = torch.where(h > zero, h, zero)
        h = r * r
        out = torch.einsum("bsf,fd->bsd", h, w2) + b2
        return out

    x = A_x
    h = x + _A_ref(_layernorm(x, axis=-1), A_c_kv, A_w_q, A_w_uk, A_w_uv, A_w_o)
    h2 = h + _B_ref(_layernorm(h, axis=-1), B_w1, B_b1, B_w2, B_b2)
    return h2
```

```cpp
#include <hip/hip_runtime.h>
#include <hip/hip_bf16.h>
#include <cstdint>
namespace ref {
typedef unsigned short u16;
__device__ __forceinline__ float bf2f(u16 b){ return __uint_as_float(((unsigned)b)<<16); }
__device__ __forceinline__ u16 f2bf(float f){ unsigned u=__float_as_uint(f); u += 0x7FFFu + ((u>>16)&1u); return (u16)(u>>16); }
constexpr int S=2048, M=576, H=32, D=128, DC=512, F=4608, HD=H*D;

template<bool INF32>
__global__ void ln_k(const void* in, u16* out){
  int row=blockIdx.x; int t=threadIdx.x;
  float v[9]; float s=0;
  for(int i=0;i<9;++i){ int c=t+64*i; v[i]= INF32? ((const float*)in)[row*M+c] : bf2f(((const u16*)in)[row*M+c]); s+=v[i]; }
  for(int o=32;o;o>>=1) s+=__shfl_xor(s,o);
  float m=s/M; float q=0; for(int i=0;i<9;++i){ float d=v[i]-m; q+=d*d; }
  for(int o=32;o;o>>=1) q+=__shfl_xor(q,o);
  float r=1.0f/sqrtf(q/M+1e-5f);
  for(int i=0;i<9;++i){ int c=t+64*i; out[row*M+c]=f2bf((v[i]-m)*r); }
}
template<int MODE>
__global__ void gemm_k(const u16* A,const u16* B,void* C,int Mm,int N,int K,const u16* bias,const void* res){
  __shared__ float As[16][17], Bs[16][17];
  int tx=threadIdx.x, ty=threadIdx.y; int row=blockIdx.y*16+ty, col=blockIdx.x*16+tx;
  float acc=0;
  for(int k0=0;k0<K;k0+=16){
    As[ty][tx]=bf2f(A[(size_t)row*K+k0+tx]); Bs[ty][tx]=bf2f(B[(size_t)(k0+ty)*N+col]);
    __syncthreads();
    for(int k=0;k<16;++k) acc+=As[ty][k]*Bs[k][tx];
    __syncthreads();
  }
  size_t o=(size_t)row*N+col;
  if(MODE==0) ((u16*)C)[o]=f2bf(acc);
  if(MODE==1){ float h=acc+bf2f(bias[col]); h=h>0?h:0; ((u16*)C)[o]=f2bf(h*h); }
  if(MODE==2){ ((float*)C)[o]=bf2f(((const u16*)res)[o])+acc; }
  if(MODE==3){ ((u16*)C)[o]=f2bf(((const float*)res)[o]+acc+bf2f(bias[col])); }
}
__global__ void attn_k(const u16* Q,const u16* K,const u16* V,u16* O){
  int q=blockIdx.x, h=blockIdx.y, t=threadIdx.x;
  __shared__ float sc[S]; __shared__ float qs[D]; __shared__ float red[256];
  if(t<D) qs[t]=bf2f(Q[(size_t)q*HD+h*D+t]);
  __syncthreads();
  float mx=-1e30f;
  for(int k=t;k<=q;k+=256){ float s=0; for(int d=0;d<D;++d) s+=qs[d]*bf2f(K[(size_t)k*HD+h*D+d]); s*=0.08838834764831845f; sc[k]=s; mx=fmaxf(mx,s); }
  red[t]=mx; __syncthreads();
  for(int o=128;o;o>>=1){ if(t<o) red[t]=fmaxf(red[t],red[t+o]); __syncthreads(); }
  mx=red[0]; __syncthreads();
  float sum=0; for(int k=t;k<=q;k+=256){ float e=__expf(sc[k]-mx); sc[k]=e; sum+=e; }
  red[t]=sum; __syncthreads();
  for(int o=128;o;o>>=1){ if(t<o) red[t]+=red[t+o]; __syncthreads(); }
  sum=red[0];
  if(t<D){ float a=0; for(int k=0;k<=q;++k) a+=sc[k]*bf2f(V[(size_t)k*HD+h*D+t]); O[(size_t)q*HD+h*D+t]=f2bf(a/sum); }
}
struct Bufs { u16 *xn,*q,*k,*v,*ctx,*hn,*f; float* h; };
inline Bufs carve(void* ws){ char* p=(char*)ws; Bufs b;
  b.xn=(u16*)p; p+=(size_t)S*M*2; b.q=(u16*)p; p+=(size_t)S*HD*2; b.k=(u16*)p; p+=(size_t)S*HD*2; b.v=(u16*)p; p+=(size_t)S*HD*2;
  b.ctx=(u16*)p; p+=(size_t)S*HD*2; b.hn=(u16*)p; p+=(size_t)S*M*2; b.f=(u16*)p; p+=(size_t)S*F*2; b.h=(float*)p; return b; }
inline void run(void* const* d_in, void* d_out, void* ws, hipStream_t st){
  const u16* x=(const u16*)d_in[0]; const u16* c=(const u16*)d_in[1]; const u16* wq=(const u16*)d_in[2]; const u16* wk=(const u16*)d_in[3];
  const u16* wv=(const u16*)d_in[4]; const u16* wo=(const u16*)d_in[5]; const u16* w1=(const u16*)d_in[7]; const u16* b1=(const u16*)d_in[8];
  const u16* w2=(const u16*)d_in[9]; const u16* b2=(const u16*)d_in[10];
  Bufs b=carve(ws); dim3 tb(16,16);
  ln_k<false><<<S,64,0,st>>>(x,b.xn);
  gemm_k<0><<<dim3(HD/16,S/16),tb,0,st>>>(b.xn,wq,b.q,S,HD,M,nullptr,nullptr);
  gemm_k<0><<<dim3(HD/16,S/16),tb,0,st>>>(c,wk,b.k,S,HD,DC,nullptr,nullptr);
  gemm_k<0><<<dim3(HD/16,S/16),tb,0,st>>>(c,wv,b.v,S,HD,DC,nullptr,nullptr);
  attn_k<<<dim3(S,H),256,0,st>>>(b.q,b.k,b.v,b.ctx);
  gemm_k<2><<<dim3(M/16,S/16),tb,0,st>>>(b.ctx,wo,b.h,S,M,HD,nullptr,x);
  ln_k<true><<<S,64,0,st>>>(b.h,b.hn);
  gemm_k<1><<<dim3(F/16,S/16),tb,0,st>>>(b.hn,w1,b.f,S,F,M,b1,nullptr);
  gemm_k<3><<<dim3(M/16,S/16),tb,0,st>>>(b.f,w2,d_out,S,M,F,b2,b.h);
}
}
extern "C" void kernel_launch(void* const* d_in, const int* in_sizes, int n_in, void* d_out, int out_size, void* d_ws, size_t ws_size, hipStream_t stream){
  ref::run(d_in,d_out,d_ws,stream);
}
```

```cpp
#include <hip/hip_runtime.h>
#include <hip/hip_bf16.h>
#include <cstdint>

typedef unsigned short u16;
typedef __bf16 bf16x8 __attribute__((ext_vector_type(8)));
typedef __bf16 bf16x2 __attribute__((ext_vector_type(2)));
typedef short s16x4 __attribute__((ext_vector_type(4)));
typedef short s16x8 __attribute__((ext_vector_type(8)));
typedef float f32x2 __attribute__((ext_vector_type(2)));
typedef float f32x4 __attribute__((ext_vector_type(4)));
typedef float f32x16 __attribute__((ext_vector_type(16)));
typedef unsigned u32x2 __attribute__((ext_vector_type(2)));
typedef unsigned u32x4 __attribute__((ext_vector_type(4)));
#define LAS __attribute__((address_space(3)))
typedef LAS const char* lds_cp;
typedef LAS char* lds_p;
#define DI __device__ __forceinline__

constexpr int S = 2048, DM = 576, NH = 32, HD = 128, DC = 512, FF = 4608, NHD = NH * HD;
constexpr float QSCALE = 0.08838834764831845f * 1.4426950408889634f;
constexpr float THR = 8.0f;

constexpr size_t WS_XN  = 0;
constexpr size_t WS_K   = WS_XN + (size_t)S * DM * 2;
constexpr size_t WS_V   = WS_K + (size_t)S * NHD * 2;
constexpr size_t WS_CTX = WS_V + (size_t)S * NHD * 2;
constexpr size_t WS_P1  = WS_CTX + (size_t)S * NHD * 2;
constexpr size_t WS_H   = WS_P1 + (size_t)4 * S * DM * 4;
constexpr size_t WS_HN  = WS_H + (size_t)S * DM * 4;
constexpr size_t WS_F   = WS_HN + (size_t)S * DM * 2;
constexpr size_t WS_P2  = WS_F + (size_t)S * FF * 2;
constexpr size_t WS_END = WS_P2 + (size_t)4 * S * DM * 4;

DI unsigned rfl(unsigned x) { return __builtin_amdgcn_readfirstlane(x); }
DI unsigned cvtpk(float lo, float hi) { f32x2 v = {lo, hi}; bf16x2 b = __builtin_convertvector(v, bf16x2); return __builtin_bit_cast(unsigned, b); }
DI float bf_lo(unsigned w) { return __uint_as_float(w << 16); }
DI float bf_hi(unsigned w) { return __uint_as_float(w & 0xffff0000u); }
#define SBAR() __builtin_amdgcn_sched_barrier(0)
#define BARRIER() asm volatile("s_barrier" ::: "memory")
#define WAITV(n) asm volatile("s_waitcnt vmcnt(%0)" :: "i"(n) : "memory")
#define WAITL0() asm volatile("s_waitcnt lgkmcnt(0)" ::: "memory")
#define MFMA32(a, b, c) __builtin_amdgcn_mfma_f32_32x32x16_bf16(a, b, c, 0, 0, 0)
#define MFMA16(a, b, c) __builtin_amdgcn_mfma_f32_16x16x32_bf16(a, b, c, 0, 0, 0)

template <int OFF> DI void glds(const void* sbase, unsigned voff, unsigned ldst) {
  unsigned keep;
  asm volatile("s_mov_b32 %0, m0\n\ts_mov_b32 m0, %3\n\ts_nop 0\n\tglobal_load_lds_dwordx4 %1, %2 offset:%4\n\ts_mov_b32 m0, %0"
               : "=&s"(keep) : "v"(voff), "s"(sbase), "s"(ldst), "i"(OFF) : "memory");
}
DI void glds4(const void* sbase, unsigned voff, unsigned ldst) {
  unsigned keep;
  asm volatile("s_mov_b32 %0, m0\n\ts_mov_b32 m0, %3\n\ts_nop 0\n\t"
               "global_load_lds_dwordx4 %1, %2\n\tglobal_load_lds_dwordx4 %1, %2 offset:1024\n\t"
               "global_load_lds_dwordx4 %1, %2 offset:2048\n\tglobal_load_lds_dwordx4 %1, %2 offset:3072\n\ts_mov_b32 m0, %0"
               : "=&s"(keep) : "v"(voff), "s"(sbase), "s"(ldst) : "memory");
}
DI void glds2(const void* sbase, unsigned voff, unsigned ldst) {
  unsigned keep;
  asm volatile("s_mov_b32 %0, m0\n\ts_mov_b32 m0, %3\n\ts_nop 0\n\t"
               "global_load_lds_dwordx4 %1, %2\n\tglobal_load_lds_dwordx4 %1, %2 offset:1024\n\ts_mov_b32 m0, %0"
               : "=&s"(keep) : "v"(voff), "s"(sbase), "s"(ldst) : "memory");
}
DI bf16x8 lds_rd128(lds_cp p) { return *(LAS const bf16x8*)p; }
DI s16x4 lds_tr(lds_cp p) { return __builtin_amdgcn_ds_read_tr16_b64_v4i16((LAS s16x4*)p); }
DI bf16x8 lds_tr2(lds_cp p0, lds_cp p1) {
  s16x4 a = lds_tr(p0), b = lds_tr(p1);
  s16x8 w = __builtin_shufflevector(a, b, 0, 1, 2, 3, 4, 5, 6, 7);
  return __builtin_bit_cast(bf16x8, w);
}
DI u32x4 pack8(const f32x16& a, int base, float sc) {
  u32x4 w;
  w[0] = cvtpk(a[base + 0] * sc, a[base + 1] * sc); w[1] = cvtpk(a[base + 2] * sc, a[base + 3] * sc);
  w[2] = cvtpk(a[base + 4] * sc, a[base + 5] * sc); w[3] = cvtpk(a[base + 6] * sc, a[base + 7] * sc);
  return w;
}
DI u32x4 pack8n(const f32x16& a, int base) {
  u32x4 w;
  w[0] = cvtpk(a[base + 0], a[base + 1]); w[1] = cvtpk(a[base + 2], a[base + 3]);
  w[2] = cvtpk(a[base + 4], a[base + 5]); w[3] = cvtpk(a[base + 6], a[base + 7]);
  return w;
}
DI float xhalf_max(float v) {
  auto rr = __builtin_amdgcn_permlane32_swap(__float_as_uint(v), __float_as_uint(v), false, false);
  return fmaxf(__uint_as_float(rr[0]), __uint_as_float(rr[1]));
}
DI float xhalf_sum(float v) {
  auto rr = __builtin_amdgcn_permlane32_swap(__float_as_uint(v), __float_as_uint(v), false, false);
  return __uint_as_float(rr[0]) + __uint_as_float(rr[1]);
}

constexpr int K1_LDS = 131072;
template <bool ISV>
DI void k1_loop(f32x16 (&acc)[2][4], lds_cp lds, const char* ckv_t, const char* wk_t, const char* wv_t, unsigned ldsb,
                unsigned avoff0, unsigned avoff1, unsigned wvoff, int wid, const unsigned (&aaddr)[4], unsigned waddr) {
#pragma unroll 1
  for (int t = 0; t < 8; ++t) {
    const unsigned cur = (unsigned)(t & 1) * 65536u, nxt = cur ^ 65536u;
    WAITV(0); WAITL0(); BARRIER();
    if (t + 1 < 8) {
      const char* a = ckv_t + (t + 1) * 128;
      const unsigned ad = ldsb + nxt + (unsigned)wid * 4096u;
      glds<0>(a, avoff0, ad); glds<0>(a, avoff1, ad + 1024u);
      glds<0>(a + 16384, avoff0, ad + 2048u); glds<0>(a + 16384, avoff1, ad + 3072u);
      const size_t wo = (size_t)(t + 1) * 64 * 8192;
      const unsigned wd = ldsb + nxt + 32768u + (unsigned)wid * 2048u;
      glds<0>(wk_t + wo, wvoff, wd); glds<0>(wk_t + wo + 128, wvoff, wd + 1024u);
      glds<0>(wv_t + wo, wvoff, wd + 16384u); glds<0>(wv_t + wo + 128, wvoff, wd + 16384u + 1024u);
    }
    lds_cp wb = lds + cur + waddr;
#pragma unroll
    for (int ks = 0; ks < 4; ++ks) {
      lds_cp ab = lds + cur + aaddr[ks];
      bf16x8 a0 = lds_rd128(ab), a1 = lds_rd128(ab + 4096);
#pragma unroll
      for (int nb = 0; nb < 4; ++nb) {
        bf16x8 w = lds_tr2(wb + ks * 4096 + nb * 512, wb + ks * 4096 + nb * 512 + 256);
        if (ISV) { acc[0][nb] = MFMA32(a0, w, acc[0][nb]); acc[1][nb] = MFMA32(a1, w, acc[1][nb]); }
        else     { acc[0][nb] = MFMA32(w, a0, acc[0][nb]); acc[1][nb] = MFMA32(w, a1, acc[1][nb]); }
      }
    }
  }
}

__global__ __launch_bounds__(512, 2) void k1_kvproj(const u16* __restrict__ x, const u16* __restrict__ ckv, const u16* __restrict__ wuk,
                                                    const u16* __restrict__ wuv, char* __restrict__ ws) {
  extern __shared__ __attribute__((aligned(16))) char smem[];
  const int tid = threadIdx.x, lane = tid & 63, r = lane & 31, hi = lane >> 5;
  const int wid = (int)rfl(tid >> 6);
  const int bid = blockIdx.x, h = (bid & 7) * 4 + ((bid >> 3) & 3), tb = bid >> 5;
  const unsigned ldsb = (unsigned)(size_t)smem;
  lds_cp lds = (lds_cp)smem;
  const char* ckv_t = (const char*)ckv + (size_t)(tb * 256 + wid * 32) * 1024;
  const unsigned avoff0 = (unsigned)(lane >> 3) * 1024u + (unsigned)(((lane & 7) ^ ((lane >> 4) & 7)) * 16);
  const unsigned avoff1 = (unsigned)(8 + (lane >> 3)) * 1024u + (unsigned)(((lane & 7) ^ ((4 + (lane >> 4)) & 7)) * 16);
  const char* wk_t = (const char*)wuk + (size_t)(wid * 8) * 8192 + h * 256;
  const char* wv_t = (const char*)wuv + (size_t)(wid * 8) * 8192 + h * 256;
  const unsigned wvoff = (unsigned)((lane >> 2) & 7) * 8192u + (unsigned)(lane >> 5) * 64u + (unsigned)(lane & 3) * 16u;
  {
    const unsigned ad = ldsb + (unsigned)wid * 4096u;
    glds<0>(ckv_t, avoff0, ad); glds<0>(ckv_t, avoff1, ad + 1024u);
    glds<0>(ckv_t + 16384, avoff0, ad + 2048u); glds<0>(ckv_t + 16384, avoff1, ad + 3072u);
    const unsigned wd = ldsb + 32768u + (unsigned)wid * 2048u;
    glds<0>(wk_t, wvoff, wd); glds<0>(wk_t + 128, wvoff, wd + 1024u);
    glds<0>(wv_t, wvoff, wd + 16384u); glds<0>(wv_t + 128, wvoff, wd + 16384u + 1024u);
  }
  {
    const int row = bid * 8 + wid;
    const u32x4* xr = (const u32x4*)(x + (size_t)row * DM);
    u32x4 c0 = xr[lane], c1 = (lane < 8) ? xr[64 + lane] : (u32x4){0u, 0u, 0u, 0u};
    float v[16];
#pragma unroll
    for (int i = 0; i < 4; ++i) { v[2 * i] = bf_lo(c0[i]); v[2 * i + 1] = bf_hi(c0[i]); v[8 + 2 * i] = bf_lo(c1[i]); v[9 + 2 * i] = bf_hi(c1[i]); }
    float s = 0.f;
#pragma unroll
    for (int i = 0; i < 16; ++i) s += v[i];
#pragma unroll
    for (int o = 32; o; o >>= 1) s += __shfl_xor(s, o);
    const float mean = s * (1.0f / DM);
    float q = 0.f;
#pragma unroll
    for (int i = 0; i < 8; ++i) { const float d = v[i] - mean; q += d * d; }
    if (lane < 8) {
#pragma unroll
      for (int i = 8; i < 16; ++i) { const float d = v[i] - mean; q += d * d; }
    }
#pragma unroll
    for (int o = 32; o; o >>= 1) q += __shfl_xor(q, o);
    const float rstd = 1.0f / sqrtf(q * (1.0f / DM) + 1e-5f);
    char* xnb = ws + WS_XN + (size_t)(row >> 5) * 36 * 1024 + (row & 31) * 16;
    u32x4 o0, o1;
#pragma unroll
    for (int i = 0; i < 4; ++i) { o0[i] = cvtpk((v[2 * i] - mean) * rstd, (v[2 * i + 1] - mean) * rstd);
                                  o1[i] = cvtpk((v[8 + 2 * i] - mean) * rstd, (v[9 + 2 * i] - mean) * rstd); }
    *(u32x4*)(xnb + (lane >> 1) * 1024 + (lane & 1) * 512) = o0;
    if (lane < 8) *(u32x4*)(xnb + (32 + (lane >> 1)) * 1024 + (lane & 1) * 512) = o1;
  }
  const int tg = wid & 3, kv = wid >> 2;
  unsigned aaddr[4];
  { const unsigned sw = (unsigned)(r >> 1) & 7u;
#pragma unroll
    for (int ks = 0; ks < 4; ++ks) aaddr[ks] = (unsigned)(tg * 64 + r) * 128u + (((unsigned)(2 * ks + hi)) ^ sw) * 16u; }
  const unsigned waddr = 32768u + (unsigned)kv * 16384u + (unsigned)hi * 2048u + (unsigned)((lane >> 2) & 3) * 64u + (unsigned)((lane >> 4) & 1) * 32u + (unsigned)(lane & 3) * 8u;
  f32x16 acc[2][4];
#pragma unroll
  for (int a = 0; a < 2; ++a)
#pragma unroll
    for (int b = 0; b < 4; ++b) acc[a][b] = (f32x16){};
  if (kv) k1_loop<true>(acc, lds, ckv_t, wk_t, wv_t, ldsb, avoff0, avoff1, wvoff, wid, aaddr, waddr);
  else    k1_loop<false>(acc, lds, ckv_t, wk_t, wv_t, ldsb, avoff0, avoff1, wvoff, wid, aaddr, waddr);
  char* ob = ws + (kv ? WS_V : WS_K) + (size_t)(h * 64 + tb * 8 + tg * 2) * 8192 + lane * 16;
#pragma unroll
  for (int a = 0; a < 2; ++a)
#pragma unroll
    for (int nb = 0; nb < 4; ++nb) {
      const u32x4 w0 = pack8n(acc[a][nb], 0), w1 = pack8n(acc[a][nb], 8);
      const int i0 = kv ? nb : 2 * nb, i1 = kv ? 4 + nb : 2 * nb + 1;
      *(u32x4*)(ob + a * 8192 + i0 * 1024) = w0;
      *(u32x4*)(ob + a * 8192 + i1 * 1024) = w1;
    }
}

constexpr int K2_LDS = 163840;
__global__ __launch_bounds__(512, 2) void k2_attn(const u16* __restrict__ wq, char* __restrict__ ws) {
  extern __shared__ __attribute__((aligned(16))) char smem[];
  const int tid = threadIdx.x, lane = tid & 63, r32 = lane & 31, hi = lane >> 5;
  const int wid = (int)rfl(tid >> 6), g = wid >> 2, r = wid & 3;
  const int bid = blockIdx.x, h = (bid & 7) * 4 + ((bid >> 3) & 3), p = bid >> 5;
  const unsigned ldsb = (unsigned)(size_t)smem;
  lds_cp lds = (lds_cp)smem;
  const int blkA = p, blkB = 15 - p;
  const int nA = g ? 100 : 2 * p + 2;
  const int kt0 = g ? 15 - 2 * p : 0;
  const char* Kh = ws + WS_K + (size_t)h * 64 * 8192 + r * 4096;
  const char* Vh = ws + WS_V + (size_t)h * 64 * 8192 + r * 4096;
  const unsigned lane16 = (unsigned)lane * 16u;
  const unsigned kreg = (unsigned)g * 16384u, vreg = 65536u + (unsigned)g * 16384u;
#define KT(i) (((i) < nA) ? (kt0 + (i)) : ((i) - nA))
#define DMA_K(i) glds4(Kh + (size_t)KT(i) * 16384, lane16, ldsb + kreg + (unsigned)((i) & 1) * 32768u + (unsigned)r * 4096u)
#define DMA_V(i) glds4(Vh + (size_t)KT(i) * 16384, lane16, ldsb + vreg + (unsigned)((i) & 1) * 32768u + (unsigned)r * 4096u)

  bf16x8 qf[8];
  {
    const int tb32 = (g ? blkB : blkA) * 4 + r;
    const char* xb = ws + WS_XN + (size_t)tb32 * 36 * 1024;
    const char* wq_t = (const char*)wq + (size_t)(wid * 8) * 8192 + h * 256;
    const unsigned wvoff = (unsigned)((lane >> 2) & 7) * 8192u + (unsigned)(lane >> 5) * 64u + (unsigned)(lane & 3) * 16u;
    bf16x8 xf[36];
#pragma unroll
    for (int c = 0; c < 9; ++c) {
      const unsigned wd = ldsb + (unsigned)c * 16384u + (unsigned)wid * 2048u;
      glds<0>(wq_t + (size_t)c * 64 * 8192, wvoff, wd); glds<0>(wq_t + (size_t)c * 64 * 8192 + 128, wvoff, wd + 1024u);
      asm volatile("global_load_dwordx4 %0, %4, %5\n\tglobal_load_dwordx4 %1, %4, %5 offset:1024\n\t"
                   "global_load_dwordx4 %2, %4, %5 offset:2048\n\tglobal_load_dwordx4 %3, %4, %5 offset:3072"
                   : "=&v"(xf[4 * c]), "=&v"(xf[4 * c + 1]), "=&v"(xf[4 * c + 2]), "=&v"(xf[4 * c + 3])
                   : "v"(lane16), "s"(xb + c * 4096) : "memory");
    }
    const unsigned waddr = (unsigned)hi * 2048u + (unsigned)((lane >> 2) & 3) * 64u + (unsigned)((lane >> 4) & 1) * 32u + (unsigned)(lane & 3) * 8u;
    f32x16 qa[4];
#pragma unroll
    for (int b = 0; b < 4; ++b) qa[b] = (f32x16){};
#pragma unroll
    for (int c = 0; c < 9; ++c) {
      constexpr int dummy = 0; (void)dummy;
      const int N = 6 * (8 - c) + 4 * ((c > 2) + (c > 4) + (c > 6));
      asm volatile("s_waitcnt vmcnt(%4) lgkmcnt(0)\n\ts_barrier" : "+v"(xf[4 * c]), "+v"(xf[4 * c + 1]), "+v"(xf[4 * c + 2]), "+v"(xf[4 * c + 3]) : "i"(N) : "memory");
      if (c == 2) DMA_K(0);
      if (c == 4) DMA_K(1);
      if (c == 6) DMA_V(0);
      lds_cp wb = lds + c * 16384 + waddr;
#pragma unroll
      for (int ks = 0; ks < 4; ++ks)
#pragma unroll
        for (int nb = 0; nb < 4; ++nb) {
          bf16x8 w = lds_tr2(wb + ks * 4096 + nb * 512, wb + ks * 4096 + nb * 512 + 256);
          qa[nb] = MFMA32(w, xf[4 * c + ks], qa[nb]);
        }
    }
#pragma unroll
    for (int nb = 0; nb < 4; ++nb) {
      qf[2 * nb] = __builtin_bit_cast(bf16x8, pack8(qa[nb], 0, QSCALE));
      qf[2 * nb + 1] = __builtin_bit_cast(bf16x8, pack8(qa[nb], 8, QSCALE));
    }
    WAITL0(); BARRIER();
    if (g) {
#pragma unroll
      for (int s = 0; s < 8; ++s) *(LAS bf16x8*)((lds_p)smem + 131072 + r * 8192 + s * 1024 + lane16) = qf[s];
    }
    asm volatile("s_waitcnt vmcnt(0) lgkmcnt(0)\n\ts_barrier" ::: "memory");
  }

  f32x16 o[4], sc[2], negm = (f32x16){};
  bf16x8 pf[4];
#pragma unroll
  for (int b = 0; b < 4; ++b) o[b] = (f32x16){};
  float mrow = 0.f, lsum = 0.f;
  bool first = true, actPrev = false;
  const int rowl = r * 32 + r32 - 4 * hi;
  if (g) BARRIER();
#pragma unroll 1
  for (int i = 0; i < 17; ++i) {
    const int last = g ? 16 : nA - 1;
    const int dt = (i == last) ? 1 : ((i == last - 1) ? 0 : -1);
    const bool act = !(dt == 1 && r < 2);
    const bool needmask = (dt == 0 && r < 2) || (dt == 1 && r >= 2);
    if (act) {
      lds_cp kb = lds + kreg + (unsigned)(i & 1) * 32768u + lane16;
#pragma unroll
      for (int s = 0; s < 8; ++s) {
        bf16x8 k0 = lds_rd128(kb + s * 1024), k1 = lds_rd128(kb + 8192 + s * 1024);
        sc[0] = MFMA32(k0, qf[s], s == 0 ? negm : sc[0]);
        sc[1] = MFMA32(k1, qf[s], s == 0 ? negm : sc[1]);
      }
    }
    if (actPrev) {
      lds_cp vb = lds + vreg + (unsigned)((i - 1) & 1) * 32768u + lane16;
#pragma unroll
      for (int ks = 0; ks < 4; ++ks)
#pragma unroll
        for (int db = 0; db < 4; ++db) { bf16x8 v = lds_rd128(vb + (ks * 4 + db) * 1024); o[db] = MFMA32(v, pf[ks], o[db]); }
    }
    asm volatile("s_waitcnt vmcnt(0) lgkmcnt(0)\n\ts_barrier" ::: "memory");
    if (i + 2 < 17) DMA_K(i + 2);
    if (i + 1 < 17) DMA_V(i + 1);
    if (i == nA) {
      const float inv = 1.0f / xhalf_sum(lsum);
      char* cb = ws + WS_CTX + (size_t)((blkA * 4 + r) * 2 + (r32 >> 4)) * 131072 + (size_t)h * 4096 + (hi * 16 + (r32 & 15)) * 16;
#pragma unroll
      for (int db = 0; db < 4; ++db) {
        *(u32x4*)(cb + db * 1024) = pack8(o[db], 0, inv);
        *(u32x4*)(cb + db * 1024 + 512) = pack8(o[db], 8, inv);
        o[db] = (f32x16){};
      }
      lsum = 0.f; mrow = 0.f;
    }
    if (act) {
      if (needmask) {
        const int dq = rowl - 64 * dt;
#pragma unroll
        for (int e = 0; e < 16; ++e) {
          const int c = (e & 3) + 8 * (e >> 2);
          if (c > dq) sc[0][e] = -INFINITY;
          if (c + 32 > dq) sc[1][e] = -INFINITY;
        }
      }
      float a = fmaxf(fmaxf(sc[0][0], sc[0][1]), sc[1][0]), b = fmaxf(fmaxf(sc[0][2], sc[0][3]), sc[1][1]);
      a = fmaxf(fmaxf(a, sc[1][2]), sc[1][3]);
#pragma unroll
      for (int e = 4; e < 16; e += 4) {
        a = fmaxf(fmaxf(a, sc[0][e]), sc[0][e + 1]); b = fmaxf(fmaxf(b, sc[0][e + 2]), sc[0][e + 3]);
        a = fmaxf(fmaxf(a, sc[1][e]), sc[1][e + 1]); b = fmaxf(fmaxf(b, sc[1][e + 2]), sc[1][e + 3]);
      }
      const float rm = xhalf_max(fmaxf(a, b));
      if (first || __any(rm > THR)) {
        const float dl = first ? rm : fmaxf(rm, 0.f);
        mrow += dl;
#pragma unroll
        for (int e = 0; e < 16; ++e) { sc[0][e] -= dl; sc[1][e] -= dl; negm[e] = -mrow; }
        if (!first) {
          const float f = __builtin_amdgcn_exp2f(-dl);
          lsum *= f;
#pragma unroll
          for (int db = 0; db < 4; ++db)
#pragma unroll
            for (int e = 0; e < 16; ++e) o[db][e] *= f;
        }
        first = false;
      }
      float s0 = 0.f, s1 = 0.f;
#pragma unroll
      for (int e = 0; e < 16; ++e) {
        sc[0][e] = __builtin_amdgcn_exp2f(sc[0][e]); sc[1][e] = __builtin_amdgcn_exp2f(sc[1][e]);
        s0 += sc[0][e]; s1 += sc[1][e];
      }
      lsum += s0 + s1;
      pf[0] = __builtin_bit_cast(bf16x8, pack8n(sc[0], 0)); pf[1] = __builtin_bit_cast(bf16x8, pack8n(sc[0], 8));
      pf[2] = __builtin_bit_cast(bf16x8, pack8n(sc[1], 0)); pf[3] = __builtin_bit_cast(bf16x8, pack8n(sc[1], 8));
    }
    actPrev = act;
    if (i == nA - 1) {
#pragma unroll
      for (int s = 0; s < 8; ++s) qf[s] = lds_rd128(lds + 131072 + r * 8192 + s * 1024 + lane16);
      negm = (f32x16){};
      first = true;
    }
    WAITL0(); BARRIER();
  }
  if (actPrev) {
    lds_cp vb = lds + vreg + lane16;
#pragma unroll
    for (int ks = 0; ks < 4; ++ks)
#pragma unroll
      for (int db = 0; db < 4; ++db) { bf16x8 v = lds_rd128(vb + (ks * 4 + db) * 1024); o[db] = MFMA32(v, pf[ks], o[db]); }
  }
  WAITL0(); BARRIER();
  if (!g) BARRIER();
  {
    lsum = xhalf_sum(lsum);
    lds_p xo = (lds_p)smem + wid * 9216 + lane16;
    if (g) { f32x16 t0 = o[0], t1 = o[1]; o[0] = o[2]; o[1] = o[3]; o[2] = t0; o[3] = t1; }
#pragma unroll
    for (int j = 0; j < 2; ++j) {
      const f32x16 t = o[2 + j];
#pragma unroll
      for (int e = 0; e < 4; ++e) *(LAS f32x4*)(xo + (j * 4 + e) * 1024) = (f32x4){t[4 * e], t[4 * e + 1], t[4 * e + 2], t[4 * e + 3]};
    }
    *(LAS f32x2*)((lds_p)smem + wid * 9216 + 8192 + lane * 8) = (f32x2){mrow, lsum};
    WAITL0(); BARRIER();
    lds_cp xi = lds + (wid ^ 4) * 9216 + lane16;
    const f32x2 ml = *(LAS const f32x2*)(lds + (wid ^ 4) * 9216 + 8192 + lane * 8);
    const float m = fmaxf(mrow, ml[0]);
    const float a0 = __builtin_amdgcn_exp2f(mrow - m), a1 = __builtin_amdgcn_exp2f(ml[0] - m);
    const float inv = 1.0f / (a0 * lsum + a1 * ml[1]);
    const float f0 = a0 * inv, f1 = a1 * inv;
    char* cb = ws + WS_CTX + (size_t)((blkB * 4 + r) * 2 + (r32 >> 4)) * 131072 + (size_t)h * 4096 + (hi * 16 + (r32 & 15)) * 16 + g * 2048;
#pragma unroll
    for (int j = 0; j < 2; ++j) {
      const f32x16 t = o[j];
      f32x16 u;
#pragma unroll
      for (int e = 0; e < 4; ++e) {
        const f32x4 w = *(LAS const f32x4*)(xi + (j * 4 + e) * 1024);
#pragma unroll
        for (int k = 0; k < 4; ++k) u[4 * e + k] = t[4 * e + k] * f0 + w[k] * f1;
      }
      *(u32x4*)(cb + j * 1024) = pack8n(u, 0);
      *(u32x4*)(cb + j * 1024 + 512) = pack8n(u, 8);
    }
  }
#undef KT
#undef DMA_K
#undef DMA_V
}

constexpr int K3_STAGE = 16384 + 18432, K3_LDS = 4 * K3_STAGE;
template <int KTOT, bool PERM32>
__global__ __launch_bounds__(512, 2) void k3_splitk(const char* __restrict__ act, const u16* __restrict__ W, float* __restrict__ part) {
  extern __shared__ __attribute__((aligned(16))) char smem[];
  constexpr int KC = KTOT / 4, NST = KC / 64, NS32 = KTOT / 32;
  const int tid = threadIdx.x, lane = tid & 63;
  const int wid = (int)rfl(tid >> 6), tg = wid & 3, kh = wid >> 2;
  const int bid = blockIdx.x, xcd = bid & 7, j = bid >> 3;
  const int split = xcd >> 1, tb = (xcd & 1) * 8 + (j >> 2), nc = j & 3;
  const unsigned ldsb = (unsigned)(size_t)smem;
  lds_cp lds = (lds_cp)smem;
  const unsigned lane16 = (unsigned)lane * 16u;
  const char* a_src = act + ((size_t)(tb * 8 + wid) * NS32 + (size_t)split * (KC / 32)) * 1024;
  const char* w_src = (const char*)W + (size_t)split * KC * (DM * 2) + nc * 288;
  unsigned wv[3];
#pragma unroll
  for (int q = 0; q < 3; ++q) { const int c = 64 * (wid + 8 * q) + lane; const int row = c / 18, cc = c - row * 18; wv[q] = (unsigned)row * (DM * 2) + (unsigned)cc * 16u; }
#define STAGE(t) do { const unsigned sb = ldsb + (unsigned)((t) & 3) * K3_STAGE; \
    glds2(a_src + (size_t)(t) * 2048, lane16, sb + (unsigned)wid * 2048u); \
    const char* ws_ = w_src + (size_t)(t) * 64 * (DM * 2); \
    glds<0>(ws_, wv[0], sb + 16384u + (unsigned)wid * 1024u); glds<0>(ws_, wv[1], sb + 16384u + 8192u + (unsigned)wid * 1024u); \
    if (wid < 2) glds<0>(ws_, wv[2], sb + 16384u + 16384u + (unsigned)wid * 1024u); } while (0)
  STAGE(0); STAGE(1); STAGE(2);
  const int q = lane >> 4;
  const unsigned krow = PERM32 ? (unsigned)(16 * (q >> 1) + 4 * (q & 1)) : (unsigned)(4 * q);
  constexpr int R2 = PERM32 ? 8 * 288 : 16 * 288;
  const unsigned waddr = 16384u + (unsigned)kh * 9216u + (krow + (unsigned)((lane >> 2) & 3)) * 288u + (unsigned)(lane & 3) * 8u;
  const unsigned aaddr = (unsigned)tg * 4096u + (unsigned)kh * 1024u + lane16;
  f32x4 acc[2][9];
#pragma unroll
  for (int a = 0; a < 2; ++a)
#pragma unroll
    for (int n = 0; n < 9; ++n) acc[a][n] = (f32x4){};
#pragma unroll 1
  for (int t = 0; t < NST; ++t) {
    if (t + 2 < NST) { if (wid < 2) WAITV(10); else WAITV(8); }
    else if (t + 1 < NST) { if (wid < 2) WAITV(5); else WAITV(4); }
    else WAITV(0);
    WAITL0(); BARRIER();
    if (t + 3 < NST) STAGE(t + 3);
    lds_cp sb = lds + (unsigned)(t & 3) * K3_STAGE;
    bf16x8 a0 = lds_rd128(sb + aaddr), a1 = lds_rd128(sb + aaddr + 2048);
#pragma unroll
    for (int n = 0; n < 9; ++n) {
      bf16x8 w = lds_tr2(sb + waddr + n * 32, sb + waddr + n * 32 + R2);
      acc[0][n] = MFMA16(w, a0, acc[0][n]); acc[1][n] = MFMA16(w, a1, acc[1][n]);
    }
  }
#undef STAGE
  WAITL0(); BARRIER();
  lds_p xo = (lds_p)smem + wid * 9216 + lane16;
#pragma unroll
  for (int n = 0; n < 9; ++n) *(LAS f32x4*)(xo + n * 1024) = kh ? acc[0][n] : acc[1][n];
  WAITL0(); BARRIER();
  lds_cp xi = lds + (wid ^ 4) * 9216 + lane16;
  char* ob = (char*)part + (((size_t)split * 128 + tb * 8 + tg * 2 + kh) * 36 + nc * 9) * 1024 + lane16;
#pragma unroll
  for (int n = 0; n < 9; ++n) {
    const f32x4 w = *(LAS const f32x4*)(xi + n * 1024);
    *(f32x4*)(ob + n * 1024) = (kh ? acc[1][n] : acc[0][n]) + w;
  }
}

__global__ __launch_bounds__(576) void k4_reduce_ln(const u16* __restrict__ x, char* __restrict__ ws) {
  __shared__ float red[2][9][8];
  const int t = threadIdx.x, tok = t & 7, q = (t >> 3) & 3, s = t >> 5, wv = t >> 6;
  const int tb16 = blockIdx.x >> 1, tok16 = (blockIdx.x & 1) * 8 + tok, row = tb16 * 16 + tok16;
  const size_t off = ((size_t)tb16 * 36 + 2 * s) * 1024 + (q * 16 + tok16) * 16;
  f32x4 a = (f32x4){}, b = (f32x4){};
#pragma unroll
  for (int sp = 0; sp < 4; ++sp) {
    const char* pp = ws + WS_P1 + (size_t)sp * S * DM * 4 + off;
    a += *(const f32x4*)pp; b += *(const f32x4*)(pp + 1024);
  }
  const u32x2 x0 = *(const u32x2*)(x + (size_t)row * DM + 32 * s + 4 * q), x1 = *(const u32x2*)(x + (size_t)row * DM + 32 * s + 16 + 4 * q);
  a += (f32x4){bf_lo(x0[0]), bf_hi(x0[0]), bf_lo(x0[1]), bf_hi(x0[1])};
  b += (f32x4){bf_lo(x1[0]), bf_hi(x1[0]), bf_lo(x1[1]), bf_hi(x1[1])};
  *(f32x4*)(ws + WS_H + off) = a; *(f32x4*)(ws + WS_H + off + 1024) = b;
  float sm = (a[0] + a[1]) + (a[2] + a[3]) + (b[0] + b[1]) + (b[2] + b[3]);
  sm += __shfl_xor(sm, 8); sm += __shfl_xor(sm, 16); sm += __shfl_xor(sm, 32);
  if ((t & 63) < 8) red[0][wv][tok] = sm;
  __syncthreads();
  float tot = 0.f;
#pragma unroll
  for (int w = 0; w < 9; ++w) tot += red[0][w][tok];
  const float mean = tot * (1.0f / DM);
  a -= mean; b -= mean;
  float sq = (a[0] * a[0] + a[1] * a[1]) + (a[2] * a[2] + a[3] * a[3]) + (b[0] * b[0] + b[1] * b[1]) + (b[2] * b[2] + b[3] * b[3]);
  sq += __shfl_xor(sq, 8); sq += __shfl_xor(sq, 16); sq += __shfl_xor(sq, 32);
  if ((t & 63) < 8) red[1][wv][tok] = sq;
  __syncthreads();
  float tq = 0.f;
#pragma unroll
  for (int w = 0; w < 9; ++w) tq += red[1][w][tok];
  const float rstd = 1.0f / sqrtf(tq * (1.0f / DM) + 1e-5f);
  u32x4 o; o[0] = cvtpk(a[0] * rstd, a[1] * rstd); o[1] = cvtpk(a[2] * rstd, a[3] * rstd); o[2] = cvtpk(b[0] * rstd, b[1] * rstd); o[3] = cvtpk(b[2] * rstd, b[3] * rstd);
  *(u32x4*)(ws + WS_HN + ((size_t)tb16 * 18 + s) * 1024 + (q * 16 + tok16) * 16) = o;
}

constexpr int K5_STAGE = 32768 + 18432, K5_LDS = 3 * K5_STAGE;
__global__ __launch_bounds__(512, 2) void k5_ffn1(const u16* __restrict__ W, const u16* __restrict__ b1, char* __restrict__ ws) {
  extern __shared__ __attribute__((aligned(16))) char smem[];
  const int tid = threadIdx.x, lane = tid & 63;
  const int wid = (int)rfl(tid >> 6);
  const int bid = blockIdx.x, nc = (bid & 7) * 4 + ((bid >> 3) & 3), tb = bid >> 5;
  const unsigned ldsb = (unsigned)(size_t)smem;
  lds_cp lds = (lds_cp)smem;
  const unsigned lane16 = (unsigned)lane * 16u;
  const char* a_src = ws + WS_HN + (size_t)(tb * 16 + wid * 2) * 18 * 1024;
  const char* w_src = (const char*)W + nc * 288;
  unsigned wv[3];
#pragma unroll
  for (int q = 0; q < 3; ++q) { const int c = 64 * (wid + 8 * q) + lane; const int row = c / 18, cc = c - row * 18; wv[q] = (unsigned)row * (FF * 2) + (unsigned)cc * 16u; }
#define STAGE(t, slot) do { const unsigned sb = ldsb + (unsigned)(slot) * K5_STAGE; \
    glds2(a_src + (size_t)(t) * 2048, lane16, sb + (unsigned)wid * 4096u); \
    glds2(a_src + 18 * 1024 + (size_t)(t) * 2048, lane16, sb + (unsigned)wid * 4096u + 2048u); \
    const char* ws_ = w_src + (size_t)(t) * 64 * (FF * 2); \
    glds<0>(ws_, wv[0], sb + 32768u + (unsigned)wid * 1024u); glds<0>(ws_, wv[1], sb + 32768u + 8192u + (unsigned)wid * 1024u); \
    if (wid < 2) glds<0>(ws_, wv[2], sb + 32768u + 16384u + (unsigned)wid * 1024u); } while (0)
  STAGE(0, 0); STAGE(1, 1);
  const int q = lane >> 4;
  const unsigned waddr = 32768u + ((unsigned)(4 * q) + (unsigned)((lane >> 2) & 3)) * 288u + (unsigned)(lane & 3) * 8u;
  const unsigned aaddr = (unsigned)wid * 4096u + lane16;
  f32x4 acc[2][9];
#pragma unroll
  for (int a = 0; a < 2; ++a)
#pragma unroll
    for (int n = 0; n < 9; ++n) acc[a][n] = (f32x4){};
  int slot = 0;
#pragma unroll 1
  for (int t = 0; t < 9; ++t) {
    if (t + 1 < 9) { if (wid < 2) WAITV(7); else WAITV(6); }
    else WAITV(0);
    WAITL0(); BARRIER();
    if (t + 2 < 9) { const int s2 = slot == 0 ? 2 : slot - 1; STAGE(t + 2, s2); }
    lds_cp sb = lds + (unsigned)slot * K5_STAGE;
#pragma unroll
    for (int ks = 0; ks < 2; ++ks) {
      bf16x8 a0 = lds_rd128(sb + aaddr + ks * 1024), a1 = lds_rd128(sb + aaddr + 2048 + ks * 1024);
#pragma unroll
      for (int n = 0; n < 9; ++n) {
        bf16x8 w = lds_tr2(sb + waddr + ks * 9216 + n * 32, sb + waddr + ks * 9216 + n * 32 + 4608);
        acc[0][n] = MFMA16(w, a0, acc[0][n]); acc[1][n] = MFMA16(w, a1, acc[1][n]);
      }
    }
    slot = slot == 2 ? 0 : slot + 1;
  }
#undef STAGE
  unsigned pk[2][9][2];
#pragma unroll
  for (int n = 0; n < 9; ++n) {
    const u32x2 bb = *(const u32x2*)(b1 + nc * 144 + n * 16 + 4 * q);
    const f32x4 bv = {bf_lo(bb[0]), bf_hi(bb[0]), bf_lo(bb[1]), bf_hi(bb[1])};
#pragma unroll
    for (int a = 0; a < 2; ++a) {
      f32x4 v = acc[a][n] + bv;
#pragma unroll
      for (int k = 0; k < 4; ++k) { const float z = fmaxf(v[k], 0.f); v[k] = z * z; }
      pk[a][n][0] = cvtpk(v[0], v[1]); pk[a][n][1] = cvtpk(v[2], v[3]);
    }
  }
  const int T0 = nc * 9;
#pragma unroll
  for (int a = 0; a < 2; ++a) {
    char* fb = ws + WS_F + (size_t)(tb * 16 + wid * 2 + a) * 144 * 1024 + lane16;
    if ((nc & 1) == 0) {
#pragma unroll
      for (int n = 0; n < 8; n += 2) *(u32x4*)(fb + ((T0 + n) >> 1) * 1024) = (u32x4){pk[a][n][0], pk[a][n][1], pk[a][n + 1][0], pk[a][n + 1][1]};
      *(u32x2*)(fb + ((T0 + 8) >> 1) * 1024) = (u32x2){pk[a][8][0], pk[a][8][1]};
    } else {
      *(u32x2*)(fb + (T0 >> 1) * 1024 + 8) = (u32x2){pk[a][0][0], pk[a][0][1]};
#pragma unroll
      for (int n = 1; n < 9; n += 2) *(u32x4*)(fb + ((T0 + n) >> 1) * 1024) = (u32x4){pk[a][n][0], pk[a][n][1], pk[a][n + 1][0], pk[a][n + 1][1]};
    }
  }
}

__global__ __launch_bounds__(576) void k7_final(const u16* __restrict__ b2, const char* __restrict__ ws, u16* __restrict__ out) {
  const int t = threadIdx.x, tok = t & 7, q = (t >> 3) & 3, s = t >> 5;
  const int tb16 = blockIdx.x >> 1, tok16 = (blockIdx.x & 1) * 8 + tok, row = tb16 * 16 + tok16;
  const size_t off = ((size_t)tb16 * 36 + 2 * s) * 1024 + (q * 16 + tok16) * 16;
  f32x4 a = *(const f32x4*)(ws + WS_H + off), b = *(const f32x4*)(ws + WS_H + off + 1024);
#pragma unroll
  for (int sp = 0; sp < 4; ++sp) {
    const char* pp = ws + WS_P2 + (size_t)sp * S * DM * 4 + off;
    a += *(const f32x4*)pp; b += *(const f32x4*)(pp + 1024);
  }
  const u32x2 x0 = *(const u32x2*)(b2 + 32 * s + 4 * q), x1 = *(const u32x2*)(b2 + 32 * s + 16 + 4 * q);
  a += (f32x4){bf_lo(x0[0]), bf_hi(x0[0]), bf_lo(x0[1]), bf_hi(x0[1])};
  b += (f32x4){bf_lo(x1[0]), bf_hi(x1[0]), bf_lo(x1[1]), bf_hi(x1[1])};
  *(u32x2*)(out + (size_t)row * DM + 32 * s + 4 * q) = (u32x2){cvtpk(a[0], a[1]), cvtpk(a[2], a[3])};
  *(u32x2*)(out + (size_t)row * DM + 32 * s + 16 + 4 * q) = (u32x2){cvtpk(b[0], b[1]), cvtpk(b[2], b[3])};
}

extern "C" void kernel_launch(void* const* d_in, const int* in_sizes, int n_in, void* d_out, int out_size, void* d_ws, size_t ws_size,
                              hipStream_t stream) {
  const u16* x = (const u16*)d_in[0];   const u16* ckv = (const u16*)d_in[1]; const u16* wq = (const u16*)d_in[2];
  const u16* wuk = (const u16*)d_in[3]; const u16* wuv = (const u16*)d_in[4]; const u16* wo = (const u16*)d_in[5];
  const u16* w1 = (const u16*)d_in[7];  const u16* b1 = (const u16*)d_in[8];  const u16* w2 = (const u16*)d_in[9];
  const u16* b2 = (const u16*)d_in[10];
  char* ws = (char*)d_ws;
  (void)hipFuncSetAttribute((const void*)k1_kvproj, hipFuncAttributeMaxDynamicSharedMemorySize, K1_LDS);
  (void)hipFuncSetAttribute((const void*)k2_attn, hipFuncAttributeMaxDynamicSharedMemorySize, K2_LDS);
  (void)hipFuncSetAttribute((const void*)k3_splitk<NHD, true>, hipFuncAttributeMaxDynamicSharedMemorySize, K3_LDS);
  (void)hipFuncSetAttribute((const void*)k3_splitk<FF, false>, hipFuncAttributeMaxDynamicSharedMemorySize, K3_LDS);
  (void)hipFuncSetAttribute((const void*)k5_ffn1, hipFuncAttributeMaxDynamicSharedMemorySize, K5_LDS);
  k1_kvproj<<<256, 512, K1_LDS, stream>>>(x, ckv, wuk, wuv, ws);
  k2_attn<<<256, 512, K2_LDS, stream>>>(wq, ws);
  k3_splitk<NHD, true><<<256, 512, K3_LDS, stream>>>(ws + WS_CTX, wo, (float*)(ws + WS_P1));
  k4_reduce_ln<<<256, 576, 0, stream>>>(x, ws);
  k5_ffn1<<<256, 512, K5_LDS, stream>>>(w1, b1, ws);
  k3_splitk<FF, false><<<256, 512, K3_LDS, stream>>>(ws + WS_F, w2, (float*)(ws + WS_P2));
  k7_final<<<256, 576, 0, stream>>>(b2, ws, (u16*)d_out);
}
```

```cpp
#include <hip/hip_runtime.h>
#include <hip/hip_bf16.h>
#include <cstdint>

typedef unsigned short u16;
typedef __bf16 bf16x8 __attribute__((ext_vector_type(8)));
typedef __bf16 bf16x2 __attribute__((ext_vector_type(2)));
typedef short s16x4 __attribute__((ext_vector_type(4)));
typedef short s16x8 __attribute__((ext_vector_type(8)));
typedef float f32x2 __attribute__((ext_vector_type(2)));
typedef float f32x4 __attribute__((ext_vector_type(4)));
typedef float f32x16 __attribute__((ext_vector_type(16)));
typedef unsigned u32x2 __attribute__((ext_vector_type(2)));
typedef unsigned u32x4 __attribute__((ext_vector_type(4)));
#define LAS __attribute__((address_space(3)))
typedef LAS const char* lds_cp;
typedef LAS char* lds_p;
#define DI __device__ __forceinline__

constexpr int S = 2048, DM = 576, NH = 32, HD = 128, DC = 512, FF = 4608, NHD = NH * HD;
constexpr float QSCALE = 0.08838834764831845f * 1.4426950408889634f;
constexpr float PSUM_MAX = 65536.0f;

constexpr size_t WS_XN  = 0;
constexpr size_t WS_K   = WS_XN + (size_t)S * DM * 2;
constexpr size_t WS_V   = WS_K + (size_t)S * NHD * 2;
constexpr size_t WS_CTX = WS_V + (size_t)S * NHD * 2;
constexpr size_t WS_P1  = WS_CTX + (size_t)S * NHD * 2;
constexpr size_t WS_H   = WS_P1 + (size_t)4 * S * DM * 2;
constexpr size_t WS_HN  = WS_H + (size_t)S * DM * 2;
constexpr size_t WS_F   = WS_HN + (size_t)S * DM * 2;
constexpr size_t WS_P2  = WS_F + (size_t)S * FF * 2;
constexpr size_t WS_END = WS_P2 + (size_t)4 * S * DM * 2;

#define STAMP(wsp, kid, idx) do {} while (0)
#define STAMPW() do {} while (0)
#define TICK(v) do {} while (0)
#define ACC_STORE(wsp, kid, idx, val) do {} while (0)
#define FSTAMP(wsp, kid, idx) do {} while (0)
DI unsigned rfl(unsigned x) { return __builtin_amdgcn_readfirstlane(x); }
DI unsigned cvtpk(float lo, float hi) { f32x2 v = {lo, hi}; bf16x2 b = __builtin_convertvector(v, bf16x2); return __builtin_bit_cast(unsigned, b); }
DI float bf_lo(unsigned w) { return __uint_as_float(w << 16); }
DI float bf_hi(unsigned w) { return __uint_as_float(w & 0xffff0000u); }
#define SBAR() __builtin_amdgcn_sched_barrier(0)
#define BARRIER() asm volatile("s_barrier" ::: "memory")
#define WAITV(n) asm volatile("s_waitcnt vmcnt(%0)" :: "i"(n) : "memory")
#define WAITL(n) asm volatile("s_waitcnt lgkmcnt(%0)" :: "i"(n) : "memory")
#define MFMA(a, b, c) __builtin_amdgcn_mfma_f32_16x16x32_bf16(a, b, c, 0, 0, 0)
#define MFMA32(a, b, c) __builtin_amdgcn_mfma_f32_32x32x16_bf16(a, b, c, 0, 0, 0)

#define STPOL 2
DI void st16(void* p, u32x4 v) {
  asm volatile("global_store_dwordx4 %0, %1, off sc1\n\ts_nop 1" :: "v"(p), "v"(v) : "memory");
}

DI void st8(void* p, u32x2 v) {
  *(u32x2*)p = v;
}
DI void store9(char* fb, int T0, const unsigned (&pk)[9][2]) {
  if ((T0 & 1) == 0) {
#pragma unroll
    for (int n = 0; n < 8; n += 2) st16(fb + ((T0 + n) >> 1) * 1024, (u32x4){pk[n][0], pk[n][1], pk[n + 1][0], pk[n + 1][1]});
    st8(fb + ((T0 + 8) >> 1) * 1024, (u32x2){pk[8][0], pk[8][1]});
  } else {
    st8(fb + (T0 >> 1) * 1024 + 8, (u32x2){pk[0][0], pk[0][1]});
#pragma unroll
    for (int n = 1; n < 9; n += 2) st16(fb + ((T0 + n) >> 1) * 1024, (u32x4){pk[n][0], pk[n][1], pk[n + 1][0], pk[n + 1][1]});
  }
}
template <int OFF> DI void glds(const void* sbase, unsigned voff, unsigned ldst) {
  unsigned keep;
  asm volatile("s_mov_b32 %0, m0\n\ts_mov_b32 m0, %3\n\ts_nop 0\n\tglobal_load_lds_dwordx4 %1, %2 offset:%4\n\ts_mov_b32 m0, %0"
               : "=&s"(keep) : "v"(voff), "s"(sbase), "s"(ldst), "i"(OFF) : "memory");
}
template <int OFF> DI void gld(bf16x8& d, const void* sbase, unsigned voff) {
  asm volatile("global_load_dwordx4 %0, %1, %2 offset:%3" : "=v"(d) : "v"(voff), "s"(sbase), "i"(OFF) : "memory");
}
DI bf16x8 lds_rd128(lds_cp p) { return *(LAS const bf16x8*)p; }
DI s16x4 lds_tr(lds_cp p) { return __builtin_amdgcn_ds_read_tr16_b64_v4i16((LAS s16x4*)p); }
DI bf16x8 lds_tr2(lds_cp p0, lds_cp p1) {
  s16x4 a = lds_tr(p0), b = lds_tr(p1);
  s16x8 w = __builtin_shufflevector(a, b, 0, 1, 2, 3, 4, 5, 6, 7);
  return __builtin_bit_cast(bf16x8, w);
}
DI u32x4 pack2(const f32x4& a, const f32x4& b) { return (u32x4){cvtpk(a[0], a[1]), cvtpk(a[2], a[3]), cvtpk(b[0], b[1]), cvtpk(b[2], b[3])}; }
DI u32x4 pack2s(const f32x4& a, const f32x4& b, float s) {
  return (u32x4){cvtpk(a[0] * s, a[1] * s), cvtpk(a[2] * s, a[3] * s), cvtpk(b[0] * s, b[1] * s), cvtpk(b[2] * s, b[3] * s)};
}
DI float xq_max(float v) {
  auto a = __builtin_amdgcn_permlane16_swap(__float_as_uint(v), __float_as_uint(v), false, false);
  v = fmaxf(__uint_as_float(a[0]), __uint_as_float(a[1]));
  auto b = __builtin_amdgcn_permlane32_swap(__float_as_uint(v), __float_as_uint(v), false, false);
  return fmaxf(__uint_as_float(b[0]), __uint_as_float(b[1]));
}
DI float xq_sum(float v) {
  auto a = __builtin_amdgcn_permlane16_swap(__float_as_uint(v), __float_as_uint(v), false, false);
  v = __uint_as_float(a[0]) + __uint_as_float(a[1]);
  auto b = __builtin_amdgcn_permlane32_swap(__float_as_uint(v), __float_as_uint(v), false, false);
  return __uint_as_float(b[0]) + __uint_as_float(b[1]);
}
DI u32x4 pack8s(const f32x16& a, int b, float s) {
  return (u32x4){cvtpk(a[b] * s, a[b + 1] * s), cvtpk(a[b + 2] * s, a[b + 3] * s), cvtpk(a[b + 4] * s, a[b + 5] * s), cvtpk(a[b + 6] * s, a[b + 7] * s)};
}
DI u32x4 pack8(const f32x16& a, int b) { return (u32x4){cvtpk(a[b], a[b + 1]), cvtpk(a[b + 2], a[b + 3]), cvtpk(a[b + 4], a[b + 5]), cvtpk(a[b + 6], a[b + 7])}; }
DI float xh_max(float v) {
  auto b = __builtin_amdgcn_permlane32_swap(__float_as_uint(v), __float_as_uint(v), false, false);
  return fmaxf(__uint_as_float(b[0]), __uint_as_float(b[1]));
}
DI float xh_sum(float v) {
  auto b = __builtin_amdgcn_permlane32_swap(__float_as_uint(v), __float_as_uint(v), false, false);
  return __uint_as_float(b[0]) + __uint_as_float(b[1]);
}
DI void gtouch(unsigned& d, const void* sbase, unsigned voff) { asm volatile("global_load_dword %0, %1, %2" : "=v"(d) : "v"(voff), "s"(sbase) : "memory"); }
DI lds_cp opq(lds_cp p) { asm volatile("" : "+v"(p)); return p; }
DI float fadd_s(float a, float b) { float r; asm("v_add_f32_e32 %0, %1, %2" : "=v"(r) : "v"(a), "v"(b)); return r; }

constexpr int K1_LDS = 163840;
struct K1C {
  const char *a_src, *w_src; unsigned avoff0, avoff1, wvoff, a_dst, w_dst;
  unsigned ab[2], wb[2];
};
template <bool ISV, int PAR>
DI void k1_interval(const K1C& c, lds_cp lds, int j, unsigned aslot_rd, unsigned aslot_wr, f32x4 (&acc)[4][8], bf16x8 (&af)[4], bf16x8 (&afn)[4], bf16x8 (&wf)[4]) {
  const unsigned wcur = 98304u + (unsigned)(j & 3) * 16384u, wnx = 98304u + (unsigned)((j + 1) & 3) * 16384u;
  lds_cp wc0 = lds + wcur + c.wb[0], wc1 = lds + wcur + c.wb[1], wn0 = lds + wnx + c.wb[0], wn1 = lds + wnx + c.wb[1];
  lds_cp an = lds + aslot_rd + c.ab[PAR ^ 1];
  const char* wsrc = c.w_src + (size_t)(j + 3) * 32 * 8192;
  const unsigned wdst = c.w_dst + (unsigned)((j + 3) & 3) * 16384u;
  const char* asrc = c.a_src + (((j + 1) >> 1) + 2) * 128 + (PAR ? 0 : 16384);
  const unsigned adst = c.a_dst + aslot_wr + (PAR ? 0u : 2048u);
#pragma unroll
  for (int n = 0; n < 8; ++n) {
    const int m = n + 3;
    if (m < 8) { lds_cp p = ((m & 1) ? wc1 : wc0) + (m >> 1) * 512; wf[m & 3] = lds_tr2(p, p + 256); }
    else       { lds_cp p = ((m & 1) ? wn1 : wn0) + ((m - 8) >> 1) * 512; wf[m & 3] = lds_tr2(p, p + 256); }
    if (n < 4) afn[n] = lds_rd128(an + n * 2048);
    if (n == 0) { if (j <= 12) glds<0>(wsrc, c.wvoff, wdst); }
    if (n == 2) { if (j <= 12) glds<0>(wsrc + 128, c.wvoff, wdst + 1024u); }
    if (n == 4) { if (j <= 10) glds<0>(asrc, c.avoff0, adst); }
    if (n == 6) { if (j <= 10) glds<0>(asrc, c.avoff1, adst + 1024u); }
#pragma unroll
    for (int tt = 0; tt < 4; ++tt) acc[tt][n] = ISV ? MFMA(af[tt], wf[n & 3], acc[tt][n]) : MFMA(wf[n & 3], af[tt], acc[tt][n]);
    SBAR();
  }
}
template <bool ISV>
DI void k1_loop(const K1C& c, lds_cp lds, f32x4 (&acc)[4][8], char* ws) {
  bf16x8 af0[4], af1[4], wf[4];
  WAITV(10); BARRIER();
  STAMP(ws, 0, 5);
#pragma unroll
  for (int n = 0; n < 4; ++n) af0[n] = lds_rd128(lds + c.ab[0] + n * 2048);
#pragma unroll
  for (int m = 0; m < 3; ++m) { lds_cp p = lds + 98304 + c.wb[m & 1] + (m >> 1) * 512; wf[m] = lds_tr2(p, p + 256); }
  unsigned s0 = 0u, s1 = 32768u, s2 = 65536u;
#pragma unroll 1
  for (int a = 0; a < 8; ++a) {
    const int j = 2 * a;
    if (a > 0) {
      if (j <= 11) WAITV(4); else if (j <= 13) WAITV(2); else WAITV(0);
      WAITL(6); BARRIER();
    }
    FSTAMP(ws, 0, 8 + j);
    k1_interval<ISV, 0>(c, lds, j, s0, s2, acc, af0, af1, wf);
    if (j + 1 <= 11) WAITV(4); else if (j + 1 <= 13) WAITV(2); else WAITV(0);
    WAITL(6); BARRIER();
    FSTAMP(ws, 0, 9 + j);
    k1_interval<ISV, 1>(c, lds, j + 1, s1, s0, acc, af1, af0, wf);
    const unsigned t = s0; s0 = s1; s1 = s2; s2 = t;
  }
}

__global__ __launch_bounds__(512, 2) void k1_kvproj(const u16* __restrict__ x, const u16* __restrict__ ckv, const u16* __restrict__ wuk,
                                                    const u16* __restrict__ wuv, const u16* __restrict__ wq, const u16* __restrict__ wo,
                                                    const u16* __restrict__ w1, const u16* __restrict__ w2, char* __restrict__ ws) {
  extern __shared__ __attribute__((aligned(16))) char smem[];
  const int tid = threadIdx.x, lane = tid & 63, r16 = lane & 15, q = lane >> 4;
  const int wid = (int)rfl(tid >> 6);
  const int bid = blockIdx.x, h = (bid & 7) * 4 + ((bid >> 3) & 3), tb = bid >> 5;
  const unsigned ldsb = (unsigned)(size_t)smem;
  lds_cp lds = (lds_cp)smem;
  STAMP(ws, 0, 0);
  const int tg = wid & 3, kv = wid >> 2;
  K1C c;
  c.a_src = (const char*)ckv + (size_t)(tb * 256 + wid * 32) * 1024;
  c.avoff0 = (unsigned)(lane >> 3) * 1024u + (unsigned)(((lane & 7) ^ ((lane >> 4) & 7)) * 16);
  c.avoff1 = (unsigned)(8 + (lane >> 3)) * 1024u + (unsigned)(((lane & 7) ^ ((4 + (lane >> 4)) & 7)) * 16);
  c.a_dst = ldsb + (unsigned)wid * 4096u;
  c.w_src = (const char*)(kv ? wuv : wuk) + (size_t)(tg * 8) * 8192 + h * 256;
  c.wvoff = (unsigned)((lane >> 2) & 7) * 8192u + (unsigned)(lane >> 5) * 64u + (unsigned)((lane & 3) ^ ((tg & 1) << 1)) * 16u;
  c.w_dst = ldsb + 98304u + (unsigned)wid * 2048u;
#define K1_W(j) do { glds<0>(c.w_src + (size_t)(j) * 32 * 8192, c.wvoff, c.w_dst + (j) * 16384u); glds<0>(c.w_src + (size_t)(j) * 32 * 8192 + 128, c.wvoff, c.w_dst + (j) * 16384u + 1024u); } while (0)
#define K1_A(a, hf) do { glds<0>(c.a_src + (a) * 128 + (hf) * 16384, c.avoff0, c.a_dst + (a) * 32768u + (hf) * 2048u); \
                         glds<0>(c.a_src + (a) * 128 + (hf) * 16384, c.avoff1, c.a_dst + (a) * 32768u + (hf) * 2048u + 1024u); } while (0)
  const int row = bid * 8 + wid;
  bf16x8 xc0, xc1;
  gld<0>(xc0, (const char*)x + (size_t)row * (DM * 2), (unsigned)lane * 16u);
  gld<1024>(xc1, (const char*)x + (size_t)row * (DM * 2), (unsigned)(lane & 7) * 16u);
  K1_W(0); K1_A(0, 0); K1_A(0, 1); K1_W(1); K1_A(1, 0); K1_A(1, 1); K1_W(2); K1_A(2, 0);
  asm volatile("s_waitcnt vmcnt(16)" : "+v"(xc0), "+v"(xc1) :: "memory");
  {
    const u32x4 c0 = __builtin_bit_cast(u32x4, xc0), c1 = __builtin_bit_cast(u32x4, xc1);
    float v[16];
#pragma unroll
    for (int i = 0; i < 4; ++i) { v[2 * i] = bf_lo(c0[i]); v[2 * i + 1] = bf_hi(c0[i]); v[8 + 2 * i] = bf_lo(c1[i]); v[9 + 2 * i] = bf_hi(c1[i]); }
    float s = 0.f, s2 = 0.f;
#pragma unroll
    for (int i = 0; i < 8; ++i) { s += v[i]; s2 += v[8 + i]; }
    if (lane < 8) s += s2;
#pragma unroll
    for (int o = 32; o; o >>= 1) s += __shfl_xor(s, o);
    const float mean = s * (1.0f / DM);
    float qq = 0.f, q2 = 0.f;
#pragma unroll
    for (int i = 0; i < 8; ++i) { const float d = v[i] - mean, d2 = v[8 + i] - mean; qq += d * d; q2 += d2 * d2; }
    if (lane < 8) qq += q2;
#pragma unroll
    for (int o = 32; o; o >>= 1) qq += __shfl_xor(qq, o);
    const float rstd = 1.0f / sqrtf(qq * (1.0f / DM) + 1e-5f);
    char* xnb = ws + WS_XN + (size_t)(row >> 5) * 36 * 1024 + (row & 31) * 16;
    u32x4 o0, o1;
#pragma unroll
    for (int i = 0; i < 4; ++i) { o0[i] = cvtpk((v[2 * i] - mean) * rstd, (v[2 * i + 1] - mean) * rstd);
                                  o1[i] = cvtpk((v[8 + 2 * i] - mean) * rstd, (v[9 + 2 * i] - mean) * rstd); }
    st16(xnb + (lane >> 1) * 1024 + (lane & 1) * 512, o0);
    if (lane < 8) st16(xnb + (32 + (lane >> 1)) * 1024 + (lane & 1) * 512, o1);
  }
#undef K1_W
#undef K1_A
  STAMP(ws, 0, 1);
  { const unsigned sw = (unsigned)(r16 >> 1) & 7u;
    c.ab[0] = (unsigned)(tg * 64 + r16) * 128u + (((unsigned)q) ^ sw) * 16u;
    c.ab[1] = (unsigned)(tg * 64 + r16) * 128u + (((unsigned)(4 + q)) ^ sw) * 16u;
    const unsigned wb = (unsigned)kv * 8192u + (unsigned)q * 2048u + (unsigned)((lane >> 2) & 3) * 64u + (unsigned)(lane & 3) * 8u;
    c.wb[0] = wb + (unsigned)(q & 1) * 32u; c.wb[1] = wb + (unsigned)((q & 1) ^ 1) * 32u; }
  f32x4 acc[4][8];
#pragma unroll
  for (int a = 0; a < 4; ++a)
#pragma unroll
    for (int b = 0; b < 8; ++b) acc[a][b] = (f32x4){};
#define K1_WARM() unsigned t0; { const unsigned gw = (unsigned)(bid * 8 + wid); \
    const unsigned o36 = (unsigned)(lane < 36 ? lane : 0) * 64u, o41 = (unsigned)(lane < 41 ? lane : 0) * 64u; \
    asm volatile("global_load_dword %0, %1, %3\n\tglobal_load_dword %0, %1, %4\n\tglobal_load_dword %0, %2, %5\n\tglobal_load_dword %0, %2, %6" \
                 : "=&v"(t0) : "v"(o36), "v"(o41), "s"((const char*)wq + (size_t)gw * 2304), "s"((const char*)wo + (size_t)gw * 2304), \
                   "s"((const char*)w1 + (size_t)gw * 2592), "s"((const char*)w2 + (size_t)gw * 2592) : "memory"); }
#define K1_WARM_END() asm volatile("s_waitcnt vmcnt(0)" : "+v"(t0) :: "memory")
  if (kv) {
    k1_loop<true>(c, lds, acc, ws);
    STAMP(ws, 0, 2);
    char* ob = ws + WS_V + (size_t)(h * 64 + tb * 8 + tg * 2) * 8192 + (q >> 1) * 4096 + (q & 1) * 512 + ((r16 >> 3) * 16 + (r16 & 7)) * 16;
#pragma unroll
    for (int ks = 0; ks < 2; ++ks)
#pragma unroll
      for (int n = 0; n < 8; ++n) st16(ob + ks * 8192 + (n >> 1) * 1024 + (n & 1) * 128, pack2(acc[2 * ks][n], acc[2 * ks + 1][n]));
  } else {
    k1_loop<false>(c, lds, acc, ws);
    STAMP(ws, 0, 2);
    char* ob = ws + WS_K + (size_t)(h * 64 + tb * 8 + tg * 2) * 8192 + (q >> 1) * 1024 + (q & 1) * 512 + ((r16 >> 3) * 16 + (r16 & 7)) * 16;
#pragma unroll
    for (int tt = 0; tt < 4; ++tt)
#pragma unroll
      for (int s = 0; s < 4; ++s) st16(ob + (tt >> 1) * 8192 + (tt & 1) * 128 + s * 2048, pack2(acc[tt][2 * s], acc[tt][2 * s + 1]));
  }
  STAMP(ws, 0, 3); STAMPW(); STAMP(ws, 0, 4);
}

constexpr int K2_LDS = 163840;
#define MNOP -1
#define MPRIO 2
#define SPRIO 0
#define QKORD 1
#define KFO(i) (QKORD ? (((i) & 1) * 8 + ((i) >> 1)) : (i))
#define FDEPTH 6
constexpr int FD = FDEPTH;
template <bool DOQK, bool DOPV>
DI void attn_mphase(lds_cp kb, lds_cp vb, bf16x8 (&fr)[FD], const bf16x8 (&qf)[8], const f32x16 (&ex)[2], const f32x16& negm,
                    f32x16 (&sc)[2], f32x16 (&o)[4]) {
  constexpr int I0 = DOQK ? 0 : 16, I1 = DOPV ? 32 : 16;
  unsigned pw[16];
  if (!DOQK) {
#pragma unroll
    for (int k = 0; k < 16; ++k) pw[k] = cvtpk(ex[k >> 3][2 * (k & 7)], ex[k >> 3][2 * (k & 7) + 1]);
  }
#pragma unroll
  for (int i = I0; i < I1; ++i) {
    const int sl = (i - I0) % FD;
    if (i < 16) {
      const int kb_ = QKORD ? (i & 1) : (i >> 3), s = QKORD ? (i >> 1) : (i & 7);
      sc[kb_] = MFMA32(fr[sl], qf[s], s == 0 ? negm : sc[kb_]);
    } else {
      const int ks = (i - 16) >> 2, db = (i - 16) & 3;
      o[db] = MFMA32(fr[sl], __builtin_bit_cast(bf16x8, (u32x4){pw[4 * ks], pw[4 * ks + 1], pw[4 * ks + 2], pw[4 * ks + 3]}), o[db]);
    }
    const int nx = i + FD;
    if (nx < I1) fr[sl] = (nx < 16) ? lds_rd128(kb + KFO(nx) * 1024) : lds_rd128(vb + (nx - 16) * 1024);
    if (DOQK && DOPV && i < 16) {
      pw[i] = cvtpk(ex[i >> 3][2 * (i & 7)], ex[i >> 3][2 * (i & 7) + 1]);
      asm volatile("" : "+v"(pw[i]));
    }
    SBAR();
  }
}
DI void set_m0(unsigned v) { asm volatile("s_mov_b32 m0, %0\n\ts_nop 0" :: "s"(v) : "memory"); }
template <int OFF> DI void glds_m(const void* sbase, unsigned voff) {
  asm volatile("global_load_lds_dwordx4 %0, %1 offset:%2" :: "v"(voff), "s"(sbase), "i"(OFF) : "memory");
}

__global__ __launch_bounds__(512, 2) void k2_attn(const u16* __restrict__ wq, const u16* __restrict__ wo, const u16* __restrict__ w1,
                                                  const u16* __restrict__ w2, char* __restrict__ ws) {
  extern __shared__ __attribute__((aligned(16))) char smem[];
  const int tid = threadIdx.x, lane = tid & 63, r32 = lane & 31, hi = lane >> 5;
  const int wid = (int)rfl(tid >> 6), g = wid >> 2, r = wid & 3;
  const int bid = blockIdx.x, h = (bid & 7) * 4 + ((bid >> 3) & 3), p = bid >> 5;
  const unsigned ldsb = (unsigned)(size_t)smem;
  lds_cp lds = (lds_cp)smem;
  const int blkA = p, blkB = 15 - p;
  const int nA = g ? 100 : 2 * p + 2;
  const int kt0 = g ? 15 - 2 * p : 0;
  const char* Kh = ws + WS_K + (size_t)h * 32 * 16384 + r * 4096;
  const char* Vh = ws + WS_V + (size_t)h * 32 * 16384 + r * 4096;
  const unsigned lane16 = (unsigned)lane * 16u;
  const unsigned kreg = (unsigned)g * 16384u, vreg = 65536u + (unsigned)g * 16384u;
#define KT(i) (((i) < nA) ? (kt0 + (i)) : ((i) - nA))
#define KSRC(i) (Kh + (size_t)KT(i) * 16384)
#define VSRC(i) (Vh + (size_t)KT(i) * 16384)
#define KDST(i) (ldsb + kreg + (unsigned)((i) & 1) * 32768u + (unsigned)r * 4096u)
#define VDST(i) (ldsb + vreg + (unsigned)((i) & 1) * 32768u + (unsigned)r * 4096u)
  STAMP(ws, 1, 0);
  bf16x8 qf[8];
  {
    const int tb32 = (g ? blkB : blkA) * 4 + r;
    const char* xb = ws + WS_XN + (size_t)tb32 * 36 * 1024;
    const char* wq_t = (const char*)wq + (size_t)(wid * 8) * 8192 + h * 256;
    const unsigned wvoff = (unsigned)((lane >> 2) & 7) * 8192u + (unsigned)(lane >> 5) * 64u + (unsigned)(lane & 3) * 16u;
    const unsigned wdst = ldsb + (unsigned)wid * 2048u;
    bf16x8 xf[9][4];
#define QW(c, b) glds<0>(wq_t + (size_t)(c) * 64 * 8192 + (b) * 128, wvoff, wdst + (c) * 16384u + (b) * 1024u)
#define QX(c, e) gld<(e) * 1024>(xf[c][e], xb + (c) * 4096, lane16)
#pragma unroll
    for (int c = 0; c < 3; ++c) { QW(c, 0); QW(c, 1); QX(c, 0); QX(c, 1); QX(c, 2); QX(c, 3); }
    const unsigned dg = 4u * (unsigned)((lane >> 1) & 1) + 2u * (unsigned)((lane >> 4) & 1) + (unsigned)(lane & 1);
    lds_cp wb = lds + (unsigned)hi * 2048u + (unsigned)((lane >> 2) & 3) * 64u + dg * 8u;
    lds_cp wbs[3] = {opq(wb), opq(wb + 49152), opq(wb + 98304)};
    f32x16 qa[4];
#pragma unroll
    for (int b = 0; b < 4; ++b) qa[b] = (f32x16){};
    bf16x8 wf[4];
    WAITV(16); BARRIER();
    STAMP(ws, 1, 5);
#define QWA(m) (wbs[((m) >> 2) / 12] + (((m) >> 2) % 12) * 4096 + ((m) & 3) * 512)
#define QWF(m) lds_tr2(QWA(m), QWA(m) + 256)
#pragma unroll
    for (int m = 0; m < 3; ++m) wf[m] = QWF(m);
#pragma unroll
    for (int c = 0; c < 9; ++c) {
      const int N = c <= 6 ? 10 : 8;
      asm volatile("s_waitcnt vmcnt(%4)\n\ts_barrier" : "+v"(xf[c][0]), "+v"(xf[c][1]), "+v"(xf[c][2]), "+v"(xf[c][3]) : "i"(N) : "memory");
#pragma unroll
      for (int n = 0; n < 16; ++n) {
        const int m = 16 * c + n + 3;
        if (m < 144) wf[m & 3] = QWF(m);
        if (c + 3 < 9) {
          if (n == 0) QW(c + 3, 0);
          if (n == 2) QW(c + 3, 1);
          if (n == 4) QX(c + 3, 0);
          if (n == 6) QX(c + 3, 1);
          if (n == 8) QX(c + 3, 2);
          if (n == 10) QX(c + 3, 3);
        } else if ((n & 1) == 0 && n < 8) {
          const int pc = n >> 1;
          if (c == 6) glds<0>(KSRC(0) + pc * 1024, lane16, KDST(0) + pc * 1024u);
          if (c == 7) glds<0>(KSRC(1) + pc * 1024, lane16, KDST(1) + pc * 1024u);
          if (c == 8) glds<0>(VSRC(0) + pc * 1024, lane16, VDST(0) + pc * 1024u);
        }
        qa[n & 3] = MFMA32(wf[n & 3], xf[c][n >> 2], qa[n & 3]);
        SBAR();
      }
    }
#undef QW
#undef QX
#undef QWF
#undef QWA
#pragma unroll
    for (int nb = 0; nb < 4; ++nb) {
      qf[2 * nb] = __builtin_bit_cast(bf16x8, pack8s(qa[nb], 0, QSCALE));
      qf[2 * nb + 1] = __builtin_bit_cast(bf16x8, pack8s(qa[nb], 8, QSCALE));
    }
    BARRIER();
    if (g) {
#pragma unroll
      for (int s = 0; s < 8; ++s) *(LAS bf16x8*)((lds_p)smem + 131072 + r * 8192 + s * 1024 + lane16) = qf[s];
    }
    asm volatile("s_waitcnt vmcnt(0) lgkmcnt(0)\n\ts_barrier" ::: "memory");
  }
  STAMP(ws, 1, 1);

  f32x16 o[4], sc[2], ex[2], negm = (f32x16){};
  bf16x8 fr[FD];
#pragma unroll
  for (int b = 0; b < 4; ++b) o[b] = (f32x16){};
  float mrow = 0.f, lsum = 0.f;
  bool first = true;
  const int rowl = r * 32 + r32 - 4 * hi;
  const int last = g ? 16 : nA - 1;
  const unsigned special = (3u << (last - 1)) | (g ? 0u : (1u << nA));
  const unsigned lanev = lane16 + (unsigned)(WS_V - WS_K);
  lds_cp krd = lds + kreg + lane16, vrd = lds + vreg + lane16;
  unsigned kdst = KDST(0), vdst = VDST(1);
  const char* vptr = KSRC(1);
#pragma unroll
  for (int i = 0; i < FD; ++i) fr[i] = lds_rd128(krd + KFO(i) * 1024);
  if (g) BARRIER();
  STAMP(ws, 1, 4);
  attn_mphase<true, false>(krd, vrd, fr, qf, ex, negm, sc, o);
  BARRIER();
#define EXP4(kb_, e) do { ex[kb_][e] = __builtin_amdgcn_exp2f(sc[kb_][e]); ex[kb_][e + 1] = __builtin_amdgcn_exp2f(sc[kb_][e + 1]); \
      ex[kb_][e + 2] = __builtin_amdgcn_exp2f(sc[kb_][e + 2]); ex[kb_][e + 3] = __builtin_amdgcn_exp2f(sc[kb_][e + 3]); } while (0)
#define ADD4(kb_, e) do { s0 = fadd_s(s0, ex[kb_][e]); s1 = fadd_s(s1, ex[kb_][e + 1]); s2 = fadd_s(s2, ex[kb_][e + 2]); s3 = fadd_s(s3, ex[kb_][e + 3]); } while (0)
#define EXPSUM(D) do { \
    D(0); EXP4(0, 0); SBAR(); EXP4(0, 4); SBAR(); \
    D(1); s0 = fadd_s(ex[0][0], ex[0][4]); s1 = fadd_s(ex[0][1], ex[0][5]); s2 = fadd_s(ex[0][2], ex[0][6]); s3 = fadd_s(ex[0][3], ex[0][7]); EXP4(0, 8); SBAR(); \
    D(2); EXP4(0, 12); ADD4(0, 8); SBAR(); \
    D(3); EXP4(1, 0); ADD4(0, 12); SBAR(); \
    D(4); EXP4(1, 4); ADD4(1, 0); SBAR(); \
    D(5); EXP4(1, 8); ADD4(1, 4); SBAR(); \
    D(6); EXP4(1, 12); ADD4(1, 8); SBAR(); \
    D(7); ADD4(1, 12); s0 = fadd_s(s0, s2); s1 = fadd_s(s1, s3); SBAR(); s0 = fadd_s(s0, s1); } while (0)
#define NODMA(n) do {} while (0)
#define RESCALE() do { \
      float a = fmaxf(fmaxf(sc[0][0], sc[0][1]), sc[1][0]), b = fmaxf(fmaxf(sc[0][2], sc[0][3]), sc[1][1]); \
      a = fmaxf(fmaxf(a, sc[1][2]), sc[1][3]); \
      _Pragma("unroll") for (int e = 4; e < 16; e += 4) { \
        a = fmaxf(fmaxf(a, sc[0][e]), sc[0][e + 1]); b = fmaxf(fmaxf(b, sc[0][e + 2]), sc[0][e + 3]); \
        a = fmaxf(fmaxf(a, sc[1][e]), sc[1][e + 1]); b = fmaxf(fmaxf(b, sc[1][e + 2]), sc[1][e + 3]); } \
      const float rm = xh_max(fmaxf(a, b)); \
      const float dl = first ? rm : fmaxf(rm, 0.f); \
      mrow += dl; \
      _Pragma("unroll") for (int e = 0; e < 16; ++e) { sc[0][e] -= dl; sc[1][e] -= dl; negm[e] = -mrow; } \
      if (!first) { const float f = __builtin_amdgcn_exp2f(-dl); lsum *= f; _Pragma("unroll") for (int db = 0; db < 4; ++db) o[db] *= f; } \
      first = false; \
      EXPSUM(NODMA); } while (0)
  auto sm_pre = [&](const int i) __attribute__((always_inline)) {
    if (i == nA) {
      const float inv = 1.0f / xh_sum(lsum);
      char* cb = ws + WS_CTX + ((size_t)((blkA * 4 + r) * 2 + (r32 >> 4)) * 128 + h * 4) * 1024 + (hi * 16 + (r32 & 15)) * 16;
#pragma unroll
      for (int db = 0; db < 4; ++db) {
        st16(cb + db * 1024, pack8s(o[db], 0, inv)); st16(cb + db * 1024 + 512, pack8s(o[db], 8, inv));
        o[db] = (f32x16){};
      }
      lsum = 0.f; mrow = 0.f;
    } else {
      const int dq = rowl - 64 * (i - (last - 1));
#pragma unroll
      for (int e = 0; e < 16; ++e) {
        const int c = 16 * ((e >> 2) & 1) + 8 * (e >> 3) + (e & 3);
        if (c > dq) sc[0][e] = -INFINITY;
        if (c + 32 > dq) sc[1][e] = -INFINITY;
      }
    }
  };
  unsigned long long tk0 = 0, tk1 = 0, tk2 = 0, tk3 = 0, tk4 = 0, aSM = 0, aW1 = 0, aM = 0, aV = 0, aW2 = 0; (void)tk0; (void)tk1; (void)tk2; (void)tk3; (void)tk4; (void)aSM; (void)aW1; (void)aM; (void)aV; (void)aW2;
  const char* kptr = KSRC(2);
  const char* wptr; unsigned wlim; unsigned tw = 0;
  {
    const unsigned gw = (unsigned)(bid * 8 + wid), sel = (unsigned)(lane < 9 ? lane : 0);
    wptr = (sel < 3 ? (const char*)wo + (size_t)gw * 2304 : sel < 6 ? (const char*)w1 + (size_t)gw * 2592 : (const char*)w2 + (size_t)gw * 2592) + (sel % 3) * 64;
    wlim = (sel < 3 ? 35u : 40u) - sel % 3;
  }
  TICK(tk0);
#pragma unroll 1
  for (int i = 0; i < 16; ++i) {
    float s0, s1, s2, s3;
    if (__builtin_expect((special >> i) & 1u, 0)) sm_pre(i);
#define FDMA(n) do { if ((n) == 0) set_m0(vdst); if ((n) == 4) set_m0(kdst); \
                     if ((n) < 4) glds_m<((n) & 3) * 1024>(vptr, lanev); else glds_m<((n) & 3) * 1024>(kptr, lane16); } while (0)
    EXPSUM(FDMA);
#undef FDMA
    { const unsigned sg = (unsigned)(3 * i) < wlim ? (unsigned)(3 * i) : wlim - wlim % 3u;
      asm volatile("global_load_dword %0, %1, off" : "+v"(tw) : "v"(wptr + sg * 64u) : "memory"); }
    if (__builtin_expect(first || __any(!(s0 <= PSUM_MAX)), 0)) RESCALE();
    lsum += s0;
    if (__builtin_expect(i == nA - 1, 0)) {
#pragma unroll
      for (int s = 0; s < 8; ++s) qf[s] = lds_rd128(lds + 131072 + r * 8192 + s * 1024 + lane16);
      negm = (f32x16){};
      first = true;
    }
    krd = (lds_cp)(size_t)((unsigned)(size_t)krd ^ 32768u);
#pragma unroll
    for (int k = 0; k < FD; ++k) fr[k] = lds_rd128(krd + KFO(k) * 1024);
    SBAR();
    TICK(tk1);
    BARRIER(); SBAR();
    TICK(tk2);
    __builtin_amdgcn_s_setprio(MPRIO);
    vptr = kptr; { const int i3 = i + 3 < 16 ? i + 3 : 16; kptr = KSRC(i3); } kdst ^= 32768u; vdst ^= 32768u;
    attn_mphase<true, true>(krd, vrd, fr, qf, ex, negm, sc, o);
    __builtin_amdgcn_s_setprio(SPRIO);
    vrd = (lds_cp)(size_t)((unsigned)(size_t)vrd ^ 32768u);
    TICK(tk3);
    asm volatile("s_waitcnt vmcnt(1)" : "+v"(tw) :: "memory");
    TICK(tk4);
    BARRIER();
    aSM += tk1 - tk0; aW1 += tk2 - tk1; aM += tk3 - tk2; aV += tk4 - tk3;
    TICK(tk0);
    aW2 += tk0 - tk4;
  }
  ACC_STORE(ws, 1, 10, aSM); ACC_STORE(ws, 1, 11, aW1); ACC_STORE(ws, 1, 12, aM); ACC_STORE(ws, 1, 13, aV); ACC_STORE(ws, 1, 14, aW2);
  {
    float s0, s1, s2, s3;
    if ((special >> 16) & 1u) sm_pre(16);
    EXPSUM(NODMA);
    if (first || __any(!(s0 <= PSUM_MAX))) RESCALE();
    lsum += s0;
  }
#pragma unroll
  for (int k = 0; k < FD; ++k) fr[k] = lds_rd128(vrd + k * 1024);
  BARRIER();
  attn_mphase<false, true>(krd, vrd, fr, qf, ex, negm, sc, o);
  asm volatile("s_waitcnt vmcnt(0)" : "+v"(tw) :: "memory");
  BARRIER();
  if (!g) BARRIER();
#undef EXP4
#undef ADD4
#undef EXPSUM
#undef NODMA
#undef RESCALE
  STAMP(ws, 1, 2);
  {
    lsum = xh_sum(lsum);
    if (g) { const f32x16 t0 = o[0], t1 = o[1]; o[0] = o[2]; o[1] = o[3]; o[2] = t0; o[3] = t1; }
    lds_p xo = (lds_p)smem + wid * 9216 + lane16;
#pragma unroll
    for (int j = 0; j < 2; ++j)
#pragma unroll
      for (int e = 0; e < 4; ++e) *(LAS f32x4*)(xo + (j * 4 + e) * 1024) = (f32x4){o[2 + j][4 * e], o[2 + j][4 * e + 1], o[2 + j][4 * e + 2], o[2 + j][4 * e + 3]};
    *(LAS f32x2*)(xo + 8192) = (f32x2){mrow, lsum};
    WAITL(0); BARRIER();
    lds_cp xi = lds + (wid ^ 4) * 9216 + lane16;
    const f32x2 ml = *(LAS const f32x2*)(xi + 8192);
    const float m = fmaxf(mrow, ml[0]);
    const float a0 = __builtin_amdgcn_exp2f(mrow - m), a1 = __builtin_amdgcn_exp2f(ml[0] - m);
    const float inv = 1.0f / (a0 * lsum + a1 * ml[1]);
    const float f0 = a0 * inv, f1 = a1 * inv;
    char* cb = ws + WS_CTX + ((size_t)((blkB * 4 + r) * 2 + (r32 >> 4)) * 128 + h * 4 + g * 2) * 1024 + (hi * 16 + (r32 & 15)) * 16;
#pragma unroll
    for (int j = 0; j < 2; ++j) {
      f32x16 u;
#pragma unroll
      for (int e = 0; e < 4; ++e) {
        const f32x4 w = *(LAS const f32x4*)(xi + (j * 4 + e) * 1024);
#pragma unroll
        for (int k = 0; k < 4; ++k) u[4 * e + k] = o[j][4 * e + k] * f0 + w[k] * f1;
      }
      st16(cb + j * 1024, pack8(u, 0)); st16(cb + j * 1024 + 512, pack8(u, 8));
    }
  }
  STAMPW(); STAMP(ws, 1, 3);
#undef KT
#undef KSRC
#undef VSRC
#undef KDST
#undef VDST
}

constexpr int WST = 18432;
constexpr int K3_LDS = 8 * WST;
template <int NST> constexpr int k3_ops(int i) { return i < 0 ? 0 : ((i + 4 < NST ? 3 : 0) + (i + 3 < NST ? 2 : 0)); }
template <int NST> constexpr int k3_wait(int t) {
  return t == 0 ? 10 : t == 1 ? 5 + k3_ops<NST>(0) : t == 2 ? k3_ops<NST>(0) + k3_ops<NST>(1) : k3_ops<NST>(t - 2) + k3_ops<NST>(t - 1);
}
template <int KTOT>
__global__ __launch_bounds__(512, 2) void k3_splitk(const char* __restrict__ act, const u16* __restrict__ W, char* __restrict__ part) {
  extern __shared__ __attribute__((aligned(16))) char smem[];
  constexpr int KC = KTOT / 4, NST = KC / 64, NS32 = KTOT / 32;
  const int tid = threadIdx.x, lane = tid & 63, q = lane >> 4;
  const int wid = (int)rfl(tid >> 6), tg = wid & 3, kh = wid >> 2;
  const int bid = blockIdx.x, xcd = bid & 7, j = bid >> 3;
  const int split = xcd >> 1, tb = (xcd & 1) * 8 + (j >> 2), nc = j & 3;
  const unsigned ldsb = (unsigned)(size_t)smem;
  lds_cp lds = (lds_cp)smem;
  const unsigned lane16 = (unsigned)lane * 16u;
  char* wsb = (char*)part - (KTOT == NHD ? WS_P1 : WS_P2); (void)wsb;
  STAMP(wsb, KTOT == NHD ? 2 : 5, 0);
  const char* a_src = act + ((size_t)(tb * 8 + tg * 2) * NS32 + (size_t)split * (KC / 32) + kh) * 1024;
  const char* w_src = (const char*)W + ((size_t)split * KC + wid * 8) * (DM * 2) + nc * 288;
  unsigned wv[3];
#pragma unroll
  for (int e = 0; e < 3; ++e) { const int c = 64 * e + lane; const int row = c / 18, cc = c - row * 18; wv[e] = (unsigned)row * (DM * 2) + (unsigned)cc * 16u; }
  const unsigned wdst = ldsb + (unsigned)wid * 2304u;
  bf16x8 af[NST][2];
#define LW(t, e) do { if ((e) < 2) glds<0>(w_src + (size_t)(t) * 64 * (DM * 2), wv[e], wdst + ((t) & 7) * WST + (e) * 1024u); \
                      else if (lane < 16) glds<0>(w_src + (size_t)(t) * 64 * (DM * 2), wv[2], wdst + ((t) & 7) * WST + 2048u); } while (0)
#define LA(t, e) gld<0>(af[t][e], a_src + (size_t)(e) * NS32 * 1024 + (t) * 2048, lane16)
#pragma unroll
  for (int t = 0; t < 3; ++t) { LW(t, 0); LW(t, 1); LW(t, 2); LA(t, 0); LA(t, 1); }
  LW(3, 0); LW(3, 1); LW(3, 2);
  lds_cp wb = lds + (unsigned)kh * 9216u + ((unsigned)(4 * q) + (unsigned)((lane >> 2) & 3)) * 288u + (unsigned)(lane & 3) * 8u;
  lds_cp wbs[3] = {opq(wb), opq(wb + 3 * WST), opq(wb + 6 * WST)};
  f32x4 acc[2][9];
#pragma unroll
  for (int n = 0; n < 9; ++n) { acc[0][n] = (f32x4){}; acc[1][n] = (f32x4){}; }
  bf16x8 wf[2][9];
  WAITV(15); BARRIER();
  STAMP(wsb, KTOT == NHD ? 2 : 5, 5);
#pragma unroll
  for (int n = 0; n < 9; ++n) wf[0][n] = lds_tr2(wbs[0] + n * 32, wbs[0] + n * 32 + 4608);
#pragma unroll
  for (int t = 0; t < NST; ++t) {
    asm volatile("s_waitcnt vmcnt(%2)\n\ts_barrier" : "+v"(af[t][0]), "+v"(af[t][1]) : "i"(k3_wait<NST>(t)) : "memory");
    FSTAMP(wsb, KTOT == NHD ? 2 : 5, 8 + t);
#pragma unroll
    for (int n = 0; n < 9; ++n) {
      if (t + 1 < NST) { lds_cp pp = wbs[((t + 1) & 7) / 3] + (((t + 1) & 7) % 3) * WST + n * 32; wf[(t + 1) & 1][n] = lds_tr2(pp, pp + 4608); }
      if (t + 4 < NST) { if (n == 0) LW(t + 4, 0); if (n == 2) LW(t + 4, 1); if (n == 4) LW(t + 4, 2); }
      if (t + 3 < NST) { if (n == 6) LA(t + 3, 0); if (n == 8) LA(t + 3, 1); }
      acc[0][n] = MFMA(wf[t & 1][n], af[t][0], acc[0][n]); acc[1][n] = MFMA(wf[t & 1][n], af[t][1], acc[1][n]);
      SBAR();
    }
  }
#undef LW
#undef LA
  BARRIER();
  STAMP(wsb, KTOT == NHD ? 2 : 5, 2);
  if (kh) {
#pragma unroll
    for (int n = 0; n < 9; ++n) { const f32x4 t = acc[0][n]; acc[0][n] = acc[1][n]; acc[1][n] = t; }
  }
  lds_p xo = (lds_p)smem + wid * 9216 + lane16;
#pragma unroll
  for (int n = 0; n < 9; ++n) *(LAS f32x4*)(xo + n * 1024) = acc[1][n];
  WAITL(0); BARRIER();
  lds_cp xi = lds + (wid ^ 4) * 9216 + lane16;
  unsigned pk[9][2];
#pragma unroll
  for (int n = 0; n < 9; ++n) {
    const f32x4 w = *(LAS const f32x4*)(xi + n * 1024) + acc[0][n];
    pk[n][0] = cvtpk(w[0], w[1]); pk[n][1] = cvtpk(w[2], w[3]);
  }
  store9(part + ((size_t)split * 128 + tb * 8 + tg * 2 + kh) * 18 * 1024 + lane16, nc * 9, pk);
  STAMP(wsb, KTOT == NHD ? 2 : 5, 3); STAMPW(); STAMP(wsb, KTOT == NHD ? 2 : 5, 4);
}

__global__ __launch_bounds__(576) void k4_reduce_ln(const u16* __restrict__ x, char* __restrict__ ws) {
  __shared__ float red[2][9][8];
  const int t = threadIdx.x, tok = t & 7, q = (t >> 3) & 3, s = t >> 5, wv = t >> 6;
  const int tb16 = blockIdx.x >> 1, tok16 = (blockIdx.x & 1) * 8 + tok, row = tb16 * 16 + tok16;
  const size_t off = ((size_t)tb16 * 18 + s) * 1024 + (q * 16 + tok16) * 16;
  STAMP(ws, 3, 0);
  u32x4 pp[4];
#pragma unroll
  for (int sp = 0; sp < 4; ++sp) pp[sp] = *(const u32x4*)(ws + WS_P1 + (size_t)sp * S * DM * 2 + off);
  const u32x2 x0 = *(const u32x2*)(x + (size_t)row * DM + 32 * s + 4 * q), x1 = *(const u32x2*)(x + (size_t)row * DM + 32 * s + 16 + 4 * q);
  f32x4 a = {bf_lo(x0[0]), bf_hi(x0[0]), bf_lo(x0[1]), bf_hi(x0[1])}, b = {bf_lo(x1[0]), bf_hi(x1[0]), bf_lo(x1[1]), bf_hi(x1[1])};
#pragma unroll
  for (int sp = 0; sp < 4; ++sp) {
    a += (f32x4){bf_lo(pp[sp][0]), bf_hi(pp[sp][0]), bf_lo(pp[sp][1]), bf_hi(pp[sp][1])};
    b += (f32x4){bf_lo(pp[sp][2]), bf_hi(pp[sp][2]), bf_lo(pp[sp][3]), bf_hi(pp[sp][3])};
  }
  st16(ws + WS_H + off, pack2(a, b));
  float sm = (a[0] + a[1]) + (a[2] + a[3]) + (b[0] + b[1]) + (b[2] + b[3]);
  sm += __shfl_xor(sm, 8); sm += __shfl_xor(sm, 16); sm += __shfl_xor(sm, 32);
  if ((t & 63) < 8) red[0][wv][tok] = sm;
  __syncthreads();
  float tot = 0.f;
#pragma unroll
  for (int w = 0; w < 9; ++w) tot += red[0][w][tok];
  const float mean = tot * (1.0f / DM);
  a -= mean; b -= mean;
  float sq = (a[0] * a[0] + a[1] * a[1]) + (a[2] * a[2] + a[3] * a[3]) + (b[0] * b[0] + b[1] * b[1]) + (b[2] * b[2] + b[3] * b[3]);
  sq += __shfl_xor(sq, 8); sq += __shfl_xor(sq, 16); sq += __shfl_xor(sq, 32);
  if ((t & 63) < 8) red[1][wv][tok] = sq;
  __syncthreads();
  float tq = 0.f;
#pragma unroll
  for (int w = 0; w < 9; ++w) tq += red[1][w][tok];
  const float rstd = 1.0f / sqrtf(tq * (1.0f / DM) + 1e-5f);
  st16(ws + WS_HN + off, pack2s(a, b, rstd));
  STAMP(ws, 3, 3);
}

constexpr int K5_LDS = 8 * WST;
constexpr int k5_ops(int u) { return u < 0 ? 0 : (((u & 1) == 0 && (u >> 1) + 3 < 9) ? 3 : 0) + (u + 4 < 18 ? 2 : 0); }
constexpr int k5_wait(int u) {
  return u == 0 ? 12 : u == 1 ? 7 + k5_ops(0) : u == 2 ? 5 + k5_ops(0) + k5_ops(1) : u == 3 ? k5_ops(0) + k5_ops(1) + k5_ops(2)
       : k5_ops(u - 3) + k5_ops(u - 2) + k5_ops(u - 1);
}
__global__ __launch_bounds__(512, 2) void k5_ffn1(const u16* __restrict__ W, const u16* __restrict__ b1, char* __restrict__ ws) {
  extern __shared__ __attribute__((aligned(16))) char smem[];
  const int tid = threadIdx.x, lane = tid & 63, q = lane >> 4;
  const int wid = (int)rfl(tid >> 6);
  const int bid = blockIdx.x, nc = (bid & 7) * 4 + ((bid >> 3) & 3), tb = bid >> 5;
  const unsigned ldsb = (unsigned)(size_t)smem;
  lds_cp lds = (lds_cp)smem;
  const unsigned lane16 = (unsigned)lane * 16u;
  STAMP(ws, 4, 0);
  const char* a_src = ws + WS_HN + (size_t)(tb * 16 + wid * 2) * 18 * 1024;
  const char* w_src = (const char*)W + (size_t)(wid * 8) * (FF * 2) + nc * 288;
  unsigned wv[3];
#pragma unroll
  for (int e = 0; e < 3; ++e) { const int c = 64 * e + lane; const int row = c / 18, cc = c - row * 18; wv[e] = (unsigned)row * (FF * 2) + (unsigned)cc * 16u; }
  const unsigned wdst = ldsb + (unsigned)wid * 2304u;
  bf16x8 af[18][2];
#define LW(t, e) do { if ((e) < 2) glds<0>(w_src + (size_t)(t) * 64 * (FF * 2), wv[e], wdst + ((t) & 7) * WST + (e) * 1024u); \
                      else if (lane < 16) glds<0>(w_src + (size_t)(t) * 64 * (FF * 2), wv[2], wdst + ((t) & 7) * WST + 2048u); } while (0)
#define LW3(t) do { LW(t, 0); LW(t, 1); LW(t, 2); } while (0)
#define LA(u, e) gld<0>(af[u][e], a_src + (e) * 18 * 1024 + (u) * 1024, lane16)
#define LA2(u) do { LA(u, 0); LA(u, 1); } while (0)
  LW3(0); LA2(0); LA2(1); LW3(1); LA2(2); LA2(3); LW3(2);
  lds_cp wb = lds + ((unsigned)(4 * q) + (unsigned)((lane >> 2) & 3)) * 288u + (unsigned)(lane & 3) * 8u;
  lds_cp wbs[3] = {opq(wb), opq(wb + 3 * WST), opq(wb + 6 * WST)};
  f32x4 acc[2][9];
#pragma unroll
  for (int n = 0; n < 9; ++n) { acc[0][n] = (f32x4){}; acc[1][n] = (f32x4){}; }
  bf16x8 wf[2][9];
  WAITV(14); BARRIER();
  STAMP(ws, 4, 5);
#pragma unroll
  for (int n = 0; n < 9; ++n) wf[0][n] = lds_tr2(wbs[0] + n * 32, wbs[0] + n * 32 + 4608);
#pragma unroll
  for (int u = 0; u < 18; ++u) {
    if (u & 1) asm volatile("s_waitcnt vmcnt(%2)\n\ts_barrier" : "+v"(af[u][0]), "+v"(af[u][1]) : "i"(k5_wait(u)) : "memory");
    else       asm volatile("s_waitcnt vmcnt(%2)" : "+v"(af[u][0]), "+v"(af[u][1]) : "i"(k5_wait(u)) : "memory");
    FSTAMP(ws, 4, 8 + u);
#pragma unroll
    for (int n = 0; n < 9; ++n) {
      if (u + 1 < 18) { lds_cp pp = wbs[(((u + 1) >> 1) & 7) / 3] + ((((u + 1) >> 1) & 7) % 3) * WST + ((u + 1) & 1) * 9216 + n * 32; wf[(u + 1) & 1][n] = lds_tr2(pp, pp + 4608); }
      if ((u & 1) == 0 && (u >> 1) + 3 < 9) { if (n == 0) LW((u >> 1) + 3, 0); if (n == 2) LW((u >> 1) + 3, 1); if (n == 4) LW((u >> 1) + 3, 2); }
      if (u + 4 < 18) { if (n == 6) LA(u + 4, 0); if (n == 8) LA(u + 4, 1); }
      acc[0][n] = MFMA(wf[u & 1][n], af[u][0], acc[0][n]); acc[1][n] = MFMA(wf[u & 1][n], af[u][1], acc[1][n]);
      SBAR();
    }
  }
#undef LW
#undef LW3
#undef LA
#undef LA2
  STAMP(ws, 4, 2);
  unsigned pk[2][9][2];
#pragma unroll
  for (int n = 0; n < 9; ++n) {
    const u32x2 bb = *(const u32x2*)(b1 + nc * 144 + n * 16 + 4 * q);
    const f32x4 bv = {bf_lo(bb[0]), bf_hi(bb[0]), bf_lo(bb[1]), bf_hi(bb[1])};
#pragma unroll
    for (int a = 0; a < 2; ++a) {
      f32x4 v = acc[a][n] + bv;
#pragma unroll
      for (int k = 0; k < 4; ++k) { const float z = fmaxf(v[k], 0.f); v[k] = z * z; }
      pk[a][n][0] = cvtpk(v[0], v[1]); pk[a][n][1] = cvtpk(v[2], v[3]);
    }
  }
#pragma unroll
  for (int a = 0; a < 2; ++a) store9(ws + WS_F + (size_t)(tb * 16 + wid * 2 + a) * 144 * 1024 + lane16, nc * 9, pk[a]);
  STAMP(ws, 4, 3); STAMPW(); STAMP(ws, 4, 4);
}

__global__ __launch_bounds__(576) void k7_final(const u16* __restrict__ b2, const char* __restrict__ ws, u16* __restrict__ out) {
  const int t = threadIdx.x, tok = t & 7, q = (t >> 3) & 3, s = t >> 5;
  const int tb16 = blockIdx.x >> 1, tok16 = (blockIdx.x & 1) * 8 + tok, row = tb16 * 16 + tok16;
  const size_t off = ((size_t)tb16 * 18 + s) * 1024 + (q * 16 + tok16) * 16;
  STAMP(ws, 6, 0);
  u32x4 pp[5];
#pragma unroll
  for (int sp = 0; sp < 4; ++sp) pp[sp] = *(const u32x4*)(ws + WS_P2 + (size_t)sp * S * DM * 2 + off);
  pp[4] = *(const u32x4*)(ws + WS_H + off);
  const u32x2 x0 = *(const u32x2*)(b2 + 32 * s + 4 * q), x1 = *(const u32x2*)(b2 + 32 * s + 16 + 4 * q);
  f32x4 a = {bf_lo(x0[0]), bf_hi(x0[0]), bf_lo(x0[1]), bf_hi(x0[1])}, b = {bf_lo(x1[0]), bf_hi(x1[0]), bf_lo(x1[1]), bf_hi(x1[1])};
#pragma unroll
  for (int sp = 0; sp < 5; ++sp) {
    a += (f32x4){bf_lo(pp[sp][0]), bf_hi(pp[sp][0]), bf_lo(pp[sp][1]), bf_hi(pp[sp][1])};
    b += (f32x4){bf_lo(pp[sp][2]), bf_hi(pp[sp][2]), bf_lo(pp[sp][3]), bf_hi(pp[sp][3])};
  }
  *(u32x2*)(out + (size_t)row * DM + 32 * s + 4 * q) = (u32x2){cvtpk(a[0], a[1]), cvtpk(a[2], a[3])};
  *(u32x2*)(out + (size_t)row * DM + 32 * s + 16 + 4 * q) = (u32x2){cvtpk(b[0], b[1]), cvtpk(b[2], b[3])};
  STAMP(ws, 6, 3);
}

extern "C" void kernel_launch(void* const* d_in, const int* in_sizes, int n_in, void* d_out, int out_size, void* d_ws, size_t ws_size,
                              hipStream_t stream) {
  const u16* x = (const u16*)d_in[0];   const u16* ckv = (const u16*)d_in[1]; const u16* wq = (const u16*)d_in[2];
  const u16* wuk = (const u16*)d_in[3]; const u16* wuv = (const u16*)d_in[4]; const u16* wo = (const u16*)d_in[5];
  const u16* w1 = (const u16*)d_in[7];  const u16* b1 = (const u16*)d_in[8];  const u16* w2 = (const u16*)d_in[9];
  const u16* b2 = (const u16*)d_in[10];
  char* ws = (char*)d_ws;
  (void)hipFuncSetAttribute((const void*)k1_kvproj, hipFuncAttributeMaxDynamicSharedMemorySize, K1_LDS);
  (void)hipFuncSetAttribute((const void*)k2_attn, hipFuncAttributeMaxDynamicSharedMemorySize, K2_LDS);
  (void)hipFuncSetAttribute((const void*)k3_splitk<NHD>, hipFuncAttributeMaxDynamicSharedMemorySize, K3_LDS);
  (void)hipFuncSetAttribute((const void*)k3_splitk<FF>, hipFuncAttributeMaxDynamicSharedMemorySize, K3_LDS);
  (void)hipFuncSetAttribute((const void*)k5_ffn1, hipFuncAttributeMaxDynamicSharedMemorySize, K5_LDS);
  k1_kvproj<<<256, 512, K1_LDS, stream>>>(x, ckv, wuk, wuv, wq, wo, w1, w2, ws);
  k2_attn<<<256, 512, K2_LDS, stream>>>(wq, wo, w1, w2, ws);
  k3_splitk<NHD><<<256, 512, K3_LDS, stream>>>(ws + WS_CTX, wo, ws + WS_P1);
  k4_reduce_ln<<<256, 576, 0, stream>>>(x, ws);
  k5_ffn1<<<256, 512, K5_LDS, stream>>>(w1, b1, ws);
  k3_splitk<FF><<<256, 512, K3_LDS, stream>>>(ws + WS_F, w2, ws + WS_P2);
  k7_final<<<256, 576, 0, stream>>>(b2, ws, (u16*)d_out);
}
```

```cpp
#include <hip/hip_runtime.h>
#include <hip/hip_bf16.h>
#include <cstdint>

typedef unsigned short u16;
typedef __bf16 bf16x8 __attribute__((ext_vector_type(8)));
typedef __bf16 bf16x2 __attribute__((ext_vector_type(2)));
typedef short s16x4 __attribute__((ext_vector_type(4)));
typedef short s16x8 __attribute__((ext_vector_type(8)));
typedef float f32x2 __attribute__((ext_vector_type(2)));
typedef float f32x4 __attribute__((ext_vector_type(4)));
typedef unsigned u32x2 __attribute__((ext_vector_type(2)));
typedef unsigned u32x4 __attribute__((ext_vector_type(4)));
#define LAS __attribute__((address_space(3)))
typedef LAS const char* lds_cp;
typedef LAS char* lds_p;
#define DI __device__ __forceinline__

constexpr int S = 2048, DM = 576, NH = 32, HD = 128, DC = 512, FF = 4608, NHD = NH * HD;
constexpr float QSCALE = 0.08838834764831845f * 1.4426950408889634f;
constexpr float PSUM_MAX = 0x1p100f, PSUM_MIN = 0x1p-40f;

constexpr size_t WS_XN  = 0;
constexpr size_t WS_K   = WS_XN + (size_t)S * DM * 2;
constexpr size_t WS_V   = WS_K + (size_t)S * NHD * 2;
constexpr size_t WS_P2  = (size_t)76 << 20;
constexpr size_t WS_CTX = (size_t)16 * S * DM * 2;
constexpr size_t WS_HN  = WS_CTX + (size_t)S * NHD * 2;
constexpr size_t WS_WO  = WS_HN + (size_t)S * DM * 2;
constexpr size_t WS_W1  = WS_WO + (size_t)NHD * DM * 2;
constexpr size_t WS_W2  = WS_W1 + (size_t)DM * FF * 2;
constexpr size_t WS_RD  = WS_W2 + (size_t)DM * FF * 2;
constexpr size_t WS_CNT = WS_RD + 262144 + 131072;
constexpr int C_O = 0, C_R = 32, C_F = 64, C_X = 160, C_W = 192, C_N = 208;

DI unsigned rfl(unsigned x) { return __builtin_amdgcn_readfirstlane(x); }
DI unsigned cvtpk(float lo, float hi) { f32x2 v = {lo, hi}; bf16x2 b = __builtin_convertvector(v, bf16x2); return __builtin_bit_cast(unsigned, b); }
DI float bf_lo(unsigned w) { return __uint_as_float(w << 16); }
DI float bf_hi(unsigned w) { return __uint_as_float(w & 0xffff0000u); }
#define SBAR() __builtin_amdgcn_sched_barrier(0)
#define WAITV(n) asm volatile("s_waitcnt vmcnt(%0)" :: "i"(n) : "memory")
#define MFMA(a, b, c) __builtin_amdgcn_mfma_f32_16x16x32_bf16(a, b, c, 0, 0, 0)

DI void st16(void* p, u32x4 v) { asm volatile("global_store_dwordx4 %0, %1, off sc1\n\ts_nop 1" :: "v"(p), "v"(v) : "memory"); }
template <int OFF> DI void gld(bf16x8& d, const void* sbase, unsigned voff) {
  asm volatile("global_load_dwordx4 %0, %1, %2 offset:%3" : "=v"(d) : "v"(voff), "s"(sbase), "i"(OFF) : "memory");
}
DI s16x4 lds_tr(lds_cp p) { return __builtin_amdgcn_ds_read_tr16_b64_v4i16((LAS s16x4*)p); }
DI u32x4 pack2(const f32x4& a, const f32x4& b) { return (u32x4){cvtpk(a[0], a[1]), cvtpk(a[2], a[3]), cvtpk(b[0], b[1]), cvtpk(b[2], b[3])}; }
DI u32x4 pack2s(const f32x4& a, const f32x4& b, float s) {
  return (u32x4){cvtpk(a[0] * s, a[1] * s), cvtpk(a[2] * s, a[3] * s), cvtpk(b[0] * s, b[1] * s), cvtpk(b[2] * s, b[3] * s)};
}
DI float xq_max(float v) {
  auto a = __builtin_amdgcn_permlane16_swap(__float_as_uint(v), __float_as_uint(v), false, false);
  v = fmaxf(__uint_as_float(a[0]), __uint_as_float(a[1]));
  auto b = __builtin_amdgcn_permlane32_swap(__float_as_uint(v), __float_as_uint(v), false, false);
  return fmaxf(__uint_as_float(b[0]), __uint_as_float(b[1]));
}
DI float xq_sum(float v) {
  auto a = __builtin_amdgcn_permlane16_swap(__float_as_uint(v), __float_as_uint(v), false, false);
  v = __uint_as_float(a[0]) + __uint_as_float(a[1]);
  auto b = __builtin_amdgcn_permlane32_swap(__float_as_uint(v), __float_as_uint(v), false, false);
  return __uint_as_float(b[0]) + __uint_as_float(b[1]);
}
DI lds_cp opq(lds_cp p) { asm volatile("" : "+v"(p)); return p; }

template <int OFF> DI void st16o(void* sbase, unsigned voff, u32x4 v) {
  asm volatile("global_store_dwordx4 %0, %1, %2 offset:%3 sc1\n\ts_nop 1" :: "v"(voff), "v"(v), "s"(sbase), "i"(OFF) : "memory");
}
#define PIN(x) asm volatile("" :: "v"(x))
#define MFI "v_mfma_f32_16x16x32_bf16 "
#define MF(d, a, b)     asm volatile(MFI "%0, %1, %2, %0" : "+v"(d) : "v"(a), "a"(b))
#define MFX(d, a, b)    asm volatile(MFI "%0, %1, %2, %0" : "+v"(d) : "a"(a), "a"(b))
#define MFV(d, a, b)    asm volatile(MFI "%0, %1, %2, %0" : "+v"(d) : "v"(a), "v"(b))
#define MF0(d, a, b)    asm volatile(MFI "%0, %1, %2, 0" : "=&v"(d) : "v"(a), "a"(b))
#define MFC(d, a, b, c) asm volatile(MFI "%0, %1, %2, %3" : "=&v"(d) : "v"(a), "a"(b), "v"(c))
#define GLDA(d, sb, vo, off)  asm volatile("global_load_dwordx4 %0, %1, %2 offset:%3" : "=a"(d) : "v"(vo), "s"(sb), "i"(off) : "memory")
#define GLDSA(d, sb, vo, off) asm volatile("global_load_dwordx4 %0, %1, %2 offset:%3 sc1" : "=a"(d) : "v"(vo), "s"(sb), "i"(off) : "memory")
#define GLDSV(d, sb, vo, off) asm volatile("global_load_dwordx4 %0, %1, %2 offset:%3 sc1" : "=v"(d) : "v"(vo), "s"(sb), "i"(off) : "memory")
#define DSR(d, ad, off)  asm volatile("ds_read_b128 %0, %1 offset:%2" : "=v"(d) : "v"(ad), "i"(off) : "memory")
#define DSRA(d, ad, off) asm volatile("ds_read_b128 %0, %1 offset:%2" : "=a"(d) : "v"(ad), "i"(off) : "memory")
#define DSW16(ad, off, ...) asm volatile("ds_write_b128 %0, %1 offset:%2" :: "v"(ad), "v"(__VA_ARGS__), "i"(off) : "memory")
#define DSW8(ad, off, ...)  asm volatile("ds_write_b64 %0, %1 offset:%2" :: "v"(ad), "v"(__VA_ARGS__), "i"(off) : "memory")

#define GLDV(d, sb, vo, off)  asm volatile("global_load_dwordx4 %0, %1, %2 offset:%3 sc1" : "=v"(d) : "v"(vo), "s"(sb), "i"(off) : "memory")
#define DSRTA(d, ad, off) asm volatile("ds_read_b128 %0, %1 offset:%2" : "+a"(d) : "v"(ad), "i"(off) : "memory")
#define DSRTV(d, ad, off) asm volatile("ds_read_b128 %0, %1 offset:%2" : "+v"(d) : "v"(ad), "i"(off) : "memory")
#define DSWA(ad, off, d) asm volatile("ds_write_b128 %0, %1 offset:%2" :: "v"(ad), "a"(d), "i"(off) : "memory")
#define DSWV(ad, off, d) asm volatile("ds_write_b128 %0, %1 offset:%2" :: "v"(ad), "v"(d), "i"(off) : "memory")

DI unsigned frag_dst(unsigned xs, unsigned T, unsigned k, unsigned nk, unsigned n0, unsigned wide, unsigned lone) {
  const unsigned w = T / 9u, t = T - 9u * w - (w & 1u);
  return (unsigned)WS_WO + ((xs * 4u + w) * 468u + n0 + (t >= 8u ? lone : wide && t < 4u ? wide * k + t : (t >> 1) * 2u * nk + (wide ? nk : 0u) + 2u * k + (t & 1u))) * 1024u;
}
DI void retile_desc(int unit, int i, const u16* wo, const u16* w1, const u16* w2, const char*& src, unsigned& rs, unsigned& dst) {
  if (unit < 1152) {
    const int k = unit / 9, G = unit - 9 * k;
    src = (const char*)wo + (size_t)k * 32 * (DM * 2) + G * 128; rs = 0;
    const unsigned T = 4u * G + i, w = T / 9u, t = T - 9u * w - (w & 1u), j = (((unsigned)k >> 2) - w) & 3u, kr = j ? 3u + 3u * (k & 3) + j : k & 3, c = t >= 8u ? 4u : t + (t >= 4u);
    dst = (unsigned)WS_WO + (((unsigned)(k >> 4) * 4u + w) * 468u + (c >= 7u ? 105u + 2u * kr : kr < 4u ? 7u * kr : c >= 5u ? 75u + 2u * kr : 8u + 5u * kr) + c) * 1024u;
  } else if (unit < 2448) {
    const int v = unit - 1152, k = v / 72, G = v - 72 * k, xs = G / 9;
    src = (const char*)w1 + (size_t)(64 * (k >> 1) + 2 * (k & 1)) * (FF * 2) + G * 128; rs = 1;
    dst = frag_dst((unsigned)xs, 4u * (G - 9 * xs) + i, (unsigned)k, 18u, 144u, 4u, 72u + k);
  } else {
    const int v = unit - 2448, k = v / 9, G = v - 9 * k, xs = k / 18, u = k - 18 * xs, hf = u / 9, c = u - 9 * hf, p = c < 4 ? c : c - 5;
    src = (const char*)w2 + (size_t)(64 * (k >> 1) + 2 * (k & 1)) * (DM * 2) + G * 128; rs = 2;
    const int tau = c == 4 ? 12 + hf : (p < 3 ? 4 * p : 14) + 2 * hf + (c > 4);
    dst = frag_dst((unsigned)xs, 4u * G + i, (unsigned)tau, 18u, 306u, 0u, 144u + tau);
  }
}
DI void set_m0(unsigned v) { asm volatile("s_mov_b32 m0, %0\n\ts_nop 0" :: "s"(v) : "memory"); }
template <int OFF> DI void glds_m(const void* sbase, unsigned voff) {
  asm volatile("global_load_lds_dwordx4 %0, %1 offset:%2" :: "v"(voff), "s"(sbase), "i"(OFF) : "memory");
}

DI float w32_sum(float v) {
  v += __uint_as_float(__builtin_amdgcn_update_dpp(0u, __float_as_uint(v), 0xb1, 0xf, 0xf, false));
  v += __uint_as_float(__builtin_amdgcn_update_dpp(0u, __float_as_uint(v), 0x4e, 0xf, 0xf, false));
  v += __uint_as_float(__builtin_amdgcn_update_dpp(0u, __float_as_uint(v), 0x124, 0xf, 0xf, false));
  v += __uint_as_float(__builtin_amdgcn_update_dpp(0u, __float_as_uint(v), 0x128, 0xf, 0xf, false));
  auto a = __builtin_amdgcn_permlane16_swap(__float_as_uint(v), __float_as_uint(v), false, false);
  return __uint_as_float(a[0]) + __uint_as_float(a[1]);
}

#define M0ADD(b, c) asm volatile("s_add_u32 m0, %0, %1" :: "s"(b), "i"(c) : "scc", "memory")
#define PINS(p) asm volatile("" : "+s"(p))
template <int OFF> DI void st16n(const void* sbase, unsigned voff, u32x4 v) {
  asm volatile("global_store_dwordx4 %0, %1, %2 offset:%3 sc1" :: "v"(voff), "v"(v), "s"(sbase), "i"(OFF) : "memory");
}
DI bf16x8 cat2(s16x4 a, s16x4 b) { return __builtin_bit_cast(bf16x8, __builtin_shufflevector(a, b, 0, 1, 2, 3, 4, 5, 6, 7)); }
constexpr int K1_LDS = 163840;
#define MG(d, a, b)  asm volatile(MFI "%0, %2, %1, %0" : "+v"(d) : "v"(a), "a"(b))
#define MG0(d, a, b) asm volatile(MFI "%0, %2, %1, 0" : "=&v"(d) : "v"(a), "a"(b))
__global__ __launch_bounds__(256, 1) __attribute__((aligned(4096))) void k1_kvproj(const u16* __restrict__ x, const u16* __restrict__ ckv, const u16* __restrict__ wuk,
                                                    const u16* __restrict__ wuv, const u16* __restrict__ wq, const u16* wo,
                                                    const u16* w1, const u16* w2, char* ws) {
  extern __shared__ __attribute__((aligned(16))) char smem[];
  int tid = threadIdx.x, lane = tid & 63, wid = (int)rfl(tid >> 6), bid = blockIdx.x;
  lds_cp lds = (lds_cp)smem;
  unsigned tw0 = 0, tw1 = 0;
  { unsigned long long pc_; unsigned len_;
    asm volatile("s_getpc_b64 %0\n.Lk1_here:\n\ts_mov_b32 %1, .Lk3_end-.Lk1_here" : "=s"(pc_), "=s"(len_));
    unsigned o0_ = (unsigned)((bid >> 3) * 32 + (tid >> 3)) * 128u; o0_ = o0_ < len_ ? o0_ : 0u;
    asm volatile("global_load_dword %0, %1, %2" : "+v"(tw1) : "v"(o0_), "s"(pc_) : "memory"); }
#define TOUCHS(vo, sb, off) asm volatile("global_load_dword %0, %1, %2 offset:%3" : "+v"(tw0) : "v"(vo), "s"(sb), "i"(off) : "memory")
#define TOUCH1() asm volatile("global_load_dword %0, %1, %2" : "+v"(tw0) : "v"(tq), "s"(wq) : "memory")
#define RT_DESC() do { const int f = lane & 15, unit = bid * 4 + wid + (lane >> 4) * 1024; const char* src; unsigned rs, dst; \
      retile_desc(unit < 3744 ? unit : unit - 1024, f & 3, wo, w1, w2, src, rs, dst); \
      const unsigned sA = rs == 0 ? 18432u : rs == 1 ? 73728u : 1152u, sB = rs == 0 ? 2304u : rs == 1 ? 36864u : 9216u, \
                     sC = rs == 0 ? 1152u : rs == 1 ? 9216u : 4608u, sQ = rs == 0 ? 4608u : rs == 1 ? 147456u : 18432u; \
      const unsigned rd = f == 0 ? (unsigned)(size_t)src : f == 1 ? (unsigned)((size_t)src >> 32) : f == 2 ? sA : f == 3 ? sB : f == 4 ? sC : f == 5 ? sQ : dst; \
      asm volatile("global_store_dword %0, %1, %2 sc1" :: "v"((unsigned)(bid * 256 + wid * 64 + f * 4 + (lane >> 4)) * 4u), "v"(rd), "s"(ws + WS_RD) : "memory"); } while (0)
  const unsigned va0 = (unsigned)(lane >> 3) * 1024u + (unsigned)(((lane & 7) ^ (lane >> 4)) * 16), va1 = (unsigned)(8 + (lane >> 3)) * 1024u + (unsigned)(((lane & 7) ^ (4 + (lane >> 4))) * 16) - 1024u,
                 va2 = va0 + 16384u - 2048u, va3 = va1 + 16384u - 2048u;
  const unsigned adst = (unsigned)(size_t)smem + 98304u + (unsigned)wid * 16384u;
  const unsigned wve = (unsigned)((lane >> 2) & 7) * 8192u + (unsigned)(lane >> 5) * 64u;
  const unsigned wv0 = wve + (unsigned)(lane & 3) * 16u, wv1 = wve + (unsigned)((lane & 3) ^ 2) * 16u + 65536u - 1024u,
                 wv2 = wv0 + 131072u - 2048u, wv3 = wv1 + 131072u - 2048u;
  const unsigned wdst = (unsigned)(size_t)smem + (unsigned)wid * 4096u;
  const unsigned xo = (unsigned)(bid * 8 + wid * 2 + (lane >> 5)) * (DM * 2) + (unsigned)((lane & 31) < 24 ? lane & 31 : 0) * 16u;
  unsigned of0 = (unsigned)((bid >> 5) * 256 + wid * 64) * 1024u, of1 = (unsigned)wid * 262144u + (unsigned)((bid & 7) * 4 + ((bid >> 3) & 3)) * 256u;
  PIN(va0); PIN(va1); PIN(va2); PIN(va3); PIN(wv0); PIN(wv1); PIN(wv2); PIN(wv3); PIN(xo); PINS(of0); PINS(of1);
  const char* a_s = (const char*)ckv + of0;
  const char* wk_s = (const char*)wuk + of1;
  unsigned ok0 = (unsigned)((bid >> 5) & 3) << 20, ok1 = (ok0 + (1u << 20)) & (3u << 20), oc0 = ok0 >> 12;
  const char *a_s0 = a_s, *a_s1 = a_s + 32768;
  bf16x8 xc[3];
#define X_LOAD() do { gld<0>(xc[0], x, xo); gld<384>(xc[1], x, xo); gld<768>(xc[2], x, xo); } while (0)
  X_LOAD();
  set_m0(adst + 0u);
  glds_m<0>(a_s0 + oc0, va0);
  glds_m<1024>(a_s0 + oc0, va1);
  glds_m<2048>(a_s0 + oc0, va2);
  glds_m<3072>(a_s0 + oc0, va3);
  set_m0(adst + 4096u);
  glds_m<0>(a_s1 + oc0, va0);
  glds_m<1024>(a_s1 + oc0, va1);
  glds_m<2048>(a_s1 + oc0, va2);
  glds_m<3072>(a_s1 + oc0, va3);
  set_m0(wdst + 0u);
  glds_m<0>(wk_s + ok0, wv0);
  glds_m<1024>(wk_s + ok0, wv1);
  glds_m<2048>(wk_s + ok0, wv2);
  glds_m<3072>(wk_s + ok0, wv3);
  set_m0(wdst + 16256u);
  glds_m<128>(wk_s + ok0, wv0);
  glds_m<1152>(wk_s + ok0, wv1);
  glds_m<2176>(wk_s + ok0, wv2);
  glds_m<3200>(wk_s + ok0, wv3);
  asm volatile("" : "+v"(tid), "+v"(lane)); asm volatile("" : "+s"(wid), "+s"(bid), "+s"(ws), "+s"(of1));
  const int r16 = lane & 15, q = lane >> 4, h = (bid & 7) * 4 + ((bid >> 3) & 3), tb = bid >> 5;
  if (tid < 32 && bid < C_N) asm volatile("global_store_dword %0, %1, %2" :: "v"((unsigned)(bid * 128 + tid * 4)), "v"(0u), "s"(ws + WS_CNT) : "memory");
  const unsigned ar0 = adst + (unsigned)r16 * 128u + ((unsigned)q ^ (unsigned)(r16 >> 1)) * 16u, ar1 = ar0 ^ 64u;
  const char* wv_s = (const char*)wuv + of1;
  lds_cp wrb = lds + (unsigned)q * 1024u + (unsigned)((lane >> 2) & 3) * 64u + (unsigned)(lane & 3) * 8u;
  lds_cp wr0[3], wr1[3];
#pragma unroll
  for (int i = 0; i < 3; ++i) { wr0[i] = opq(wrb + (unsigned)(q & 1) * 32u + i * 65536); wr1[i] = opq(wrb + (unsigned)((q & 1) ^ 1) * 32u + i * 65536); }
  char* ko = ws + WS_K + (size_t)(h * 64 + tb * 8 + wid * 2) * 8192;
  char* vo = ws + WS_V + (size_t)(h * 64 + tb * 8 + wid * 2) * 8192;
  const unsigned lo0 = (unsigned)lane * 16u, lo1 = lo0 + 4096u, lo2 = lo0 + 8192u, lo3 = lo0 + 12288u;
  const int xl = lane & 31, row = bid * 8 + wid * 2 + (lane >> 5);
  const bool xact = xl < 24;
#define X_LN() do { float v[24]; float s = 0.f, qq = 0.f; \
    _Pragma("unroll") for (int i = 0; i < 12; ++i) { const unsigned w_ = __builtin_bit_cast(u32x4, xc[i >> 2])[i & 3]; v[2 * i] = bf_lo(w_); v[2 * i + 1] = bf_hi(w_); s += v[2 * i] + v[2 * i + 1]; } \
    const float mean = w32_sum(xact ? s : 0.f) * (1.0f / DM); \
    _Pragma("unroll") for (int i = 0; i < 24; ++i) { v[i] -= mean; qq += v[i] * v[i]; } \
    const float rstd = QSCALE / sqrtf(w32_sum(xact ? qq : 0.f) * (1.0f / DM) + 1e-5f); \
    char* xnb = ws + WS_XN + (size_t)(row >> 4) * 18 * 1024 + (row & 15) * 16 + xl * 256; \
    if (xact) { _Pragma("unroll") for (int c = 0; c < 3; ++c) \
      st16(xnb + c * 6144, (u32x4){cvtpk(v[8 * c] * rstd, v[8 * c + 1] * rstd), cvtpk(v[8 * c + 2] * rstd, v[8 * c + 3] * rstd), \
                                    cvtpk(v[8 * c + 4] * rstd, v[8 * c + 5] * rstd), cvtpk(v[8 * c + 6] * rstd, v[8 * c + 7] * rstd)}); } } while (0)
  f32x4 ak0[4], ak1[4], ak2[4], ak3[4], ak4[4], ak5[4], ak6[4], ak7[4], av0[4], av1[4], av2[4], av3[4];
  s16x4 wl[6], wh[6]; bf16x8 bq[16][4]; unsigned pk[2][4]; const char* dp;
  unsigned tq = (unsigned)((bid * 4 + wid) * 36 + (lane < 36 ? lane : 0)) * 128u; PIN(tq);  PIN(lo1); PIN(lo2); PIN(lo3); PINS(ko); PINS(vo);
  unsigned ok2 = ok0 ^ (2u << 20), ok3 = ok1 ^ (2u << 20), oc1 = ok1 >> 12, oc2 = ok2 >> 12, oc3 = ok3 >> 12;
  const int hv = tb >> 2;
  const char *wv_h0 = wv_s + hv * 128, *wv_h1 = wv_s + (1 - hv) * 128; char *vo0 = vo + hv * 4096, *vo1 = vo + (1 - hv) * 4096;
  PINS(ok2); PINS(ok3); PINS(oc1); PINS(oc2); PINS(oc3); PINS(wv_h0); PINS(wv_h1); PINS(vo0); PINS(vo1);
  set_m0(adst + 8064u);
  glds_m<128>(a_s0 + oc0, va0);
  glds_m<1152>(a_s0 + oc0, va1);
  glds_m<2176>(a_s0 + oc0, va2);
  glds_m<3200>(a_s0 + oc0, va3);
  set_m0(adst + 12160u);
  glds_m<128>(a_s1 + oc0, va0);
  glds_m<1152>(a_s1 + oc0, va1);
  glds_m<2176>(a_s1 + oc0, va2);
  glds_m<3200>(a_s1 + oc0, va3);
  SBAR(); PINS(wo); PINS(w1); PINS(w2);
  RT_DESC(); SBAR();
  set_m0(wdst + 32768u);
  glds_m<0>(wk_s + ok1, wv0);
  glds_m<1024>(wk_s + ok1, wv1);
  glds_m<2048>(wk_s + ok1, wv2);
  glds_m<3072>(wk_s + ok1, wv3);
  SBAR();
  asm volatile("s_waitcnt vmcnt(29)" : "+v"(xc[0]), "+v"(xc[1]), "+v"(xc[2]) :: "memory");
  X_LN();
  SBAR();
  set_m0(wdst + 49024u);
  glds_m<128>(wk_s + ok1, wv0);
  glds_m<1152>(wk_s + ok1, wv1);
  glds_m<2176>(wk_s + ok1, wv2);
  glds_m<3200>(wk_s + ok1, wv3);
  TOUCHS((unsigned)(lane & 15) * 262144u + (unsigned)(bid * 128), (wid & 1) ? wq : wuv, 0);
  asm volatile("s_waitcnt vmcnt(29)" :  :: "memory");
  DSRA(bq[0][0], ar0, 0);
  DSRA(bq[0][1], ar0, 2048);
  DSRA(bq[0][2], ar0, 4096);
  DSRA(bq[0][3], ar0, 6144);
  DSRA(bq[1][0], ar1, 0);
  DSRA(bq[1][1], ar1, 2048);
  DSRA(bq[1][2], ar1, 4096);
  DSRA(bq[1][3], ar1, 6144);
  asm volatile("s_waitcnt vmcnt(21) lgkmcnt(0)\n\ts_barrier" :  :: "memory");
  wl[0] = lds_tr(wr0[0] + 0);
  wh[0] = lds_tr(wr0[0] + 256);
  wl[1] = lds_tr(wr1[0] + 0);
  wh[1] = lds_tr(wr1[0] + 256);
  wl[2] = lds_tr(wr0[0] + 512);
  wh[2] = lds_tr(wr0[0] + 768);
  wl[3] = lds_tr(wr1[0] + 512);
  wh[3] = lds_tr(wr1[0] + 768);
  MF0(ak0[0], cat2(wl[0], wh[0]), bq[0][0]); SBAR();
  wl[4] = lds_tr(wr0[0] + 16384);
  MF0(ak0[1], cat2(wl[0], wh[0]), bq[0][1]); SBAR();
  wh[4] = lds_tr(wr0[0] + 16640);
  MF0(ak0[2], cat2(wl[0], wh[0]), bq[0][2]); SBAR();
  dp = a_s0 + oc1; PINS(dp);
  MF0(ak0[3], cat2(wl[0], wh[0]), bq[0][3]); SBAR();
  M0ADD(adst, 0);
  MF0(ak1[0], cat2(wl[1], wh[1]), bq[0][0]); SBAR();
  wl[5] = lds_tr(wr1[0] + 16384);
  MF0(ak1[1], cat2(wl[1], wh[1]), bq[0][1]); SBAR();
  wh[5] = lds_tr(wr1[0] + 16640);
  MF0(ak1[2], cat2(wl[1], wh[1]), bq[0][2]); SBAR();
  glds_m<0>(dp, va0);
  MF0(ak1[3], cat2(wl[1], wh[1]), bq[0][3]); SBAR();
  glds_m<1024>(dp, va1);
  MF0(ak2[0], cat2(wl[2], wh[2]), bq[0][0]); SBAR();
  wl[0] = lds_tr(wr0[0] + 16896);
  MF0(ak2[1], cat2(wl[2], wh[2]), bq[0][1]); SBAR();
  wh[0] = lds_tr(wr0[0] + 17152);
  MF0(ak2[2], cat2(wl[2], wh[2]), bq[0][2]); SBAR();
  glds_m<2048>(dp, va2);
  MF0(ak2[3], cat2(wl[2], wh[2]), bq[0][3]); SBAR();
  glds_m<3072>(dp, va3);
  MF0(ak3[0], cat2(wl[3], wh[3]), bq[0][0]); SBAR();
  wl[1] = lds_tr(wr1[0] + 16896);
  MF0(ak3[1], cat2(wl[3], wh[3]), bq[0][1]); SBAR();
  wh[1] = lds_tr(wr1[0] + 17152);
  MF0(ak3[2], cat2(wl[3], wh[3]), bq[0][2]); SBAR();
  dp = a_s1 + oc1; PINS(dp);
  MF0(ak3[3], cat2(wl[3], wh[3]), bq[0][3]); SBAR();
  M0ADD(adst, 4096);
  MF0(ak4[0], cat2(wl[4], wh[4]), bq[0][0]); SBAR();
  wl[2] = lds_tr(wr0[0] + 4096);
  MF0(ak4[1], cat2(wl[4], wh[4]), bq[0][1]); SBAR();
  wh[2] = lds_tr(wr0[0] + 4352);
  MF0(ak4[2], cat2(wl[4], wh[4]), bq[0][2]); SBAR();
  glds_m<0>(dp, va0);
  MF0(ak4[3], cat2(wl[4], wh[4]), bq[0][3]); SBAR();
  glds_m<1024>(dp, va1);
  MF0(ak5[0], cat2(wl[5], wh[5]), bq[0][0]); SBAR();
  wl[3] = lds_tr(wr1[0] + 4096);
  MF0(ak5[1], cat2(wl[5], wh[5]), bq[0][1]); SBAR();
  wh[3] = lds_tr(wr1[0] + 4352);
  MF0(ak5[2], cat2(wl[5], wh[5]), bq[0][2]); SBAR();
  glds_m<2048>(dp, va2);
  MF0(ak5[3], cat2(wl[5], wh[5]), bq[0][3]); SBAR();
  glds_m<3072>(dp, va3);
  MF0(ak6[0], cat2(wl[0], wh[0]), bq[0][0]); SBAR();
  wl[4] = lds_tr(wr0[0] + 4608);
  MF0(ak6[1], cat2(wl[0], wh[0]), bq[0][1]); SBAR();
  wh[4] = lds_tr(wr0[0] + 4864);
  MF0(ak6[2], cat2(wl[0], wh[0]), bq[0][2]); SBAR();
  dp = wk_s + ok2; PINS(dp);
  MF0(ak6[3], cat2(wl[0], wh[0]), bq[0][3]); SBAR();
  M0ADD(wdst, 65536);
  MF0(ak7[0], cat2(wl[1], wh[1]), bq[0][0]); SBAR();
  wl[5] = lds_tr(wr1[0] + 4608);
  MF0(ak7[1], cat2(wl[1], wh[1]), bq[0][1]); SBAR();
  wh[5] = lds_tr(wr1[0] + 4864);
  MF0(ak7[2], cat2(wl[1], wh[1]), bq[0][2]); SBAR();
  glds_m<0>(dp, wv0);
  MF0(ak7[3], cat2(wl[1], wh[1]), bq[0][3]); SBAR();
  glds_m<1024>(dp, wv1);
  MF(ak0[0], cat2(wl[2], wh[2]), bq[1][0]); SBAR();
  wl[0] = lds_tr(wr0[0] + 20480);
  MF(ak0[1], cat2(wl[2], wh[2]), bq[1][1]); SBAR();
  wh[0] = lds_tr(wr0[0] + 20736);
  MF(ak0[2], cat2(wl[2], wh[2]), bq[1][2]); SBAR();
  glds_m<2048>(dp, wv2);
  MF(ak0[3], cat2(wl[2], wh[2]), bq[1][3]); SBAR();
  glds_m<3072>(dp, wv3);
  MF(ak1[0], cat2(wl[3], wh[3]), bq[1][0]); SBAR();
  wl[1] = lds_tr(wr1[0] + 20480);
  asm volatile("s_waitcnt vmcnt(25)" :  :: "memory");
  MF(ak1[1], cat2(wl[3], wh[3]), bq[1][1]); SBAR();
  wh[1] = lds_tr(wr1[0] + 20736);
  MF(ak1[2], cat2(wl[3], wh[3]), bq[1][2]); SBAR();
  DSRA(bq[2][0], ar0, 8192);
  MF(ak1[3], cat2(wl[3], wh[3]), bq[1][3]); SBAR();
  DSRA(bq[2][1], ar0, 10240);
  MF(ak2[0], cat2(wl[4], wh[4]), bq[1][0]); SBAR();
  wl[2] = lds_tr(wr0[0] + 20992);
  MF(ak2[1], cat2(wl[4], wh[4]), bq[1][1]); SBAR();
  wh[2] = lds_tr(wr0[0] + 21248);
  MF(ak2[2], cat2(wl[4], wh[4]), bq[1][2]); SBAR();
  DSRA(bq[2][2], ar0, 12288);
  MF(ak2[3], cat2(wl[4], wh[4]), bq[1][3]); SBAR();
  DSRA(bq[2][3], ar0, 14336);
  MF(ak3[0], cat2(wl[5], wh[5]), bq[1][0]); SBAR();
  wl[3] = lds_tr(wr1[0] + 20992);
  MF(ak3[1], cat2(wl[5], wh[5]), bq[1][1]); SBAR();
  wh[3] = lds_tr(wr1[0] + 21248);
  MF(ak3[2], cat2(wl[5], wh[5]), bq[1][2]); SBAR();
  DSRA(bq[3][0], ar1, 8192);
  MF(ak3[3], cat2(wl[5], wh[5]), bq[1][3]); SBAR();
  DSRA(bq[3][1], ar1, 10240);
  MF(ak4[0], cat2(wl[0], wh[0]), bq[1][0]); SBAR();
  wl[4] = lds_tr(wr0[0] + 8192);
  MF(ak4[1], cat2(wl[0], wh[0]), bq[1][1]); SBAR();
  wh[4] = lds_tr(wr0[0] + 8448);
  MF(ak4[2], cat2(wl[0], wh[0]), bq[1][2]); SBAR();
  DSRA(bq[3][2], ar1, 12288);
  MF(ak4[3], cat2(wl[0], wh[0]), bq[1][3]); SBAR();
  DSRA(bq[3][3], ar1, 14336);
  MF(ak5[0], cat2(wl[1], wh[1]), bq[1][0]); SBAR();
  wl[5] = lds_tr(wr1[0] + 8192);
  MF(ak5[1], cat2(wl[1], wh[1]), bq[1][1]); SBAR();
  wh[5] = lds_tr(wr1[0] + 8448);
  MF(ak5[2], cat2(wl[1], wh[1]), bq[1][2]); SBAR();
  MF(ak5[3], cat2(wl[1], wh[1]), bq[1][3]); SBAR();
  MF(ak6[0], cat2(wl[2], wh[2]), bq[1][0]); SBAR();
  wl[0] = lds_tr(wr0[0] + 8704);
  MF(ak6[1], cat2(wl[2], wh[2]), bq[1][1]); SBAR();
  wh[0] = lds_tr(wr0[0] + 8960);
  MF(ak6[2], cat2(wl[2], wh[2]), bq[1][2]); SBAR();
  MF(ak6[3], cat2(wl[2], wh[2]), bq[1][3]); SBAR();
  asm volatile("s_waitcnt lgkmcnt(4)" :  :: "memory");
  MF(ak7[0], cat2(wl[3], wh[3]), bq[1][0]); SBAR();
  wl[1] = lds_tr(wr1[0] + 8704);
  MF(ak7[1], cat2(wl[3], wh[3]), bq[1][1]); SBAR();
  wh[1] = lds_tr(wr1[0] + 8960);
  MF(ak7[2], cat2(wl[3], wh[3]), bq[1][2]); SBAR();
  dp = a_s0 + oc1; PINS(dp);
  MF(ak7[3], cat2(wl[3], wh[3]), bq[1][3]); SBAR();
  asm volatile("s_waitcnt lgkmcnt(14)" :  :: "memory");
  MF(ak0[0], cat2(wl[4], wh[4]), bq[2][0]); SBAR();
  wl[2] = lds_tr(wr0[0] + 24576);
  M0ADD(adst, 8064);
  MF(ak0[1], cat2(wl[4], wh[4]), bq[2][1]); SBAR();
  wh[2] = lds_tr(wr0[0] + 24832);
  MF(ak0[2], cat2(wl[4], wh[4]), bq[2][2]); SBAR();
  glds_m<128>(dp, va0);
  MF(ak0[3], cat2(wl[4], wh[4]), bq[2][3]); SBAR();
  glds_m<1152>(dp, va1);
  MF(ak1[0], cat2(wl[5], wh[5]), bq[2][0]); SBAR();
  wl[3] = lds_tr(wr1[0] + 24576);
  MF(ak1[1], cat2(wl[5], wh[5]), bq[2][1]); SBAR();
  wh[3] = lds_tr(wr1[0] + 24832);
  MF(ak1[2], cat2(wl[5], wh[5]), bq[2][2]); SBAR();
  glds_m<2176>(dp, va2);
  MF(ak1[3], cat2(wl[5], wh[5]), bq[2][3]); SBAR();
  glds_m<3200>(dp, va3);
  MF(ak2[0], cat2(wl[0], wh[0]), bq[2][0]); SBAR();
  wl[4] = lds_tr(wr0[0] + 25088);
  MF(ak2[1], cat2(wl[0], wh[0]), bq[2][1]); SBAR();
  wh[4] = lds_tr(wr0[0] + 25344);
  MF(ak2[2], cat2(wl[0], wh[0]), bq[2][2]); SBAR();
  dp = a_s1 + oc1; PINS(dp);
  MF(ak2[3], cat2(wl[0], wh[0]), bq[2][3]); SBAR();
  M0ADD(adst, 12160);
  MF(ak3[0], cat2(wl[1], wh[1]), bq[2][0]); SBAR();
  wl[5] = lds_tr(wr1[0] + 25088);
  MF(ak3[1], cat2(wl[1], wh[1]), bq[2][1]); SBAR();
  wh[5] = lds_tr(wr1[0] + 25344);
  MF(ak3[2], cat2(wl[1], wh[1]), bq[2][2]); SBAR();
  glds_m<128>(dp, va0);
  MF(ak3[3], cat2(wl[1], wh[1]), bq[2][3]); SBAR();
  glds_m<1152>(dp, va1);
  MF(ak4[0], cat2(wl[2], wh[2]), bq[2][0]); SBAR();
  wl[0] = lds_tr(wr0[0] + 12288);
  MF(ak4[1], cat2(wl[2], wh[2]), bq[2][1]); SBAR();
  wh[0] = lds_tr(wr0[0] + 12544);
  MF(ak4[2], cat2(wl[2], wh[2]), bq[2][2]); SBAR();
  glds_m<2176>(dp, va2);
  MF(ak4[3], cat2(wl[2], wh[2]), bq[2][3]); SBAR();
  glds_m<3200>(dp, va3);
  MF(ak5[0], cat2(wl[3], wh[3]), bq[2][0]); SBAR();
  wl[1] = lds_tr(wr1[0] + 12288);
  MF(ak5[1], cat2(wl[3], wh[3]), bq[2][1]); SBAR();
  wh[1] = lds_tr(wr1[0] + 12544);
  MF(ak5[2], cat2(wl[3], wh[3]), bq[2][2]); SBAR();
  dp = wk_s + ok2; PINS(dp);
  MF(ak5[3], cat2(wl[3], wh[3]), bq[2][3]); SBAR();
  M0ADD(wdst, 81792);
  MF(ak6[0], cat2(wl[4], wh[4]), bq[2][0]); SBAR();
  wl[2] = lds_tr(wr0[0] + 12800);
  MF(ak6[1], cat2(wl[4], wh[4]), bq[2][1]); SBAR();
  wh[2] = lds_tr(wr0[0] + 13056);
  MF(ak6[2], cat2(wl[4], wh[4]), bq[2][2]); SBAR();
  glds_m<128>(dp, wv0);
  MF(ak6[3], cat2(wl[4], wh[4]), bq[2][3]); SBAR();
  glds_m<1152>(dp, wv1);
  MF(ak7[0], cat2(wl[5], wh[5]), bq[2][0]); SBAR();
  wl[3] = lds_tr(wr1[0] + 12800);
  MF(ak7[1], cat2(wl[5], wh[5]), bq[2][1]); SBAR();
  wh[3] = lds_tr(wr1[0] + 13056);
  MF(ak7[2], cat2(wl[5], wh[5]), bq[2][2]); SBAR();
  asm volatile("s_waitcnt vmcnt(23)\n\ts_barrier" :  :: "memory");
  MF(ak7[3], cat2(wl[5], wh[5]), bq[2][3]); SBAR();
  asm volatile("s_waitcnt lgkmcnt(15)" :  :: "memory");
  MF(ak0[0], cat2(wl[0], wh[0]), bq[3][0]); SBAR();
  wl[4] = lds_tr(wr0[0] + 28672);
  MF(ak0[1], cat2(wl[0], wh[0]), bq[3][1]); SBAR();
  wh[4] = lds_tr(wr0[0] + 28928);
  MF(ak0[2], cat2(wl[0], wh[0]), bq[3][2]); SBAR();
  glds_m<2176>(dp, wv2);
  MF(ak0[3], cat2(wl[0], wh[0]), bq[3][3]); SBAR();
  glds_m<3200>(dp, wv3);
  MF(ak1[0], cat2(wl[1], wh[1]), bq[3][0]); SBAR();
  wl[5] = lds_tr(wr1[0] + 28672);
  asm volatile("s_waitcnt vmcnt(16)" :  :: "memory");
  MF(ak1[1], cat2(wl[1], wh[1]), bq[3][1]); SBAR();
  wh[5] = lds_tr(wr1[0] + 28928);
  MF(ak1[2], cat2(wl[1], wh[1]), bq[3][2]); SBAR();
  DSRA(bq[4][0], ar0, 0);
  MF(ak1[3], cat2(wl[1], wh[1]), bq[3][3]); SBAR();
  DSRA(bq[4][1], ar0, 2048);
  MF(ak2[0], cat2(wl[2], wh[2]), bq[3][0]); SBAR();
  wl[0] = lds_tr(wr0[0] + 29184);
  MF(ak2[1], cat2(wl[2], wh[2]), bq[3][1]); SBAR();
  wh[0] = lds_tr(wr0[0] + 29440);
  MF(ak2[2], cat2(wl[2], wh[2]), bq[3][2]); SBAR();
  DSRA(bq[4][2], ar0, 4096);
  MF(ak2[3], cat2(wl[2], wh[2]), bq[3][3]); SBAR();
  DSRA(bq[4][3], ar0, 6144);
  MF(ak3[0], cat2(wl[3], wh[3]), bq[3][0]); SBAR();
  wl[1] = lds_tr(wr1[0] + 29184);
  MF(ak3[1], cat2(wl[3], wh[3]), bq[3][1]); SBAR();
  wh[1] = lds_tr(wr1[0] + 29440);
  MF(ak3[2], cat2(wl[3], wh[3]), bq[3][2]); SBAR();
  DSRA(bq[5][0], ar1, 0);
  MF(ak3[3], cat2(wl[3], wh[3]), bq[3][3]); SBAR();
  DSRA(bq[5][1], ar1, 2048);
  MF(ak4[0], cat2(wl[4], wh[4]), bq[3][0]); SBAR();
  wl[2] = lds_tr(wr0[0] + 32768);
  MF(ak4[1], cat2(wl[4], wh[4]), bq[3][1]); SBAR();
  wh[2] = lds_tr(wr0[0] + 33024);
  MF(ak4[2], cat2(wl[4], wh[4]), bq[3][2]); SBAR();
  DSRA(bq[5][2], ar1, 4096);
  MF(ak4[3], cat2(wl[4], wh[4]), bq[3][3]); SBAR();
  DSRA(bq[5][3], ar1, 6144);
  MF(ak5[0], cat2(wl[5], wh[5]), bq[3][0]); SBAR();
  wl[3] = lds_tr(wr1[0] + 32768);
  MF(ak5[1], cat2(wl[5], wh[5]), bq[3][1]); SBAR();
  wh[3] = lds_tr(wr1[0] + 33024);
  MF(ak5[2], cat2(wl[5], wh[5]), bq[3][2]); SBAR();
  MF(ak5[3], cat2(wl[5], wh[5]), bq[3][3]); SBAR();
  MF(ak6[0], cat2(wl[0], wh[0]), bq[3][0]); SBAR();
  wl[4] = lds_tr(wr0[0] + 33280);
  MF(ak6[1], cat2(wl[0], wh[0]), bq[3][1]); SBAR();
  wh[4] = lds_tr(wr0[0] + 33536);
  MF(ak6[2], cat2(wl[0], wh[0]), bq[3][2]); SBAR();
  MF(ak6[3], cat2(wl[0], wh[0]), bq[3][3]); SBAR();
  asm volatile("s_waitcnt lgkmcnt(4)" :  :: "memory");
  MF(ak7[0], cat2(wl[1], wh[1]), bq[3][0]); SBAR();
  wl[5] = lds_tr(wr1[0] + 33280);
  MF(ak7[1], cat2(wl[1], wh[1]), bq[3][1]); SBAR();
  wh[5] = lds_tr(wr1[0] + 33536);
  MF(ak7[2], cat2(wl[1], wh[1]), bq[3][2]); SBAR();
  dp = a_s0 + oc2; PINS(dp);
  MF(ak7[3], cat2(wl[1], wh[1]), bq[3][3]); SBAR();
  asm volatile("s_waitcnt lgkmcnt(14)" :  :: "memory");
  MF(ak0[0], cat2(wl[2], wh[2]), bq[4][0]); SBAR();
  wl[0] = lds_tr(wr0[0] + 49152);
  M0ADD(adst, 0);
  MF(ak0[1], cat2(wl[2], wh[2]), bq[4][1]); SBAR();
  wh[0] = lds_tr(wr0[0] + 49408);
  MF(ak0[2], cat2(wl[2], wh[2]), bq[4][2]); SBAR();
  glds_m<0>(dp, va0);
  MF(ak0[3], cat2(wl[2], wh[2]), bq[4][3]); SBAR();
  glds_m<1024>(dp, va1);
  MF(ak1[0], cat2(wl[3], wh[3]), bq[4][0]); SBAR();
  wl[1] = lds_tr(wr1[0] + 49152);
  MF(ak1[1], cat2(wl[3], wh[3]), bq[4][1]); SBAR();
  wh[1] = lds_tr(wr1[0] + 49408);
  MF(ak1[2], cat2(wl[3], wh[3]), bq[4][2]); SBAR();
  glds_m<2048>(dp, va2);
  MF(ak1[3], cat2(wl[3], wh[3]), bq[4][3]); SBAR();
  glds_m<3072>(dp, va3);
  MF(ak2[0], cat2(wl[4], wh[4]), bq[4][0]); SBAR();
  wl[2] = lds_tr(wr0[0] + 49664);
  MF(ak2[1], cat2(wl[4], wh[4]), bq[4][1]); SBAR();
  wh[2] = lds_tr(wr0[0] + 49920);
  MF(ak2[2], cat2(wl[4], wh[4]), bq[4][2]); SBAR();
  dp = a_s1 + oc2; PINS(dp);
  MF(ak2[3], cat2(wl[4], wh[4]), bq[4][3]); SBAR();
  M0ADD(adst, 4096);
  MF(ak3[0], cat2(wl[5], wh[5]), bq[4][0]); SBAR();
  wl[3] = lds_tr(wr1[0] + 49664);
  MF(ak3[1], cat2(wl[5], wh[5]), bq[4][1]); SBAR();
  wh[3] = lds_tr(wr1[0] + 49920);
  MF(ak3[2], cat2(wl[5], wh[5]), bq[4][2]); SBAR();
  glds_m<0>(dp, va0);
  MF(ak3[3], cat2(wl[5], wh[5]), bq[4][3]); SBAR();
  glds_m<1024>(dp, va1);
  MF(ak4[0], cat2(wl[0], wh[0]), bq[4][0]); SBAR();
  wl[4] = lds_tr(wr0[0] + 36864);
  MF(ak4[1], cat2(wl[0], wh[0]), bq[4][1]); SBAR();
  wh[4] = lds_tr(wr0[0] + 37120);
  MF(ak4[2], cat2(wl[0], wh[0]), bq[4][2]); SBAR();
  glds_m<2048>(dp, va2);
  MF(ak4[3], cat2(wl[0], wh[0]), bq[4][3]); SBAR();
  glds_m<3072>(dp, va3);
  MF(ak5[0], cat2(wl[1], wh[1]), bq[4][0]); SBAR();
  wl[5] = lds_tr(wr1[0] + 36864);
  MF(ak5[1], cat2(wl[1], wh[1]), bq[4][1]); SBAR();
  wh[5] = lds_tr(wr1[0] + 37120);
  MF(ak5[2], cat2(wl[1], wh[1]), bq[4][2]); SBAR();
  MF(ak5[3], cat2(wl[1], wh[1]), bq[4][3]); SBAR();
  MF(ak6[0], cat2(wl[2], wh[2]), bq[4][0]); SBAR();
  wl[0] = lds_tr(wr0[0] + 37376);
  MF(ak6[1], cat2(wl[2], wh[2]), bq[4][1]); SBAR();
  wh[0] = lds_tr(wr0[0] + 37632);
  MF(ak6[2], cat2(wl[2], wh[2]), bq[4][2]); SBAR();
  MF(ak6[3], cat2(wl[2], wh[2]), bq[4][3]); SBAR();
  MF(ak7[0], cat2(wl[3], wh[3]), bq[4][0]); SBAR();
  wl[1] = lds_tr(wr1[0] + 37376);
  MF(ak7[1], cat2(wl[3], wh[3]), bq[4][1]); SBAR();
  wh[1] = lds_tr(wr1[0] + 37632);
  MF(ak7[2], cat2(wl[3], wh[3]), bq[4][2]); SBAR();
  asm volatile("s_barrier" :  :: "memory");
  MF(ak7[3], cat2(wl[3], wh[3]), bq[4][3]); SBAR();
  asm volatile("s_waitcnt lgkmcnt(15)" :  :: "memory");
  MF(ak0[0], cat2(wl[4], wh[4]), bq[5][0]); SBAR();
  wl[2] = lds_tr(wr0[0] + 53248);
  MF(ak0[1], cat2(wl[4], wh[4]), bq[5][1]); SBAR();
  wh[2] = lds_tr(wr0[0] + 53504);
  MF(ak0[2], cat2(wl[4], wh[4]), bq[5][2]); SBAR();
  dp = wk_s + ok3; PINS(dp);
  MF(ak0[3], cat2(wl[4], wh[4]), bq[5][3]); SBAR();
  M0ADD(wdst, 0);
  MF(ak1[0], cat2(wl[5], wh[5]), bq[5][0]); SBAR();
  wl[3] = lds_tr(wr1[0] + 53248);
  MF(ak1[1], cat2(wl[5], wh[5]), bq[5][1]); SBAR();
  wh[3] = lds_tr(wr1[0] + 53504);
  MF(ak1[2], cat2(wl[5], wh[5]), bq[5][2]); SBAR();
  glds_m<0>(dp, wv0);
  MF(ak1[3], cat2(wl[5], wh[5]), bq[5][3]); SBAR();
  glds_m<1024>(dp, wv1);
  MF(ak2[0], cat2(wl[0], wh[0]), bq[5][0]); SBAR();
  wl[4] = lds_tr(wr0[0] + 53760);
  MF(ak2[1], cat2(wl[0], wh[0]), bq[5][1]); SBAR();
  wh[4] = lds_tr(wr0[0] + 54016);
  MF(ak2[2], cat2(wl[0], wh[0]), bq[5][2]); SBAR();
  glds_m<2048>(dp, wv2);
  MF(ak2[3], cat2(wl[0], wh[0]), bq[5][3]); SBAR();
  glds_m<3072>(dp, wv3);
  MF(ak3[0], cat2(wl[1], wh[1]), bq[5][0]); SBAR();
  wl[5] = lds_tr(wr1[0] + 53760);
  asm volatile("s_waitcnt vmcnt(16)" :  :: "memory");
  MF(ak3[1], cat2(wl[1], wh[1]), bq[5][1]); SBAR();
  wh[5] = lds_tr(wr1[0] + 54016);
  MF(ak3[2], cat2(wl[1], wh[1]), bq[5][2]); SBAR();
  DSRA(bq[6][0], ar0, 8192);
  MF(ak3[3], cat2(wl[1], wh[1]), bq[5][3]); SBAR();
  DSRA(bq[6][1], ar0, 10240);
  MF(ak4[0], cat2(wl[2], wh[2]), bq[5][0]); SBAR();
  wl[0] = lds_tr(wr0[0] + 40960);
  MF(ak4[1], cat2(wl[2], wh[2]), bq[5][1]); SBAR();
  wh[0] = lds_tr(wr0[0] + 41216);
  MF(ak4[2], cat2(wl[2], wh[2]), bq[5][2]); SBAR();
  DSRA(bq[6][2], ar0, 12288);
  MF(ak4[3], cat2(wl[2], wh[2]), bq[5][3]); SBAR();
  DSRA(bq[6][3], ar0, 14336);
  MF(ak5[0], cat2(wl[3], wh[3]), bq[5][0]); SBAR();
  wl[1] = lds_tr(wr1[0] + 40960);
  MF(ak5[1], cat2(wl[3], wh[3]), bq[5][1]); SBAR();
  wh[1] = lds_tr(wr1[0] + 41216);
  MF(ak5[2], cat2(wl[3], wh[3]), bq[5][2]); SBAR();
  DSRA(bq[7][0], ar1, 8192);
  MF(ak5[3], cat2(wl[3], wh[3]), bq[5][3]); SBAR();
  DSRA(bq[7][1], ar1, 10240);
  MF(ak6[0], cat2(wl[4], wh[4]), bq[5][0]); SBAR();
  wl[2] = lds_tr(wr0[0] + 41472);
  MF(ak6[1], cat2(wl[4], wh[4]), bq[5][1]); SBAR();
  wh[2] = lds_tr(wr0[0] + 41728);
  MF(ak6[2], cat2(wl[4], wh[4]), bq[5][2]); SBAR();
  DSRA(bq[7][2], ar1, 12288);
  MF(ak6[3], cat2(wl[4], wh[4]), bq[5][3]); SBAR();
  DSRA(bq[7][3], ar1, 14336);
  MF(ak7[0], cat2(wl[5], wh[5]), bq[5][0]); SBAR();
  wl[3] = lds_tr(wr1[0] + 41472);
  asm volatile("s_waitcnt lgkmcnt(1)" :  :: "memory");
  MF(ak7[1], cat2(wl[5], wh[5]), bq[5][1]); SBAR();
  wh[3] = lds_tr(wr1[0] + 41728);
  MF(ak7[2], cat2(wl[5], wh[5]), bq[5][2]); SBAR();
  dp = a_s0 + oc2; PINS(dp);
  MF(ak7[3], cat2(wl[5], wh[5]), bq[5][3]); SBAR();
  asm volatile("s_waitcnt lgkmcnt(10)" :  :: "memory");
  MF(ak0[0], cat2(wl[0], wh[0]), bq[6][0]); SBAR();
  wl[4] = lds_tr(wr0[0] + 57344);
  M0ADD(adst, 8064);
  MF(ak0[1], cat2(wl[0], wh[0]), bq[6][1]); SBAR();
  wh[4] = lds_tr(wr0[0] + 57600);
  MF(ak0[2], cat2(wl[0], wh[0]), bq[6][2]); SBAR();
  glds_m<128>(dp, va0);
  MF(ak0[3], cat2(wl[0], wh[0]), bq[6][3]); SBAR();
  glds_m<1152>(dp, va1);
  MF(ak1[0], cat2(wl[1], wh[1]), bq[6][0]); SBAR();
  wl[5] = lds_tr(wr1[0] + 57344);
  MF(ak1[1], cat2(wl[1], wh[1]), bq[6][1]); SBAR();
  wh[5] = lds_tr(wr1[0] + 57600);
  MF(ak1[2], cat2(wl[1], wh[1]), bq[6][2]); SBAR();
  glds_m<2176>(dp, va2);
  MF(ak1[3], cat2(wl[1], wh[1]), bq[6][3]); SBAR();
  glds_m<3200>(dp, va3);
  MF(ak2[0], cat2(wl[2], wh[2]), bq[6][0]); SBAR();
  wl[0] = lds_tr(wr0[0] + 57856);
  MF(ak2[1], cat2(wl[2], wh[2]), bq[6][1]); SBAR();
  wh[0] = lds_tr(wr0[0] + 58112);
  MF(ak2[2], cat2(wl[2], wh[2]), bq[6][2]); SBAR();
  dp = a_s1 + oc2; PINS(dp);
  MF(ak2[3], cat2(wl[2], wh[2]), bq[6][3]); SBAR();
  M0ADD(adst, 12160);
  MF(ak3[0], cat2(wl[3], wh[3]), bq[6][0]); SBAR();
  wl[1] = lds_tr(wr1[0] + 57856);
  MF(ak3[1], cat2(wl[3], wh[3]), bq[6][1]); SBAR();
  wh[1] = lds_tr(wr1[0] + 58112);
  MF(ak3[2], cat2(wl[3], wh[3]), bq[6][2]); SBAR();
  glds_m<128>(dp, va0);
  MF(ak3[3], cat2(wl[3], wh[3]), bq[6][3]); SBAR();
  glds_m<1152>(dp, va1);
  MF(ak4[0], cat2(wl[4], wh[4]), bq[6][0]); SBAR();
  wl[2] = lds_tr(wr0[0] + 45056);
  MF(ak4[1], cat2(wl[4], wh[4]), bq[6][1]); SBAR();
  wh[2] = lds_tr(wr0[0] + 45312);
  MF(ak4[2], cat2(wl[4], wh[4]), bq[6][2]); SBAR();
  glds_m<2176>(dp, va2);
  MF(ak4[3], cat2(wl[4], wh[4]), bq[6][3]); SBAR();
  glds_m<3200>(dp, va3);
  MF(ak5[0], cat2(wl[5], wh[5]), bq[6][0]); SBAR();
  wl[3] = lds_tr(wr1[0] + 45056);
  MF(ak5[1], cat2(wl[5], wh[5]), bq[6][1]); SBAR();
  wh[3] = lds_tr(wr1[0] + 45312);
  MF(ak5[2], cat2(wl[5], wh[5]), bq[6][2]); SBAR();
  dp = wk_s + ok3; PINS(dp);
  MF(ak5[3], cat2(wl[5], wh[5]), bq[6][3]); SBAR();
  M0ADD(wdst, 16256);
  MF(ak6[0], cat2(wl[0], wh[0]), bq[6][0]); SBAR();
  wl[4] = lds_tr(wr0[0] + 45568);
  MF(ak6[1], cat2(wl[0], wh[0]), bq[6][1]); SBAR();
  wh[4] = lds_tr(wr0[0] + 45824);
  MF(ak6[2], cat2(wl[0], wh[0]), bq[6][2]); SBAR();
  glds_m<128>(dp, wv0);
  MF(ak6[3], cat2(wl[0], wh[0]), bq[6][3]); SBAR();
  glds_m<1152>(dp, wv1);
  MF(ak7[0], cat2(wl[1], wh[1]), bq[6][0]); SBAR();
  wl[5] = lds_tr(wr1[0] + 45568);
  MF(ak7[1], cat2(wl[1], wh[1]), bq[6][1]); SBAR();
  wh[5] = lds_tr(wr1[0] + 45824);
  MF(ak7[2], cat2(wl[1], wh[1]), bq[6][2]); SBAR();
  asm volatile("s_waitcnt vmcnt(22)\n\ts_barrier" :  :: "memory");
  MF(ak7[3], cat2(wl[1], wh[1]), bq[6][3]); SBAR();
  asm volatile("s_waitcnt lgkmcnt(15)" :  :: "memory");
  MF(ak0[0], cat2(wl[2], wh[2]), bq[7][0]); SBAR();
  wl[0] = lds_tr(wr0[0] + 61440);
  MF(ak0[1], cat2(wl[2], wh[2]), bq[7][1]); SBAR();
  wh[0] = lds_tr(wr0[0] + 61696);
  MF(ak0[2], cat2(wl[2], wh[2]), bq[7][2]); SBAR();
  glds_m<2176>(dp, wv2);
  MF(ak0[3], cat2(wl[2], wh[2]), bq[7][3]); SBAR();
  glds_m<3200>(dp, wv3);
  MF(ak1[0], cat2(wl[3], wh[3]), bq[7][0]); SBAR();
  wl[1] = lds_tr(wr1[0] + 61440);
  asm volatile("s_waitcnt vmcnt(16)" :  :: "memory");
  MF(ak1[1], cat2(wl[3], wh[3]), bq[7][1]); SBAR();
  wh[1] = lds_tr(wr1[0] + 61696);
  MF(ak1[2], cat2(wl[3], wh[3]), bq[7][2]); SBAR();
  DSRA(bq[8][0], ar0, 0);
  MF(ak1[3], cat2(wl[3], wh[3]), bq[7][3]); SBAR();
  DSRA(bq[8][1], ar0, 2048);
  MF(ak2[0], cat2(wl[4], wh[4]), bq[7][0]); SBAR();
  wl[2] = lds_tr(wr0[0] + 61952);
  MF(ak2[1], cat2(wl[4], wh[4]), bq[7][1]); SBAR();
  wh[2] = lds_tr(wr0[0] + 62208);
  MF(ak2[2], cat2(wl[4], wh[4]), bq[7][2]); SBAR();
  DSRA(bq[8][2], ar0, 4096);
  MF(ak2[3], cat2(wl[4], wh[4]), bq[7][3]); SBAR();
  DSRA(bq[8][3], ar0, 6144);
  MF(ak3[0], cat2(wl[5], wh[5]), bq[7][0]); SBAR();
  wl[3] = lds_tr(wr1[0] + 61952);
  MF(ak3[1], cat2(wl[5], wh[5]), bq[7][1]); SBAR();
  wh[3] = lds_tr(wr1[0] + 62208);
  MF(ak3[2], cat2(wl[5], wh[5]), bq[7][2]); SBAR();
  DSRA(bq[9][0], ar1, 0);
  MF(ak3[3], cat2(wl[5], wh[5]), bq[7][3]); SBAR();
  DSRA(bq[9][1], ar1, 2048);
  MF(ak4[0], cat2(wl[0], wh[0]), bq[7][0]); SBAR();
  wl[4] = lds_tr(wr0[1] + 0);
  MF(ak4[1], cat2(wl[0], wh[0]), bq[7][1]); SBAR();
  wh[4] = lds_tr(wr0[1] + 256);
  MF(ak4[2], cat2(wl[0], wh[0]), bq[7][2]); SBAR();
  DSRA(bq[9][2], ar1, 4096);
  MF(ak4[3], cat2(wl[0], wh[0]), bq[7][3]); SBAR();
  DSRA(bq[9][3], ar1, 6144);
  MF(ak5[0], cat2(wl[1], wh[1]), bq[7][0]); SBAR();
  wl[5] = lds_tr(wr1[1] + 0);
  MF(ak5[1], cat2(wl[1], wh[1]), bq[7][1]); SBAR();
  wh[5] = lds_tr(wr1[1] + 256);
  MF(ak5[2], cat2(wl[1], wh[1]), bq[7][2]); SBAR();
  MF(ak5[3], cat2(wl[1], wh[1]), bq[7][3]); SBAR();
  MF(ak6[0], cat2(wl[2], wh[2]), bq[7][0]); SBAR();
  wl[0] = lds_tr(wr0[1] + 512);
  MF(ak6[1], cat2(wl[2], wh[2]), bq[7][1]); SBAR();
  wh[0] = lds_tr(wr0[1] + 768);
  MF(ak6[2], cat2(wl[2], wh[2]), bq[7][2]); SBAR();
  MF(ak6[3], cat2(wl[2], wh[2]), bq[7][3]); SBAR();
  asm volatile("s_waitcnt lgkmcnt(4)" :  :: "memory");
  MF(ak7[0], cat2(wl[3], wh[3]), bq[7][0]); SBAR();
  wl[1] = lds_tr(wr1[1] + 512);
  MF(ak7[1], cat2(wl[3], wh[3]), bq[7][1]); SBAR();
  wh[1] = lds_tr(wr1[1] + 768);
  MF(ak7[2], cat2(wl[3], wh[3]), bq[7][2]); SBAR();
  dp = a_s0 + oc3; PINS(dp);
  MF(ak7[3], cat2(wl[3], wh[3]), bq[7][3]); SBAR();
  asm volatile("s_waitcnt lgkmcnt(14)" :  :: "memory");
  MF(ak0[0], cat2(wl[4], wh[4]), bq[8][0]); SBAR();
  wl[2] = lds_tr(wr0[1] + 16384);
  M0ADD(adst, 0);
  MF(ak0[1], cat2(wl[4], wh[4]), bq[8][1]); SBAR();
  wh[2] = lds_tr(wr0[1] + 16640);
  MF(ak0[2], cat2(wl[4], wh[4]), bq[8][2]); SBAR();
  glds_m<0>(dp, va0);
  MF(ak0[3], cat2(wl[4], wh[4]), bq[8][3]); SBAR();
  glds_m<1024>(dp, va1);
  MF(ak1[0], cat2(wl[5], wh[5]), bq[8][0]); SBAR();
  wl[3] = lds_tr(wr1[1] + 16384);
  MF(ak1[1], cat2(wl[5], wh[5]), bq[8][1]); SBAR();
  wh[3] = lds_tr(wr1[1] + 16640);
  MF(ak1[2], cat2(wl[5], wh[5]), bq[8][2]); SBAR();
  glds_m<2048>(dp, va2);
  MF(ak1[3], cat2(wl[5], wh[5]), bq[8][3]); SBAR();
  glds_m<3072>(dp, va3);
  MF(ak2[0], cat2(wl[0], wh[0]), bq[8][0]); SBAR();
  wl[4] = lds_tr(wr0[1] + 16896);
  MF(ak2[1], cat2(wl[0], wh[0]), bq[8][1]); SBAR();
  wh[4] = lds_tr(wr0[1] + 17152);
  MF(ak2[2], cat2(wl[0], wh[0]), bq[8][2]); SBAR();
  dp = a_s1 + oc3; PINS(dp);
  MF(ak2[3], cat2(wl[0], wh[0]), bq[8][3]); SBAR();
  M0ADD(adst, 4096);
  MF(ak3[0], cat2(wl[1], wh[1]), bq[8][0]); SBAR();
  wl[5] = lds_tr(wr1[1] + 16896);
  MF(ak3[1], cat2(wl[1], wh[1]), bq[8][1]); SBAR();
  wh[5] = lds_tr(wr1[1] + 17152);
  MF(ak3[2], cat2(wl[1], wh[1]), bq[8][2]); SBAR();
  glds_m<0>(dp, va0);
  MF(ak3[3], cat2(wl[1], wh[1]), bq[8][3]); SBAR();
  glds_m<1024>(dp, va1);
  MF(ak4[0], cat2(wl[2], wh[2]), bq[8][0]); SBAR();
  wl[0] = lds_tr(wr0[1] + 4096);
  MF(ak4[1], cat2(wl[2], wh[2]), bq[8][1]); SBAR();
  wh[0] = lds_tr(wr0[1] + 4352);
  MF(ak4[2], cat2(wl[2], wh[2]), bq[8][2]); SBAR();
  glds_m<2048>(dp, va2);
  MF(ak4[3], cat2(wl[2], wh[2]), bq[8][3]); SBAR();
  glds_m<3072>(dp, va3);
  MF(ak5[0], cat2(wl[3], wh[3]), bq[8][0]); SBAR();
  wl[1] = lds_tr(wr1[1] + 4096);
  MF(ak5[1], cat2(wl[3], wh[3]), bq[8][1]); SBAR();
  wh[1] = lds_tr(wr1[1] + 4352);
  MF(ak5[2], cat2(wl[3], wh[3]), bq[8][2]); SBAR();
  MF(ak5[3], cat2(wl[3], wh[3]), bq[8][3]); SBAR();
  MF(ak6[0], cat2(wl[4], wh[4]), bq[8][0]); SBAR();
  wl[2] = lds_tr(wr0[1] + 4608);
  MF(ak6[1], cat2(wl[4], wh[4]), bq[8][1]); SBAR();
  wh[2] = lds_tr(wr0[1] + 4864);
  MF(ak6[2], cat2(wl[4], wh[4]), bq[8][2]); SBAR();
  MF(ak6[3], cat2(wl[4], wh[4]), bq[8][3]); SBAR();
  MF(ak7[0], cat2(wl[5], wh[5]), bq[8][0]); SBAR();
  wl[3] = lds_tr(wr1[1] + 4608);
  MF(ak7[1], cat2(wl[5], wh[5]), bq[8][1]); SBAR();
  wh[3] = lds_tr(wr1[1] + 4864);
  MF(ak7[2], cat2(wl[5], wh[5]), bq[8][2]); SBAR();
  asm volatile("s_barrier" :  :: "memory");
  MF(ak7[3], cat2(wl[5], wh[5]), bq[8][3]); SBAR();
  asm volatile("s_waitcnt lgkmcnt(15)" :  :: "memory");
  MF(ak0[0], cat2(wl[0], wh[0]), bq[9][0]); SBAR();
  wl[4] = lds_tr(wr0[1] + 20480);
  MF(ak0[1], cat2(wl[0], wh[0]), bq[9][1]); SBAR();
  wh[4] = lds_tr(wr0[1] + 20736);
  MF(ak0[2], cat2(wl[0], wh[0]), bq[9][2]); SBAR();
  dp = wv_h0 + ok0; PINS(dp);
  MF(ak0[3], cat2(wl[0], wh[0]), bq[9][3]); SBAR();
  M0ADD(wdst, 32768);
  MF(ak1[0], cat2(wl[1], wh[1]), bq[9][0]); SBAR();
  wl[5] = lds_tr(wr1[1] + 20480);
  MF(ak1[1], cat2(wl[1], wh[1]), bq[9][1]); SBAR();
  wh[5] = lds_tr(wr1[1] + 20736);
  MF(ak1[2], cat2(wl[1], wh[1]), bq[9][2]); SBAR();
  glds_m<0>(dp, wv0);
  MF(ak1[3], cat2(wl[1], wh[1]), bq[9][3]); SBAR();
  glds_m<1024>(dp, wv1);
  MF(ak2[0], cat2(wl[2], wh[2]), bq[9][0]); SBAR();
  wl[0] = lds_tr(wr0[1] + 20992);
  MF(ak2[1], cat2(wl[2], wh[2]), bq[9][1]); SBAR();
  wh[0] = lds_tr(wr0[1] + 21248);
  MF(ak2[2], cat2(wl[2], wh[2]), bq[9][2]); SBAR();
  glds_m<2048>(dp, wv2);
  MF(ak2[3], cat2(wl[2], wh[2]), bq[9][3]); SBAR();
  glds_m<3072>(dp, wv3);
  MF(ak3[0], cat2(wl[3], wh[3]), bq[9][0]); SBAR();
  wl[1] = lds_tr(wr1[1] + 20992);
  asm volatile("s_waitcnt vmcnt(16)" :  :: "memory");
  MF(ak3[1], cat2(wl[3], wh[3]), bq[9][1]); SBAR();
  wh[1] = lds_tr(wr1[1] + 21248);
  MF(ak3[2], cat2(wl[3], wh[3]), bq[9][2]); SBAR();
  DSRA(bq[10][0], ar0, 8192);
  MF(ak3[3], cat2(wl[3], wh[3]), bq[9][3]); SBAR();
  DSRA(bq[10][1], ar0, 10240);
  MF(ak4[0], cat2(wl[4], wh[4]), bq[9][0]); SBAR();
  wl[2] = lds_tr(wr0[1] + 8192);
  MF(ak4[1], cat2(wl[4], wh[4]), bq[9][1]); SBAR();
  wh[2] = lds_tr(wr0[1] + 8448);
  MF(ak4[2], cat2(wl[4], wh[4]), bq[9][2]); SBAR();
  DSRA(bq[10][2], ar0, 12288);
  MF(ak4[3], cat2(wl[4], wh[4]), bq[9][3]); SBAR();
  DSRA(bq[10][3], ar0, 14336);
  MF(ak5[0], cat2(wl[5], wh[5]), bq[9][0]); SBAR();
  wl[3] = lds_tr(wr1[1] + 8192);
  MF(ak5[1], cat2(wl[5], wh[5]), bq[9][1]); SBAR();
  wh[3] = lds_tr(wr1[1] + 8448);
  MF(ak5[2], cat2(wl[5], wh[5]), bq[9][2]); SBAR();
  DSRA(bq[11][0], ar1, 8192);
  MF(ak5[3], cat2(wl[5], wh[5]), bq[9][3]); SBAR();
  DSRA(bq[11][1], ar1, 10240);
  MF(ak6[0], cat2(wl[0], wh[0]), bq[9][0]); SBAR();
  wl[4] = lds_tr(wr0[1] + 8704);
  MF(ak6[1], cat2(wl[0], wh[0]), bq[9][1]); SBAR();
  wh[4] = lds_tr(wr0[1] + 8960);
  MF(ak6[2], cat2(wl[0], wh[0]), bq[9][2]); SBAR();
  DSRA(bq[11][2], ar1, 12288);
  MF(ak6[3], cat2(wl[0], wh[0]), bq[9][3]); SBAR();
  DSRA(bq[11][3], ar1, 14336);
  MF(ak7[0], cat2(wl[1], wh[1]), bq[9][0]); SBAR();
  wl[5] = lds_tr(wr1[1] + 8704);
  asm volatile("s_waitcnt lgkmcnt(1)" :  :: "memory");
  MF(ak7[1], cat2(wl[1], wh[1]), bq[9][1]); SBAR();
  wh[5] = lds_tr(wr1[1] + 8960);
  MF(ak7[2], cat2(wl[1], wh[1]), bq[9][2]); SBAR();
  dp = a_s0 + oc3; PINS(dp);
  MF(ak7[3], cat2(wl[1], wh[1]), bq[9][3]); SBAR();
  asm volatile("s_waitcnt lgkmcnt(10)" :  :: "memory");
  MF(ak0[0], cat2(wl[2], wh[2]), bq[10][0]); SBAR();
  wl[0] = lds_tr(wr0[1] + 24576);
  M0ADD(adst, 8064);
  MF(ak0[1], cat2(wl[2], wh[2]), bq[10][1]); SBAR();
  wh[0] = lds_tr(wr0[1] + 24832);
  MF(ak0[2], cat2(wl[2], wh[2]), bq[10][2]); SBAR();
  glds_m<128>(dp, va0);
  MF(ak0[3], cat2(wl[2], wh[2]), bq[10][3]); SBAR();
  glds_m<1152>(dp, va1);
  MF(ak1[0], cat2(wl[3], wh[3]), bq[10][0]); SBAR();
  wl[1] = lds_tr(wr1[1] + 24576);
  MF(ak1[1], cat2(wl[3], wh[3]), bq[10][1]); SBAR();
  wh[1] = lds_tr(wr1[1] + 24832);
  MF(ak1[2], cat2(wl[3], wh[3]), bq[10][2]); SBAR();
  glds_m<2176>(dp, va2);
  MF(ak1[3], cat2(wl[3], wh[3]), bq[10][3]); SBAR();
  glds_m<3200>(dp, va3);
  MF(ak2[0], cat2(wl[4], wh[4]), bq[10][0]); SBAR();
  wl[2] = lds_tr(wr0[1] + 25088);
  MF(ak2[1], cat2(wl[4], wh[4]), bq[10][1]); SBAR();
  wh[2] = lds_tr(wr0[1] + 25344);
  MF(ak2[2], cat2(wl[4], wh[4]), bq[10][2]); SBAR();
  dp = a_s1 + oc3; PINS(dp);
  MF(ak2[3], cat2(wl[4], wh[4]), bq[10][3]); SBAR();
  M0ADD(adst, 12160);
  MF(ak3[0], cat2(wl[5], wh[5]), bq[10][0]); SBAR();
  wl[3] = lds_tr(wr1[1] + 25088);
  MF(ak3[1], cat2(wl[5], wh[5]), bq[10][1]); SBAR();
  wh[3] = lds_tr(wr1[1] + 25344);
  MF(ak3[2], cat2(wl[5], wh[5]), bq[10][2]); SBAR();
  glds_m<128>(dp, va0);
  MF(ak3[3], cat2(wl[5], wh[5]), bq[10][3]); SBAR();
  glds_m<1152>(dp, va1);
  MF(ak4[0], cat2(wl[0], wh[0]), bq[10][0]); SBAR();
  wl[4] = lds_tr(wr0[1] + 12288);
  MF(ak4[1], cat2(wl[0], wh[0]), bq[10][1]); SBAR();
  wh[4] = lds_tr(wr0[1] + 12544);
  MF(ak4[2], cat2(wl[0], wh[0]), bq[10][2]); SBAR();
  glds_m<2176>(dp, va2);
  MF(ak4[3], cat2(wl[0], wh[0]), bq[10][3]); SBAR();
  glds_m<3200>(dp, va3);
  MF(ak5[0], cat2(wl[1], wh[1]), bq[10][0]); SBAR();
  wl[5] = lds_tr(wr1[1] + 12288);
  MF(ak5[1], cat2(wl[1], wh[1]), bq[10][1]); SBAR();
  wh[5] = lds_tr(wr1[1] + 12544);
  MF(ak5[2], cat2(wl[1], wh[1]), bq[10][2]); SBAR();
  dp = wv_h0 + ok1; PINS(dp);
  MF(ak5[3], cat2(wl[1], wh[1]), bq[10][3]); SBAR();
  M0ADD(wdst, 49152);
  MF(ak6[0], cat2(wl[2], wh[2]), bq[10][0]); SBAR();
  wl[0] = lds_tr(wr0[1] + 12800);
  MF(ak6[1], cat2(wl[2], wh[2]), bq[10][1]); SBAR();
  wh[0] = lds_tr(wr0[1] + 13056);
  MF(ak6[2], cat2(wl[2], wh[2]), bq[10][2]); SBAR();
  glds_m<0>(dp, wv0);
  MF(ak6[3], cat2(wl[2], wh[2]), bq[10][3]); SBAR();
  glds_m<1024>(dp, wv1);
  MF(ak7[0], cat2(wl[3], wh[3]), bq[10][0]); SBAR();
  wl[1] = lds_tr(wr1[1] + 12800);
  MF(ak7[1], cat2(wl[3], wh[3]), bq[10][1]); SBAR();
  wh[1] = lds_tr(wr1[1] + 13056);
  MF(ak7[2], cat2(wl[3], wh[3]), bq[10][2]); SBAR();
  asm volatile("s_waitcnt vmcnt(22)\n\ts_barrier" :  :: "memory");
  MF(ak7[3], cat2(wl[3], wh[3]), bq[10][3]); SBAR();
  asm volatile("s_waitcnt lgkmcnt(15)" :  :: "memory");
  MF(ak0[0], cat2(wl[4], wh[4]), bq[11][0]); SBAR();
  wl[2] = lds_tr(wr0[1] + 28672);
  MF(ak0[1], cat2(wl[4], wh[4]), bq[11][1]); SBAR();
  wh[2] = lds_tr(wr0[1] + 28928);
  MF(ak0[2], cat2(wl[4], wh[4]), bq[11][2]); SBAR();
  glds_m<2048>(dp, wv2);
  MF(ak0[3], cat2(wl[4], wh[4]), bq[11][3]); SBAR();
  glds_m<3072>(dp, wv3);
  MF(ak1[0], cat2(wl[5], wh[5]), bq[11][0]); SBAR();
  wl[3] = lds_tr(wr1[1] + 28672);
  asm volatile("s_waitcnt vmcnt(16)" :  :: "memory");
  MF(ak1[1], cat2(wl[5], wh[5]), bq[11][1]); SBAR();
  wh[3] = lds_tr(wr1[1] + 28928);
  MF(ak1[2], cat2(wl[5], wh[5]), bq[11][2]); SBAR();
  DSRA(bq[12][0], ar0, 0);
  MF(ak1[3], cat2(wl[5], wh[5]), bq[11][3]); SBAR();
  DSRA(bq[12][1], ar0, 2048);
  MF(ak2[0], cat2(wl[0], wh[0]), bq[11][0]); SBAR();
  wl[4] = lds_tr(wr0[1] + 29184);
  MF(ak2[1], cat2(wl[0], wh[0]), bq[11][1]); SBAR();
  wh[4] = lds_tr(wr0[1] + 29440);
  MF(ak2[2], cat2(wl[0], wh[0]), bq[11][2]); SBAR();
  DSRA(bq[12][2], ar0, 4096);
  MF(ak2[3], cat2(wl[0], wh[0]), bq[11][3]); SBAR();
  DSRA(bq[12][3], ar0, 6144);
  MF(ak3[0], cat2(wl[1], wh[1]), bq[11][0]); SBAR();
  wl[5] = lds_tr(wr1[1] + 29184);
  MF(ak3[1], cat2(wl[1], wh[1]), bq[11][1]); SBAR();
  wh[5] = lds_tr(wr1[1] + 29440);
  MF(ak3[2], cat2(wl[1], wh[1]), bq[11][2]); SBAR();
  DSRA(bq[13][0], ar1, 0);
  MF(ak3[3], cat2(wl[1], wh[1]), bq[11][3]); SBAR();
  DSRA(bq[13][1], ar1, 2048);
  MF(ak4[0], cat2(wl[2], wh[2]), bq[11][0]); SBAR();
  wl[0] = lds_tr(wr0[0] + 0);
  MF(ak4[1], cat2(wl[2], wh[2]), bq[11][1]); SBAR();
  wh[0] = lds_tr(wr0[0] + 256);
  MF(ak4[2], cat2(wl[2], wh[2]), bq[11][2]); SBAR();
  DSRA(bq[13][2], ar1, 4096);
  MF(ak4[3], cat2(wl[2], wh[2]), bq[11][3]); SBAR();
  DSRA(bq[13][3], ar1, 6144);
  MF(ak5[0], cat2(wl[3], wh[3]), bq[11][0]); SBAR();
  wl[1] = lds_tr(wr1[0] + 0);
  MF(ak5[1], cat2(wl[3], wh[3]), bq[11][1]); SBAR();
  wh[1] = lds_tr(wr1[0] + 256);
  MF(ak5[2], cat2(wl[3], wh[3]), bq[11][2]); SBAR();
  MF(ak5[3], cat2(wl[3], wh[3]), bq[11][3]); SBAR();
  MF(ak6[0], cat2(wl[4], wh[4]), bq[11][0]); SBAR();
  wl[2] = lds_tr(wr0[0] + 512);
  MF(ak6[1], cat2(wl[4], wh[4]), bq[11][1]); SBAR();
  wh[2] = lds_tr(wr0[0] + 768);
  MF(ak6[2], cat2(wl[4], wh[4]), bq[11][2]); SBAR();
  MF(ak6[3], cat2(wl[4], wh[4]), bq[11][3]); SBAR();
  MF(ak7[0], cat2(wl[5], wh[5]), bq[11][0]); SBAR();
  wl[3] = lds_tr(wr1[0] + 512);
  MF(ak7[1], cat2(wl[5], wh[5]), bq[11][1]); SBAR();
  wh[3] = lds_tr(wr1[0] + 768);
  MF(ak7[2], cat2(wl[5], wh[5]), bq[11][2]); SBAR();
  MF(ak7[3], cat2(wl[5], wh[5]), bq[11][3]); SBAR();
  asm volatile("s_waitcnt lgkmcnt(14)" :  :: "memory");
  MF(ak0[0], cat2(wl[0], wh[0]), bq[12][0]); SBAR();
  wl[4] = lds_tr(wr0[0] + 16384);
  MF(ak0[1], cat2(wl[0], wh[0]), bq[12][1]); SBAR();
  wh[4] = lds_tr(wr0[0] + 16640);
  MF(ak0[2], cat2(wl[0], wh[0]), bq[12][2]); SBAR();
  MF(ak0[3], cat2(wl[0], wh[0]), bq[12][3]); SBAR();
  MF(ak1[0], cat2(wl[1], wh[1]), bq[12][0]); SBAR();
  wl[5] = lds_tr(wr1[0] + 16384);
  MF(ak1[1], cat2(wl[1], wh[1]), bq[12][1]); SBAR();
  wh[5] = lds_tr(wr1[0] + 16640);
  MF(ak1[2], cat2(wl[1], wh[1]), bq[12][2]); SBAR();
  MF(ak1[3], cat2(wl[1], wh[1]), bq[12][3]); SBAR();
  MF(ak2[0], cat2(wl[2], wh[2]), bq[12][0]); SBAR();
  wl[0] = lds_tr(wr0[0] + 16896);
  MF(ak2[1], cat2(wl[2], wh[2]), bq[12][1]); SBAR();
  wh[0] = lds_tr(wr0[0] + 17152);
  MF(ak2[2], cat2(wl[2], wh[2]), bq[12][2]); SBAR();
  MF(ak2[3], cat2(wl[2], wh[2]), bq[12][3]); SBAR();
  MF(ak3[0], cat2(wl[3], wh[3]), bq[12][0]); SBAR();
  wl[1] = lds_tr(wr1[0] + 16896);
  MF(ak3[1], cat2(wl[3], wh[3]), bq[12][1]); SBAR();
  wh[1] = lds_tr(wr1[0] + 17152);
  MF(ak3[2], cat2(wl[3], wh[3]), bq[12][2]); SBAR();
  MF(ak3[3], cat2(wl[3], wh[3]), bq[12][3]); SBAR();
  MF(ak4[0], cat2(wl[4], wh[4]), bq[12][0]); SBAR();
  wl[2] = lds_tr(wr0[0] + 4096);
  MF(ak4[1], cat2(wl[4], wh[4]), bq[12][1]); SBAR();
  wh[2] = lds_tr(wr0[0] + 4352);
  MF(ak4[2], cat2(wl[4], wh[4]), bq[12][2]); SBAR();
  TOUCH1();
  MF(ak4[3], cat2(wl[4], wh[4]), bq[12][3]); SBAR();
  MF(ak5[0], cat2(wl[5], wh[5]), bq[12][0]); SBAR();
  wl[3] = lds_tr(wr1[0] + 4096);
  MF(ak5[1], cat2(wl[5], wh[5]), bq[12][1]); SBAR();
  wh[3] = lds_tr(wr1[0] + 4352);
  MF(ak5[2], cat2(wl[5], wh[5]), bq[12][2]); SBAR();
  MF(ak5[3], cat2(wl[5], wh[5]), bq[12][3]); SBAR();
  MF(ak6[0], cat2(wl[0], wh[0]), bq[12][0]); SBAR();
  wl[4] = lds_tr(wr0[0] + 4608);
  MF(ak6[1], cat2(wl[0], wh[0]), bq[12][1]); SBAR();
  wh[4] = lds_tr(wr0[0] + 4864);
  MF(ak6[2], cat2(wl[0], wh[0]), bq[12][2]); SBAR();
  MF(ak6[3], cat2(wl[0], wh[0]), bq[12][3]); SBAR();
  MF(ak7[0], cat2(wl[1], wh[1]), bq[12][0]); SBAR();
  wl[5] = lds_tr(wr1[0] + 4608);
  MF(ak7[1], cat2(wl[1], wh[1]), bq[12][1]); SBAR();
  wh[5] = lds_tr(wr1[0] + 4864);
  MF(ak7[2], cat2(wl[1], wh[1]), bq[12][2]); SBAR();
  asm volatile("s_barrier" :  :: "memory");
  MF(ak7[3], cat2(wl[1], wh[1]), bq[12][3]); SBAR();
  asm volatile("s_waitcnt lgkmcnt(15)" :  :: "memory");
  MF(ak0[0], cat2(wl[2], wh[2]), bq[13][0]); SBAR();
  wl[0] = lds_tr(wr0[0] + 20480);
  MF(ak0[1], cat2(wl[2], wh[2]), bq[13][1]); SBAR();
  wh[0] = lds_tr(wr0[0] + 20736);
  MF(ak0[2], cat2(wl[2], wh[2]), bq[13][2]); SBAR();
  dp = wv_h0 + ok2; PINS(dp);
  MF(ak0[3], cat2(wl[2], wh[2]), bq[13][3]); SBAR();
  M0ADD(wdst, 65536);
  MF(ak1[0], cat2(wl[3], wh[3]), bq[13][0]); SBAR();
  wl[1] = lds_tr(wr1[0] + 20480);
  MF(ak1[1], cat2(wl[3], wh[3]), bq[13][1]); SBAR();
  wh[1] = lds_tr(wr1[0] + 20736);
  MF(ak1[2], cat2(wl[3], wh[3]), bq[13][2]); SBAR();
  glds_m<0>(dp, wv0);
  MF(ak1[3], cat2(wl[3], wh[3]), bq[13][3]); SBAR();
  glds_m<1024>(dp, wv1);
  MF(ak2[0], cat2(wl[4], wh[4]), bq[13][0]); SBAR();
  wl[2] = lds_tr(wr0[0] + 20992);
  MF(ak2[1], cat2(wl[4], wh[4]), bq[13][1]); SBAR();
  wh[2] = lds_tr(wr0[0] + 21248);
  MF(ak2[2], cat2(wl[4], wh[4]), bq[13][2]); SBAR();
  glds_m<2048>(dp, wv2);
  MF(ak2[3], cat2(wl[4], wh[4]), bq[13][3]); SBAR();
  glds_m<3072>(dp, wv3);
  MF(ak3[0], cat2(wl[5], wh[5]), bq[13][0]); SBAR();
  wl[3] = lds_tr(wr1[0] + 20992);
  asm volatile("s_waitcnt vmcnt(9)" :  :: "memory");
  MF(ak3[1], cat2(wl[5], wh[5]), bq[13][1]); SBAR();
  wh[3] = lds_tr(wr1[0] + 21248);
  MF(ak3[2], cat2(wl[5], wh[5]), bq[13][2]); SBAR();
  DSRA(bq[14][0], ar0, 8192);
  MF(ak3[3], cat2(wl[5], wh[5]), bq[13][3]); SBAR();
  DSRA(bq[14][1], ar0, 10240);
  MF(ak4[0], cat2(wl[0], wh[0]), bq[13][0]); SBAR();
  wl[4] = lds_tr(wr0[0] + 8192);
  MF(ak4[1], cat2(wl[0], wh[0]), bq[13][1]); SBAR();
  wh[4] = lds_tr(wr0[0] + 8448);
  MF(ak4[2], cat2(wl[0], wh[0]), bq[13][2]); SBAR();
  DSRA(bq[14][2], ar0, 12288);
  MF(ak4[3], cat2(wl[0], wh[0]), bq[13][3]); SBAR();
  DSRA(bq[14][3], ar0, 14336);
  MF(ak5[0], cat2(wl[1], wh[1]), bq[13][0]); SBAR();
  wl[5] = lds_tr(wr1[0] + 8192);
  MF(ak5[1], cat2(wl[1], wh[1]), bq[13][1]); SBAR();
  wh[5] = lds_tr(wr1[0] + 8448);
  MF(ak5[2], cat2(wl[1], wh[1]), bq[13][2]); SBAR();
  DSRA(bq[15][0], ar1, 8192);
  MF(ak5[3], cat2(wl[1], wh[1]), bq[13][3]); SBAR();
  DSRA(bq[15][1], ar1, 10240);
  MF(ak6[0], cat2(wl[2], wh[2]), bq[13][0]); SBAR();
  wl[0] = lds_tr(wr0[0] + 8704);
  MF(ak6[1], cat2(wl[2], wh[2]), bq[13][1]); SBAR();
  wh[0] = lds_tr(wr0[0] + 8960);
  MF(ak6[2], cat2(wl[2], wh[2]), bq[13][2]); SBAR();
  DSRA(bq[15][2], ar1, 12288);
  MF(ak6[3], cat2(wl[2], wh[2]), bq[13][3]); SBAR();
  DSRA(bq[15][3], ar1, 14336);
  MF(ak7[0], cat2(wl[3], wh[3]), bq[13][0]); SBAR();
  wl[1] = lds_tr(wr1[0] + 8704);
  MF(ak7[1], cat2(wl[3], wh[3]), bq[13][1]); SBAR();
  wh[1] = lds_tr(wr1[0] + 8960);
  MF(ak7[2], cat2(wl[3], wh[3]), bq[13][2]); SBAR();
  dp = wv_h0 + ok3; PINS(dp);
  MF(ak7[3], cat2(wl[3], wh[3]), bq[13][3]); SBAR();
  asm volatile("s_waitcnt lgkmcnt(10)" :  :: "memory");
  MF(ak0[0], cat2(wl[4], wh[4]), bq[14][0]); SBAR();
  wl[2] = lds_tr(wr0[0] + 24576);
  M0ADD(wdst, 81920);
  MF(ak0[1], cat2(wl[4], wh[4]), bq[14][1]); SBAR();
  wh[2] = lds_tr(wr0[0] + 24832);
  MF(ak0[2], cat2(wl[4], wh[4]), bq[14][2]); SBAR();
  glds_m<0>(dp, wv0);
  MF(ak0[3], cat2(wl[4], wh[4]), bq[14][3]); SBAR();
  glds_m<1024>(dp, wv1);
  MF(ak1[0], cat2(wl[5], wh[5]), bq[14][0]); SBAR();
  wl[3] = lds_tr(wr1[0] + 24576);
  MF(ak1[1], cat2(wl[5], wh[5]), bq[14][1]); SBAR();
  wh[3] = lds_tr(wr1[0] + 24832);
  MF(ak1[2], cat2(wl[5], wh[5]), bq[14][2]); SBAR();
  glds_m<2048>(dp, wv2);
  MF(ak1[3], cat2(wl[5], wh[5]), bq[14][3]); SBAR();
  glds_m<3072>(dp, wv3);
  MF(ak2[0], cat2(wl[0], wh[0]), bq[14][0]); SBAR();
  wl[4] = lds_tr(wr0[0] + 25088);
  MF(ak2[1], cat2(wl[0], wh[0]), bq[14][1]); SBAR();
  wh[4] = lds_tr(wr0[0] + 25344);
  MF(ak2[2], cat2(wl[0], wh[0]), bq[14][2]); SBAR();
  MF(ak2[3], cat2(wl[0], wh[0]), bq[14][3]); SBAR();
  MF(ak3[0], cat2(wl[1], wh[1]), bq[14][0]); SBAR();
  wl[5] = lds_tr(wr1[0] + 25088);
  MF(ak3[1], cat2(wl[1], wh[1]), bq[14][1]); SBAR();
  wh[5] = lds_tr(wr1[0] + 25344);
  MF(ak3[2], cat2(wl[1], wh[1]), bq[14][2]); SBAR();
  MF(ak3[3], cat2(wl[1], wh[1]), bq[14][3]); SBAR();
  MF(ak4[0], cat2(wl[2], wh[2]), bq[14][0]); SBAR();
  wl[0] = lds_tr(wr0[0] + 12288);
  MF(ak4[1], cat2(wl[2], wh[2]), bq[14][1]); SBAR();
  wh[0] = lds_tr(wr0[0] + 12544);
  MF(ak4[2], cat2(wl[2], wh[2]), bq[14][2]); SBAR();
  MF(ak4[3], cat2(wl[2], wh[2]), bq[14][3]); SBAR();
  MF(ak5[0], cat2(wl[3], wh[3]), bq[14][0]); SBAR();
  wl[1] = lds_tr(wr1[0] + 12288);
  MF(ak5[1], cat2(wl[3], wh[3]), bq[14][1]); SBAR();
  wh[1] = lds_tr(wr1[0] + 12544);
  MF(ak5[2], cat2(wl[3], wh[3]), bq[14][2]); SBAR();
  MF(ak5[3], cat2(wl[3], wh[3]), bq[14][3]); SBAR();
  MF(ak6[0], cat2(wl[4], wh[4]), bq[14][0]); SBAR();
  wl[2] = lds_tr(wr0[0] + 12800);
  MF(ak6[1], cat2(wl[4], wh[4]), bq[14][1]); SBAR();
  wh[2] = lds_tr(wr0[0] + 13056);
  MF(ak6[2], cat2(wl[4], wh[4]), bq[14][2]); SBAR();
  MF(ak6[3], cat2(wl[4], wh[4]), bq[14][3]); SBAR();
  MF(ak7[0], cat2(wl[5], wh[5]), bq[14][0]); SBAR();
  wl[3] = lds_tr(wr1[0] + 12800);
  MF(ak7[1], cat2(wl[5], wh[5]), bq[14][1]); SBAR();
  wh[3] = lds_tr(wr1[0] + 13056);
  MF(ak7[2], cat2(wl[5], wh[5]), bq[14][2]); SBAR();
  asm volatile("s_waitcnt vmcnt(9)\n\ts_barrier" :  :: "memory");
  MF(ak7[3], cat2(wl[5], wh[5]), bq[14][3]); SBAR();
  asm volatile("s_waitcnt lgkmcnt(15)" :  :: "memory");
  MF(ak0[0], cat2(wl[0], wh[0]), bq[15][0]); SBAR();
  wl[4] = lds_tr(wr0[0] + 28672);
  MF(ak0[1], cat2(wl[0], wh[0]), bq[15][1]); SBAR();
  wh[4] = lds_tr(wr0[0] + 28928);
  MF(ak0[2], cat2(wl[0], wh[0]), bq[15][2]); SBAR();
  dp = wv_h1 + ok0; PINS(dp);
  MF(ak0[3], cat2(wl[0], wh[0]), bq[15][3]); SBAR();
  M0ADD(wdst, 98304);
  MF(ak1[0], cat2(wl[1], wh[1]), bq[15][0]); SBAR();
  wl[5] = lds_tr(wr1[0] + 28672);
  MF(ak1[1], cat2(wl[1], wh[1]), bq[15][1]); SBAR();
  wh[5] = lds_tr(wr1[0] + 28928);
  MF(ak1[2], cat2(wl[1], wh[1]), bq[15][2]); SBAR();
  glds_m<0>(dp, wv0);
  MF(ak1[3], cat2(wl[1], wh[1]), bq[15][3]); SBAR();
  glds_m<1024>(dp, wv1);
  MF(ak2[0], cat2(wl[2], wh[2]), bq[15][0]); SBAR();
  wl[0] = lds_tr(wr0[0] + 29184);
  MF(ak2[1], cat2(wl[2], wh[2]), bq[15][1]); SBAR();
  wh[0] = lds_tr(wr0[0] + 29440);
  MF(ak2[2], cat2(wl[2], wh[2]), bq[15][2]); SBAR();
  glds_m<2048>(dp, wv2);
  MF(ak2[3], cat2(wl[2], wh[2]), bq[15][3]); SBAR();
  glds_m<3072>(dp, wv3);
  MF(ak3[0], cat2(wl[3], wh[3]), bq[15][0]); SBAR();
  wl[1] = lds_tr(wr1[0] + 29184);
  MF(ak3[1], cat2(wl[3], wh[3]), bq[15][1]); SBAR();
  wh[1] = lds_tr(wr1[0] + 29440);
  MF(ak3[2], cat2(wl[3], wh[3]), bq[15][2]); SBAR();
  MF(ak3[3], cat2(wl[3], wh[3]), bq[15][3]); SBAR();
  MF(ak4[0], cat2(wl[4], wh[4]), bq[15][0]); SBAR();
  wl[2] = lds_tr(wr0[0] + 32768);
  MF(ak4[1], cat2(wl[4], wh[4]), bq[15][1]); SBAR();
  wh[2] = lds_tr(wr0[0] + 33024);
  MF(ak4[2], cat2(wl[4], wh[4]), bq[15][2]); SBAR();
  dp = wv_h1 + ok1; PINS(dp);
  MF(ak4[3], cat2(wl[4], wh[4]), bq[15][3]); SBAR();
  M0ADD(wdst, 114688);
  MF(ak5[0], cat2(wl[5], wh[5]), bq[15][0]); SBAR();
  wl[3] = lds_tr(wr1[0] + 32768);
  MF(ak5[1], cat2(wl[5], wh[5]), bq[15][1]); SBAR();
  wh[3] = lds_tr(wr1[0] + 33024);
  MF(ak5[2], cat2(wl[5], wh[5]), bq[15][2]); SBAR();
  glds_m<0>(dp, wv0);
  MF(ak5[3], cat2(wl[5], wh[5]), bq[15][3]); SBAR();
  glds_m<1024>(dp, wv1);
  MF(ak6[0], cat2(wl[0], wh[0]), bq[15][0]); SBAR();
  wl[4] = lds_tr(wr0[0] + 36864);
  MF(ak6[1], cat2(wl[0], wh[0]), bq[15][1]); SBAR();
  wh[4] = lds_tr(wr0[0] + 37120);
  MF(ak6[2], cat2(wl[0], wh[0]), bq[15][2]); SBAR();
  glds_m<2048>(dp, wv2);
  MF(ak6[3], cat2(wl[0], wh[0]), bq[15][3]); SBAR();
  glds_m<3072>(dp, wv3);
  MF(ak7[0], cat2(wl[1], wh[1]), bq[15][0]); SBAR();
  wl[5] = lds_tr(wr1[0] + 36864);
  MF(ak7[1], cat2(wl[1], wh[1]), bq[15][1]); SBAR();
  wh[5] = lds_tr(wr1[0] + 37120);
  MF(ak7[2], cat2(wl[1], wh[1]), bq[15][2]); SBAR();
  MF(ak7[3], cat2(wl[1], wh[1]), bq[15][3]); SBAR();
  MG0(av0[0], cat2(wl[2], wh[2]), bq[0][0]); SBAR();
  wl[0] = lds_tr(wr0[0] + 40960);
  MG0(av0[1], cat2(wl[2], wh[2]), bq[0][1]); SBAR();
  wh[0] = lds_tr(wr0[0] + 41216);
  MG0(av0[2], cat2(wl[2], wh[2]), bq[0][2]); SBAR();
  MG0(av0[3], cat2(wl[2], wh[2]), bq[0][3]); SBAR();
  MG0(av1[0], cat2(wl[3], wh[3]), bq[0][0]); SBAR();
  wl[1] = lds_tr(wr1[0] + 40960);
  MG0(av1[1], cat2(wl[3], wh[3]), bq[0][1]); SBAR();
  wh[1] = lds_tr(wr1[0] + 41216);
  MG0(av1[2], cat2(wl[3], wh[3]), bq[0][2]); SBAR();
  dp = wv_h1 + ok2; PINS(dp);
  MG0(av1[3], cat2(wl[3], wh[3]), bq[0][3]); SBAR();
  M0ADD(wdst, 131072);
  MG(av0[0], cat2(wl[4], wh[4]), bq[1][0]); SBAR();
  wl[2] = lds_tr(wr0[0] + 45056);
  pk[0][0] = cvtpk(ak0[0][0], ak0[0][1]); PIN(pk[0][0]);
  MG(av0[1], cat2(wl[4], wh[4]), bq[1][1]); SBAR();
  wh[2] = lds_tr(wr0[0] + 45312);
  pk[0][1] = cvtpk(ak0[0][2], ak0[0][3]); PIN(pk[0][1]);
  MG(av0[2], cat2(wl[4], wh[4]), bq[1][2]); SBAR();
  glds_m<0>(dp, wv0);
  pk[0][2] = cvtpk(ak1[0][0], ak1[0][1]); PIN(pk[0][2]);
  MG(av0[3], cat2(wl[4], wh[4]), bq[1][3]); SBAR();
  glds_m<1024>(dp, wv1);
  MG(av1[0], cat2(wl[5], wh[5]), bq[1][0]); SBAR();
  wl[3] = lds_tr(wr1[0] + 45056);
  pk[0][3] = cvtpk(ak1[0][2], ak1[0][3]); PIN(pk[0][3]);
  MG(av1[1], cat2(wl[5], wh[5]), bq[1][1]); SBAR();
  wh[3] = lds_tr(wr1[0] + 45312);
  MG(av1[2], cat2(wl[5], wh[5]), bq[1][2]); SBAR();
  glds_m<2048>(dp, wv2);
  st16n<0>(ko, lo0, (u32x4){pk[0][0], pk[0][1], pk[0][2], pk[0][3]});
  MG(av1[3], cat2(wl[5], wh[5]), bq[1][3]); SBAR();
  glds_m<3072>(dp, wv3);
  MG(av0[0], cat2(wl[0], wh[0]), bq[2][0]); SBAR();
  wl[4] = lds_tr(wr0[0] + 49152);
  MG(av0[1], cat2(wl[0], wh[0]), bq[2][1]); SBAR();
  wh[4] = lds_tr(wr0[0] + 49408);
  MG(av0[2], cat2(wl[0], wh[0]), bq[2][2]); SBAR();
  MG(av0[3], cat2(wl[0], wh[0]), bq[2][3]); SBAR();
  MG(av1[0], cat2(wl[1], wh[1]), bq[2][0]); SBAR();
  wl[5] = lds_tr(wr1[0] + 49152);
  MG(av1[1], cat2(wl[1], wh[1]), bq[2][1]); SBAR();
  wh[5] = lds_tr(wr1[0] + 49408);
  MG(av1[2], cat2(wl[1], wh[1]), bq[2][2]); SBAR();
  MG(av1[3], cat2(wl[1], wh[1]), bq[2][3]); SBAR();
  MG(av0[0], cat2(wl[2], wh[2]), bq[3][0]); SBAR();
  wl[0] = lds_tr(wr0[0] + 53248);
  MG(av0[1], cat2(wl[2], wh[2]), bq[3][1]); SBAR();
  wh[0] = lds_tr(wr0[0] + 53504);
  MG(av0[2], cat2(wl[2], wh[2]), bq[3][2]); SBAR();
  dp = wv_h1 + ok3; PINS(dp);
  MG(av0[3], cat2(wl[2], wh[2]), bq[3][3]); SBAR();
  M0ADD(wdst, 147456);
  MG(av1[0], cat2(wl[3], wh[3]), bq[3][0]); SBAR();
  wl[1] = lds_tr(wr1[0] + 53248);
  pk[1][0] = cvtpk(ak0[1][0], ak0[1][1]); PIN(pk[1][0]);
  MG(av1[1], cat2(wl[3], wh[3]), bq[3][1]); SBAR();
  wh[1] = lds_tr(wr1[0] + 53504);
  pk[1][1] = cvtpk(ak0[1][2], ak0[1][3]); PIN(pk[1][1]);
  MG(av1[2], cat2(wl[3], wh[3]), bq[3][2]); SBAR();
  asm volatile("s_waitcnt vmcnt(13)\n\ts_barrier" :  :: "memory");
  MG(av1[3], cat2(wl[3], wh[3]), bq[3][3]); SBAR();
  glds_m<0>(dp, wv0);
  MG(av0[0], cat2(wl[4], wh[4]), bq[4][0]); SBAR();
  wl[2] = lds_tr(wr0[0] + 57344);
  pk[1][2] = cvtpk(ak1[1][0], ak1[1][1]); PIN(pk[1][2]);
  MG(av0[1], cat2(wl[4], wh[4]), bq[4][1]); SBAR();
  wh[2] = lds_tr(wr0[0] + 57600);
  pk[1][3] = cvtpk(ak1[1][2], ak1[1][3]); PIN(pk[1][3]);
  MG(av0[2], cat2(wl[4], wh[4]), bq[4][2]); SBAR();
  glds_m<1024>(dp, wv1);
  st16n<0>(ko, lo1, (u32x4){pk[1][0], pk[1][1], pk[1][2], pk[1][3]});
  MG(av0[3], cat2(wl[4], wh[4]), bq[4][3]); SBAR();
  glds_m<2048>(dp, wv2);
  MG(av1[0], cat2(wl[5], wh[5]), bq[4][0]); SBAR();
  wl[3] = lds_tr(wr1[0] + 57344);
  MG(av1[1], cat2(wl[5], wh[5]), bq[4][1]); SBAR();
  wh[3] = lds_tr(wr1[0] + 57600);
  MG(av1[2], cat2(wl[5], wh[5]), bq[4][2]); SBAR();
  glds_m<3072>(dp, wv3);
  MG(av1[3], cat2(wl[5], wh[5]), bq[4][3]); SBAR();
  MG(av0[0], cat2(wl[0], wh[0]), bq[5][0]); SBAR();
  wl[4] = lds_tr(wr0[0] + 61440);
  MG(av0[1], cat2(wl[0], wh[0]), bq[5][1]); SBAR();
  wh[4] = lds_tr(wr0[0] + 61696);
  MG(av0[2], cat2(wl[0], wh[0]), bq[5][2]); SBAR();
  MG(av0[3], cat2(wl[0], wh[0]), bq[5][3]); SBAR();
  MG(av1[0], cat2(wl[1], wh[1]), bq[5][0]); SBAR();
  wl[5] = lds_tr(wr1[0] + 61440);
  MG(av1[1], cat2(wl[1], wh[1]), bq[5][1]); SBAR();
  wh[5] = lds_tr(wr1[0] + 61696);
  MG(av1[2], cat2(wl[1], wh[1]), bq[5][2]); SBAR();
  pk[0][0] = cvtpk(ak0[2][0], ak0[2][1]); PIN(pk[0][0]);
  pk[0][1] = cvtpk(ak0[2][2], ak0[2][3]); PIN(pk[0][1]);
  MG(av1[3], cat2(wl[1], wh[1]), bq[5][3]); SBAR();
  pk[0][2] = cvtpk(ak1[2][0], ak1[2][1]); PIN(pk[0][2]);
  MG(av0[0], cat2(wl[2], wh[2]), bq[6][0]); SBAR();
  wl[0] = lds_tr(wr0[1] + 0);
  pk[0][3] = cvtpk(ak1[2][2], ak1[2][3]); PIN(pk[0][3]);
  MG(av0[1], cat2(wl[2], wh[2]), bq[6][1]); SBAR();
  wh[0] = lds_tr(wr0[1] + 256);
  MG(av0[2], cat2(wl[2], wh[2]), bq[6][2]); SBAR();
  st16n<0>(ko, lo2, (u32x4){pk[0][0], pk[0][1], pk[0][2], pk[0][3]});
  MG(av0[3], cat2(wl[2], wh[2]), bq[6][3]); SBAR();
  MG(av1[0], cat2(wl[3], wh[3]), bq[6][0]); SBAR();
  wl[1] = lds_tr(wr1[1] + 0);
  MG(av1[1], cat2(wl[3], wh[3]), bq[6][1]); SBAR();
  wh[1] = lds_tr(wr1[1] + 256);
  MG(av1[2], cat2(wl[3], wh[3]), bq[6][2]); SBAR();
  MG(av1[3], cat2(wl[3], wh[3]), bq[6][3]); SBAR();
  MG(av0[0], cat2(wl[4], wh[4]), bq[7][0]); SBAR();
  wl[2] = lds_tr(wr0[1] + 4096);
  MG(av0[1], cat2(wl[4], wh[4]), bq[7][1]); SBAR();
  wh[2] = lds_tr(wr0[1] + 4352);
  MG(av0[2], cat2(wl[4], wh[4]), bq[7][2]); SBAR();
  MG(av0[3], cat2(wl[4], wh[4]), bq[7][3]); SBAR();
  MG(av1[0], cat2(wl[5], wh[5]), bq[7][0]); SBAR();
  wl[3] = lds_tr(wr1[1] + 4096);
  MG(av1[1], cat2(wl[5], wh[5]), bq[7][1]); SBAR();
  wh[3] = lds_tr(wr1[1] + 4352);
  MG(av1[2], cat2(wl[5], wh[5]), bq[7][2]); SBAR();
  pk[1][0] = cvtpk(ak0[3][0], ak0[3][1]); PIN(pk[1][0]);
  pk[1][1] = cvtpk(ak0[3][2], ak0[3][3]); PIN(pk[1][1]);
  MG(av1[3], cat2(wl[5], wh[5]), bq[7][3]); SBAR();
  pk[1][2] = cvtpk(ak1[3][0], ak1[3][1]); PIN(pk[1][2]);
  MG(av0[0], cat2(wl[0], wh[0]), bq[8][0]); SBAR();
  wl[4] = lds_tr(wr0[1] + 8192);
  pk[1][3] = cvtpk(ak1[3][2], ak1[3][3]); PIN(pk[1][3]);
  MG(av0[1], cat2(wl[0], wh[0]), bq[8][1]); SBAR();
  wh[4] = lds_tr(wr0[1] + 8448);
  MG(av0[2], cat2(wl[0], wh[0]), bq[8][2]); SBAR();
  st16n<0>(ko, lo3, (u32x4){pk[1][0], pk[1][1], pk[1][2], pk[1][3]});
  MG(av0[3], cat2(wl[0], wh[0]), bq[8][3]); SBAR();
  MG(av1[0], cat2(wl[1], wh[1]), bq[8][0]); SBAR();
  wl[5] = lds_tr(wr1[1] + 8192);
  MG(av1[1], cat2(wl[1], wh[1]), bq[8][1]); SBAR();
  wh[5] = lds_tr(wr1[1] + 8448);
  MG(av1[2], cat2(wl[1], wh[1]), bq[8][2]); SBAR();
  MG(av1[3], cat2(wl[1], wh[1]), bq[8][3]); SBAR();
  MG(av0[0], cat2(wl[2], wh[2]), bq[9][0]); SBAR();
  wl[0] = lds_tr(wr0[1] + 12288);
  MG(av0[1], cat2(wl[2], wh[2]), bq[9][1]); SBAR();
  wh[0] = lds_tr(wr0[1] + 12544);
  MG(av0[2], cat2(wl[2], wh[2]), bq[9][2]); SBAR();
  MG(av0[3], cat2(wl[2], wh[2]), bq[9][3]); SBAR();
  MG(av1[0], cat2(wl[3], wh[3]), bq[9][0]); SBAR();
  wl[1] = lds_tr(wr1[1] + 12288);
  MG(av1[1], cat2(wl[3], wh[3]), bq[9][1]); SBAR();
  wh[1] = lds_tr(wr1[1] + 12544);
  MG(av1[2], cat2(wl[3], wh[3]), bq[9][2]); SBAR();
  pk[0][0] = cvtpk(ak2[0][0], ak2[0][1]); PIN(pk[0][0]);
  pk[0][1] = cvtpk(ak2[0][2], ak2[0][3]); PIN(pk[0][1]);
  MG(av1[3], cat2(wl[3], wh[3]), bq[9][3]); SBAR();
  pk[0][2] = cvtpk(ak3[0][0], ak3[0][1]); PIN(pk[0][2]);
  MG(av0[0], cat2(wl[4], wh[4]), bq[10][0]); SBAR();
  wl[2] = lds_tr(wr0[1] + 16384);
  pk[0][3] = cvtpk(ak3[0][2], ak3[0][3]); PIN(pk[0][3]);
  MG(av0[1], cat2(wl[4], wh[4]), bq[10][1]); SBAR();
  wh[2] = lds_tr(wr0[1] + 16640);
  MG(av0[2], cat2(wl[4], wh[4]), bq[10][2]); SBAR();
  st16n<1024>(ko, lo0, (u32x4){pk[0][0], pk[0][1], pk[0][2], pk[0][3]});
  MG(av0[3], cat2(wl[4], wh[4]), bq[10][3]); SBAR();
  MG(av1[0], cat2(wl[5], wh[5]), bq[10][0]); SBAR();
  wl[3] = lds_tr(wr1[1] + 16384);
  MG(av1[1], cat2(wl[5], wh[5]), bq[10][1]); SBAR();
  wh[3] = lds_tr(wr1[1] + 16640);
  MG(av1[2], cat2(wl[5], wh[5]), bq[10][2]); SBAR();
  MG(av1[3], cat2(wl[5], wh[5]), bq[10][3]); SBAR();
  MG(av0[0], cat2(wl[0], wh[0]), bq[11][0]); SBAR();
  wl[4] = lds_tr(wr0[1] + 20480);
  MG(av0[1], cat2(wl[0], wh[0]), bq[11][1]); SBAR();
  wh[4] = lds_tr(wr0[1] + 20736);
  MG(av0[2], cat2(wl[0], wh[0]), bq[11][2]); SBAR();
  MG(av0[3], cat2(wl[0], wh[0]), bq[11][3]); SBAR();
  MG(av1[0], cat2(wl[1], wh[1]), bq[11][0]); SBAR();
  wl[5] = lds_tr(wr1[1] + 20480);
  MG(av1[1], cat2(wl[1], wh[1]), bq[11][1]); SBAR();
  wh[5] = lds_tr(wr1[1] + 20736);
  MG(av1[2], cat2(wl[1], wh[1]), bq[11][2]); SBAR();
  pk[1][0] = cvtpk(ak2[1][0], ak2[1][1]); PIN(pk[1][0]);
  pk[1][1] = cvtpk(ak2[1][2], ak2[1][3]); PIN(pk[1][1]);
  MG(av1[3], cat2(wl[1], wh[1]), bq[11][3]); SBAR();
  pk[1][2] = cvtpk(ak3[1][0], ak3[1][1]); PIN(pk[1][2]);
  MG(av0[0], cat2(wl[2], wh[2]), bq[12][0]); SBAR();
  wl[0] = lds_tr(wr0[1] + 24576);
  pk[1][3] = cvtpk(ak3[1][2], ak3[1][3]); PIN(pk[1][3]);
  MG(av0[1], cat2(wl[2], wh[2]), bq[12][1]); SBAR();
  wh[0] = lds_tr(wr0[1] + 24832);
  MG(av0[2], cat2(wl[2], wh[2]), bq[12][2]); SBAR();
  st16n<1024>(ko, lo1, (u32x4){pk[1][0], pk[1][1], pk[1][2], pk[1][3]});
  MG(av0[3], cat2(wl[2], wh[2]), bq[12][3]); SBAR();
  MG(av1[0], cat2(wl[3], wh[3]), bq[12][0]); SBAR();
  wl[1] = lds_tr(wr1[1] + 24576);
  MG(av1[1], cat2(wl[3], wh[3]), bq[12][1]); SBAR();
  wh[1] = lds_tr(wr1[1] + 24832);
  MG(av1[2], cat2(wl[3], wh[3]), bq[12][2]); SBAR();
  MG(av1[3], cat2(wl[3], wh[3]), bq[12][3]); SBAR();
  MG(av0[0], cat2(wl[4], wh[4]), bq[13][0]); SBAR();
  wl[2] = lds_tr(wr0[1] + 28672);
  MG(av0[1], cat2(wl[4], wh[4]), bq[13][1]); SBAR();
  wh[2] = lds_tr(wr0[1] + 28928);
  MG(av0[2], cat2(wl[4], wh[4]), bq[13][2]); SBAR();
  MG(av0[3], cat2(wl[4], wh[4]), bq[13][3]); SBAR();
  MG(av1[0], cat2(wl[5], wh[5]), bq[13][0]); SBAR();
  wl[3] = lds_tr(wr1[1] + 28672);
  MG(av1[1], cat2(wl[5], wh[5]), bq[13][1]); SBAR();
  wh[3] = lds_tr(wr1[1] + 28928);
  MG(av1[2], cat2(wl[5], wh[5]), bq[13][2]); SBAR();
  pk[0][0] = cvtpk(ak2[2][0], ak2[2][1]); PIN(pk[0][0]);
  pk[0][1] = cvtpk(ak2[2][2], ak2[2][3]); PIN(pk[0][1]);
  MG(av1[3], cat2(wl[5], wh[5]), bq[13][3]); SBAR();
  pk[0][2] = cvtpk(ak3[2][0], ak3[2][1]); PIN(pk[0][2]);
  MG(av0[0], cat2(wl[0], wh[0]), bq[14][0]); SBAR();
  wl[4] = lds_tr(wr0[0] + 33280);
  pk[0][3] = cvtpk(ak3[2][2], ak3[2][3]); PIN(pk[0][3]);
  MG(av0[1], cat2(wl[0], wh[0]), bq[14][1]); SBAR();
  wh[4] = lds_tr(wr0[0] + 33536);
  MG(av0[2], cat2(wl[0], wh[0]), bq[14][2]); SBAR();
  st16n<1024>(ko, lo2, (u32x4){pk[0][0], pk[0][1], pk[0][2], pk[0][3]});
  MG(av0[3], cat2(wl[0], wh[0]), bq[14][3]); SBAR();
  MG(av1[0], cat2(wl[1], wh[1]), bq[14][0]); SBAR();
  wl[5] = lds_tr(wr1[0] + 33280);
  MG(av1[1], cat2(wl[1], wh[1]), bq[14][1]); SBAR();
  wh[5] = lds_tr(wr1[0] + 33536);
  MG(av1[2], cat2(wl[1], wh[1]), bq[14][2]); SBAR();
  MG(av1[3], cat2(wl[1], wh[1]), bq[14][3]); SBAR();
  MG(av0[0], cat2(wl[2], wh[2]), bq[15][0]); SBAR();
  wl[0] = lds_tr(wr0[0] + 37376);
  MG(av0[1], cat2(wl[2], wh[2]), bq[15][1]); SBAR();
  wh[0] = lds_tr(wr0[0] + 37632);
  MG(av0[2], cat2(wl[2], wh[2]), bq[15][2]); SBAR();
  MG(av0[3], cat2(wl[2], wh[2]), bq[15][3]); SBAR();
  MG(av1[0], cat2(wl[3], wh[3]), bq[15][0]); SBAR();
  wl[1] = lds_tr(wr1[0] + 37376);
  MG(av1[1], cat2(wl[3], wh[3]), bq[15][1]); SBAR();
  wh[1] = lds_tr(wr1[0] + 37632);
  MG(av1[2], cat2(wl[3], wh[3]), bq[15][2]); SBAR();
  pk[1][0] = cvtpk(ak2[3][0], ak2[3][1]); PIN(pk[1][0]);
  pk[1][1] = cvtpk(ak2[3][2], ak2[3][3]); PIN(pk[1][1]);
  MG(av1[3], cat2(wl[3], wh[3]), bq[15][3]); SBAR();
  pk[1][2] = cvtpk(ak3[3][0], ak3[3][1]); PIN(pk[1][2]);
  MG0(av2[0], cat2(wl[4], wh[4]), bq[0][0]); SBAR();
  wl[2] = lds_tr(wr0[0] + 41472);
  pk[1][3] = cvtpk(ak3[3][2], ak3[3][3]); PIN(pk[1][3]);
  MG0(av2[1], cat2(wl[4], wh[4]), bq[0][1]); SBAR();
  wh[2] = lds_tr(wr0[0] + 41728);
  MG0(av2[2], cat2(wl[4], wh[4]), bq[0][2]); SBAR();
  st16n<1024>(ko, lo3, (u32x4){pk[1][0], pk[1][1], pk[1][2], pk[1][3]});
  MG0(av2[3], cat2(wl[4], wh[4]), bq[0][3]); SBAR();
  MG0(av3[0], cat2(wl[5], wh[5]), bq[0][0]); SBAR();
  wl[3] = lds_tr(wr1[0] + 41472);
  MG0(av3[1], cat2(wl[5], wh[5]), bq[0][1]); SBAR();
  wh[3] = lds_tr(wr1[0] + 41728);
  MG0(av3[2], cat2(wl[5], wh[5]), bq[0][2]); SBAR();
  MG0(av3[3], cat2(wl[5], wh[5]), bq[0][3]); SBAR();
  MG(av2[0], cat2(wl[0], wh[0]), bq[1][0]); SBAR();
  wl[4] = lds_tr(wr0[0] + 45568);
  MG(av2[1], cat2(wl[0], wh[0]), bq[1][1]); SBAR();
  wh[4] = lds_tr(wr0[0] + 45824);
  MG(av2[2], cat2(wl[0], wh[0]), bq[1][2]); SBAR();
  MG(av2[3], cat2(wl[0], wh[0]), bq[1][3]); SBAR();
  MG(av3[0], cat2(wl[1], wh[1]), bq[1][0]); SBAR();
  wl[5] = lds_tr(wr1[0] + 45568);
  MG(av3[1], cat2(wl[1], wh[1]), bq[1][1]); SBAR();
  wh[5] = lds_tr(wr1[0] + 45824);
  MG(av3[2], cat2(wl[1], wh[1]), bq[1][2]); SBAR();
  pk[0][0] = cvtpk(av0[0][0], av0[0][1]); PIN(pk[0][0]);
  pk[0][1] = cvtpk(av0[0][2], av0[0][3]); PIN(pk[0][1]);
  MG(av3[3], cat2(wl[1], wh[1]), bq[1][3]); SBAR();
  pk[0][2] = cvtpk(av0[1][0], av0[1][1]); PIN(pk[0][2]);
  MG(av2[0], cat2(wl[2], wh[2]), bq[2][0]); SBAR();
  wl[0] = lds_tr(wr0[0] + 49664);
  pk[0][3] = cvtpk(av0[1][2], av0[1][3]); PIN(pk[0][3]);
  MG(av2[1], cat2(wl[2], wh[2]), bq[2][1]); SBAR();
  wh[0] = lds_tr(wr0[0] + 49920);
  MG(av2[2], cat2(wl[2], wh[2]), bq[2][2]); SBAR();
  st16n<0>(vo0, lo0, (u32x4){pk[0][0], pk[0][1], pk[0][2], pk[0][3]});
  MG(av2[3], cat2(wl[2], wh[2]), bq[2][3]); SBAR();
  MG(av3[0], cat2(wl[3], wh[3]), bq[2][0]); SBAR();
  wl[1] = lds_tr(wr1[0] + 49664);
  MG(av3[1], cat2(wl[3], wh[3]), bq[2][1]); SBAR();
  wh[1] = lds_tr(wr1[0] + 49920);
  MG(av3[2], cat2(wl[3], wh[3]), bq[2][2]); SBAR();
  MG(av3[3], cat2(wl[3], wh[3]), bq[2][3]); SBAR();
  MG(av2[0], cat2(wl[4], wh[4]), bq[3][0]); SBAR();
  wl[2] = lds_tr(wr0[0] + 53760);
  MG(av2[1], cat2(wl[4], wh[4]), bq[3][1]); SBAR();
  wh[2] = lds_tr(wr0[0] + 54016);
  MG(av2[2], cat2(wl[4], wh[4]), bq[3][2]); SBAR();
  MG(av2[3], cat2(wl[4], wh[4]), bq[3][3]); SBAR();
  MG(av3[0], cat2(wl[5], wh[5]), bq[3][0]); SBAR();
  wl[3] = lds_tr(wr1[0] + 53760);
  MG(av3[1], cat2(wl[5], wh[5]), bq[3][1]); SBAR();
  wh[3] = lds_tr(wr1[0] + 54016);
  MG(av3[2], cat2(wl[5], wh[5]), bq[3][2]); SBAR();
  pk[1][0] = cvtpk(av0[2][0], av0[2][1]); PIN(pk[1][0]);
  pk[1][1] = cvtpk(av0[2][2], av0[2][3]); PIN(pk[1][1]);
  MG(av3[3], cat2(wl[5], wh[5]), bq[3][3]); SBAR();
  pk[1][2] = cvtpk(av0[3][0], av0[3][1]); PIN(pk[1][2]);
  MG(av2[0], cat2(wl[0], wh[0]), bq[4][0]); SBAR();
  wl[4] = lds_tr(wr0[0] + 57856);
  pk[1][3] = cvtpk(av0[3][2], av0[3][3]); PIN(pk[1][3]);
  MG(av2[1], cat2(wl[0], wh[0]), bq[4][1]); SBAR();
  wh[4] = lds_tr(wr0[0] + 58112);
  MG(av2[2], cat2(wl[0], wh[0]), bq[4][2]); SBAR();
  st16n<0>(vo0, lo2, (u32x4){pk[1][0], pk[1][1], pk[1][2], pk[1][3]});
  MG(av2[3], cat2(wl[0], wh[0]), bq[4][3]); SBAR();
  MG(av3[0], cat2(wl[1], wh[1]), bq[4][0]); SBAR();
  wl[5] = lds_tr(wr1[0] + 57856);
  MG(av3[1], cat2(wl[1], wh[1]), bq[4][1]); SBAR();
  wh[5] = lds_tr(wr1[0] + 58112);
  MG(av3[2], cat2(wl[1], wh[1]), bq[4][2]); SBAR();
  MG(av3[3], cat2(wl[1], wh[1]), bq[4][3]); SBAR();
  MG(av2[0], cat2(wl[2], wh[2]), bq[5][0]); SBAR();
  wl[0] = lds_tr(wr0[0] + 61952);
  MG(av2[1], cat2(wl[2], wh[2]), bq[5][1]); SBAR();
  wh[0] = lds_tr(wr0[0] + 62208);
  MG(av2[2], cat2(wl[2], wh[2]), bq[5][2]); SBAR();
  MG(av2[3], cat2(wl[2], wh[2]), bq[5][3]); SBAR();
  MG(av3[0], cat2(wl[3], wh[3]), bq[5][0]); SBAR();
  wl[1] = lds_tr(wr1[0] + 61952);
  MG(av3[1], cat2(wl[3], wh[3]), bq[5][1]); SBAR();
  wh[1] = lds_tr(wr1[0] + 62208);
  MG(av3[2], cat2(wl[3], wh[3]), bq[5][2]); SBAR();
  pk[0][0] = cvtpk(av1[0][0], av1[0][1]); PIN(pk[0][0]);
  pk[0][1] = cvtpk(av1[0][2], av1[0][3]); PIN(pk[0][1]);
  MG(av3[3], cat2(wl[3], wh[3]), bq[5][3]); SBAR();
  pk[0][2] = cvtpk(av1[1][0], av1[1][1]); PIN(pk[0][2]);
  MG(av2[0], cat2(wl[4], wh[4]), bq[6][0]); SBAR();
  wl[2] = lds_tr(wr0[1] + 512);
  pk[0][3] = cvtpk(av1[1][2], av1[1][3]); PIN(pk[0][3]);
  MG(av2[1], cat2(wl[4], wh[4]), bq[6][1]); SBAR();
  wh[2] = lds_tr(wr0[1] + 768);
  MG(av2[2], cat2(wl[4], wh[4]), bq[6][2]); SBAR();
  st16n<1024>(vo0, lo0, (u32x4){pk[0][0], pk[0][1], pk[0][2], pk[0][3]});
  MG(av2[3], cat2(wl[4], wh[4]), bq[6][3]); SBAR();
  MG(av3[0], cat2(wl[5], wh[5]), bq[6][0]); SBAR();
  wl[3] = lds_tr(wr1[1] + 512);
  MG(av3[1], cat2(wl[5], wh[5]), bq[6][1]); SBAR();
  wh[3] = lds_tr(wr1[1] + 768);
  MG(av3[2], cat2(wl[5], wh[5]), bq[6][2]); SBAR();
  MG(av3[3], cat2(wl[5], wh[5]), bq[6][3]); SBAR();
  MG(av2[0], cat2(wl[0], wh[0]), bq[7][0]); SBAR();
  wl[4] = lds_tr(wr0[1] + 4608);
  MG(av2[1], cat2(wl[0], wh[0]), bq[7][1]); SBAR();
  wh[4] = lds_tr(wr0[1] + 4864);
  MG(av2[2], cat2(wl[0], wh[0]), bq[7][2]); SBAR();
  MG(av2[3], cat2(wl[0], wh[0]), bq[7][3]); SBAR();
  MG(av3[0], cat2(wl[1], wh[1]), bq[7][0]); SBAR();
  wl[5] = lds_tr(wr1[1] + 4608);
  MG(av3[1], cat2(wl[1], wh[1]), bq[7][1]); SBAR();
  wh[5] = lds_tr(wr1[1] + 4864);
  MG(av3[2], cat2(wl[1], wh[1]), bq[7][2]); SBAR();
  asm volatile("s_waitcnt vmcnt(9)\n\ts_barrier" :  :: "memory");
  MG(av3[3], cat2(wl[1], wh[1]), bq[7][3]); SBAR();
  pk[1][0] = cvtpk(av1[2][0], av1[2][1]); PIN(pk[1][0]);
  MG(av2[0], cat2(wl[2], wh[2]), bq[8][0]); SBAR();
  wl[0] = lds_tr(wr0[1] + 8704);
  pk[1][1] = cvtpk(av1[2][2], av1[2][3]); PIN(pk[1][1]);
  MG(av2[1], cat2(wl[2], wh[2]), bq[8][1]); SBAR();
  wh[0] = lds_tr(wr0[1] + 8960);
  pk[1][2] = cvtpk(av1[3][0], av1[3][1]); PIN(pk[1][2]);
  MG(av2[2], cat2(wl[2], wh[2]), bq[8][2]); SBAR();
  pk[1][3] = cvtpk(av1[3][2], av1[3][3]); PIN(pk[1][3]);
  MG(av2[3], cat2(wl[2], wh[2]), bq[8][3]); SBAR();
  st16n<1024>(vo0, lo2, (u32x4){pk[1][0], pk[1][1], pk[1][2], pk[1][3]});
  MG(av3[0], cat2(wl[3], wh[3]), bq[8][0]); SBAR();
  wl[1] = lds_tr(wr1[1] + 8704);
  MG(av3[1], cat2(wl[3], wh[3]), bq[8][1]); SBAR();
  wh[1] = lds_tr(wr1[1] + 8960);
  MG(av3[2], cat2(wl[3], wh[3]), bq[8][2]); SBAR();
  MG(av3[3], cat2(wl[3], wh[3]), bq[8][3]); SBAR();
  MG(av2[0], cat2(wl[4], wh[4]), bq[9][0]); SBAR();
  wl[2] = lds_tr(wr0[1] + 12800);
  MG(av2[1], cat2(wl[4], wh[4]), bq[9][1]); SBAR();
  wh[2] = lds_tr(wr0[1] + 13056);
  MG(av2[2], cat2(wl[4], wh[4]), bq[9][2]); SBAR();
  MG(av2[3], cat2(wl[4], wh[4]), bq[9][3]); SBAR();
  MG(av3[0], cat2(wl[5], wh[5]), bq[9][0]); SBAR();
  wl[3] = lds_tr(wr1[1] + 12800);
  MG(av3[1], cat2(wl[5], wh[5]), bq[9][1]); SBAR();
  wh[3] = lds_tr(wr1[1] + 13056);
  MG(av3[2], cat2(wl[5], wh[5]), bq[9][2]); SBAR();
  MG(av3[3], cat2(wl[5], wh[5]), bq[9][3]); SBAR();
  pk[0][0] = cvtpk(ak4[0][0], ak4[0][1]); PIN(pk[0][0]);
  MG(av2[0], cat2(wl[0], wh[0]), bq[10][0]); SBAR();
  wl[4] = lds_tr(wr0[1] + 16896);
  pk[0][1] = cvtpk(ak4[0][2], ak4[0][3]); PIN(pk[0][1]);
  MG(av2[1], cat2(wl[0], wh[0]), bq[10][1]); SBAR();
  wh[4] = lds_tr(wr0[1] + 17152);
  pk[0][2] = cvtpk(ak5[0][0], ak5[0][1]); PIN(pk[0][2]);
  MG(av2[2], cat2(wl[0], wh[0]), bq[10][2]); SBAR();
  pk[0][3] = cvtpk(ak5[0][2], ak5[0][3]); PIN(pk[0][3]);
  MG(av2[3], cat2(wl[0], wh[0]), bq[10][3]); SBAR();
  st16n<2048>(ko, lo0, (u32x4){pk[0][0], pk[0][1], pk[0][2], pk[0][3]});
  MG(av3[0], cat2(wl[1], wh[1]), bq[10][0]); SBAR();
  wl[5] = lds_tr(wr1[1] + 16896);
  MG(av3[1], cat2(wl[1], wh[1]), bq[10][1]); SBAR();
  wh[5] = lds_tr(wr1[1] + 17152);
  MG(av3[2], cat2(wl[1], wh[1]), bq[10][2]); SBAR();
  MG(av3[3], cat2(wl[1], wh[1]), bq[10][3]); SBAR();
  MG(av2[0], cat2(wl[2], wh[2]), bq[11][0]); SBAR();
  wl[0] = lds_tr(wr0[1] + 20992);
  MG(av2[1], cat2(wl[2], wh[2]), bq[11][1]); SBAR();
  wh[0] = lds_tr(wr0[1] + 21248);
  MG(av2[2], cat2(wl[2], wh[2]), bq[11][2]); SBAR();
  MG(av2[3], cat2(wl[2], wh[2]), bq[11][3]); SBAR();
  MG(av3[0], cat2(wl[3], wh[3]), bq[11][0]); SBAR();
  wl[1] = lds_tr(wr1[1] + 20992);
  MG(av3[1], cat2(wl[3], wh[3]), bq[11][1]); SBAR();
  wh[1] = lds_tr(wr1[1] + 21248);
  MG(av3[2], cat2(wl[3], wh[3]), bq[11][2]); SBAR();
  MG(av3[3], cat2(wl[3], wh[3]), bq[11][3]); SBAR();
  pk[1][0] = cvtpk(ak4[1][0], ak4[1][1]); PIN(pk[1][0]);
  MG(av2[0], cat2(wl[4], wh[4]), bq[12][0]); SBAR();
  wl[2] = lds_tr(wr0[1] + 25088);
  pk[1][1] = cvtpk(ak4[1][2], ak4[1][3]); PIN(pk[1][1]);
  MG(av2[1], cat2(wl[4], wh[4]), bq[12][1]); SBAR();
  wh[2] = lds_tr(wr0[1] + 25344);
  pk[1][2] = cvtpk(ak5[1][0], ak5[1][1]); PIN(pk[1][2]);
  MG(av2[2], cat2(wl[4], wh[4]), bq[12][2]); SBAR();
  pk[1][3] = cvtpk(ak5[1][2], ak5[1][3]); PIN(pk[1][3]);
  MG(av2[3], cat2(wl[4], wh[4]), bq[12][3]); SBAR();
  st16n<2048>(ko, lo1, (u32x4){pk[1][0], pk[1][1], pk[1][2], pk[1][3]});
  MG(av3[0], cat2(wl[5], wh[5]), bq[12][0]); SBAR();
  wl[3] = lds_tr(wr1[1] + 25088);
  MG(av3[1], cat2(wl[5], wh[5]), bq[12][1]); SBAR();
  wh[3] = lds_tr(wr1[1] + 25344);
  MG(av3[2], cat2(wl[5], wh[5]), bq[12][2]); SBAR();
  MG(av3[3], cat2(wl[5], wh[5]), bq[12][3]); SBAR();
  MG(av2[0], cat2(wl[0], wh[0]), bq[13][0]); SBAR();
  wl[4] = lds_tr(wr0[1] + 29184);
  MG(av2[1], cat2(wl[0], wh[0]), bq[13][1]); SBAR();
  wh[4] = lds_tr(wr0[1] + 29440);
  MG(av2[2], cat2(wl[0], wh[0]), bq[13][2]); SBAR();
  MG(av2[3], cat2(wl[0], wh[0]), bq[13][3]); SBAR();
  MG(av3[0], cat2(wl[1], wh[1]), bq[13][0]); SBAR();
  wl[5] = lds_tr(wr1[1] + 29184);
  MG(av3[1], cat2(wl[1], wh[1]), bq[13][1]); SBAR();
  wh[5] = lds_tr(wr1[1] + 29440);
  MG(av3[2], cat2(wl[1], wh[1]), bq[13][2]); SBAR();
  MG(av3[3], cat2(wl[1], wh[1]), bq[13][3]); SBAR();
  pk[0][0] = cvtpk(ak4[2][0], ak4[2][1]); PIN(pk[0][0]);
  MG(av2[0], cat2(wl[2], wh[2]), bq[14][0]); SBAR();
  wl[0] = lds_tr(wr0[1] + 32768);
  pk[0][1] = cvtpk(ak4[2][2], ak4[2][3]); PIN(pk[0][1]);
  MG(av2[1], cat2(wl[2], wh[2]), bq[14][1]); SBAR();
  wh[0] = lds_tr(wr0[1] + 33024);
  pk[0][2] = cvtpk(ak5[2][0], ak5[2][1]); PIN(pk[0][2]);
  MG(av2[2], cat2(wl[2], wh[2]), bq[14][2]); SBAR();
  pk[0][3] = cvtpk(ak5[2][2], ak5[2][3]); PIN(pk[0][3]);
  MG(av2[3], cat2(wl[2], wh[2]), bq[14][3]); SBAR();
  st16n<2048>(ko, lo2, (u32x4){pk[0][0], pk[0][1], pk[0][2], pk[0][3]});
  MG(av3[0], cat2(wl[3], wh[3]), bq[14][0]); SBAR();
  wl[1] = lds_tr(wr1[1] + 32768);
  MG(av3[1], cat2(wl[3], wh[3]), bq[14][1]); SBAR();
  wh[1] = lds_tr(wr1[1] + 33024);
  MG(av3[2], cat2(wl[3], wh[3]), bq[14][2]); SBAR();
  MG(av3[3], cat2(wl[3], wh[3]), bq[14][3]); SBAR();
  MG(av2[0], cat2(wl[4], wh[4]), bq[15][0]); SBAR();
  wl[2] = lds_tr(wr0[1] + 36864);
  MG(av2[1], cat2(wl[4], wh[4]), bq[15][1]); SBAR();
  wh[2] = lds_tr(wr0[1] + 37120);
  MG(av2[2], cat2(wl[4], wh[4]), bq[15][2]); SBAR();
  MG(av2[3], cat2(wl[4], wh[4]), bq[15][3]); SBAR();
  MG(av3[0], cat2(wl[5], wh[5]), bq[15][0]); SBAR();
  wl[3] = lds_tr(wr1[1] + 36864);
  MG(av3[1], cat2(wl[5], wh[5]), bq[15][1]); SBAR();
  wh[3] = lds_tr(wr1[1] + 37120);
  MG(av3[2], cat2(wl[5], wh[5]), bq[15][2]); SBAR();
  MG(av3[3], cat2(wl[5], wh[5]), bq[15][3]); SBAR();
  pk[1][0] = cvtpk(ak4[3][0], ak4[3][1]); PIN(pk[1][0]);
  MG0(av0[0], cat2(wl[0], wh[0]), bq[0][0]); SBAR();
  wl[4] = lds_tr(wr0[1] + 40960);
  pk[1][1] = cvtpk(ak4[3][2], ak4[3][3]); PIN(pk[1][1]);
  MG0(av0[1], cat2(wl[0], wh[0]), bq[0][1]); SBAR();
  wh[4] = lds_tr(wr0[1] + 41216);
  pk[1][2] = cvtpk(ak5[3][0], ak5[3][1]); PIN(pk[1][2]);
  MG0(av0[2], cat2(wl[0], wh[0]), bq[0][2]); SBAR();
  pk[1][3] = cvtpk(ak5[3][2], ak5[3][3]); PIN(pk[1][3]);
  MG0(av0[3], cat2(wl[0], wh[0]), bq[0][3]); SBAR();
  st16n<2048>(ko, lo3, (u32x4){pk[1][0], pk[1][1], pk[1][2], pk[1][3]});
  MG0(av1[0], cat2(wl[1], wh[1]), bq[0][0]); SBAR();
  wl[5] = lds_tr(wr1[1] + 40960);
  MG0(av1[1], cat2(wl[1], wh[1]), bq[0][1]); SBAR();
  wh[5] = lds_tr(wr1[1] + 41216);
  MG0(av1[2], cat2(wl[1], wh[1]), bq[0][2]); SBAR();
  MG0(av1[3], cat2(wl[1], wh[1]), bq[0][3]); SBAR();
  MG(av0[0], cat2(wl[2], wh[2]), bq[1][0]); SBAR();
  wl[0] = lds_tr(wr0[1] + 45056);
  MG(av0[1], cat2(wl[2], wh[2]), bq[1][1]); SBAR();
  wh[0] = lds_tr(wr0[1] + 45312);
  MG(av0[2], cat2(wl[2], wh[2]), bq[1][2]); SBAR();
  MG(av0[3], cat2(wl[2], wh[2]), bq[1][3]); SBAR();
  MG(av1[0], cat2(wl[3], wh[3]), bq[1][0]); SBAR();
  wl[1] = lds_tr(wr1[1] + 45056);
  MG(av1[1], cat2(wl[3], wh[3]), bq[1][1]); SBAR();
  wh[1] = lds_tr(wr1[1] + 45312);
  MG(av1[2], cat2(wl[3], wh[3]), bq[1][2]); SBAR();
  MG(av1[3], cat2(wl[3], wh[3]), bq[1][3]); SBAR();
  pk[0][0] = cvtpk(av2[0][0], av2[0][1]); PIN(pk[0][0]);
  MG(av0[0], cat2(wl[4], wh[4]), bq[2][0]); SBAR();
  wl[2] = lds_tr(wr0[1] + 49152);
  pk[0][1] = cvtpk(av2[0][2], av2[0][3]); PIN(pk[0][1]);
  MG(av0[1], cat2(wl[4], wh[4]), bq[2][1]); SBAR();
  wh[2] = lds_tr(wr0[1] + 49408);
  pk[0][2] = cvtpk(av2[1][0], av2[1][1]); PIN(pk[0][2]);
  MG(av0[2], cat2(wl[4], wh[4]), bq[2][2]); SBAR();
  pk[0][3] = cvtpk(av2[1][2], av2[1][3]); PIN(pk[0][3]);
  MG(av0[3], cat2(wl[4], wh[4]), bq[2][3]); SBAR();
  st16n<2048>(vo0, lo0, (u32x4){pk[0][0], pk[0][1], pk[0][2], pk[0][3]});
  MG(av1[0], cat2(wl[5], wh[5]), bq[2][0]); SBAR();
  wl[3] = lds_tr(wr1[1] + 49152);
  MG(av1[1], cat2(wl[5], wh[5]), bq[2][1]); SBAR();
  wh[3] = lds_tr(wr1[1] + 49408);
  MG(av1[2], cat2(wl[5], wh[5]), bq[2][2]); SBAR();
  MG(av1[3], cat2(wl[5], wh[5]), bq[2][3]); SBAR();
  MG(av0[0], cat2(wl[0], wh[0]), bq[3][0]); SBAR();
  wl[4] = lds_tr(wr0[1] + 53248);
  MG(av0[1], cat2(wl[0], wh[0]), bq[3][1]); SBAR();
  wh[4] = lds_tr(wr0[1] + 53504);
  MG(av0[2], cat2(wl[0], wh[0]), bq[3][2]); SBAR();
  MG(av0[3], cat2(wl[0], wh[0]), bq[3][3]); SBAR();
  MG(av1[0], cat2(wl[1], wh[1]), bq[3][0]); SBAR();
  wl[5] = lds_tr(wr1[1] + 53248);
  MG(av1[1], cat2(wl[1], wh[1]), bq[3][1]); SBAR();
  wh[5] = lds_tr(wr1[1] + 53504);
  MG(av1[2], cat2(wl[1], wh[1]), bq[3][2]); SBAR();
  MG(av1[3], cat2(wl[1], wh[1]), bq[3][3]); SBAR();
  pk[1][0] = cvtpk(av2[2][0], av2[2][1]); PIN(pk[1][0]);
  MG(av0[0], cat2(wl[2], wh[2]), bq[4][0]); SBAR();
  wl[0] = lds_tr(wr0[1] + 57344);
  pk[1][1] = cvtpk(av2[2][2], av2[2][3]); PIN(pk[1][1]);
  MG(av0[1], cat2(wl[2], wh[2]), bq[4][1]); SBAR();
  wh[0] = lds_tr(wr0[1] + 57600);
  pk[1][2] = cvtpk(av2[3][0], av2[3][1]); PIN(pk[1][2]);
  MG(av0[2], cat2(wl[2], wh[2]), bq[4][2]); SBAR();
  pk[1][3] = cvtpk(av2[3][2], av2[3][3]); PIN(pk[1][3]);
  MG(av0[3], cat2(wl[2], wh[2]), bq[4][3]); SBAR();
  st16n<2048>(vo0, lo2, (u32x4){pk[1][0], pk[1][1], pk[1][2], pk[1][3]});
  MG(av1[0], cat2(wl[3], wh[3]), bq[4][0]); SBAR();
  wl[1] = lds_tr(wr1[1] + 57344);
  MG(av1[1], cat2(wl[3], wh[3]), bq[4][1]); SBAR();
  wh[1] = lds_tr(wr1[1] + 57600);
  MG(av1[2], cat2(wl[3], wh[3]), bq[4][2]); SBAR();
  MG(av1[3], cat2(wl[3], wh[3]), bq[4][3]); SBAR();
  MG(av0[0], cat2(wl[4], wh[4]), bq[5][0]); SBAR();
  wl[2] = lds_tr(wr0[1] + 61440);
  MG(av0[1], cat2(wl[4], wh[4]), bq[5][1]); SBAR();
  wh[2] = lds_tr(wr0[1] + 61696);
  MG(av0[2], cat2(wl[4], wh[4]), bq[5][2]); SBAR();
  MG(av0[3], cat2(wl[4], wh[4]), bq[5][3]); SBAR();
  MG(av1[0], cat2(wl[5], wh[5]), bq[5][0]); SBAR();
  wl[3] = lds_tr(wr1[1] + 61440);
  MG(av1[1], cat2(wl[5], wh[5]), bq[5][1]); SBAR();
  wh[3] = lds_tr(wr1[1] + 61696);
  MG(av1[2], cat2(wl[5], wh[5]), bq[5][2]); SBAR();
  MG(av1[3], cat2(wl[5], wh[5]), bq[5][3]); SBAR();
  pk[0][0] = cvtpk(av3[0][0], av3[0][1]); PIN(pk[0][0]);
  MG(av0[0], cat2(wl[0], wh[0]), bq[6][0]); SBAR();
  wl[4] = lds_tr(wr0[2] + 0);
  pk[0][1] = cvtpk(av3[0][2], av3[0][3]); PIN(pk[0][1]);
  MG(av0[1], cat2(wl[0], wh[0]), bq[6][1]); SBAR();
  wh[4] = lds_tr(wr0[2] + 256);
  pk[0][2] = cvtpk(av3[1][0], av3[1][1]); PIN(pk[0][2]);
  MG(av0[2], cat2(wl[0], wh[0]), bq[6][2]); SBAR();
  pk[0][3] = cvtpk(av3[1][2], av3[1][3]); PIN(pk[0][3]);
  MG(av0[3], cat2(wl[0], wh[0]), bq[6][3]); SBAR();
  st16n<3072>(vo0, lo0, (u32x4){pk[0][0], pk[0][1], pk[0][2], pk[0][3]});
  MG(av1[0], cat2(wl[1], wh[1]), bq[6][0]); SBAR();
  wl[5] = lds_tr(wr1[2] + 0);
  MG(av1[1], cat2(wl[1], wh[1]), bq[6][1]); SBAR();
  wh[5] = lds_tr(wr1[2] + 256);
  MG(av1[2], cat2(wl[1], wh[1]), bq[6][2]); SBAR();
  MG(av1[3], cat2(wl[1], wh[1]), bq[6][3]); SBAR();
  MG(av0[0], cat2(wl[2], wh[2]), bq[7][0]); SBAR();
  wl[0] = lds_tr(wr0[2] + 4096);
  MG(av0[1], cat2(wl[2], wh[2]), bq[7][1]); SBAR();
  wh[0] = lds_tr(wr0[2] + 4352);
  MG(av0[2], cat2(wl[2], wh[2]), bq[7][2]); SBAR();
  MG(av0[3], cat2(wl[2], wh[2]), bq[7][3]); SBAR();
  MG(av1[0], cat2(wl[3], wh[3]), bq[7][0]); SBAR();
  wl[1] = lds_tr(wr1[2] + 4096);
  MG(av1[1], cat2(wl[3], wh[3]), bq[7][1]); SBAR();
  wh[1] = lds_tr(wr1[2] + 4352);
  MG(av1[2], cat2(wl[3], wh[3]), bq[7][2]); SBAR();
  MG(av1[3], cat2(wl[3], wh[3]), bq[7][3]); SBAR();
  pk[1][0] = cvtpk(av3[2][0], av3[2][1]); PIN(pk[1][0]);
  MG(av0[0], cat2(wl[4], wh[4]), bq[8][0]); SBAR();
  wl[2] = lds_tr(wr0[2] + 8192);
  pk[1][1] = cvtpk(av3[2][2], av3[2][3]); PIN(pk[1][1]);
  MG(av0[1], cat2(wl[4], wh[4]), bq[8][1]); SBAR();
  wh[2] = lds_tr(wr0[2] + 8448);
  pk[1][2] = cvtpk(av3[3][0], av3[3][1]); PIN(pk[1][2]);
  MG(av0[2], cat2(wl[4], wh[4]), bq[8][2]); SBAR();
  pk[1][3] = cvtpk(av3[3][2], av3[3][3]); PIN(pk[1][3]);
  MG(av0[3], cat2(wl[4], wh[4]), bq[8][3]); SBAR();
  st16n<3072>(vo0, lo2, (u32x4){pk[1][0], pk[1][1], pk[1][2], pk[1][3]});
  MG(av1[0], cat2(wl[5], wh[5]), bq[8][0]); SBAR();
  wl[3] = lds_tr(wr1[2] + 8192);
  MG(av1[1], cat2(wl[5], wh[5]), bq[8][1]); SBAR();
  wh[3] = lds_tr(wr1[2] + 8448);
  MG(av1[2], cat2(wl[5], wh[5]), bq[8][2]); SBAR();
  MG(av1[3], cat2(wl[5], wh[5]), bq[8][3]); SBAR();
  MG(av0[0], cat2(wl[0], wh[0]), bq[9][0]); SBAR();
  wl[4] = lds_tr(wr0[2] + 12288);
  MG(av0[1], cat2(wl[0], wh[0]), bq[9][1]); SBAR();
  wh[4] = lds_tr(wr0[2] + 12544);
  MG(av0[2], cat2(wl[0], wh[0]), bq[9][2]); SBAR();
  MG(av0[3], cat2(wl[0], wh[0]), bq[9][3]); SBAR();
  MG(av1[0], cat2(wl[1], wh[1]), bq[9][0]); SBAR();
  wl[5] = lds_tr(wr1[2] + 12288);
  MG(av1[1], cat2(wl[1], wh[1]), bq[9][1]); SBAR();
  wh[5] = lds_tr(wr1[2] + 12544);
  MG(av1[2], cat2(wl[1], wh[1]), bq[9][2]); SBAR();
  MG(av1[3], cat2(wl[1], wh[1]), bq[9][3]); SBAR();
  pk[0][0] = cvtpk(ak6[0][0], ak6[0][1]); PIN(pk[0][0]);
  MG(av0[0], cat2(wl[2], wh[2]), bq[10][0]); SBAR();
  wl[0] = lds_tr(wr0[2] + 16384);
  pk[0][1] = cvtpk(ak6[0][2], ak6[0][3]); PIN(pk[0][1]);
  MG(av0[1], cat2(wl[2], wh[2]), bq[10][1]); SBAR();
  wh[0] = lds_tr(wr0[2] + 16640);
  pk[0][2] = cvtpk(ak7[0][0], ak7[0][1]); PIN(pk[0][2]);
  MG(av0[2], cat2(wl[2], wh[2]), bq[10][2]); SBAR();
  pk[0][3] = cvtpk(ak7[0][2], ak7[0][3]); PIN(pk[0][3]);
  MG(av0[3], cat2(wl[2], wh[2]), bq[10][3]); SBAR();
  st16n<3072>(ko, lo0, (u32x4){pk[0][0], pk[0][1], pk[0][2], pk[0][3]});
  MG(av1[0], cat2(wl[3], wh[3]), bq[10][0]); SBAR();
  wl[1] = lds_tr(wr1[2] + 16384);
  MG(av1[1], cat2(wl[3], wh[3]), bq[10][1]); SBAR();
  wh[1] = lds_tr(wr1[2] + 16640);
  MG(av1[2], cat2(wl[3], wh[3]), bq[10][2]); SBAR();
  MG(av1[3], cat2(wl[3], wh[3]), bq[10][3]); SBAR();
  MG(av0[0], cat2(wl[4], wh[4]), bq[11][0]); SBAR();
  wl[2] = lds_tr(wr0[2] + 20480);
  MG(av0[1], cat2(wl[4], wh[4]), bq[11][1]); SBAR();
  wh[2] = lds_tr(wr0[2] + 20736);
  MG(av0[2], cat2(wl[4], wh[4]), bq[11][2]); SBAR();
  MG(av0[3], cat2(wl[4], wh[4]), bq[11][3]); SBAR();
  MG(av1[0], cat2(wl[5], wh[5]), bq[11][0]); SBAR();
  wl[3] = lds_tr(wr1[2] + 20480);
  MG(av1[1], cat2(wl[5], wh[5]), bq[11][1]); SBAR();
  wh[3] = lds_tr(wr1[2] + 20736);
  MG(av1[2], cat2(wl[5], wh[5]), bq[11][2]); SBAR();
  MG(av1[3], cat2(wl[5], wh[5]), bq[11][3]); SBAR();
  pk[1][0] = cvtpk(ak6[1][0], ak6[1][1]); PIN(pk[1][0]);
  MG(av0[0], cat2(wl[0], wh[0]), bq[12][0]); SBAR();
  wl[4] = lds_tr(wr0[2] + 24576);
  pk[1][1] = cvtpk(ak6[1][2], ak6[1][3]); PIN(pk[1][1]);
  MG(av0[1], cat2(wl[0], wh[0]), bq[12][1]); SBAR();
  wh[4] = lds_tr(wr0[2] + 24832);
  pk[1][2] = cvtpk(ak7[1][0], ak7[1][1]); PIN(pk[1][2]);
  MG(av0[2], cat2(wl[0], wh[0]), bq[12][2]); SBAR();
  pk[1][3] = cvtpk(ak7[1][2], ak7[1][3]); PIN(pk[1][3]);
  MG(av0[3], cat2(wl[0], wh[0]), bq[12][3]); SBAR();
  st16n<3072>(ko, lo1, (u32x4){pk[1][0], pk[1][1], pk[1][2], pk[1][3]});
  MG(av1[0], cat2(wl[1], wh[1]), bq[12][0]); SBAR();
  wl[5] = lds_tr(wr1[2] + 24576);
  MG(av1[1], cat2(wl[1], wh[1]), bq[12][1]); SBAR();
  wh[5] = lds_tr(wr1[2] + 24832);
  MG(av1[2], cat2(wl[1], wh[1]), bq[12][2]); SBAR();
  MG(av1[3], cat2(wl[1], wh[1]), bq[12][3]); SBAR();
  MG(av0[0], cat2(wl[2], wh[2]), bq[13][0]); SBAR();
  wl[0] = lds_tr(wr0[2] + 28672);
  MG(av0[1], cat2(wl[2], wh[2]), bq[13][1]); SBAR();
  wh[0] = lds_tr(wr0[2] + 28928);
  MG(av0[2], cat2(wl[2], wh[2]), bq[13][2]); SBAR();
  MG(av0[3], cat2(wl[2], wh[2]), bq[13][3]); SBAR();
  MG(av1[0], cat2(wl[3], wh[3]), bq[13][0]); SBAR();
  wl[1] = lds_tr(wr1[2] + 28672);
  MG(av1[1], cat2(wl[3], wh[3]), bq[13][1]); SBAR();
  wh[1] = lds_tr(wr1[2] + 28928);
  MG(av1[2], cat2(wl[3], wh[3]), bq[13][2]); SBAR();
  MG(av1[3], cat2(wl[3], wh[3]), bq[13][3]); SBAR();
  pk[0][0] = cvtpk(ak6[2][0], ak6[2][1]); PIN(pk[0][0]);
  MG(av0[0], cat2(wl[4], wh[4]), bq[14][0]); SBAR();
  wl[2] = lds_tr(wr0[1] + 33280);
  pk[0][1] = cvtpk(ak6[2][2], ak6[2][3]); PIN(pk[0][1]);
  MG(av0[1], cat2(wl[4], wh[4]), bq[14][1]); SBAR();
  wh[2] = lds_tr(wr0[1] + 33536);
  pk[0][2] = cvtpk(ak7[2][0], ak7[2][1]); PIN(pk[0][2]);
  MG(av0[2], cat2(wl[4], wh[4]), bq[14][2]); SBAR();
  pk[0][3] = cvtpk(ak7[2][2], ak7[2][3]); PIN(pk[0][3]);
  MG(av0[3], cat2(wl[4], wh[4]), bq[14][3]); SBAR();
  st16n<3072>(ko, lo2, (u32x4){pk[0][0], pk[0][1], pk[0][2], pk[0][3]});
  MG(av1[0], cat2(wl[5], wh[5]), bq[14][0]); SBAR();
  wl[3] = lds_tr(wr1[1] + 33280);
  MG(av1[1], cat2(wl[5], wh[5]), bq[14][1]); SBAR();
  wh[3] = lds_tr(wr1[1] + 33536);
  MG(av1[2], cat2(wl[5], wh[5]), bq[14][2]); SBAR();
  MG(av1[3], cat2(wl[5], wh[5]), bq[14][3]); SBAR();
  MG(av0[0], cat2(wl[0], wh[0]), bq[15][0]); SBAR();
  wl[4] = lds_tr(wr0[1] + 37376);
  MG(av0[1], cat2(wl[0], wh[0]), bq[15][1]); SBAR();
  wh[4] = lds_tr(wr0[1] + 37632);
  MG(av0[2], cat2(wl[0], wh[0]), bq[15][2]); SBAR();
  MG(av0[3], cat2(wl[0], wh[0]), bq[15][3]); SBAR();
  MG(av1[0], cat2(wl[1], wh[1]), bq[15][0]); SBAR();
  wl[5] = lds_tr(wr1[1] + 37376);
  MG(av1[1], cat2(wl[1], wh[1]), bq[15][1]); SBAR();
  wh[5] = lds_tr(wr1[1] + 37632);
  MG(av1[2], cat2(wl[1], wh[1]), bq[15][2]); SBAR();
  MG(av1[3], cat2(wl[1], wh[1]), bq[15][3]); SBAR();
  pk[1][0] = cvtpk(ak6[3][0], ak6[3][1]); PIN(pk[1][0]);
  MG0(av2[0], cat2(wl[2], wh[2]), bq[0][0]); SBAR();
  wl[0] = lds_tr(wr0[1] + 41472);
  pk[1][1] = cvtpk(ak6[3][2], ak6[3][3]); PIN(pk[1][1]);
  MG0(av2[1], cat2(wl[2], wh[2]), bq[0][1]); SBAR();
  wh[0] = lds_tr(wr0[1] + 41728);
  pk[1][2] = cvtpk(ak7[3][0], ak7[3][1]); PIN(pk[1][2]);
  MG0(av2[2], cat2(wl[2], wh[2]), bq[0][2]); SBAR();
  pk[1][3] = cvtpk(ak7[3][2], ak7[3][3]); PIN(pk[1][3]);
  MG0(av2[3], cat2(wl[2], wh[2]), bq[0][3]); SBAR();
  st16n<3072>(ko, lo3, (u32x4){pk[1][0], pk[1][1], pk[1][2], pk[1][3]});
  MG0(av3[0], cat2(wl[3], wh[3]), bq[0][0]); SBAR();
  wl[1] = lds_tr(wr1[1] + 41472);
  MG0(av3[1], cat2(wl[3], wh[3]), bq[0][1]); SBAR();
  wh[1] = lds_tr(wr1[1] + 41728);
  MG0(av3[2], cat2(wl[3], wh[3]), bq[0][2]); SBAR();
  MG0(av3[3], cat2(wl[3], wh[3]), bq[0][3]); SBAR();
  MG(av2[0], cat2(wl[4], wh[4]), bq[1][0]); SBAR();
  wl[2] = lds_tr(wr0[1] + 45568);
  MG(av2[1], cat2(wl[4], wh[4]), bq[1][1]); SBAR();
  wh[2] = lds_tr(wr0[1] + 45824);
  MG(av2[2], cat2(wl[4], wh[4]), bq[1][2]); SBAR();
  MG(av2[3], cat2(wl[4], wh[4]), bq[1][3]); SBAR();
  MG(av3[0], cat2(wl[5], wh[5]), bq[1][0]); SBAR();
  wl[3] = lds_tr(wr1[1] + 45568);
  MG(av3[1], cat2(wl[5], wh[5]), bq[1][1]); SBAR();
  wh[3] = lds_tr(wr1[1] + 45824);
  MG(av3[2], cat2(wl[5], wh[5]), bq[1][2]); SBAR();
  MG(av3[3], cat2(wl[5], wh[5]), bq[1][3]); SBAR();
  pk[0][0] = cvtpk(av0[0][0], av0[0][1]); PIN(pk[0][0]);
  MG(av2[0], cat2(wl[0], wh[0]), bq[2][0]); SBAR();
  wl[4] = lds_tr(wr0[1] + 49664);
  pk[0][1] = cvtpk(av0[0][2], av0[0][3]); PIN(pk[0][1]);
  MG(av2[1], cat2(wl[0], wh[0]), bq[2][1]); SBAR();
  wh[4] = lds_tr(wr0[1] + 49920);
  pk[0][2] = cvtpk(av0[1][0], av0[1][1]); PIN(pk[0][2]);
  MG(av2[2], cat2(wl[0], wh[0]), bq[2][2]); SBAR();
  pk[0][3] = cvtpk(av0[1][2], av0[1][3]); PIN(pk[0][3]);
  MG(av2[3], cat2(wl[0], wh[0]), bq[2][3]); SBAR();
  st16n<0>(vo1, lo0, (u32x4){pk[0][0], pk[0][1], pk[0][2], pk[0][3]});
  MG(av3[0], cat2(wl[1], wh[1]), bq[2][0]); SBAR();
  wl[5] = lds_tr(wr1[1] + 49664);
  MG(av3[1], cat2(wl[1], wh[1]), bq[2][1]); SBAR();
  wh[5] = lds_tr(wr1[1] + 49920);
  MG(av3[2], cat2(wl[1], wh[1]), bq[2][2]); SBAR();
  MG(av3[3], cat2(wl[1], wh[1]), bq[2][3]); SBAR();
  MG(av2[0], cat2(wl[2], wh[2]), bq[3][0]); SBAR();
  wl[0] = lds_tr(wr0[1] + 53760);
  MG(av2[1], cat2(wl[2], wh[2]), bq[3][1]); SBAR();
  wh[0] = lds_tr(wr0[1] + 54016);
  MG(av2[2], cat2(wl[2], wh[2]), bq[3][2]); SBAR();
  MG(av2[3], cat2(wl[2], wh[2]), bq[3][3]); SBAR();
  MG(av3[0], cat2(wl[3], wh[3]), bq[3][0]); SBAR();
  wl[1] = lds_tr(wr1[1] + 53760);
  MG(av3[1], cat2(wl[3], wh[3]), bq[3][1]); SBAR();
  wh[1] = lds_tr(wr1[1] + 54016);
  MG(av3[2], cat2(wl[3], wh[3]), bq[3][2]); SBAR();
  MG(av3[3], cat2(wl[3], wh[3]), bq[3][3]); SBAR();
  pk[1][0] = cvtpk(av0[2][0], av0[2][1]); PIN(pk[1][0]);
  MG(av2[0], cat2(wl[4], wh[4]), bq[4][0]); SBAR();
  wl[2] = lds_tr(wr0[1] + 57856);
  pk[1][1] = cvtpk(av0[2][2], av0[2][3]); PIN(pk[1][1]);
  MG(av2[1], cat2(wl[4], wh[4]), bq[4][1]); SBAR();
  wh[2] = lds_tr(wr0[1] + 58112);
  pk[1][2] = cvtpk(av0[3][0], av0[3][1]); PIN(pk[1][2]);
  MG(av2[2], cat2(wl[4], wh[4]), bq[4][2]); SBAR();
  pk[1][3] = cvtpk(av0[3][2], av0[3][3]); PIN(pk[1][3]);
  MG(av2[3], cat2(wl[4], wh[4]), bq[4][3]); SBAR();
  st16n<0>(vo1, lo2, (u32x4){pk[1][0], pk[1][1], pk[1][2], pk[1][3]});
  MG(av3[0], cat2(wl[5], wh[5]), bq[4][0]); SBAR();
  wl[3] = lds_tr(wr1[1] + 57856);
  MG(av3[1], cat2(wl[5], wh[5]), bq[4][1]); SBAR();
  wh[3] = lds_tr(wr1[1] + 58112);
  MG(av3[2], cat2(wl[5], wh[5]), bq[4][2]); SBAR();
  MG(av3[3], cat2(wl[5], wh[5]), bq[4][3]); SBAR();
  MG(av2[0], cat2(wl[0], wh[0]), bq[5][0]); SBAR();
  wl[4] = lds_tr(wr0[1] + 61952);
  MG(av2[1], cat2(wl[0], wh[0]), bq[5][1]); SBAR();
  wh[4] = lds_tr(wr0[1] + 62208);
  MG(av2[2], cat2(wl[0], wh[0]), bq[5][2]); SBAR();
  MG(av2[3], cat2(wl[0], wh[0]), bq[5][3]); SBAR();
  MG(av3[0], cat2(wl[1], wh[1]), bq[5][0]); SBAR();
  wl[5] = lds_tr(wr1[1] + 61952);
  MG(av3[1], cat2(wl[1], wh[1]), bq[5][1]); SBAR();
  wh[5] = lds_tr(wr1[1] + 62208);
  MG(av3[2], cat2(wl[1], wh[1]), bq[5][2]); SBAR();
  MG(av3[3], cat2(wl[1], wh[1]), bq[5][3]); SBAR();
  pk[0][0] = cvtpk(av1[0][0], av1[0][1]); PIN(pk[0][0]);
  MG(av2[0], cat2(wl[2], wh[2]), bq[6][0]); SBAR();
  wl[0] = lds_tr(wr0[2] + 512);
  pk[0][1] = cvtpk(av1[0][2], av1[0][3]); PIN(pk[0][1]);
  MG(av2[1], cat2(wl[2], wh[2]), bq[6][1]); SBAR();
  wh[0] = lds_tr(wr0[2] + 768);
  pk[0][2] = cvtpk(av1[1][0], av1[1][1]); PIN(pk[0][2]);
  MG(av2[2], cat2(wl[2], wh[2]), bq[6][2]); SBAR();
  pk[0][3] = cvtpk(av1[1][2], av1[1][3]); PIN(pk[0][3]);
  MG(av2[3], cat2(wl[2], wh[2]), bq[6][3]); SBAR();
  st16n<1024>(vo1, lo0, (u32x4){pk[0][0], pk[0][1], pk[0][2], pk[0][3]});
  MG(av3[0], cat2(wl[3], wh[3]), bq[6][0]); SBAR();
  wl[1] = lds_tr(wr1[2] + 512);
  MG(av3[1], cat2(wl[3], wh[3]), bq[6][1]); SBAR();
  wh[1] = lds_tr(wr1[2] + 768);
  MG(av3[2], cat2(wl[3], wh[3]), bq[6][2]); SBAR();
  MG(av3[3], cat2(wl[3], wh[3]), bq[6][3]); SBAR();
  MG(av2[0], cat2(wl[4], wh[4]), bq[7][0]); SBAR();
  wl[2] = lds_tr(wr0[2] + 4608);
  MG(av2[1], cat2(wl[4], wh[4]), bq[7][1]); SBAR();
  wh[2] = lds_tr(wr0[2] + 4864);
  MG(av2[2], cat2(wl[4], wh[4]), bq[7][2]); SBAR();
  MG(av2[3], cat2(wl[4], wh[4]), bq[7][3]); SBAR();
  MG(av3[0], cat2(wl[5], wh[5]), bq[7][0]); SBAR();
  wl[3] = lds_tr(wr1[2] + 4608);
  MG(av3[1], cat2(wl[5], wh[5]), bq[7][1]); SBAR();
  wh[3] = lds_tr(wr1[2] + 4864);
  MG(av3[2], cat2(wl[5], wh[5]), bq[7][2]); SBAR();
  MG(av3[3], cat2(wl[5], wh[5]), bq[7][3]); SBAR();
  pk[1][0] = cvtpk(av1[2][0], av1[2][1]); PIN(pk[1][0]);
  MG(av2[0], cat2(wl[0], wh[0]), bq[8][0]); SBAR();
  wl[4] = lds_tr(wr0[2] + 8704);
  pk[1][1] = cvtpk(av1[2][2], av1[2][3]); PIN(pk[1][1]);
  MG(av2[1], cat2(wl[0], wh[0]), bq[8][1]); SBAR();
  wh[4] = lds_tr(wr0[2] + 8960);
  pk[1][2] = cvtpk(av1[3][0], av1[3][1]); PIN(pk[1][2]);
  MG(av2[2], cat2(wl[0], wh[0]), bq[8][2]); SBAR();
  pk[1][3] = cvtpk(av1[3][2], av1[3][3]); PIN(pk[1][3]);
  MG(av2[3], cat2(wl[0], wh[0]), bq[8][3]); SBAR();
  st16n<1024>(vo1, lo2, (u32x4){pk[1][0], pk[1][1], pk[1][2], pk[1][3]});
  MG(av3[0], cat2(wl[1], wh[1]), bq[8][0]); SBAR();
  wl[5] = lds_tr(wr1[2] + 8704);
  MG(av3[1], cat2(wl[1], wh[1]), bq[8][1]); SBAR();
  wh[5] = lds_tr(wr1[2] + 8960);
  MG(av3[2], cat2(wl[1], wh[1]), bq[8][2]); SBAR();
  MG(av3[3], cat2(wl[1], wh[1]), bq[8][3]); SBAR();
  MG(av2[0], cat2(wl[2], wh[2]), bq[9][0]); SBAR();
  wl[0] = lds_tr(wr0[2] + 12800);
  MG(av2[1], cat2(wl[2], wh[2]), bq[9][1]); SBAR();
  wh[0] = lds_tr(wr0[2] + 13056);
  MG(av2[2], cat2(wl[2], wh[2]), bq[9][2]); SBAR();
  MG(av2[3], cat2(wl[2], wh[2]), bq[9][3]); SBAR();
  MG(av3[0], cat2(wl[3], wh[3]), bq[9][0]); SBAR();
  wl[1] = lds_tr(wr1[2] + 12800);
  MG(av3[1], cat2(wl[3], wh[3]), bq[9][1]); SBAR();
  wh[1] = lds_tr(wr1[2] + 13056);
  MG(av3[2], cat2(wl[3], wh[3]), bq[9][2]); SBAR();
  MG(av3[3], cat2(wl[3], wh[3]), bq[9][3]); SBAR();
  MG(av2[0], cat2(wl[4], wh[4]), bq[10][0]); SBAR();
  wl[2] = lds_tr(wr0[2] + 16896);
  MG(av2[1], cat2(wl[4], wh[4]), bq[10][1]); SBAR();
  wh[2] = lds_tr(wr0[2] + 17152);
  MG(av2[2], cat2(wl[4], wh[4]), bq[10][2]); SBAR();
  MG(av2[3], cat2(wl[4], wh[4]), bq[10][3]); SBAR();
  MG(av3[0], cat2(wl[5], wh[5]), bq[10][0]); SBAR();
  wl[3] = lds_tr(wr1[2] + 16896);
  MG(av3[1], cat2(wl[5], wh[5]), bq[10][1]); SBAR();
  wh[3] = lds_tr(wr1[2] + 17152);
  MG(av3[2], cat2(wl[5], wh[5]), bq[10][2]); SBAR();
  MG(av3[3], cat2(wl[5], wh[5]), bq[10][3]); SBAR();
  MG(av2[0], cat2(wl[0], wh[0]), bq[11][0]); SBAR();
  wl[4] = lds_tr(wr0[2] + 20992);
  MG(av2[1], cat2(wl[0], wh[0]), bq[11][1]); SBAR();
  wh[4] = lds_tr(wr0[2] + 21248);
  MG(av2[2], cat2(wl[0], wh[0]), bq[11][2]); SBAR();
  MG(av2[3], cat2(wl[0], wh[0]), bq[11][3]); SBAR();
  MG(av3[0], cat2(wl[1], wh[1]), bq[11][0]); SBAR();
  wl[5] = lds_tr(wr1[2] + 20992);
  MG(av3[1], cat2(wl[1], wh[1]), bq[11][1]); SBAR();
  wh[5] = lds_tr(wr1[2] + 21248);
  MG(av3[2], cat2(wl[1], wh[1]), bq[11][2]); SBAR();
  MG(av3[3], cat2(wl[1], wh[1]), bq[11][3]); SBAR();
  MG(av2[0], cat2(wl[2], wh[2]), bq[12][0]); SBAR();
  wl[0] = lds_tr(wr0[2] + 25088);
  MG(av2[1], cat2(wl[2], wh[2]), bq[12][1]); SBAR();
  wh[0] = lds_tr(wr0[2] + 25344);
  MG(av2[2], cat2(wl[2], wh[2]), bq[12][2]); SBAR();
  MG(av2[3], cat2(wl[2], wh[2]), bq[12][3]); SBAR();
  MG(av3[0], cat2(wl[3], wh[3]), bq[12][0]); SBAR();
  wl[1] = lds_tr(wr1[2] + 25088);
  MG(av3[1], cat2(wl[3], wh[3]), bq[12][1]); SBAR();
  wh[1] = lds_tr(wr1[2] + 25344);
  MG(av3[2], cat2(wl[3], wh[3]), bq[12][2]); SBAR();
  MG(av3[3], cat2(wl[3], wh[3]), bq[12][3]); SBAR();
  MG(av2[0], cat2(wl[4], wh[4]), bq[13][0]); SBAR();
  wl[2] = lds_tr(wr0[2] + 29184);
  MG(av2[1], cat2(wl[4], wh[4]), bq[13][1]); SBAR();
  wh[2] = lds_tr(wr0[2] + 29440);
  MG(av2[2], cat2(wl[4], wh[4]), bq[13][2]); SBAR();
  MG(av2[3], cat2(wl[4], wh[4]), bq[13][3]); SBAR();
  MG(av3[0], cat2(wl[5], wh[5]), bq[13][0]); SBAR();
  wl[3] = lds_tr(wr1[2] + 29184);
  MG(av3[1], cat2(wl[5], wh[5]), bq[13][1]); SBAR();
  wh[3] = lds_tr(wr1[2] + 29440);
  MG(av3[2], cat2(wl[5], wh[5]), bq[13][2]); SBAR();
  MG(av3[3], cat2(wl[5], wh[5]), bq[13][3]); SBAR();
  MG(av2[0], cat2(wl[0], wh[0]), bq[14][0]); SBAR();
  MG(av2[1], cat2(wl[0], wh[0]), bq[14][1]); SBAR();
  MG(av2[2], cat2(wl[0], wh[0]), bq[14][2]); SBAR();
  MG(av2[3], cat2(wl[0], wh[0]), bq[14][3]); SBAR();
  MG(av3[0], cat2(wl[1], wh[1]), bq[14][0]); SBAR();
  MG(av3[1], cat2(wl[1], wh[1]), bq[14][1]); SBAR();
  MG(av3[2], cat2(wl[1], wh[1]), bq[14][2]); SBAR();
  MG(av3[3], cat2(wl[1], wh[1]), bq[14][3]); SBAR();
  MG(av2[0], cat2(wl[2], wh[2]), bq[15][0]); SBAR();
  MG(av2[1], cat2(wl[2], wh[2]), bq[15][1]); SBAR();
  MG(av2[2], cat2(wl[2], wh[2]), bq[15][2]); SBAR();
  MG(av2[3], cat2(wl[2], wh[2]), bq[15][3]); SBAR();
  MG(av3[0], cat2(wl[3], wh[3]), bq[15][0]); SBAR();
  MG(av3[1], cat2(wl[3], wh[3]), bq[15][1]); SBAR();
  MG(av3[2], cat2(wl[3], wh[3]), bq[15][2]); SBAR();
  MG(av3[3], cat2(wl[3], wh[3]), bq[15][3]); SBAR();
  asm volatile("s_nop 7\n\ts_nop 3" ::: "memory");
  pk[0][0] = cvtpk(av2[0][0], av2[0][1]); PIN(pk[0][0]);
  pk[0][1] = cvtpk(av2[0][2], av2[0][3]); PIN(pk[0][1]);
  pk[0][2] = cvtpk(av2[1][0], av2[1][1]); PIN(pk[0][2]);
  pk[0][3] = cvtpk(av2[1][2], av2[1][3]); PIN(pk[0][3]);
  st16o<2048>(vo1, lo0, (u32x4){pk[0][0], pk[0][1], pk[0][2], pk[0][3]});
  pk[1][0] = cvtpk(av2[2][0], av2[2][1]); PIN(pk[1][0]);
  pk[1][1] = cvtpk(av2[2][2], av2[2][3]); PIN(pk[1][1]);
  pk[1][2] = cvtpk(av2[3][0], av2[3][1]); PIN(pk[1][2]);
  pk[1][3] = cvtpk(av2[3][2], av2[3][3]); PIN(pk[1][3]);
  st16o<2048>(vo1, lo2, (u32x4){pk[1][0], pk[1][1], pk[1][2], pk[1][3]});
  pk[0][0] = cvtpk(av3[0][0], av3[0][1]); PIN(pk[0][0]);
  pk[0][1] = cvtpk(av3[0][2], av3[0][3]); PIN(pk[0][1]);
  pk[0][2] = cvtpk(av3[1][0], av3[1][1]); PIN(pk[0][2]);
  pk[0][3] = cvtpk(av3[1][2], av3[1][3]); PIN(pk[0][3]);
  st16o<3072>(vo1, lo0, (u32x4){pk[0][0], pk[0][1], pk[0][2], pk[0][3]});
  pk[1][0] = cvtpk(av3[2][0], av3[2][1]); PIN(pk[1][0]);
  pk[1][1] = cvtpk(av3[2][2], av3[2][3]); PIN(pk[1][1]);
  pk[1][2] = cvtpk(av3[3][0], av3[3][1]); PIN(pk[1][2]);
  pk[1][3] = cvtpk(av3[3][2], av3[3][3]); PIN(pk[1][3]);
  st16o<3072>(vo1, lo2, (u32x4){pk[1][0], pk[1][1], pk[1][2], pk[1][3]});
  asm volatile("s_waitcnt vmcnt(56)" : "+v"(tw0), "+v"(tw1) :: "memory");
}

constexpr int T_SLOT = 18432, T_F = 81920, T_RED = T_F + 4 * T_SLOT, T_B1 = T_RED + 256, T_LDS = T_B1 + 2304;
template <int OFF> DI void gld_sc1(bf16x8& d, const void* sbase, unsigned voff) {
  asm volatile("global_load_dwordx4 %0, %1, %2 offset:%3 sc1" : "=v"(d) : "v"(voff), "s"(sbase), "i"(OFF) : "memory");
}
DI void gld2x8(bf16x8& d, const void* sbase, unsigned voff) {
  u32x2 lo, hi;
  asm volatile("global_load_dwordx2 %0, %2, %4 sc1\n\tglobal_load_dwordx2 %1, %3, %4 sc1" : "=&v"(lo), "=&v"(hi) : "v"(voff), "v"(voff + 2048u), "s"(sbase) : "memory");
  d = __builtin_bit_cast(bf16x8, (u32x4){lo[0], lo[1], hi[0], hi[1]});
}
DI void poll_issue(unsigned& v, const void* cnt, unsigned off) { asm volatile("global_load_dword %0, %1, %2 sc1" : "=v"(v) : "v"(off), "s"(cnt) : "memory"); }
DI void spin_until(const void* cnt, unsigned off) {
#pragma unroll 1
  for (unsigned n = 0; n < (1u << 22); ++n) {
    unsigned v;
    asm volatile("global_load_dword %0, %1, %2 sc1\n\ts_waitcnt vmcnt(0)" : "=v"(v) : "v"(off), "s"(cnt) : "memory");
    if (__builtin_amdgcn_ballot_w64(v == 0u) == 0ull) break;
    __builtin_amdgcn_s_sleep(1);
  }
}
DI void spin4(const void* cnt, unsigned off) {
#pragma unroll 1
  for (unsigned n = 0; n < (1u << 22); ++n) {
    u32x4 v;
    asm volatile("global_load_dwordx4 %0, %1, %2 sc1\n\ts_waitcnt vmcnt(0)" : "=v"(v) : "v"(off), "s"(cnt) : "memory");
    if (__builtin_amdgcn_ballot_w64(min(min(v[0], v[1]), min(v[2], v[3])) == 0u) == 0ull) break;
    __builtin_amdgcn_s_sleep(8);
  }
}
DI unsigned relu2(unsigned a) { unsigned r; asm volatile("v_pk_max_i16 %0, %1, 0" : "=v"(r) : "v"(a)); return r; }
DI float x8_sum(float v) {
  v += __uint_as_float(__builtin_amdgcn_update_dpp(0u, __float_as_uint(v), 0x128, 0xf, 0xf, false));
  return xq_sum(v);
}

constexpr int K2_LDS = 163840;

DI void slow_tile(const u16* wq, char* ws, int h, int tb16, int lane, char* cb) {
  const int r16 = lane & 15, q = lane >> 4;
  f32x4 o[8];
#pragma unroll
  for (int i = 0; i < 8; ++i) o[i] = (f32x4){};
#pragma unroll 1
  for (int ks = 0; ks < 18; ++ks) {
    const bf16x8 xf = *(const bf16x8*)(ws + WS_XN + ((size_t)tb16 * 18 + ks) * 1024 + lane * 16);
    const u16* wp = wq + (size_t)(ks * 32 + q * 8) * NHD + h * HD + r16;
#pragma unroll
    for (int dt = 0; dt < 8; ++dt) {
      s16x8 w;
#pragma unroll
      for (int j = 0; j < 8; ++j) w[j] = (short)wp[j * NHD + dt * 16];
      o[dt] = MFMA(__builtin_bit_cast(bf16x8, w), xf, o[dt]);
    }
  }
  bf16x8 qf[4];
#pragma unroll
  for (int i = 0; i < 4; ++i) { qf[i] = __builtin_bit_cast(bf16x8, pack2(o[2 * i], o[2 * i + 1])); o[2 * i] = (f32x4){}; o[2 * i + 1] = (f32x4){}; }
  float m = -__builtin_inff(), l = 0.f;
  const char* kb = ws + WS_K + (size_t)h * 64 * 8192 + lane * 16;
#pragma unroll 1
  for (int u = 0; u <= (tb16 >> 1); ++u) {
    f32x4 sc[2] = {};
#pragma unroll
    for (int j = 0; j < 8; ++j) sc[j >> 2] = MFMA(*(const bf16x8*)(kb + (size_t)u * 8192 + j * 1024), qf[j & 3], sc[j >> 2]);
    float mx = m;
#pragma unroll
    for (int j = 0; j < 8; ++j) {
      if (u * 32 + (j >> 2) * 16 + q * 4 + (j & 3) > tb16 * 16 + r16) sc[j >> 2][j & 3] = -__builtin_inff();
      mx = fmaxf(mx, sc[j >> 2][j & 3]);
    }
    mx = xq_max(mx);
    const float al = __builtin_amdgcn_exp2f(m - mx);
    m = mx; l *= al;
#pragma unroll
    for (int j = 0; j < 8; ++j) { sc[j >> 2][j & 3] = __builtin_amdgcn_exp2f(sc[j >> 2][j & 3] - mx); l += sc[j >> 2][j & 3]; }
    const bf16x8 pf = __builtin_bit_cast(bf16x8, pack2(sc[0], sc[1]));
#pragma unroll
    for (int dt = 0; dt < 8; ++dt) o[dt] = MFMA(*(const bf16x8*)(kb + (WS_V - WS_K) + (size_t)u * 8192 + dt * 1024), pf, o[dt] * al);
  }
  const float inv = 1.0f / xq_sum(l);
#pragma unroll
  for (int i = 0; i < 4; ++i) st16(cb + i * 1024, pack2s(o[2 * i], o[2 * i + 1], inv));
}

__global__ __launch_bounds__(256, 1) __attribute__((aligned(4096))) void k2_rest(const u16* __restrict__ wq, char* ws, const u16* x_, const u16* b1_, const u16* b2_, u16* out_) {
  extern __shared__ __attribute__((aligned(16))) char smem[];
  const int tid = threadIdx.x; int lane = tid & 63, wid = (int)rfl(tid >> 6), bid = blockIdx.x;
  const unsigned ldsb = (unsigned)(size_t)smem, lane16 = (unsigned)lane * 16u;
  unsigned tw = 0;
  { unsigned long long pc_; unsigned len_;
    asm volatile("s_getpc_b64 %0\n.Lk2_here:\n\ts_mov_b32 %1, .Lk2_end-.Lk2_here" : "=s"(pc_), "=s"(len_));
    unsigned o0_ = (unsigned)(((bid >> 3) & 15) * 32 + (tid >> 3)) * 128u; o0_ = o0_ < len_ ? o0_ : 0u;
    asm volatile("global_load_dword %0, %1, %2" : "+v"(tw) : "v"(o0_), "s"(pc_) : "memory"); }
  bf16x8 xf[18][4];
  const unsigned wqe = (unsigned)((lane >> 2) & 7) * 8192u + (unsigned)(lane >> 5) * 64u + 4096u;
  const unsigned wqv0 = wqe + (unsigned)(lane & 3) * 16u, wqv1 = wqv0 + 128u - 1024u;
  const unsigned wqv2 = wqe + (unsigned)((lane & 3) ^ 2) * 16u + 65536u - 2048u, wqv3 = wqv2 + 128u - 1024u;
  const unsigned xv0 = lane16, xv1 = lane16 + 36864u, xv2 = lane16 + 73728u, xv3 = lane16 + 110592u;
  const unsigned wdq = ldsb + (unsigned)wid * 4096u;
  unsigned of0 = (unsigned)wid * 131072u + (unsigned)((bid & 7) * 4 + ((bid >> 3) & 3)) * 256u;
  unsigned of1 = (unsigned)WS_XN + (unsigned)(((wid & 2) ? 15 - (bid >> 5) : bid >> 5) * 8 + (wid & 1)) * 18432u;
  PIN(wqv0); PIN(wqv1); PIN(wqv2); PIN(wqv3); PIN(xv1); PIN(xv2); PIN(xv3); PINS(of0); PINS(of1);
  const char* wq_t = (const char*)wq + of0 - 4096;
  const char* xb0 = ws + of1;
  set_m0(wdq + 0u);
  glds_m<0>(wq_t + 0, wqv0);
  glds_m<1024>(wq_t + 0, wqv1);
  glds_m<2048>(wq_t + 0, wqv2);
  glds_m<3072>(wq_t + 0, wqv3);
  GLDA(xf[0][0], xb0, xv0, 0);
  GLDA(xf[0][1], xb0, xv1, 0);
  GLDA(xf[0][2], xb0, xv2, 0);
  GLDA(xf[0][3], xb0, xv3, 0);
  set_m0(wdq + 16384u);
  glds_m<0>(wq_t + 524288, wqv0);
  glds_m<1024>(wq_t + 524288, wqv1);
  glds_m<2048>(wq_t + 524288, wqv2);
  glds_m<3072>(wq_t + 524288, wqv3);
  GLDA(xf[1][0], xb0, xv0, 1024);
  GLDA(xf[1][1], xb0, xv1, 1024);
  GLDA(xf[1][2], xb0, xv2, 1024);
  GLDA(xf[1][3], xb0, xv3, 1024);
  GLDA(xf[2][0], xb0, xv0, 2048);
  GLDA(xf[2][1], xb0, xv1, 2048);
  GLDA(xf[2][2], xb0, xv2, 2048);
  GLDA(xf[2][3], xb0, xv3, 2048);
  set_m0(wdq + 32768u);
  glds_m<0>(wq_t + 1048576, wqv0);
  glds_m<1024>(wq_t + 1048576, wqv1);
  glds_m<2048>(wq_t + 1048576, wqv2);
  glds_m<3072>(wq_t + 1048576, wqv3);
  GLDA(xf[3][0], xb0, xv0, 3072);
  GLDA(xf[3][1], xb0, xv1, 3072);
  GLDA(xf[3][2], xb0, xv2, 3072);
  GLDA(xf[3][3], xb0, xv3, 3072);
  asm volatile("" : "+v"(lane)); asm volatile("" : "+s"(wid), "+s"(bid), "+s"(ws));
  const int q = lane >> 4, g = wid >> 1, r = wid & 1;
  const int h = (bid & 7) * 4 + ((bid >> 3) & 3), p = bid >> 5;
  lds_cp lds = (lds_cp)smem;
  const int blk = g ? 15 - p : p;
  const int nA = g ? 100 : 15 - 2 * p;
  const int kt0 = g ? 15 - 2 * p : 0;
  const char* Kh = ws + WS_K + (size_t)h * 32 * 16384 + r * 4096;
  const unsigned lanev = lane16 + (unsigned)(WS_V - WS_K);
  const unsigned greg = (unsigned)g * 16384u, wdst0 = ldsb + greg + (unsigned)r * 4096u;
#define KT(i) (((i) < nA) ? (kt0 + (i)) : ((i) - nA))
#define KSRC(i) (Kh + (size_t)KT(i) * 16384)
  bf16x8 cq[16]; u32x4 rdesc;
  {
    lds_cp wrb = lds + (unsigned)q * 2048u + (unsigned)((lane >> 2) & 3) * 64u + (unsigned)(lane & 3) * 8u;
    lds_cp wr0[3], wr1[3];
#pragma unroll
    for (int i = 0; i < 3; ++i) { wr0[i] = opq(wrb + (unsigned)(q & 1) * 32u + i * 65536); wr1[i] = opq(wrb + (unsigned)((q & 1) ^ 1) * 32u + i * 65536); }
    f32x4 qa[8][4]; s16x4 wl[6], wh[6]; unsigned qp[64]; const char* dp;
    const char *xb1 = ws + (of1 + 4096u), *xb2 = ws + (of1 + 8192u), *xb3 = ws + (of1 + 12288u), *xb4 = ws + (of1 + 16384u), *ks0 = KSRC(0), *ks1 = KSRC(1);
    PINS(xb1); PINS(xb2); PINS(xb3); PINS(xb4); PINS(ks0); PINS(ks1);
    unsigned long long gm = 0ull; unsigned qx = ldsb + 8192u + (unsigned)r * 32768u + lane16; PINS(gm); gm -= (unsigned long long)g; PINS(gm); PIN(qx);
#define QW(off, ...) asm volatile("s_mov_b64 exec, %3\n\tds_write_b128 %0, %1 offset:%2\n\ts_mov_b64 exec, -1" :: "v"(qx), "v"(__VA_ARGS__), "i"(off), "s"(gm) : "memory")
#define RD_LOAD() asm volatile("global_load_dwordx4 %0, %1, %2" : "=v"(rdesc) : "v"((unsigned)(bid * 64 + wid * 16 + (lane & 15)) * 16u), "s"(ws + WS_RD) : "memory")
  RD_LOAD();
  asm volatile("s_mov_b64 exec, 0\n\ts_cmp_eq_u32 %0, 0\n\ts_cbranch_scc1 .Lk2_p0\n\ts_cmp_eq_u32 %0, 1\n\ts_cbranch_scc1 .Lk2_p1\n\ts_cmp_eq_u32 %0, 2\n\ts_cbranch_scc1 .Lk2_p2\n\ts_cmp_eq_u32 %0, 3\n\ts_cbranch_scc1 .Lk2_p3\n\t.Lk2_pr:\n\ts_mov_b64 exec, -1" :: "s"(wid) : "scc", "memory");
  asm volatile("s_waitcnt vmcnt(25)\n\ts_barrier" : "+v"(tw) :: "memory");
  wl[0] = lds_tr(wr0[0] + 0);
  wh[0] = lds_tr(wr0[0] + 256);
  wl[1] = lds_tr(wr1[0] + 0);
  wh[1] = lds_tr(wr1[0] + 256);
  wl[2] = lds_tr(wr0[0] + 512);
  wh[2] = lds_tr(wr0[0] + 768);
  wl[3] = lds_tr(wr1[0] + 512);
  wh[3] = lds_tr(wr1[0] + 768);
  asm volatile("s_waitcnt vmcnt(21)" :  :: "memory");
  MF0(qa[0][0], cat2(wl[0], wh[0]), xf[0][0]); SBAR();
  wl[4] = lds_tr(wr0[0] + 1024);
  MF0(qa[0][1], cat2(wl[0], wh[0]), xf[0][1]); SBAR();
  wh[4] = lds_tr(wr0[0] + 1280);
  MF0(qa[0][2], cat2(wl[0], wh[0]), xf[0][2]); SBAR();
  dp = wq_t + 1572864; PINS(dp);
  MF0(qa[0][3], cat2(wl[0], wh[0]), xf[0][3]); SBAR();
  M0ADD(wdq, 49152);
  MF0(qa[1][0], cat2(wl[1], wh[1]), xf[0][0]); SBAR();
  wl[5] = lds_tr(wr1[0] + 1024);
  MF0(qa[1][1], cat2(wl[1], wh[1]), xf[0][1]); SBAR();
  wh[5] = lds_tr(wr1[0] + 1280);
  MF0(qa[1][2], cat2(wl[1], wh[1]), xf[0][2]); SBAR();
  glds_m<0>(dp, wqv0);
  MF0(qa[1][3], cat2(wl[1], wh[1]), xf[0][3]); SBAR();
  MF0(qa[2][0], cat2(wl[2], wh[2]), xf[0][0]); SBAR();
  wl[0] = lds_tr(wr0[0] + 1536);
  MF0(qa[2][1], cat2(wl[2], wh[2]), xf[0][1]); SBAR();
  wh[0] = lds_tr(wr0[0] + 1792);
  MF0(qa[2][2], cat2(wl[2], wh[2]), xf[0][2]); SBAR();
  glds_m<1024>(dp, wqv1);
  MF0(qa[2][3], cat2(wl[2], wh[2]), xf[0][3]); SBAR();
  MF0(qa[3][0], cat2(wl[3], wh[3]), xf[0][0]); SBAR();
  wl[1] = lds_tr(wr1[0] + 1536);
  MF0(qa[3][1], cat2(wl[3], wh[3]), xf[0][1]); SBAR();
  wh[1] = lds_tr(wr1[0] + 1792);
  MF0(qa[3][2], cat2(wl[3], wh[3]), xf[0][2]); SBAR();
  glds_m<2048>(dp, wqv2);
  MF0(qa[3][3], cat2(wl[3], wh[3]), xf[0][3]); SBAR();
  MF0(qa[4][0], cat2(wl[4], wh[4]), xf[0][0]); SBAR();
  wl[2] = lds_tr(wr0[0] + 8192);
  MF0(qa[4][1], cat2(wl[4], wh[4]), xf[0][1]); SBAR();
  wh[2] = lds_tr(wr0[0] + 8448);
  MF0(qa[4][2], cat2(wl[4], wh[4]), xf[0][2]); SBAR();
  glds_m<3072>(dp, wqv3);
  MF0(qa[4][3], cat2(wl[4], wh[4]), xf[0][3]); SBAR();
  MF0(qa[5][0], cat2(wl[5], wh[5]), xf[0][0]); SBAR();
  wl[3] = lds_tr(wr1[0] + 8192);
  MF0(qa[5][1], cat2(wl[5], wh[5]), xf[0][1]); SBAR();
  wh[3] = lds_tr(wr1[0] + 8448);
  MF0(qa[5][2], cat2(wl[5], wh[5]), xf[0][2]); SBAR();
  GLDA(xf[4][0], xb1, xv0, 0);
  MF0(qa[5][3], cat2(wl[5], wh[5]), xf[0][3]); SBAR();
  MF0(qa[6][0], cat2(wl[0], wh[0]), xf[0][0]); SBAR();
  wl[4] = lds_tr(wr0[0] + 8704);
  MF0(qa[6][1], cat2(wl[0], wh[0]), xf[0][1]); SBAR();
  wh[4] = lds_tr(wr0[0] + 8960);
  MF0(qa[6][2], cat2(wl[0], wh[0]), xf[0][2]); SBAR();
  GLDA(xf[4][1], xb1, xv1, 0);
  MF0(qa[6][3], cat2(wl[0], wh[0]), xf[0][3]); SBAR();
  MF0(qa[7][0], cat2(wl[1], wh[1]), xf[0][0]); SBAR();
  wl[5] = lds_tr(wr1[0] + 8704);
  MF0(qa[7][1], cat2(wl[1], wh[1]), xf[0][1]); SBAR();
  wh[5] = lds_tr(wr1[0] + 8960);
  MF0(qa[7][2], cat2(wl[1], wh[1]), xf[0][2]); SBAR();
  asm volatile("s_waitcnt vmcnt(23)\n\ts_barrier" :  :: "memory");
  MF0(qa[7][3], cat2(wl[1], wh[1]), xf[0][3]); SBAR();
  asm volatile("s_waitcnt vmcnt(19)" :  :: "memory");
  MF(qa[0][0], cat2(wl[2], wh[2]), xf[1][0]); SBAR();
  wl[0] = lds_tr(wr0[0] + 9216);
  MF(qa[0][1], cat2(wl[2], wh[2]), xf[1][1]); SBAR();
  wh[0] = lds_tr(wr0[0] + 9472);
  MF(qa[0][2], cat2(wl[2], wh[2]), xf[1][2]); SBAR();
  GLDA(xf[4][2], xb1, xv2, 0);
  MF(qa[0][3], cat2(wl[2], wh[2]), xf[1][3]); SBAR();
  MF(qa[1][0], cat2(wl[3], wh[3]), xf[1][0]); SBAR();
  wl[1] = lds_tr(wr1[0] + 9216);
  MF(qa[1][1], cat2(wl[3], wh[3]), xf[1][1]); SBAR();
  wh[1] = lds_tr(wr1[0] + 9472);
  MF(qa[1][2], cat2(wl[3], wh[3]), xf[1][2]); SBAR();
  GLDA(xf[4][3], xb1, xv3, 0);
  MF(qa[1][3], cat2(wl[3], wh[3]), xf[1][3]); SBAR();
  MF(qa[2][0], cat2(wl[4], wh[4]), xf[1][0]); SBAR();
  wl[2] = lds_tr(wr0[0] + 9728);
  MF(qa[2][1], cat2(wl[4], wh[4]), xf[1][1]); SBAR();
  wh[2] = lds_tr(wr0[0] + 9984);
  MF(qa[2][2], cat2(wl[4], wh[4]), xf[1][2]); SBAR();
  GLDA(xf[5][0], xb1, xv0, 1024);
  MF(qa[2][3], cat2(wl[4], wh[4]), xf[1][3]); SBAR();
  MF(qa[3][0], cat2(wl[5], wh[5]), xf[1][0]); SBAR();
  wl[3] = lds_tr(wr1[0] + 9728);
  MF(qa[3][1], cat2(wl[5], wh[5]), xf[1][1]); SBAR();
  wh[3] = lds_tr(wr1[0] + 9984);
  MF(qa[3][2], cat2(wl[5], wh[5]), xf[1][2]); SBAR();
  GLDA(xf[5][1], xb1, xv1, 1024);
  MF(qa[3][3], cat2(wl[5], wh[5]), xf[1][3]); SBAR();
  MF(qa[4][0], cat2(wl[0], wh[0]), xf[1][0]); SBAR();
  wl[4] = lds_tr(wr0[0] + 16384);
  MF(qa[4][1], cat2(wl[0], wh[0]), xf[1][1]); SBAR();
  wh[4] = lds_tr(wr0[0] + 16640);
  MF(qa[4][2], cat2(wl[0], wh[0]), xf[1][2]); SBAR();
  GLDA(xf[5][2], xb1, xv2, 1024);
  MF(qa[4][3], cat2(wl[0], wh[0]), xf[1][3]); SBAR();
  MF(qa[5][0], cat2(wl[1], wh[1]), xf[1][0]); SBAR();
  wl[5] = lds_tr(wr1[0] + 16384);
  MF(qa[5][1], cat2(wl[1], wh[1]), xf[1][1]); SBAR();
  wh[5] = lds_tr(wr1[0] + 16640);
  MF(qa[5][2], cat2(wl[1], wh[1]), xf[1][2]); SBAR();
  GLDA(xf[5][3], xb1, xv3, 1024);
  MF(qa[5][3], cat2(wl[1], wh[1]), xf[1][3]); SBAR();
  MF(qa[6][0], cat2(wl[2], wh[2]), xf[1][0]); SBAR();
  wl[0] = lds_tr(wr0[0] + 16896);
  MF(qa[6][1], cat2(wl[2], wh[2]), xf[1][1]); SBAR();
  wh[0] = lds_tr(wr0[0] + 17152);
  MF(qa[6][2], cat2(wl[2], wh[2]), xf[1][2]); SBAR();
  dp = wq_t + 2097152; PINS(dp);
  MF(qa[6][3], cat2(wl[2], wh[2]), xf[1][3]); SBAR();
  M0ADD(wdq, 65536);
  MF(qa[7][0], cat2(wl[3], wh[3]), xf[1][0]); SBAR();
  wl[1] = lds_tr(wr1[0] + 16896);
  MF(qa[7][1], cat2(wl[3], wh[3]), xf[1][1]); SBAR();
  wh[1] = lds_tr(wr1[0] + 17152);
  MF(qa[7][2], cat2(wl[3], wh[3]), xf[1][2]); SBAR();
  glds_m<0>(dp, wqv0);
  MF(qa[7][3], cat2(wl[3], wh[3]), xf[1][3]); SBAR();
  asm volatile("s_waitcnt vmcnt(22)" :  :: "memory");
  MF(qa[0][0], cat2(wl[4], wh[4]), xf[2][0]); SBAR();
  wl[2] = lds_tr(wr0[0] + 17408);
  MF(qa[0][1], cat2(wl[4], wh[4]), xf[2][1]); SBAR();
  wh[2] = lds_tr(wr0[0] + 17664);
  MF(qa[0][2], cat2(wl[4], wh[4]), xf[2][2]); SBAR();
  glds_m<1024>(dp, wqv1);
  MF(qa[0][3], cat2(wl[4], wh[4]), xf[2][3]); SBAR();
  MF(qa[1][0], cat2(wl[5], wh[5]), xf[2][0]); SBAR();
  wl[3] = lds_tr(wr1[0] + 17408);
  MF(qa[1][1], cat2(wl[5], wh[5]), xf[2][1]); SBAR();
  wh[3] = lds_tr(wr1[0] + 17664);
  MF(qa[1][2], cat2(wl[5], wh[5]), xf[2][2]); SBAR();
  glds_m<2048>(dp, wqv2);
  MF(qa[1][3], cat2(wl[5], wh[5]), xf[2][3]); SBAR();
  MF(qa[2][0], cat2(wl[0], wh[0]), xf[2][0]); SBAR();
  wl[4] = lds_tr(wr0[0] + 17920);
  MF(qa[2][1], cat2(wl[0], wh[0]), xf[2][1]); SBAR();
  wh[4] = lds_tr(wr0[0] + 18176);
  MF(qa[2][2], cat2(wl[0], wh[0]), xf[2][2]); SBAR();
  glds_m<3072>(dp, wqv3);
  MF(qa[2][3], cat2(wl[0], wh[0]), xf[2][3]); SBAR();
  MF(qa[3][0], cat2(wl[1], wh[1]), xf[2][0]); SBAR();
  wl[5] = lds_tr(wr1[0] + 17920);
  MF(qa[3][1], cat2(wl[1], wh[1]), xf[2][1]); SBAR();
  wh[5] = lds_tr(wr1[0] + 18176);
  MF(qa[3][2], cat2(wl[1], wh[1]), xf[2][2]); SBAR();
  GLDA(xf[6][0], xb1, xv0, 2048);
  MF(qa[3][3], cat2(wl[1], wh[1]), xf[2][3]); SBAR();
  MF(qa[4][0], cat2(wl[2], wh[2]), xf[2][0]); SBAR();
  wl[0] = lds_tr(wr0[0] + 24576);
  MF(qa[4][1], cat2(wl[2], wh[2]), xf[2][1]); SBAR();
  wh[0] = lds_tr(wr0[0] + 24832);
  MF(qa[4][2], cat2(wl[2], wh[2]), xf[2][2]); SBAR();
  GLDA(xf[6][1], xb1, xv1, 2048);
  MF(qa[4][3], cat2(wl[2], wh[2]), xf[2][3]); SBAR();
  MF(qa[5][0], cat2(wl[3], wh[3]), xf[2][0]); SBAR();
  wl[1] = lds_tr(wr1[0] + 24576);
  MF(qa[5][1], cat2(wl[3], wh[3]), xf[2][1]); SBAR();
  wh[1] = lds_tr(wr1[0] + 24832);
  MF(qa[5][2], cat2(wl[3], wh[3]), xf[2][2]); SBAR();
  GLDA(xf[6][2], xb1, xv2, 2048);
  MF(qa[5][3], cat2(wl[3], wh[3]), xf[2][3]); SBAR();
  MF(qa[6][0], cat2(wl[4], wh[4]), xf[2][0]); SBAR();
  wl[2] = lds_tr(wr0[0] + 25088);
  MF(qa[6][1], cat2(wl[4], wh[4]), xf[2][1]); SBAR();
  wh[2] = lds_tr(wr0[0] + 25344);
  MF(qa[6][2], cat2(wl[4], wh[4]), xf[2][2]); SBAR();
  GLDA(xf[6][3], xb1, xv3, 2048);
  MF(qa[6][3], cat2(wl[4], wh[4]), xf[2][3]); SBAR();
  MF(qa[7][0], cat2(wl[5], wh[5]), xf[2][0]); SBAR();
  wl[3] = lds_tr(wr1[0] + 25088);
  MF(qa[7][1], cat2(wl[5], wh[5]), xf[2][1]); SBAR();
  wh[3] = lds_tr(wr1[0] + 25344);
  MF(qa[7][2], cat2(wl[5], wh[5]), xf[2][2]); SBAR();
  asm volatile("s_waitcnt vmcnt(25)\n\ts_barrier" :  :: "memory");
  MF(qa[7][3], cat2(wl[5], wh[5]), xf[2][3]); SBAR();
  asm volatile("s_waitcnt vmcnt(21)" :  :: "memory");
  MF(qa[0][0], cat2(wl[0], wh[0]), xf[3][0]); SBAR();
  wl[4] = lds_tr(wr0[0] + 25600);
  MF(qa[0][1], cat2(wl[0], wh[0]), xf[3][1]); SBAR();
  wh[4] = lds_tr(wr0[0] + 25856);
  MF(qa[0][2], cat2(wl[0], wh[0]), xf[3][2]); SBAR();
  GLDA(xf[7][0], xb1, xv0, 3072);
  MF(qa[0][3], cat2(wl[0], wh[0]), xf[3][3]); SBAR();
  MF(qa[1][0], cat2(wl[1], wh[1]), xf[3][0]); SBAR();
  wl[5] = lds_tr(wr1[0] + 25600);
  MF(qa[1][1], cat2(wl[1], wh[1]), xf[3][1]); SBAR();
  wh[5] = lds_tr(wr1[0] + 25856);
  MF(qa[1][2], cat2(wl[1], wh[1]), xf[3][2]); SBAR();
  GLDA(xf[7][1], xb1, xv1, 3072);
  MF(qa[1][3], cat2(wl[1], wh[1]), xf[3][3]); SBAR();
  MF(qa[2][0], cat2(wl[2], wh[2]), xf[3][0]); SBAR();
  wl[0] = lds_tr(wr0[0] + 26112);
  MF(qa[2][1], cat2(wl[2], wh[2]), xf[3][1]); SBAR();
  wh[0] = lds_tr(wr0[0] + 26368);
  MF(qa[2][2], cat2(wl[2], wh[2]), xf[3][2]); SBAR();
  GLDA(xf[7][2], xb1, xv2, 3072);
  MF(qa[2][3], cat2(wl[2], wh[2]), xf[3][3]); SBAR();
  MF(qa[3][0], cat2(wl[3], wh[3]), xf[3][0]); SBAR();
  wl[1] = lds_tr(wr1[0] + 26112);
  MF(qa[3][1], cat2(wl[3], wh[3]), xf[3][1]); SBAR();
  wh[1] = lds_tr(wr1[0] + 26368);
  MF(qa[3][2], cat2(wl[3], wh[3]), xf[3][2]); SBAR();
  GLDA(xf[7][3], xb1, xv3, 3072);
  MF(qa[3][3], cat2(wl[3], wh[3]), xf[3][3]); SBAR();
  MF(qa[4][0], cat2(wl[4], wh[4]), xf[3][0]); SBAR();
  wl[2] = lds_tr(wr0[0] + 32768);
  MF(qa[4][1], cat2(wl[4], wh[4]), xf[3][1]); SBAR();
  wh[2] = lds_tr(wr0[0] + 33024);
  MF(qa[4][2], cat2(wl[4], wh[4]), xf[3][2]); SBAR();
  dp = wq_t + 2621440; PINS(dp);
  MF(qa[4][3], cat2(wl[4], wh[4]), xf[3][3]); SBAR();
  M0ADD(wdq, 81920);
  MF(qa[5][0], cat2(wl[5], wh[5]), xf[3][0]); SBAR();
  wl[3] = lds_tr(wr1[0] + 32768);
  MF(qa[5][1], cat2(wl[5], wh[5]), xf[3][1]); SBAR();
  wh[3] = lds_tr(wr1[0] + 33024);
  MF(qa[5][2], cat2(wl[5], wh[5]), xf[3][2]); SBAR();
  glds_m<0>(dp, wqv0);
  MF(qa[5][3], cat2(wl[5], wh[5]), xf[3][3]); SBAR();
  MF(qa[6][0], cat2(wl[0], wh[0]), xf[3][0]); SBAR();
  wl[4] = lds_tr(wr0[0] + 33280);
  MF(qa[6][1], cat2(wl[0], wh[0]), xf[3][1]); SBAR();
  wh[4] = lds_tr(wr0[0] + 33536);
  MF(qa[6][2], cat2(wl[0], wh[0]), xf[3][2]); SBAR();
  glds_m<1024>(dp, wqv1);
  MF(qa[6][3], cat2(wl[0], wh[0]), xf[3][3]); SBAR();
  MF(qa[7][0], cat2(wl[1], wh[1]), xf[3][0]); SBAR();
  wl[5] = lds_tr(wr1[0] + 33280);
  MF(qa[7][1], cat2(wl[1], wh[1]), xf[3][1]); SBAR();
  wh[5] = lds_tr(wr1[0] + 33536);
  MF(qa[7][2], cat2(wl[1], wh[1]), xf[3][2]); SBAR();
  glds_m<2048>(dp, wqv2);
  MF(qa[7][3], cat2(wl[1], wh[1]), xf[3][3]); SBAR();
  asm volatile("s_waitcnt vmcnt(19)" :  :: "memory");
  MF(qa[0][0], cat2(wl[2], wh[2]), xf[4][0]); SBAR();
  wl[0] = lds_tr(wr0[0] + 33792);
  MF(qa[0][1], cat2(wl[2], wh[2]), xf[4][1]); SBAR();
  wh[0] = lds_tr(wr0[0] + 34048);
  MF(qa[0][2], cat2(wl[2], wh[2]), xf[4][2]); SBAR();
  glds_m<3072>(dp, wqv3);
  MF(qa[0][3], cat2(wl[2], wh[2]), xf[4][3]); SBAR();
  MF(qa[1][0], cat2(wl[3], wh[3]), xf[4][0]); SBAR();
  wl[1] = lds_tr(wr1[0] + 33792);
  MF(qa[1][1], cat2(wl[3], wh[3]), xf[4][1]); SBAR();
  wh[1] = lds_tr(wr1[0] + 34048);
  MF(qa[1][2], cat2(wl[3], wh[3]), xf[4][2]); SBAR();
  GLDA(xf[8][0], xb2, xv0, 0);
  MF(qa[1][3], cat2(wl[3], wh[3]), xf[4][3]); SBAR();
  MF(qa[2][0], cat2(wl[4], wh[4]), xf[4][0]); SBAR();
  wl[2] = lds_tr(wr0[0] + 34304);
  MF(qa[2][1], cat2(wl[4], wh[4]), xf[4][1]); SBAR();
  wh[2] = lds_tr(wr0[0] + 34560);
  MF(qa[2][2], cat2(wl[4], wh[4]), xf[4][2]); SBAR();
  GLDA(xf[8][1], xb2, xv1, 0);
  MF(qa[2][3], cat2(wl[4], wh[4]), xf[4][3]); SBAR();
  MF(qa[3][0], cat2(wl[5], wh[5]), xf[4][0]); SBAR();
  wl[3] = lds_tr(wr1[0] + 34304);
  MF(qa[3][1], cat2(wl[5], wh[5]), xf[4][1]); SBAR();
  wh[3] = lds_tr(wr1[0] + 34560);
  MF(qa[3][2], cat2(wl[5], wh[5]), xf[4][2]); SBAR();
  GLDA(xf[8][2], xb2, xv2, 0);
  MF(qa[3][3], cat2(wl[5], wh[5]), xf[4][3]); SBAR();
  MF(qa[4][0], cat2(wl[0], wh[0]), xf[4][0]); SBAR();
  wl[4] = lds_tr(wr0[0] + 40960);
  MF(qa[4][1], cat2(wl[0], wh[0]), xf[4][1]); SBAR();
  wh[4] = lds_tr(wr0[0] + 41216);
  MF(qa[4][2], cat2(wl[0], wh[0]), xf[4][2]); SBAR();
  GLDA(xf[8][3], xb2, xv3, 0);
  MF(qa[4][3], cat2(wl[0], wh[0]), xf[4][3]); SBAR();
  MF(qa[5][0], cat2(wl[1], wh[1]), xf[4][0]); SBAR();
  wl[5] = lds_tr(wr1[0] + 40960);
  MF(qa[5][1], cat2(wl[1], wh[1]), xf[4][1]); SBAR();
  wh[5] = lds_tr(wr1[0] + 41216);
  MF(qa[5][2], cat2(wl[1], wh[1]), xf[4][2]); SBAR();
  GLDA(xf[9][0], xb2, xv0, 1024);
  MF(qa[5][3], cat2(wl[1], wh[1]), xf[4][3]); SBAR();
  MF(qa[6][0], cat2(wl[2], wh[2]), xf[4][0]); SBAR();
  wl[0] = lds_tr(wr0[0] + 41472);
  MF(qa[6][1], cat2(wl[2], wh[2]), xf[4][1]); SBAR();
  wh[0] = lds_tr(wr0[0] + 41728);
  MF(qa[6][2], cat2(wl[2], wh[2]), xf[4][2]); SBAR();
  GLDA(xf[9][1], xb2, xv1, 1024);
  MF(qa[6][3], cat2(wl[2], wh[2]), xf[4][3]); SBAR();
  MF(qa[7][0], cat2(wl[3], wh[3]), xf[4][0]); SBAR();
  wl[1] = lds_tr(wr1[0] + 41472);
  MF(qa[7][1], cat2(wl[3], wh[3]), xf[4][1]); SBAR();
  wh[1] = lds_tr(wr1[0] + 41728);
  MF(qa[7][2], cat2(wl[3], wh[3]), xf[4][2]); SBAR();
  asm volatile("s_waitcnt vmcnt(30)\n\ts_barrier" :  :: "memory");
  MF(qa[7][3], cat2(wl[3], wh[3]), xf[4][3]); SBAR();
  asm volatile("s_waitcnt vmcnt(22)" :  :: "memory");
  MF(qa[0][0], cat2(wl[4], wh[4]), xf[5][0]); SBAR();
  wl[2] = lds_tr(wr0[0] + 41984);
  MF(qa[0][1], cat2(wl[4], wh[4]), xf[5][1]); SBAR();
  wh[2] = lds_tr(wr0[0] + 42240);
  MF(qa[0][2], cat2(wl[4], wh[4]), xf[5][2]); SBAR();
  GLDA(xf[9][2], xb2, xv2, 1024);
  MF(qa[0][3], cat2(wl[4], wh[4]), xf[5][3]); SBAR();
  MF(qa[1][0], cat2(wl[5], wh[5]), xf[5][0]); SBAR();
  wl[3] = lds_tr(wr1[0] + 41984);
  MF(qa[1][1], cat2(wl[5], wh[5]), xf[5][1]); SBAR();
  wh[3] = lds_tr(wr1[0] + 42240);
  MF(qa[1][2], cat2(wl[5], wh[5]), xf[5][2]); SBAR();
  GLDA(xf[9][3], xb2, xv3, 1024);
  MF(qa[1][3], cat2(wl[5], wh[5]), xf[5][3]); SBAR();
  MF(qa[2][0], cat2(wl[0], wh[0]), xf[5][0]); SBAR();
  wl[4] = lds_tr(wr0[0] + 42496);
  MF(qa[2][1], cat2(wl[0], wh[0]), xf[5][1]); SBAR();
  wh[4] = lds_tr(wr0[0] + 42752);
  MF(qa[2][2], cat2(wl[0], wh[0]), xf[5][2]); SBAR();
  dp = wq_t + 3145728; PINS(dp);
  MF(qa[2][3], cat2(wl[0], wh[0]), xf[5][3]); SBAR();
  M0ADD(wdq, 98304);
  MF(qa[3][0], cat2(wl[1], wh[1]), xf[5][0]); SBAR();
  wl[5] = lds_tr(wr1[0] + 42496);
  MF(qa[3][1], cat2(wl[1], wh[1]), xf[5][1]); SBAR();
  wh[5] = lds_tr(wr1[0] + 42752);
  MF(qa[3][2], cat2(wl[1], wh[1]), xf[5][2]); SBAR();
  glds_m<0>(dp, wqv0);
  MF(qa[3][3], cat2(wl[1], wh[1]), xf[5][3]); SBAR();
  MF(qa[4][0], cat2(wl[2], wh[2]), xf[5][0]); SBAR();
  wl[0] = lds_tr(wr0[0] + 49152);
  MF(qa[4][1], cat2(wl[2], wh[2]), xf[5][1]); SBAR();
  wh[0] = lds_tr(wr0[0] + 49408);
  MF(qa[4][2], cat2(wl[2], wh[2]), xf[5][2]); SBAR();
  glds_m<1024>(dp, wqv1);
  MF(qa[4][3], cat2(wl[2], wh[2]), xf[5][3]); SBAR();
  MF(qa[5][0], cat2(wl[3], wh[3]), xf[5][0]); SBAR();
  wl[1] = lds_tr(wr1[0] + 49152);
  MF(qa[5][1], cat2(wl[3], wh[3]), xf[5][1]); SBAR();
  wh[1] = lds_tr(wr1[0] + 49408);
  MF(qa[5][2], cat2(wl[3], wh[3]), xf[5][2]); SBAR();
  glds_m<2048>(dp, wqv2);
  MF(qa[5][3], cat2(wl[3], wh[3]), xf[5][3]); SBAR();
  MF(qa[6][0], cat2(wl[4], wh[4]), xf[5][0]); SBAR();
  wl[2] = lds_tr(wr0[0] + 49664);
  MF(qa[6][1], cat2(wl[4], wh[4]), xf[5][1]); SBAR();
  wh[2] = lds_tr(wr0[0] + 49920);
  MF(qa[6][2], cat2(wl[4], wh[4]), xf[5][2]); SBAR();
  glds_m<3072>(dp, wqv3);
  MF(qa[6][3], cat2(wl[4], wh[4]), xf[5][3]); SBAR();
  MF(qa[7][0], cat2(wl[5], wh[5]), xf[5][0]); SBAR();
  wl[3] = lds_tr(wr1[0] + 49664);
  MF(qa[7][1], cat2(wl[5], wh[5]), xf[5][1]); SBAR();
  wh[3] = lds_tr(wr1[0] + 49920);
  MF(qa[7][2], cat2(wl[5], wh[5]), xf[5][2]); SBAR();
  GLDA(xf[10][0], xb2, xv0, 2048);
  MF(qa[7][3], cat2(wl[5], wh[5]), xf[5][3]); SBAR();
  asm volatile("s_waitcnt vmcnt(21)" :  :: "memory");
  MF(qa[0][0], cat2(wl[0], wh[0]), xf[6][0]); SBAR();
  wl[4] = lds_tr(wr0[0] + 50176);
  asm volatile(".Lk2_p0:\n\ts_cbranch_execz .Lk2_p4" ::: "memory");
  MF(qa[0][1], cat2(wl[0], wh[0]), xf[6][1]); SBAR();
  wh[4] = lds_tr(wr0[0] + 50432);
  MF(qa[0][2], cat2(wl[0], wh[0]), xf[6][2]); SBAR();
  GLDA(xf[10][1], xb2, xv1, 2048);
  MF(qa[0][3], cat2(wl[0], wh[0]), xf[6][3]); SBAR();
  MF(qa[1][0], cat2(wl[1], wh[1]), xf[6][0]); SBAR();
  wl[5] = lds_tr(wr1[0] + 50176);
  MF(qa[1][1], cat2(wl[1], wh[1]), xf[6][1]); SBAR();
  wh[5] = lds_tr(wr1[0] + 50432);
  MF(qa[1][2], cat2(wl[1], wh[1]), xf[6][2]); SBAR();
  GLDA(xf[10][2], xb2, xv2, 2048);
  MF(qa[1][3], cat2(wl[1], wh[1]), xf[6][3]); SBAR();
  MF(qa[2][0], cat2(wl[2], wh[2]), xf[6][0]); SBAR();
  wl[0] = lds_tr(wr0[0] + 50688);
  MF(qa[2][1], cat2(wl[2], wh[2]), xf[6][1]); SBAR();
  wh[0] = lds_tr(wr0[0] + 50944);
  MF(qa[2][2], cat2(wl[2], wh[2]), xf[6][2]); SBAR();
  GLDA(xf[10][3], xb2, xv3, 2048);
  MF(qa[2][3], cat2(wl[2], wh[2]), xf[6][3]); SBAR();
  MF(qa[3][0], cat2(wl[3], wh[3]), xf[6][0]); SBAR();
  wl[1] = lds_tr(wr1[0] + 50688);
  MF(qa[3][1], cat2(wl[3], wh[3]), xf[6][1]); SBAR();
  wh[1] = lds_tr(wr1[0] + 50944);
  MF(qa[3][2], cat2(wl[3], wh[3]), xf[6][2]); SBAR();
  GLDA(xf[11][0], xb2, xv0, 3072);
  MF(qa[3][3], cat2(wl[3], wh[3]), xf[6][3]); SBAR();
  MF(qa[4][0], cat2(wl[4], wh[4]), xf[6][0]); SBAR();
  wl[2] = lds_tr(wr0[0] + 57344);
  MF(qa[4][1], cat2(wl[4], wh[4]), xf[6][1]); SBAR();
  wh[2] = lds_tr(wr0[0] + 57600);
  MF(qa[4][2], cat2(wl[4], wh[4]), xf[6][2]); SBAR();
  GLDA(xf[11][1], xb2, xv1, 3072);
  MF(qa[4][3], cat2(wl[4], wh[4]), xf[6][3]); SBAR();
  MF(qa[5][0], cat2(wl[5], wh[5]), xf[6][0]); SBAR();
  wl[3] = lds_tr(wr1[0] + 57344);
  MF(qa[5][1], cat2(wl[5], wh[5]), xf[6][1]); SBAR();
  wh[3] = lds_tr(wr1[0] + 57600);
  MF(qa[5][2], cat2(wl[5], wh[5]), xf[6][2]); SBAR();
  GLDA(xf[11][2], xb2, xv2, 3072);
  MF(qa[5][3], cat2(wl[5], wh[5]), xf[6][3]); SBAR();
  MF(qa[6][0], cat2(wl[0], wh[0]), xf[6][0]); SBAR();
  wl[4] = lds_tr(wr0[0] + 57856);
  MF(qa[6][1], cat2(wl[0], wh[0]), xf[6][1]); SBAR();
  wh[4] = lds_tr(wr0[0] + 58112);
  MF(qa[6][2], cat2(wl[0], wh[0]), xf[6][2]); SBAR();
  GLDA(xf[11][3], xb2, xv3, 3072);
  MF(qa[6][3], cat2(wl[0], wh[0]), xf[6][3]); SBAR();
  MF(qa[7][0], cat2(wl[1], wh[1]), xf[6][0]); SBAR();
  wl[5] = lds_tr(wr1[0] + 57856);
  MF(qa[7][1], cat2(wl[1], wh[1]), xf[6][1]); SBAR();
  wh[5] = lds_tr(wr1[0] + 58112);
  MF(qa[7][2], cat2(wl[1], wh[1]), xf[6][2]); SBAR();
  asm volatile("s_waitcnt vmcnt(32)\n\ts_barrier" :  :: "memory");
  MF(qa[7][3], cat2(wl[1], wh[1]), xf[6][3]); SBAR();
  asm volatile("s_waitcnt vmcnt(24)" :  :: "memory");
  MF(qa[0][0], cat2(wl[2], wh[2]), xf[7][0]); SBAR();
  wl[0] = lds_tr(wr0[0] + 58368);
  MF(qa[0][1], cat2(wl[2], wh[2]), xf[7][1]); SBAR();
  wh[0] = lds_tr(wr0[0] + 58624);
  MF(qa[0][2], cat2(wl[2], wh[2]), xf[7][2]); SBAR();
  dp = wq_t + 3670016; PINS(dp);
  MF(qa[0][3], cat2(wl[2], wh[2]), xf[7][3]); SBAR();
  M0ADD(wdq, 114688);
  MF(qa[1][0], cat2(wl[3], wh[3]), xf[7][0]); SBAR();
  wl[1] = lds_tr(wr1[0] + 58368);
  MF(qa[1][1], cat2(wl[3], wh[3]), xf[7][1]); SBAR();
  wh[1] = lds_tr(wr1[0] + 58624);
  MF(qa[1][2], cat2(wl[3], wh[3]), xf[7][2]); SBAR();
  glds_m<0>(dp, wqv0);
  MF(qa[1][3], cat2(wl[3], wh[3]), xf[7][3]); SBAR();
  MF(qa[2][0], cat2(wl[4], wh[4]), xf[7][0]); SBAR();
  wl[2] = lds_tr(wr0[0] + 58880);
  MF(qa[2][1], cat2(wl[4], wh[4]), xf[7][1]); SBAR();
  wh[2] = lds_tr(wr0[0] + 59136);
  MF(qa[2][2], cat2(wl[4], wh[4]), xf[7][2]); SBAR();
  glds_m<1024>(dp, wqv1);
  MF(qa[2][3], cat2(wl[4], wh[4]), xf[7][3]); SBAR();
  MF(qa[3][0], cat2(wl[5], wh[5]), xf[7][0]); SBAR();
  wl[3] = lds_tr(wr1[0] + 58880);
  MF(qa[3][1], cat2(wl[5], wh[5]), xf[7][1]); SBAR();
  wh[3] = lds_tr(wr1[0] + 59136);
  MF(qa[3][2], cat2(wl[5], wh[5]), xf[7][2]); SBAR();
  glds_m<2048>(dp, wqv2);
  MF(qa[3][3], cat2(wl[5], wh[5]), xf[7][3]); SBAR();
  MF(qa[4][0], cat2(wl[0], wh[0]), xf[7][0]); SBAR();
  wl[4] = lds_tr(wr0[1] + 0);
  MF(qa[4][1], cat2(wl[0], wh[0]), xf[7][1]); SBAR();
  wh[4] = lds_tr(wr0[1] + 256);
  MF(qa[4][2], cat2(wl[0], wh[0]), xf[7][2]); SBAR();
  glds_m<3072>(dp, wqv3);
  MF(qa[4][3], cat2(wl[0], wh[0]), xf[7][3]); SBAR();
  MF(qa[5][0], cat2(wl[1], wh[1]), xf[7][0]); SBAR();
  wl[5] = lds_tr(wr1[1] + 0);
  MF(qa[5][1], cat2(wl[1], wh[1]), xf[7][1]); SBAR();
  wh[5] = lds_tr(wr1[1] + 256);
  MF(qa[5][2], cat2(wl[1], wh[1]), xf[7][2]); SBAR();
  GLDA(xf[12][0], xb3, xv0, 0);
  MF(qa[5][3], cat2(wl[1], wh[1]), xf[7][3]); SBAR();
  MF(qa[6][0], cat2(wl[2], wh[2]), xf[7][0]); SBAR();
  wl[0] = lds_tr(wr0[1] + 512);
  MF(qa[6][1], cat2(wl[2], wh[2]), xf[7][1]); SBAR();
  wh[0] = lds_tr(wr0[1] + 768);
  MF(qa[6][2], cat2(wl[2], wh[2]), xf[7][2]); SBAR();
  GLDA(xf[12][1], xb3, xv1, 0);
  MF(qa[6][3], cat2(wl[2], wh[2]), xf[7][3]); SBAR();
  MF(qa[7][0], cat2(wl[3], wh[3]), xf[7][0]); SBAR();
  wl[1] = lds_tr(wr1[1] + 512);
  MF(qa[7][1], cat2(wl[3], wh[3]), xf[7][1]); SBAR();
  wh[1] = lds_tr(wr1[1] + 768);
  MF(qa[7][2], cat2(wl[3], wh[3]), xf[7][2]); SBAR();
  GLDA(xf[12][2], xb3, xv2, 0);
  MF(qa[7][3], cat2(wl[3], wh[3]), xf[7][3]); SBAR();
  asm volatile("s_waitcnt vmcnt(23)" :  :: "memory");
  MF(qa[0][0], cat2(wl[4], wh[4]), xf[8][0]); SBAR();
  wl[2] = lds_tr(wr0[1] + 1024);
  MF(qa[0][1], cat2(wl[4], wh[4]), xf[8][1]); SBAR();
  wh[2] = lds_tr(wr0[1] + 1280);
  MF(qa[0][2], cat2(wl[4], wh[4]), xf[8][2]); SBAR();
  GLDA(xf[12][3], xb3, xv3, 0);
  MF(qa[0][3], cat2(wl[4], wh[4]), xf[8][3]); SBAR();
  MF(qa[1][0], cat2(wl[5], wh[5]), xf[8][0]); SBAR();
  wl[3] = lds_tr(wr1[1] + 1024);
  MF(qa[1][1], cat2(wl[5], wh[5]), xf[8][1]); SBAR();
  wh[3] = lds_tr(wr1[1] + 1280);
  MF(qa[1][2], cat2(wl[5], wh[5]), xf[8][2]); SBAR();
  GLDA(xf[13][0], xb3, xv0, 1024);
  MF(qa[1][3], cat2(wl[5], wh[5]), xf[8][3]); SBAR();
  MF(qa[2][0], cat2(wl[0], wh[0]), xf[8][0]); SBAR();
  wl[4] = lds_tr(wr0[1] + 1536);
  MF(qa[2][1], cat2(wl[0], wh[0]), xf[8][1]); SBAR();
  wh[4] = lds_tr(wr0[1] + 1792);
  MF(qa[2][2], cat2(wl[0], wh[0]), xf[8][2]); SBAR();
  GLDA(xf[13][1], xb3, xv1, 1024);
  MF(qa[2][3], cat2(wl[0], wh[0]), xf[8][3]); SBAR();
  MF(qa[3][0], cat2(wl[1], wh[1]), xf[8][0]); SBAR();
  wl[5] = lds_tr(wr1[1] + 1536);
  MF(qa[3][1], cat2(wl[1], wh[1]), xf[8][1]); SBAR();
  wh[5] = lds_tr(wr1[1] + 1792);
  MF(qa[3][2], cat2(wl[1], wh[1]), xf[8][2]); SBAR();
  GLDA(xf[13][2], xb3, xv2, 1024);
  MF(qa[3][3], cat2(wl[1], wh[1]), xf[8][3]); SBAR();
  MF(qa[4][0], cat2(wl[2], wh[2]), xf[8][0]); SBAR();
  wl[0] = lds_tr(wr0[1] + 8192);
  MF(qa[4][1], cat2(wl[2], wh[2]), xf[8][1]); SBAR();
  wh[0] = lds_tr(wr0[1] + 8448);
  MF(qa[4][2], cat2(wl[2], wh[2]), xf[8][2]); SBAR();
  GLDA(xf[13][3], xb3, xv3, 1024);
  MF(qa[4][3], cat2(wl[2], wh[2]), xf[8][3]); SBAR();
  MF(qa[5][0], cat2(wl[3], wh[3]), xf[8][0]); SBAR();
  wl[1] = lds_tr(wr1[1] + 8192);
  MF(qa[5][1], cat2(wl[3], wh[3]), xf[8][1]); SBAR();
  wh[1] = lds_tr(wr1[1] + 8448);
  MF(qa[5][2], cat2(wl[3], wh[3]), xf[8][2]); SBAR();
  dp = wq_t + 4194304; PINS(dp);
  MF(qa[5][3], cat2(wl[3], wh[3]), xf[8][3]); SBAR();
  M0ADD(wdq, 131072);
  MF(qa[6][0], cat2(wl[4], wh[4]), xf[8][0]); SBAR();
  wl[2] = lds_tr(wr0[1] + 8704);
  MF(qa[6][1], cat2(wl[4], wh[4]), xf[8][1]); SBAR();
  wh[2] = lds_tr(wr0[1] + 8960);
  MF(qa[6][2], cat2(wl[4], wh[4]), xf[8][2]); SBAR();
  glds_m<0>(dp, wqv0);
  MF(qa[6][3], cat2(wl[4], wh[4]), xf[8][3]); SBAR();
  MF(qa[7][0], cat2(wl[5], wh[5]), xf[8][0]); SBAR();
  wl[3] = lds_tr(wr1[1] + 8704);
  MF(qa[7][1], cat2(wl[5], wh[5]), xf[8][1]); SBAR();
  wh[3] = lds_tr(wr1[1] + 8960);
  MF(qa[7][2], cat2(wl[5], wh[5]), xf[8][2]); SBAR();
  asm volatile("s_waitcnt vmcnt(33)\n\ts_barrier" :  :: "memory");
  MF(qa[7][3], cat2(wl[5], wh[5]), xf[8][3]); SBAR();
  asm volatile("s_waitcnt vmcnt(25)" :  :: "memory");
  MF(qa[0][0], cat2(wl[0], wh[0]), xf[9][0]); SBAR();
  wl[4] = lds_tr(wr0[1] + 9216);
  MF(qa[0][1], cat2(wl[0], wh[0]), xf[9][1]); SBAR();
  wh[4] = lds_tr(wr0[1] + 9472);
  MF(qa[0][2], cat2(wl[0], wh[0]), xf[9][2]); SBAR();
  glds_m<1024>(dp, wqv1);
  MF(qa[0][3], cat2(wl[0], wh[0]), xf[9][3]); SBAR();
  MF(qa[1][0], cat2(wl[1], wh[1]), xf[9][0]); SBAR();
  wl[5] = lds_tr(wr1[1] + 9216);
  MF(qa[1][1], cat2(wl[1], wh[1]), xf[9][1]); SBAR();
  wh[5] = lds_tr(wr1[1] + 9472);
  MF(qa[1][2], cat2(wl[1], wh[1]), xf[9][2]); SBAR();
  glds_m<2048>(dp, wqv2);
  MF(qa[1][3], cat2(wl[1], wh[1]), xf[9][3]); SBAR();
  MF(qa[2][0], cat2(wl[2], wh[2]), xf[9][0]); SBAR();
  wl[0] = lds_tr(wr0[1] + 9728);
  MF(qa[2][1], cat2(wl[2], wh[2]), xf[9][1]); SBAR();
  wh[0] = lds_tr(wr0[1] + 9984);
  MF(qa[2][2], cat2(wl[2], wh[2]), xf[9][2]); SBAR();
  glds_m<3072>(dp, wqv3);
  MF(qa[2][3], cat2(wl[2], wh[2]), xf[9][3]); SBAR();
  MF(qa[3][0], cat2(wl[3], wh[3]), xf[9][0]); SBAR();
  wl[1] = lds_tr(wr1[1] + 9728);
  MF(qa[3][1], cat2(wl[3], wh[3]), xf[9][1]); SBAR();
  wh[1] = lds_tr(wr1[1] + 9984);
  MF(qa[3][2], cat2(wl[3], wh[3]), xf[9][2]); SBAR();
  GLDA(xf[14][0], xb3, xv0, 2048);
  MF(qa[3][3], cat2(wl[3], wh[3]), xf[9][3]); SBAR();
  MF(qa[4][0], cat2(wl[4], wh[4]), xf[9][0]); SBAR();
  wl[2] = lds_tr(wr0[1] + 16384);
  MF(qa[4][1], cat2(wl[4], wh[4]), xf[9][1]); SBAR();
  wh[2] = lds_tr(wr0[1] + 16640);
  MF(qa[4][2], cat2(wl[4], wh[4]), xf[9][2]); SBAR();
  GLDA(xf[14][1], xb3, xv1, 2048);
  MF(qa[4][3], cat2(wl[4], wh[4]), xf[9][3]); SBAR();
  MF(qa[5][0], cat2(wl[5], wh[5]), xf[9][0]); SBAR();
  wl[3] = lds_tr(wr1[1] + 16384);
  MF(qa[5][1], cat2(wl[5], wh[5]), xf[9][1]); SBAR();
  wh[3] = lds_tr(wr1[1] + 16640);
  MF(qa[5][2], cat2(wl[5], wh[5]), xf[9][2]); SBAR();
  GLDA(xf[14][2], xb3, xv2, 2048);
  MF(qa[5][3], cat2(wl[5], wh[5]), xf[9][3]); SBAR();
  MF(qa[6][0], cat2(wl[0], wh[0]), xf[9][0]); SBAR();
  wl[4] = lds_tr(wr0[1] + 16896);
  MF(qa[6][1], cat2(wl[0], wh[0]), xf[9][1]); SBAR();
  wh[4] = lds_tr(wr0[1] + 17152);
  MF(qa[6][2], cat2(wl[0], wh[0]), xf[9][2]); SBAR();
  GLDA(xf[14][3], xb3, xv3, 2048);
  MF(qa[6][3], cat2(wl[0], wh[0]), xf[9][3]); SBAR();
  MF(qa[7][0], cat2(wl[1], wh[1]), xf[9][0]); SBAR();
  wl[5] = lds_tr(wr1[1] + 16896);
  MF(qa[7][1], cat2(wl[1], wh[1]), xf[9][1]); SBAR();
  wh[5] = lds_tr(wr1[1] + 17152);
  MF(qa[7][2], cat2(wl[1], wh[1]), xf[9][2]); SBAR();
  GLDA(xf[15][0], xb3, xv0, 3072);
  MF(qa[7][3], cat2(wl[1], wh[1]), xf[9][3]); SBAR();
  asm volatile("s_waitcnt vmcnt(25)" :  :: "memory");
  MF(qa[0][0], cat2(wl[2], wh[2]), xf[10][0]); SBAR();
  wl[0] = lds_tr(wr0[1] + 17408);
  MF(qa[0][1], cat2(wl[2], wh[2]), xf[10][1]); SBAR();
  wh[0] = lds_tr(wr0[1] + 17664);
  MF(qa[0][2], cat2(wl[2], wh[2]), xf[10][2]); SBAR();
  GLDA(xf[15][1], xb3, xv1, 3072);
  MF(qa[0][3], cat2(wl[2], wh[2]), xf[10][3]); SBAR();
  MF(qa[1][0], cat2(wl[3], wh[3]), xf[10][0]); SBAR();
  wl[1] = lds_tr(wr1[1] + 17408);
  MF(qa[1][1], cat2(wl[3], wh[3]), xf[10][1]); SBAR();
  wh[1] = lds_tr(wr1[1] + 17664);
  MF(qa[1][2], cat2(wl[3], wh[3]), xf[10][2]); SBAR();
  GLDA(xf[15][2], xb3, xv2, 3072);
  MF(qa[1][3], cat2(wl[3], wh[3]), xf[10][3]); SBAR();
  MF(qa[2][0], cat2(wl[4], wh[4]), xf[10][0]); SBAR();
  wl[2] = lds_tr(wr0[1] + 17920);
  MF(qa[2][1], cat2(wl[4], wh[4]), xf[10][1]); SBAR();
  wh[2] = lds_tr(wr0[1] + 18176);
  MF(qa[2][2], cat2(wl[4], wh[4]), xf[10][2]); SBAR();
  GLDA(xf[15][3], xb3, xv3, 3072);
  MF(qa[2][3], cat2(wl[4], wh[4]), xf[10][3]); SBAR();
  MF(qa[3][0], cat2(wl[5], wh[5]), xf[10][0]); SBAR();
  wl[3] = lds_tr(wr1[1] + 17920);
  MF(qa[3][1], cat2(wl[5], wh[5]), xf[10][1]); SBAR();
  wh[3] = lds_tr(wr1[1] + 18176);
  MF(qa[3][2], cat2(wl[5], wh[5]), xf[10][2]); SBAR();
  GLDA(xf[16][0], xb4, xv0, 0);
  MF(qa[3][3], cat2(wl[5], wh[5]), xf[10][3]); SBAR();
  MF(qa[4][0], cat2(wl[0], wh[0]), xf[10][0]); SBAR();
  wl[4] = lds_tr(wr0[1] + 24576);
  MF(qa[4][1], cat2(wl[0], wh[0]), xf[10][1]); SBAR();
  wh[4] = lds_tr(wr0[1] + 24832);
  MF(qa[4][2], cat2(wl[0], wh[0]), xf[10][2]); SBAR();
  GLDA(xf[16][1], xb4, xv1, 0);
  MF(qa[4][3], cat2(wl[0], wh[0]), xf[10][3]); SBAR();
  MF(qa[5][0], cat2(wl[1], wh[1]), xf[10][0]); SBAR();
  wl[5] = lds_tr(wr1[1] + 24576);
  MF(qa[5][1], cat2(wl[1], wh[1]), xf[10][1]); SBAR();
  wh[5] = lds_tr(wr1[1] + 24832);
  MF(qa[5][2], cat2(wl[1], wh[1]), xf[10][2]); SBAR();
  GLDA(xf[16][2], xb4, xv2, 0);
  MF(qa[5][3], cat2(wl[1], wh[1]), xf[10][3]); SBAR();
  MF(qa[6][0], cat2(wl[2], wh[2]), xf[10][0]); SBAR();
  wl[0] = lds_tr(wr0[1] + 25088);
  MF(qa[6][1], cat2(wl[2], wh[2]), xf[10][1]); SBAR();
  wh[0] = lds_tr(wr0[1] + 25344);
  MF(qa[6][2], cat2(wl[2], wh[2]), xf[10][2]); SBAR();
  GLDA(xf[16][3], xb4, xv3, 0);
  MF(qa[6][3], cat2(wl[2], wh[2]), xf[10][3]); SBAR();
  MF(qa[7][0], cat2(wl[3], wh[3]), xf[10][0]); SBAR();
  wl[1] = lds_tr(wr1[1] + 25088);
  MF(qa[7][1], cat2(wl[3], wh[3]), xf[10][1]); SBAR();
  wh[1] = lds_tr(wr1[1] + 25344);
  MF(qa[7][2], cat2(wl[3], wh[3]), xf[10][2]); SBAR();
  asm volatile("s_waitcnt vmcnt(36)\n\ts_barrier" :  :: "memory");
  MF(qa[7][3], cat2(wl[3], wh[3]), xf[10][3]); SBAR();
  asm volatile("s_waitcnt vmcnt(28)" :  :: "memory");
  MF(qa[0][0], cat2(wl[4], wh[4]), xf[11][0]); SBAR();
  wl[2] = lds_tr(wr0[1] + 25600);
  MF(qa[0][1], cat2(wl[4], wh[4]), xf[11][1]); SBAR();
  wh[2] = lds_tr(wr0[1] + 25856);
  MF(qa[0][2], cat2(wl[4], wh[4]), xf[11][2]); SBAR();
  GLDA(xf[17][0], xb4, xv0, 1024);
  MF(qa[0][3], cat2(wl[4], wh[4]), xf[11][3]); SBAR();
  MF(qa[1][0], cat2(wl[5], wh[5]), xf[11][0]); SBAR();
  wl[3] = lds_tr(wr1[1] + 25600);
  MF(qa[1][1], cat2(wl[5], wh[5]), xf[11][1]); SBAR();
  wh[3] = lds_tr(wr1[1] + 25856);
  MF(qa[1][2], cat2(wl[5], wh[5]), xf[11][2]); SBAR();
  GLDA(xf[17][1], xb4, xv1, 1024);
  MF(qa[1][3], cat2(wl[5], wh[5]), xf[11][3]); SBAR();
  MF(qa[2][0], cat2(wl[0], wh[0]), xf[11][0]); SBAR();
  wl[4] = lds_tr(wr0[1] + 26112);
  MF(qa[2][1], cat2(wl[0], wh[0]), xf[11][1]); SBAR();
  wh[4] = lds_tr(wr0[1] + 26368);
  MF(qa[2][2], cat2(wl[0], wh[0]), xf[11][2]); SBAR();
  GLDA(xf[17][2], xb4, xv2, 1024);
  MF(qa[2][3], cat2(wl[0], wh[0]), xf[11][3]); SBAR();
  MF(qa[3][0], cat2(wl[1], wh[1]), xf[11][0]); SBAR();
  wl[5] = lds_tr(wr1[1] + 26112);
  MF(qa[3][1], cat2(wl[1], wh[1]), xf[11][1]); SBAR();
  wh[5] = lds_tr(wr1[1] + 26368);
  MF(qa[3][2], cat2(wl[1], wh[1]), xf[11][2]); SBAR();
  GLDA(xf[17][3], xb4, xv3, 1024);
  MF(qa[3][3], cat2(wl[1], wh[1]), xf[11][3]); SBAR();
  MF(qa[4][0], cat2(wl[2], wh[2]), xf[11][0]); SBAR();
  wl[0] = lds_tr(wr0[1] + 32768);
  MF(qa[4][1], cat2(wl[2], wh[2]), xf[11][1]); SBAR();
  wh[0] = lds_tr(wr0[1] + 33024);
  MF(qa[4][2], cat2(wl[2], wh[2]), xf[11][2]); SBAR();
  dp = ks0 + 0; PINS(dp);
  MF(qa[4][3], cat2(wl[2], wh[2]), xf[11][3]); SBAR();
  M0ADD(wdst0, 0);
  MF(qa[5][0], cat2(wl[3], wh[3]), xf[11][0]); SBAR();
  wl[1] = lds_tr(wr1[1] + 32768);
  MF(qa[5][1], cat2(wl[3], wh[3]), xf[11][1]); SBAR();
  wh[1] = lds_tr(wr1[1] + 33024);
  MF(qa[5][2], cat2(wl[3], wh[3]), xf[11][2]); SBAR();
  glds_m<0>(dp, lane16);
  MF(qa[5][3], cat2(wl[3], wh[3]), xf[11][3]); SBAR();
  MF(qa[6][0], cat2(wl[4], wh[4]), xf[11][0]); SBAR();
  wl[2] = lds_tr(wr0[1] + 33280);
  MF(qa[6][1], cat2(wl[4], wh[4]), xf[11][1]); SBAR();
  wh[2] = lds_tr(wr0[1] + 33536);
  MF(qa[6][2], cat2(wl[4], wh[4]), xf[11][2]); SBAR();
  glds_m<1024>(dp, lane16);
  MF(qa[6][3], cat2(wl[4], wh[4]), xf[11][3]); SBAR();
  MF(qa[7][0], cat2(wl[5], wh[5]), xf[11][0]); SBAR();
  wl[3] = lds_tr(wr1[1] + 33280);
  MF(qa[7][1], cat2(wl[5], wh[5]), xf[11][1]); SBAR();
  wh[3] = lds_tr(wr1[1] + 33536);
  MF(qa[7][2], cat2(wl[5], wh[5]), xf[11][2]); SBAR();
  glds_m<2048>(dp, lane16);
  MF(qa[7][3], cat2(wl[5], wh[5]), xf[11][3]); SBAR();
  asm volatile("s_waitcnt vmcnt(27)" :  :: "memory");
  MF(qa[0][0], cat2(wl[0], wh[0]), xf[12][0]); SBAR();
  wl[4] = lds_tr(wr0[1] + 33792);
  MF(qa[0][1], cat2(wl[0], wh[0]), xf[12][1]); SBAR();
  wh[4] = lds_tr(wr0[1] + 34048);
  MF(qa[0][2], cat2(wl[0], wh[0]), xf[12][2]); SBAR();
  glds_m<3072>(dp, lane16);
  MF(qa[0][3], cat2(wl[0], wh[0]), xf[12][3]); SBAR();
  MF(qa[1][0], cat2(wl[1], wh[1]), xf[12][0]); SBAR();
  wl[5] = lds_tr(wr1[1] + 33792);
  MF(qa[1][1], cat2(wl[1], wh[1]), xf[12][1]); SBAR();
  wh[5] = lds_tr(wr1[1] + 34048);
  MF(qa[1][2], cat2(wl[1], wh[1]), xf[12][2]); SBAR();
  dp = ks0 + 8192; PINS(dp);
  MF(qa[1][3], cat2(wl[1], wh[1]), xf[12][3]); SBAR();
  M0ADD(wdst0, 32768);
  MF(qa[2][0], cat2(wl[2], wh[2]), xf[12][0]); SBAR();
  wl[0] = lds_tr(wr0[1] + 34304);
  MF(qa[2][1], cat2(wl[2], wh[2]), xf[12][1]); SBAR();
  wh[0] = lds_tr(wr0[1] + 34560);
  MF(qa[2][2], cat2(wl[2], wh[2]), xf[12][2]); SBAR();
  glds_m<0>(dp, lane16);
  MF(qa[2][3], cat2(wl[2], wh[2]), xf[12][3]); SBAR();
  MF(qa[3][0], cat2(wl[3], wh[3]), xf[12][0]); SBAR();
  wl[1] = lds_tr(wr1[1] + 34304);
  MF(qa[3][1], cat2(wl[3], wh[3]), xf[12][1]); SBAR();
  wh[1] = lds_tr(wr1[1] + 34560);
  MF(qa[3][2], cat2(wl[3], wh[3]), xf[12][2]); SBAR();
  glds_m<1024>(dp, lane16);
  MF(qa[3][3], cat2(wl[3], wh[3]), xf[12][3]); SBAR();
  MF(qa[4][0], cat2(wl[4], wh[4]), xf[12][0]); SBAR();
  wl[2] = lds_tr(wr0[1] + 40960);
  MF(qa[4][1], cat2(wl[4], wh[4]), xf[12][1]); SBAR();
  wh[2] = lds_tr(wr0[1] + 41216);
  MF(qa[4][2], cat2(wl[4], wh[4]), xf[12][2]); SBAR();
  glds_m<2048>(dp, lane16);
  MF(qa[4][3], cat2(wl[4], wh[4]), xf[12][3]); SBAR();
  MF(qa[5][0], cat2(wl[5], wh[5]), xf[12][0]); SBAR();
  wl[3] = lds_tr(wr1[1] + 40960);
  MF(qa[5][1], cat2(wl[5], wh[5]), xf[12][1]); SBAR();
  wh[3] = lds_tr(wr1[1] + 41216);
  MF(qa[5][2], cat2(wl[5], wh[5]), xf[12][2]); SBAR();
  glds_m<3072>(dp, lane16);
  MF(qa[5][3], cat2(wl[5], wh[5]), xf[12][3]); SBAR();
  MF(qa[6][0], cat2(wl[0], wh[0]), xf[12][0]); SBAR();
  wl[4] = lds_tr(wr0[1] + 41472);
  MF(qa[6][1], cat2(wl[0], wh[0]), xf[12][1]); SBAR();
  wh[4] = lds_tr(wr0[1] + 41728);
  MF(qa[6][2], cat2(wl[0], wh[0]), xf[12][2]); SBAR();
  MF(qa[6][3], cat2(wl[0], wh[0]), xf[12][3]); SBAR();
  MF(qa[7][0], cat2(wl[1], wh[1]), xf[12][0]); SBAR();
  wl[5] = lds_tr(wr1[1] + 41472);
  MF(qa[7][1], cat2(wl[1], wh[1]), xf[12][1]); SBAR();
  wh[5] = lds_tr(wr1[1] + 41728);
  MF(qa[7][2], cat2(wl[1], wh[1]), xf[12][2]); SBAR();
  asm volatile("s_waitcnt vmcnt(24)\n\ts_barrier" :  :: "memory");
  MF(qa[7][3], cat2(wl[1], wh[1]), xf[12][3]); SBAR();
  asm volatile("s_waitcnt vmcnt(28)" :  :: "memory");
  MF(qa[0][0], cat2(wl[2], wh[2]), xf[13][0]); SBAR();
  wl[0] = lds_tr(wr0[1] + 41984);
  MF(qa[0][1], cat2(wl[2], wh[2]), xf[13][1]); SBAR();
  wh[0] = lds_tr(wr0[1] + 42240);
  MF(qa[0][2], cat2(wl[2], wh[2]), xf[13][2]); SBAR();
  dp = ks1 + 0; PINS(dp);
  MF(qa[0][3], cat2(wl[2], wh[2]), xf[13][3]); SBAR();
  M0ADD(wdst0, 65536);
  MF(qa[1][0], cat2(wl[3], wh[3]), xf[13][0]); SBAR();
  wl[1] = lds_tr(wr1[1] + 41984);
  MF(qa[1][1], cat2(wl[3], wh[3]), xf[13][1]); SBAR();
  wh[1] = lds_tr(wr1[1] + 42240);
  MF(qa[1][2], cat2(wl[3], wh[3]), xf[13][2]); SBAR();
  glds_m<0>(dp, lane16);
  MF(qa[1][3], cat2(wl[3], wh[3]), xf[13][3]); SBAR();
  MF(qa[2][0], cat2(wl[4], wh[4]), xf[13][0]); SBAR();
  wl[2] = lds_tr(wr0[1] + 42496);
  MF(qa[2][1], cat2(wl[4], wh[4]), xf[13][1]); SBAR();
  wh[2] = lds_tr(wr0[1] + 42752);
  MF(qa[2][2], cat2(wl[4], wh[4]), xf[13][2]); SBAR();
  glds_m<1024>(dp, lane16);
  MF(qa[2][3], cat2(wl[4], wh[4]), xf[13][3]); SBAR();
  MF(qa[3][0], cat2(wl[5], wh[5]), xf[13][0]); SBAR();
  wl[3] = lds_tr(wr1[1] + 42496);
  MF(qa[3][1], cat2(wl[5], wh[5]), xf[13][1]); SBAR();
  wh[3] = lds_tr(wr1[1] + 42752);
  MF(qa[3][2], cat2(wl[5], wh[5]), xf[13][2]); SBAR();
  glds_m<2048>(dp, lane16);
  MF(qa[3][3], cat2(wl[5], wh[5]), xf[13][3]); SBAR();
  MF(qa[4][0], cat2(wl[0], wh[0]), xf[13][0]); SBAR();
  wl[4] = lds_tr(wr0[1] + 49152);
  MF(qa[4][1], cat2(wl[0], wh[0]), xf[13][1]); SBAR();
  wh[4] = lds_tr(wr0[1] + 49408);
  MF(qa[4][2], cat2(wl[0], wh[0]), xf[13][2]); SBAR();
  glds_m<3072>(dp, lane16);
  MF(qa[4][3], cat2(wl[0], wh[0]), xf[13][3]); SBAR();
  MF(qa[5][0], cat2(wl[1], wh[1]), xf[13][0]); SBAR();
  wl[5] = lds_tr(wr0[1] + 57344);
  MF(qa[5][1], cat2(wl[1], wh[1]), xf[13][1]); SBAR();
  wh[5] = lds_tr(wr0[1] + 57600);
  MF(qa[5][2], cat2(wl[1], wh[1]), xf[13][2]); SBAR();
  dp = ks0 + 0; PINS(dp);
  MF(qa[5][3], cat2(wl[1], wh[1]), xf[13][3]); SBAR();
  M0ADD(wdst0, 73728);
  MF(qa[6][0], cat2(wl[2], wh[2]), xf[13][0]); SBAR();
  wl[0] = lds_tr(wr0[2] + 0);
  MF(qa[6][1], cat2(wl[2], wh[2]), xf[13][1]); SBAR();
  wh[0] = lds_tr(wr0[2] + 256);
  MF(qa[6][2], cat2(wl[2], wh[2]), xf[13][2]); SBAR();
  glds_m<0>(dp, lanev);
  MF(qa[6][3], cat2(wl[2], wh[2]), xf[13][3]); SBAR();
  MF(qa[7][0], cat2(wl[3], wh[3]), xf[13][0]); SBAR();
  wl[1] = lds_tr(wr0[2] + 8192);
  MF(qa[7][1], cat2(wl[3], wh[3]), xf[13][1]); SBAR();
  wh[1] = lds_tr(wr0[2] + 8448);
  MF(qa[7][2], cat2(wl[3], wh[3]), xf[13][2]); SBAR();
  glds_m<1024>(dp, lanev);
  MF(qa[7][3], cat2(wl[3], wh[3]), xf[13][3]); SBAR();
  asm volatile("s_waitcnt vmcnt(26)" :  :: "memory");
  MF(qa[0][0], cat2(wl[4], wh[4]), xf[14][0]); SBAR();
  wl[2] = lds_tr(wr1[1] + 49152);
  MF(qa[0][1], cat2(wl[4], wh[4]), xf[14][1]); SBAR();
  wh[2] = lds_tr(wr1[1] + 49408);
  MF(qa[0][2], cat2(wl[4], wh[4]), xf[14][2]); SBAR();
  glds_m<2048>(dp, lanev);
  MF(qa[0][3], cat2(wl[4], wh[4]), xf[14][3]); SBAR();
  asm volatile("s_waitcnt vmcnt(23)" :  :: "memory");
  MF(qa[0][0], cat2(wl[5], wh[5]), xf[15][0]); SBAR();
  wl[3] = lds_tr(wr1[1] + 57344);
  MF(qa[0][1], cat2(wl[5], wh[5]), xf[15][1]); SBAR();
  wh[3] = lds_tr(wr1[1] + 57600);
  MF(qa[0][2], cat2(wl[5], wh[5]), xf[15][2]); SBAR();
  glds_m<3072>(dp, lanev);
  MF(qa[0][3], cat2(wl[5], wh[5]), xf[15][3]); SBAR();
  asm volatile("s_waitcnt vmcnt(20)" :  :: "memory");
  MF(qa[0][0], cat2(wl[0], wh[0]), xf[16][0]); SBAR();
  wl[4] = lds_tr(wr1[2] + 0);
  MF(qa[0][1], cat2(wl[0], wh[0]), xf[16][1]); SBAR();
  wh[4] = lds_tr(wr1[2] + 256);
  MF(qa[0][2], cat2(wl[0], wh[0]), xf[16][2]); SBAR();
  MF(qa[0][3], cat2(wl[0], wh[0]), xf[16][3]); SBAR();
  asm volatile("s_waitcnt vmcnt(16)" :  :: "memory");
  MF(qa[0][0], cat2(wl[1], wh[1]), xf[17][0]); SBAR();
  wl[5] = lds_tr(wr1[2] + 8192);
  asm volatile(".Lk2_p1:\n\ts_cbranch_execz .Lk2_p5" ::: "memory");
  MF(qa[0][1], cat2(wl[1], wh[1]), xf[17][1]); SBAR();
  wh[5] = lds_tr(wr1[2] + 8448);
  MF(qa[0][2], cat2(wl[1], wh[1]), xf[17][2]); SBAR();
  MF(qa[0][3], cat2(wl[1], wh[1]), xf[17][3]); SBAR();
  MF(qa[1][0], cat2(wl[2], wh[2]), xf[14][0]); SBAR();
  wl[0] = lds_tr(wr0[1] + 49664);
  MF(qa[1][1], cat2(wl[2], wh[2]), xf[14][1]); SBAR();
  wh[0] = lds_tr(wr0[1] + 49920);
  MF(qa[1][2], cat2(wl[2], wh[2]), xf[14][2]); SBAR();
  MF(qa[1][3], cat2(wl[2], wh[2]), xf[14][3]); SBAR();
  MF(qa[1][0], cat2(wl[3], wh[3]), xf[15][0]); SBAR();
  wl[1] = lds_tr(wr0[1] + 57856);
  MF(qa[1][1], cat2(wl[3], wh[3]), xf[15][1]); SBAR();
  wh[1] = lds_tr(wr0[1] + 58112);
  MF(qa[1][2], cat2(wl[3], wh[3]), xf[15][2]); SBAR();
  MF(qa[1][3], cat2(wl[3], wh[3]), xf[15][3]); SBAR();
  MF(qa[1][0], cat2(wl[4], wh[4]), xf[16][0]); SBAR();
  wl[2] = lds_tr(wr0[2] + 512);
  MF(qa[1][1], cat2(wl[4], wh[4]), xf[16][1]); SBAR();
  wh[2] = lds_tr(wr0[2] + 768);
  MF(qa[1][2], cat2(wl[4], wh[4]), xf[16][2]); SBAR();
  MF(qa[1][3], cat2(wl[4], wh[4]), xf[16][3]); SBAR();
  MF(qa[1][0], cat2(wl[5], wh[5]), xf[17][0]); SBAR();
  wl[3] = lds_tr(wr0[2] + 8704);
  MF(qa[1][1], cat2(wl[5], wh[5]), xf[17][1]); SBAR();
  wh[3] = lds_tr(wr0[2] + 8960);
  MF(qa[1][2], cat2(wl[5], wh[5]), xf[17][2]); SBAR();
  MF(qa[1][3], cat2(wl[5], wh[5]), xf[17][3]); SBAR();
  MF(qa[2][0], cat2(wl[0], wh[0]), xf[14][0]); SBAR();
  wl[4] = lds_tr(wr1[1] + 49664);
  MF(qa[2][1], cat2(wl[0], wh[0]), xf[14][1]); SBAR();
  wh[4] = lds_tr(wr1[1] + 49920);
  MF(qa[2][2], cat2(wl[0], wh[0]), xf[14][2]); SBAR();
  { unsigned t_ = cvtpk(qa[0][0][0], qa[0][0][1]); PIN(t_); qp[0] = t_; }
  { unsigned t_ = cvtpk(qa[0][0][2], qa[0][0][3]); PIN(t_); qp[1] = t_; }
  MF(qa[2][3], cat2(wl[0], wh[0]), xf[14][3]); SBAR();
  { unsigned t_ = cvtpk(qa[1][0][0], qa[1][0][1]); PIN(t_); qp[2] = t_; }
  MF(qa[2][0], cat2(wl[1], wh[1]), xf[15][0]); SBAR();
  wl[5] = lds_tr(wr1[1] + 57856);
  { unsigned t_ = cvtpk(qa[1][0][2], qa[1][0][3]); PIN(t_); qp[3] = t_; }
  MF(qa[2][1], cat2(wl[1], wh[1]), xf[15][1]); SBAR();
  wh[5] = lds_tr(wr1[1] + 58112);
  MF(qa[2][2], cat2(wl[1], wh[1]), xf[15][2]); SBAR();
  QW(0, (u32x4){qp[0], qp[1], qp[2], qp[3]});
  MF(qa[2][3], cat2(wl[1], wh[1]), xf[15][3]); SBAR();
  MF(qa[2][0], cat2(wl[2], wh[2]), xf[16][0]); SBAR();
  wl[0] = lds_tr(wr1[2] + 512);
  MF(qa[2][1], cat2(wl[2], wh[2]), xf[16][1]); SBAR();
  wh[0] = lds_tr(wr1[2] + 768);
  MF(qa[2][2], cat2(wl[2], wh[2]), xf[16][2]); SBAR();
  cq[0] = __builtin_bit_cast(bf16x8, (u32x4){qp[0], qp[1], qp[2], qp[3]}); asm volatile("" : "+{a[128:131]}"(cq[0]));
  MF(qa[2][3], cat2(wl[2], wh[2]), xf[16][3]); SBAR();
  { unsigned t_ = cvtpk(qa[0][1][0], qa[0][1][1]); PIN(t_); qp[16] = t_; }
  MF(qa[2][0], cat2(wl[3], wh[3]), xf[17][0]); SBAR();
  wl[1] = lds_tr(wr1[2] + 8704);
  { unsigned t_ = cvtpk(qa[0][1][2], qa[0][1][3]); PIN(t_); qp[17] = t_; }
  MF(qa[2][1], cat2(wl[3], wh[3]), xf[17][1]); SBAR();
  wh[1] = lds_tr(wr1[2] + 8960);
  { unsigned t_ = cvtpk(qa[1][1][0], qa[1][1][1]); PIN(t_); qp[18] = t_; }
  MF(qa[2][2], cat2(wl[3], wh[3]), xf[17][2]); SBAR();
  { unsigned t_ = cvtpk(qa[1][1][2], qa[1][1][3]); PIN(t_); qp[19] = t_; }
  MF(qa[2][3], cat2(wl[3], wh[3]), xf[17][3]); SBAR();
  MF(qa[3][0], cat2(wl[4], wh[4]), xf[14][0]); SBAR();
  wl[2] = lds_tr(wr0[1] + 50176);
  MF(qa[3][1], cat2(wl[4], wh[4]), xf[14][1]); SBAR();
  wh[2] = lds_tr(wr0[1] + 50432);
  MF(qa[3][2], cat2(wl[4], wh[4]), xf[14][2]); SBAR();
  QW(4096, (u32x4){qp[16], qp[17], qp[18], qp[19]});
  MF(qa[3][3], cat2(wl[4], wh[4]), xf[14][3]); SBAR();
  MF(qa[3][0], cat2(wl[5], wh[5]), xf[15][0]); SBAR();
  wl[3] = lds_tr(wr0[1] + 58368);
  MF(qa[3][1], cat2(wl[5], wh[5]), xf[15][1]); SBAR();
  wh[3] = lds_tr(wr0[1] + 58624);
  MF(qa[3][2], cat2(wl[5], wh[5]), xf[15][2]); SBAR();
  cq[4] = __builtin_bit_cast(bf16x8, (u32x4){qp[16], qp[17], qp[18], qp[19]}); asm volatile("" : "+{a[144:147]}"(cq[4]));
  MF(qa[3][3], cat2(wl[5], wh[5]), xf[15][3]); SBAR();
  { unsigned t_ = cvtpk(qa[0][2][0], qa[0][2][1]); PIN(t_); qp[32] = t_; }
  MF(qa[3][0], cat2(wl[0], wh[0]), xf[16][0]); SBAR();
  wl[4] = lds_tr(wr0[2] + 1024);
  { unsigned t_ = cvtpk(qa[0][2][2], qa[0][2][3]); PIN(t_); qp[33] = t_; }
  MF(qa[3][1], cat2(wl[0], wh[0]), xf[16][1]); SBAR();
  wh[4] = lds_tr(wr0[2] + 1280);
  { unsigned t_ = cvtpk(qa[1][2][0], qa[1][2][1]); PIN(t_); qp[34] = t_; }
  MF(qa[3][2], cat2(wl[0], wh[0]), xf[16][2]); SBAR();
  { unsigned t_ = cvtpk(qa[1][2][2], qa[1][2][3]); PIN(t_); qp[35] = t_; }
  MF(qa[3][3], cat2(wl[0], wh[0]), xf[16][3]); SBAR();
  MF(qa[3][0], cat2(wl[1], wh[1]), xf[17][0]); SBAR();
  wl[5] = lds_tr(wr0[2] + 9216);
  MF(qa[3][1], cat2(wl[1], wh[1]), xf[17][1]); SBAR();
  wh[5] = lds_tr(wr0[2] + 9472);
  MF(qa[3][2], cat2(wl[1], wh[1]), xf[17][2]); SBAR();
  QW(16384, (u32x4){qp[32], qp[33], qp[34], qp[35]});
  MF(qa[3][3], cat2(wl[1], wh[1]), xf[17][3]); SBAR();
  MF(qa[4][0], cat2(wl[2], wh[2]), xf[14][0]); SBAR();
  wl[0] = lds_tr(wr1[1] + 50176);
  MF(qa[4][1], cat2(wl[2], wh[2]), xf[14][1]); SBAR();
  wh[0] = lds_tr(wr1[1] + 50432);
  MF(qa[4][2], cat2(wl[2], wh[2]), xf[14][2]); SBAR();
  cq[8] = __builtin_bit_cast(bf16x8, (u32x4){qp[32], qp[33], qp[34], qp[35]}); asm volatile("" : "+{a[160:163]}"(cq[8]));
  MF(qa[4][3], cat2(wl[2], wh[2]), xf[14][3]); SBAR();
  { unsigned t_ = cvtpk(qa[0][3][0], qa[0][3][1]); PIN(t_); qp[48] = t_; }
  MF(qa[4][0], cat2(wl[3], wh[3]), xf[15][0]); SBAR();
  wl[1] = lds_tr(wr1[1] + 58368);
  { unsigned t_ = cvtpk(qa[0][3][2], qa[0][3][3]); PIN(t_); qp[49] = t_; }
  MF(qa[4][1], cat2(wl[3], wh[3]), xf[15][1]); SBAR();
  wh[1] = lds_tr(wr1[1] + 58624);
  { unsigned t_ = cvtpk(qa[1][3][0], qa[1][3][1]); PIN(t_); qp[50] = t_; }
  MF(qa[4][2], cat2(wl[3], wh[3]), xf[15][2]); SBAR();
  { unsigned t_ = cvtpk(qa[1][3][2], qa[1][3][3]); PIN(t_); qp[51] = t_; }
  MF(qa[4][3], cat2(wl[3], wh[3]), xf[15][3]); SBAR();
  MF(qa[4][0], cat2(wl[4], wh[4]), xf[16][0]); SBAR();
  wl[2] = lds_tr(wr1[2] + 1024);
  MF(qa[4][1], cat2(wl[4], wh[4]), xf[16][1]); SBAR();
  wh[2] = lds_tr(wr1[2] + 1280);
  MF(qa[4][2], cat2(wl[4], wh[4]), xf[16][2]); SBAR();
  QW(20480, (u32x4){qp[48], qp[49], qp[50], qp[51]});
  MF(qa[4][3], cat2(wl[4], wh[4]), xf[16][3]); SBAR();
  MF(qa[4][0], cat2(wl[5], wh[5]), xf[17][0]); SBAR();
  wl[3] = lds_tr(wr1[2] + 9216);
  MF(qa[4][1], cat2(wl[5], wh[5]), xf[17][1]); SBAR();
  wh[3] = lds_tr(wr1[2] + 9472);
  MF(qa[4][2], cat2(wl[5], wh[5]), xf[17][2]); SBAR();
  cq[12] = __builtin_bit_cast(bf16x8, (u32x4){qp[48], qp[49], qp[50], qp[51]}); asm volatile("" : "+{a[176:179]}"(cq[12]));
  MF(qa[4][3], cat2(wl[5], wh[5]), xf[17][3]); SBAR();
  { unsigned t_ = cvtpk(qa[2][0][0], qa[2][0][1]); PIN(t_); qp[4] = t_; }
  MF(qa[5][0], cat2(wl[0], wh[0]), xf[14][0]); SBAR();
  wl[4] = lds_tr(wr0[1] + 50688);
  { unsigned t_ = cvtpk(qa[2][0][2], qa[2][0][3]); PIN(t_); qp[5] = t_; }
  MF(qa[5][1], cat2(wl[0], wh[0]), xf[14][1]); SBAR();
  wh[4] = lds_tr(wr0[1] + 50944);
  { unsigned t_ = cvtpk(qa[3][0][0], qa[3][0][1]); PIN(t_); qp[6] = t_; }
  MF(qa[5][2], cat2(wl[0], wh[0]), xf[14][2]); SBAR();
  { unsigned t_ = cvtpk(qa[3][0][2], qa[3][0][3]); PIN(t_); qp[7] = t_; }
  MF(qa[5][3], cat2(wl[0], wh[0]), xf[14][3]); SBAR();
  MF(qa[5][0], cat2(wl[1], wh[1]), xf[15][0]); SBAR();
  wl[5] = lds_tr(wr0[1] + 58880);
  MF(qa[5][1], cat2(wl[1], wh[1]), xf[15][1]); SBAR();
  wh[5] = lds_tr(wr0[1] + 59136);
  MF(qa[5][2], cat2(wl[1], wh[1]), xf[15][2]); SBAR();
  QW(1024, (u32x4){qp[4], qp[5], qp[6], qp[7]});
  MF(qa[5][3], cat2(wl[1], wh[1]), xf[15][3]); SBAR();
  MF(qa[5][0], cat2(wl[2], wh[2]), xf[16][0]); SBAR();
  wl[0] = lds_tr(wr0[2] + 1536);
  MF(qa[5][1], cat2(wl[2], wh[2]), xf[16][1]); SBAR();
  wh[0] = lds_tr(wr0[2] + 1792);
  MF(qa[5][2], cat2(wl[2], wh[2]), xf[16][2]); SBAR();
  cq[1] = __builtin_bit_cast(bf16x8, (u32x4){qp[4], qp[5], qp[6], qp[7]}); asm volatile("" : "+{a[132:135]}"(cq[1]));
  MF(qa[5][3], cat2(wl[2], wh[2]), xf[16][3]); SBAR();
  { unsigned t_ = cvtpk(qa[2][1][0], qa[2][1][1]); PIN(t_); qp[20] = t_; }
  MF(qa[5][0], cat2(wl[3], wh[3]), xf[17][0]); SBAR();
  wl[1] = lds_tr(wr0[2] + 9728);
  { unsigned t_ = cvtpk(qa[2][1][2], qa[2][1][3]); PIN(t_); qp[21] = t_; }
  MF(qa[5][1], cat2(wl[3], wh[3]), xf[17][1]); SBAR();
  wh[1] = lds_tr(wr0[2] + 9984);
  { unsigned t_ = cvtpk(qa[3][1][0], qa[3][1][1]); PIN(t_); qp[22] = t_; }
  MF(qa[5][2], cat2(wl[3], wh[3]), xf[17][2]); SBAR();
  { unsigned t_ = cvtpk(qa[3][1][2], qa[3][1][3]); PIN(t_); qp[23] = t_; }
  MF(qa[5][3], cat2(wl[3], wh[3]), xf[17][3]); SBAR();
  MF(qa[6][0], cat2(wl[4], wh[4]), xf[14][0]); SBAR();
  wl[2] = lds_tr(wr1[1] + 50688);
  MF(qa[6][1], cat2(wl[4], wh[4]), xf[14][1]); SBAR();
  wh[2] = lds_tr(wr1[1] + 50944);
  MF(qa[6][2], cat2(wl[4], wh[4]), xf[14][2]); SBAR();
  QW(5120, (u32x4){qp[20], qp[21], qp[22], qp[23]});
  MF(qa[6][3], cat2(wl[4], wh[4]), xf[14][3]); SBAR();
  MF(qa[6][0], cat2(wl[5], wh[5]), xf[15][0]); SBAR();
  wl[3] = lds_tr(wr1[1] + 58880);
  MF(qa[6][1], cat2(wl[5], wh[5]), xf[15][1]); SBAR();
  wh[3] = lds_tr(wr1[1] + 59136);
  MF(qa[6][2], cat2(wl[5], wh[5]), xf[15][2]); SBAR();
  cq[5] = __builtin_bit_cast(bf16x8, (u32x4){qp[20], qp[21], qp[22], qp[23]}); asm volatile("" : "+{a[148:151]}"(cq[5]));
  MF(qa[6][3], cat2(wl[5], wh[5]), xf[15][3]); SBAR();
  { unsigned t_ = cvtpk(qa[2][2][0], qa[2][2][1]); PIN(t_); qp[36] = t_; }
  MF(qa[6][0], cat2(wl[0], wh[0]), xf[16][0]); SBAR();
  wl[4] = lds_tr(wr1[2] + 1536);
  { unsigned t_ = cvtpk(qa[2][2][2], qa[2][2][3]); PIN(t_); qp[37] = t_; }
  MF(qa[6][1], cat2(wl[0], wh[0]), xf[16][1]); SBAR();
  wh[4] = lds_tr(wr1[2] + 1792);
  { unsigned t_ = cvtpk(qa[3][2][0], qa[3][2][1]); PIN(t_); qp[38] = t_; }
  MF(qa[6][2], cat2(wl[0], wh[0]), xf[16][2]); SBAR();
  { unsigned t_ = cvtpk(qa[3][2][2], qa[3][2][3]); PIN(t_); qp[39] = t_; }
  MF(qa[6][3], cat2(wl[0], wh[0]), xf[16][3]); SBAR();
  MF(qa[6][0], cat2(wl[1], wh[1]), xf[17][0]); SBAR();
  wl[5] = lds_tr(wr1[2] + 9728);
  MF(qa[6][1], cat2(wl[1], wh[1]), xf[17][1]); SBAR();
  wh[5] = lds_tr(wr1[2] + 9984);
  MF(qa[6][2], cat2(wl[1], wh[1]), xf[17][2]); SBAR();
  QW(17408, (u32x4){qp[36], qp[37], qp[38], qp[39]});
  MF(qa[6][3], cat2(wl[1], wh[1]), xf[17][3]); SBAR();
  MF(qa[7][0], cat2(wl[2], wh[2]), xf[14][0]); SBAR();
  cq[9] = __builtin_bit_cast(bf16x8, (u32x4){qp[36], qp[37], qp[38], qp[39]}); asm volatile("" : "+{a[164:167]}"(cq[9]));
  MF(qa[7][1], cat2(wl[2], wh[2]), xf[14][1]); SBAR();
  { unsigned t_ = cvtpk(qa[2][3][0], qa[2][3][1]); PIN(t_); qp[52] = t_; }
  { unsigned t_ = cvtpk(qa[2][3][2], qa[2][3][3]); PIN(t_); qp[53] = t_; }
  MF(qa[7][2], cat2(wl[2], wh[2]), xf[14][2]); SBAR();
  { unsigned t_ = cvtpk(qa[3][3][0], qa[3][3][1]); PIN(t_); qp[54] = t_; }
  { unsigned t_ = cvtpk(qa[3][3][2], qa[3][3][3]); PIN(t_); qp[55] = t_; }
  MF(qa[7][3], cat2(wl[2], wh[2]), xf[14][3]); SBAR();
  MF(qa[7][0], cat2(wl[3], wh[3]), xf[15][0]); SBAR();
  QW(21504, (u32x4){qp[52], qp[53], qp[54], qp[55]});
  MF(qa[7][1], cat2(wl[3], wh[3]), xf[15][1]); SBAR();
  cq[13] = __builtin_bit_cast(bf16x8, (u32x4){qp[52], qp[53], qp[54], qp[55]}); asm volatile("" : "+{a[180:183]}"(cq[13]));
  MF(qa[7][2], cat2(wl[3], wh[3]), xf[15][2]); SBAR();
  { unsigned t_ = cvtpk(qa[4][0][0], qa[4][0][1]); PIN(t_); qp[8] = t_; }
  { unsigned t_ = cvtpk(qa[4][0][2], qa[4][0][3]); PIN(t_); qp[9] = t_; }
  MF(qa[7][3], cat2(wl[3], wh[3]), xf[15][3]); SBAR();
  { unsigned t_ = cvtpk(qa[5][0][0], qa[5][0][1]); PIN(t_); qp[10] = t_; }
  MF(qa[7][0], cat2(wl[4], wh[4]), xf[16][0]); SBAR();
  { unsigned t_ = cvtpk(qa[5][0][2], qa[5][0][3]); PIN(t_); qp[11] = t_; }
  MF(qa[7][1], cat2(wl[4], wh[4]), xf[16][1]); SBAR();
  QW(2048, (u32x4){qp[8], qp[9], qp[10], qp[11]});
  MF(qa[7][2], cat2(wl[4], wh[4]), xf[16][2]); SBAR();
  cq[2] = __builtin_bit_cast(bf16x8, (u32x4){qp[8], qp[9], qp[10], qp[11]}); asm volatile("" : "+{a[136:139]}"(cq[2]));
  MF(qa[7][3], cat2(wl[4], wh[4]), xf[16][3]); SBAR();
  { unsigned t_ = cvtpk(qa[4][1][0], qa[4][1][1]); PIN(t_); qp[24] = t_; }
  MF(qa[7][0], cat2(wl[5], wh[5]), xf[17][0]); SBAR();
  { unsigned t_ = cvtpk(qa[4][1][2], qa[4][1][3]); PIN(t_); qp[25] = t_; }
  { unsigned t_ = cvtpk(qa[5][1][0], qa[5][1][1]); PIN(t_); qp[26] = t_; }
  MF(qa[7][1], cat2(wl[5], wh[5]), xf[17][1]); SBAR();
  { unsigned t_ = cvtpk(qa[5][1][2], qa[5][1][3]); PIN(t_); qp[27] = t_; }
  MF(qa[7][2], cat2(wl[5], wh[5]), xf[17][2]); SBAR();
  QW(6144, (u32x4){qp[24], qp[25], qp[26], qp[27]});
  MF(qa[7][3], cat2(wl[5], wh[5]), xf[17][3]); SBAR();
  cq[6] = __builtin_bit_cast(bf16x8, (u32x4){qp[24], qp[25], qp[26], qp[27]}); asm volatile("" : "+{a[152:155]}"(cq[6]));
  { unsigned t_ = cvtpk(qa[4][2][0], qa[4][2][1]); PIN(t_); qp[40] = t_; }
  { unsigned t_ = cvtpk(qa[4][2][2], qa[4][2][3]); PIN(t_); qp[41] = t_; }
  { unsigned t_ = cvtpk(qa[5][2][0], qa[5][2][1]); PIN(t_); qp[42] = t_; }
  { unsigned t_ = cvtpk(qa[5][2][2], qa[5][2][3]); PIN(t_); qp[43] = t_; }
  QW(18432, (u32x4){qp[40], qp[41], qp[42], qp[43]});
  cq[10] = __builtin_bit_cast(bf16x8, (u32x4){qp[40], qp[41], qp[42], qp[43]}); asm volatile("" : "+{a[168:171]}"(cq[10]));
  { unsigned t_ = cvtpk(qa[4][3][0], qa[4][3][1]); PIN(t_); qp[56] = t_; }
  { unsigned t_ = cvtpk(qa[4][3][2], qa[4][3][3]); PIN(t_); qp[57] = t_; }
  { unsigned t_ = cvtpk(qa[5][3][0], qa[5][3][1]); PIN(t_); qp[58] = t_; }
  { unsigned t_ = cvtpk(qa[5][3][2], qa[5][3][3]); PIN(t_); qp[59] = t_; }
  QW(22528, (u32x4){qp[56], qp[57], qp[58], qp[59]});
  cq[14] = __builtin_bit_cast(bf16x8, (u32x4){qp[56], qp[57], qp[58], qp[59]}); asm volatile("" : "+{a[184:187]}"(cq[14]));
#pragma unroll
    for (int qt = 0; qt < 4; ++qt) {
      if (qt >= 2) asm volatile("s_nop 1" : "+v"(qa[7][qt]), "+v"(qp[16 * qt - 4]), "+v"(qp[16 * qt - 3]), "+v"(qp[16 * qt - 2]), "+v"(qp[16 * qt - 1]), "+v"(qp[12]), "+v"(qp[13]), "+v"(qp[14]), "+v"(qp[15]));
#pragma unroll
      for (int k = 0; k < 4; ++k) qp[16 * qt + 12 + k] = cvtpk(qa[6 + (k >> 1)][qt][2 * (k & 1)], qa[6 + (k >> 1)][qt][2 * (k & 1) + 1]);
    }
#pragma unroll
    for (int i = 3; i < 16; i += 4) cq[i] = __builtin_bit_cast(bf16x8, (u32x4){qp[4 * i], qp[4 * i + 1], qp[4 * i + 2], qp[4 * i + 3]});
    asm volatile("" : "+{a[128:131]}"(cq[0]), "+{a[132:135]}"(cq[1]), "+{a[136:139]}"(cq[2]), "+{a[140:143]}"(cq[3]), "+{a[144:147]}"(cq[4]), "+{a[148:151]}"(cq[5]), "+{a[152:155]}"(cq[6]), "+{a[156:159]}"(cq[7]), "+{a[160:163]}"(cq[8]), "+{a[164:167]}"(cq[9]), "+{a[168:171]}"(cq[10]), "+{a[172:175]}"(cq[11]), "+{a[176:179]}"(cq[12]), "+{a[180:183]}"(cq[13]), "+{a[184:187]}"(cq[14]), "+{a[188:191]}"(cq[15]));
    asm volatile("" : "+v"(rdesc) :: "memory");
  }

  const unsigned special = g ? 0u : 3u << nA;
  const unsigned rbase = ldsb + greg + lane16, lsa = ldsb + 65536u + (unsigned)(r + 2 - 2 * g) * 1024u + lane16;
  const char* ctxa = ws + WS_CTX + ((size_t)(p * 16 + wid * 4) * 128 + h * 4) * 1024;
  unsigned redo;
  unsigned rfo = (wid | lane) ? 0x100000u + (unsigned)(bid * 4 + wid) * 128u : 0x60000u + (unsigned)bid * 4u, cto;
  { unsigned len_; asm volatile("s_mov_b32 %0, .Lk3_end-.Lk2_tch" : "=s"(len_));
    cto = (unsigned)(((bid >> 3) & 15) * 32 + wid * 8 + (lane >> 3)) * 128u; cto = cto < len_ ? cto : 0u; }
  const char* const __attribute__((address_space(4)))* ka = (const char* const __attribute__((address_space(4)))*)__builtin_amdgcn_kernarg_segment_ptr();
  asm volatile(
    "s_cmp_lt_i32 %[na], 50\n"
    "s_cbranch_scc1 .Lnq%=\n"
    "ds_write_b128 %[qx], a[140:143] offset:3072\n"
    "ds_write_b128 %[qx], a[156:159] offset:7168\n"
    "ds_write_b128 %[qx], a[172:175] offset:19456\n"
    "ds_write_b128 %[qx], a[188:191] offset:23552\n"
    "s_branch .Lnr%=\n"
    ".Lnq%=:\n"
    "v_accvgpr_mov_b32 a192, a128\n"
    "v_accvgpr_mov_b32 a193, a129\n"
    "v_accvgpr_mov_b32 a194, a130\n"
    "v_accvgpr_mov_b32 a195, a131\n"
    "v_accvgpr_mov_b32 a196, a132\n"
    "v_accvgpr_mov_b32 a197, a133\n"
    "v_accvgpr_mov_b32 a198, a134\n"
    "v_accvgpr_mov_b32 a199, a135\n"
    "v_accvgpr_mov_b32 a200, a136\n"
    "v_accvgpr_mov_b32 a201, a137\n"
    "v_accvgpr_mov_b32 a202, a138\n"
    "v_accvgpr_mov_b32 a203, a139\n"
    "v_accvgpr_mov_b32 a204, a140\n"
    "v_accvgpr_mov_b32 a205, a141\n"
    "v_accvgpr_mov_b32 a206, a142\n"
    "v_accvgpr_mov_b32 a207, a143\n"
    "v_accvgpr_mov_b32 a208, a144\n"
    "v_accvgpr_mov_b32 a209, a145\n"
    "v_accvgpr_mov_b32 a210, a146\n"
    "v_accvgpr_mov_b32 a211, a147\n"
    "v_accvgpr_mov_b32 a212, a148\n"
    "v_accvgpr_mov_b32 a213, a149\n"
    "v_accvgpr_mov_b32 a214, a150\n"
    "v_accvgpr_mov_b32 a215, a151\n"
    "v_accvgpr_mov_b32 a216, a152\n"
    "v_accvgpr_mov_b32 a217, a153\n"
    "v_accvgpr_mov_b32 a218, a154\n"
    "v_accvgpr_mov_b32 a219, a155\n"
    "v_accvgpr_mov_b32 a220, a156\n"
    "v_accvgpr_mov_b32 a221, a157\n"
    "v_accvgpr_mov_b32 a222, a158\n"
    "v_accvgpr_mov_b32 a223, a159\n"
    "v_accvgpr_mov_b32 a224, a160\n"
    "v_accvgpr_mov_b32 a225, a161\n"
    "v_accvgpr_mov_b32 a226, a162\n"
    "v_accvgpr_mov_b32 a227, a163\n"
    "v_accvgpr_mov_b32 a228, a164\n"
    "v_accvgpr_mov_b32 a229, a165\n"
    "v_accvgpr_mov_b32 a230, a166\n"
    "v_accvgpr_mov_b32 a231, a167\n"
    "v_accvgpr_mov_b32 a232, a168\n"
    "v_accvgpr_mov_b32 a233, a169\n"
    "v_accvgpr_mov_b32 a234, a170\n"
    "v_accvgpr_mov_b32 a235, a171\n"
    "v_accvgpr_mov_b32 a236, a172\n"
    "v_accvgpr_mov_b32 a237, a173\n"
    "v_accvgpr_mov_b32 a238, a174\n"
    "v_accvgpr_mov_b32 a239, a175\n"
    "v_accvgpr_mov_b32 a240, a176\n"
    "v_accvgpr_mov_b32 a241, a177\n"
    "v_accvgpr_mov_b32 a242, a178\n"
    "v_accvgpr_mov_b32 a243, a179\n"
    "v_accvgpr_mov_b32 a244, a180\n"
    "v_accvgpr_mov_b32 a245, a181\n"
    "v_accvgpr_mov_b32 a246, a182\n"
    "v_accvgpr_mov_b32 a247, a183\n"
    "v_accvgpr_mov_b32 a248, a184\n"
    "v_accvgpr_mov_b32 a249, a185\n"
    "v_accvgpr_mov_b32 a250, a186\n"
    "v_accvgpr_mov_b32 a251, a187\n"
    "v_accvgpr_mov_b32 a252, a188\n"
    "v_accvgpr_mov_b32 a253, a189\n"
    "v_accvgpr_mov_b32 a254, a190\n"
    "v_accvgpr_mov_b32 a255, a191\n"
    ".Lnr%=:\n"
    "s_waitcnt vmcnt(0) lgkmcnt(0)\n"
    "s_barrier\n"
    "ds_read_b128 v[96:99], %[rbase] offset:0\n"
    "ds_read_b128 v[104:107], %[rbase] offset:1024\n"
    "ds_read_b128 v[112:115], %[rbase] offset:2048\n"
    "ds_read_b128 v[120:123], %[rbase] offset:3072\n"
    "s_cbranch_scc0 .Lni%=\n"
    "ds_read_b128 a[128:131], %[qx] offset:0\n"
    "ds_read_b128 a[132:135], %[qx] offset:1024\n"
    "ds_read_b128 a[136:139], %[qx] offset:2048\n"
    "ds_read_b128 a[140:143], %[qx] offset:3072\n"
    "ds_read_b128 a[144:147], %[qx] offset:4096\n"
    "ds_read_b128 a[148:151], %[qx] offset:5120\n"
    "ds_read_b128 a[152:155], %[qx] offset:6144\n"
    "ds_read_b128 a[156:159], %[qx] offset:7168\n"
    "ds_read_b128 a[160:163], %[qx] offset:16384\n"
    "ds_read_b128 a[164:167], %[qx] offset:17408\n"
    "ds_read_b128 a[168:171], %[qx] offset:18432\n"
    "ds_read_b128 a[172:175], %[qx] offset:19456\n"
    "ds_read_b128 a[176:179], %[qx] offset:20480\n"
    "ds_read_b128 a[180:183], %[qx] offset:21504\n"
    "ds_read_b128 a[184:187], %[qx] offset:22528\n"
    "ds_read_b128 a[188:191], %[qx] offset:23552\n"
    ".Lni%=:\n"
    "v_mov_b32 v128, 0\n"
    "v_mov_b32 v129, 0\n"
    "v_mov_b32 v130, 0\n"
    "v_mov_b32 v131, 0\n"
    "v_mov_b32 v200, 0\n"
    "v_mov_b32 v201, 0\n"
    "v_mov_b32 v202, 0\n"
    "v_mov_b32 v203, 0\n"
    "v_bfe_u32 v164, %[lane16], 4, 2\n"
    "v_mov_b32 v165, 0x3f803f80\n"
    "v_mfma_f32_32x32x16_bf16 a[0:15], v[200:203], v[200:203], 0\n"
    "v_cmp_eq_u32 s[84:85], 0, v164\n"
    "v_cmp_eq_u32 s[86:87], 1, v164\n"
    "v_cmp_eq_u32 s[88:89], 2, v164\n"
    "v_cmp_eq_u32 s[90:91], 3, v164\n"
    "v_cndmask_b32 v132, 0, v165, s[84:85]\n"
    "v_cndmask_b32 v133, 0, v165, s[84:85]\n"
    "v_mfma_f32_32x32x16_bf16 a[16:31], v[200:203], v[200:203], 0\n"
    "v_cndmask_b32 v134, 0, v165, s[84:85]\n"
    "v_cndmask_b32 v135, 0, v165, s[84:85]\n"
    "v_cndmask_b32 v136, 0, v165, s[86:87]\n"
    "v_cndmask_b32 v137, 0, v165, s[86:87]\n"
    "v_cndmask_b32 v138, 0, v165, s[86:87]\n"
    "v_cndmask_b32 v139, 0, v165, s[86:87]\n"
    "v_mfma_f32_32x32x16_bf16 a[32:47], v[200:203], v[200:203], 0\n"
    "v_cndmask_b32 v140, 0, v165, s[88:89]\n"
    "v_cndmask_b32 v141, 0, v165, s[88:89]\n"
    "v_cndmask_b32 v142, 0, v165, s[88:89]\n"
    "v_cndmask_b32 v143, 0, v165, s[88:89]\n"
    "v_cndmask_b32 v144, 0, v165, s[90:91]\n"
    "v_cndmask_b32 v145, 0, v165, s[90:91]\n"
    "v_mfma_f32_32x32x16_bf16 a[48:63], v[200:203], v[200:203], 0\n"
    "v_cndmask_b32 v146, 0, v165, s[90:91]\n"
    "v_cndmask_b32 v147, 0, v165, s[90:91]\n"
    "v_mov_b32 v166, 0xff800000\n"
    "v_cmp_gt_i32 s[84:85], 0, %[dm]\n"
    "v_cmp_gt_i32 s[86:87], 1, %[dm]\n"
    "v_cmp_gt_i32 s[88:89], 2, %[dm]\n"
    "v_mfma_f32_32x32x16_bf16 a[64:79], v[200:203], v[200:203], 0\n"
    "v_cmp_gt_i32 s[90:91], 3, %[dm]\n"
    "v_cndmask_b32 v222, 0, v166, s[84:85]\n"
    "v_cndmask_b32 v223, 0, v166, s[86:87]\n"
    "v_cndmask_b32 v224, 0, v166, s[88:89]\n"
    "v_cndmask_b32 v225, 0, v166, s[90:91]\n"
    "v_cmp_gt_i32 s[84:85], 16, %[dm]\n"
    "v_mfma_f32_32x32x16_bf16 a[80:95], v[200:203], v[200:203], 0\n"
    "v_cmp_gt_i32 s[86:87], 17, %[dm]\n"
    "v_cmp_gt_i32 s[88:89], 18, %[dm]\n"
    "v_cmp_gt_i32 s[90:91], 19, %[dm]\n"
    "v_cndmask_b32 v226, 0, v166, s[84:85]\n"
    "v_cndmask_b32 v227, 0, v166, s[86:87]\n"
    "v_cndmask_b32 v228, 0, v166, s[88:89]\n"
    "v_mfma_f32_32x32x16_bf16 a[96:111], v[200:203], v[200:203], 0\n"
    "v_cndmask_b32 v229, 0, v166, s[90:91]\n"
    "s_sub_i32 s45, 0, %[na]\n"
    "s_mov_b32 s60, 0x05040100\n"
    "s_mov_b32 s61, 0x07060302\n"
    "s_mov_b32 s40, 0\n"
    "s_cmp_lt_i32 s40, %[na]\n"
    "v_mfma_f32_32x32x16_bf16 a[112:127], v[200:203], v[200:203], 0\n"
    "s_cselect_b32 s53, %[kt0], s45\n"
    "s_add_i32 s53, s53, s40\n"
    "s_lshl_b32 s53, s53, 14\n"
    "s_add_u32 s46, %[khlo], s53\n"
    "s_addc_u32 s47, %[khhi], 0\n"
    "s_mov_b32 s56, 1\n"
    "s_cmp_lt_i32 s56, %[na]\n"
    "s_cselect_b32 s53, %[kt0], s45\n"
    "s_add_i32 s53, s53, s56\n"
    "s_lshl_b32 s53, s53, 14\n"
    "s_add_u32 s48, %[khlo], s53\n"
    "s_addc_u32 s49, %[khhi], 0\n"
    "v_add_u32 v204, 0x10000, %[rbase]\n"
    "v_add_u32 v205, 0x2000, %[lane16]\n"
    "v_add_u32 v207, 0x2000, %[lanev]\n"
    "s_waitcnt lgkmcnt(0)\n"
    "s_waitcnt lgkmcnt(3)\n"
    "v_mfma_f32_16x16x32_bf16 v[0:3], v[96:99], a[128:131], 0\n"
    "s_add_u32 m0, %[wdst0], 106496\n"
    "s_add_u32 s57, s40, 2\n"
    "v_mfma_f32_16x16x32_bf16 v[4:7], v[96:99], a[144:147], 0\n"
    "s_min_u32 s57, s57, 16\n"
    "v_mfma_f32_16x16x32_bf16 v[8:11], v[96:99], a[160:163], 0\n"
    "s_cmp_lt_i32 s57, %[na]\n"
    "s_cselect_b32 s53, %[kt0], s45\n"
    "v_mfma_f32_16x16x32_bf16 v[12:15], v[96:99], a[176:179], 0\n"
    "s_add_i32 s53, s53, s57\n"
    "s_lshl_b32 s53, s53, 14\n"
    "ds_read_b128 v[96:99], %[rbase] offset:4096\n"
    "s_waitcnt lgkmcnt(3)\n"
    "v_mfma_f32_16x16x32_bf16 v[0:3], v[104:107], a[132:135], v[0:3]\n"
    "v_mfma_f32_16x16x32_bf16 v[4:7], v[104:107], a[148:151], v[4:7]\n"
    "v_mfma_f32_16x16x32_bf16 v[8:11], v[104:107], a[164:167], v[8:11]\n"
    "v_mfma_f32_16x16x32_bf16 v[12:15], v[104:107], a[180:183], v[12:15]\n"
    "ds_read_b128 v[104:107], %[rbase] offset:5120\n"
    "s_waitcnt lgkmcnt(3)\n"
    "v_mfma_f32_16x16x32_bf16 v[0:3], v[112:115], a[136:139], v[0:3]\n"
    "v_mfma_f32_16x16x32_bf16 v[4:7], v[112:115], a[152:155], v[4:7]\n"
    "v_mfma_f32_16x16x32_bf16 v[8:11], v[112:115], a[168:171], v[8:11]\n"
    "v_mfma_f32_16x16x32_bf16 v[12:15], v[112:115], a[184:187], v[12:15]\n"
    "ds_read_b128 v[112:115], %[rbase] offset:6144\n"
    "s_waitcnt lgkmcnt(3)\n"
    "v_mfma_f32_16x16x32_bf16 v[0:3], v[120:123], a[140:143], v[0:3]\n"
    "v_mfma_f32_16x16x32_bf16 v[4:7], v[120:123], a[156:159], v[4:7]\n"
    "v_mfma_f32_16x16x32_bf16 v[8:11], v[120:123], a[172:175], v[8:11]\n"
    ".Lk2_p2:\n\ts_cbranch_execz .Lk2_p6\n"
    "v_mfma_f32_16x16x32_bf16 v[12:15], v[120:123], a[188:191], v[12:15]\n"
    "s_waitcnt vmcnt(1) lgkmcnt(0)\n"
    "s_barrier\n"
    "s_nop 0\n"
    "global_load_lds_dwordx4 v207, s[46:47] offset:0\n"
    "ds_read_b128 v[120:123], %[rbase] offset:7168\n"
    "s_waitcnt lgkmcnt(3)\n"
    "v_mfma_f32_16x16x32_bf16 v[16:19], v[96:99], a[128:131], 0\n"
    "v_mfma_f32_16x16x32_bf16 v[20:23], v[96:99], a[144:147], 0\n"
    "v_mfma_f32_16x16x32_bf16 v[24:27], v[96:99], a[160:163], 0\n"
    "global_load_lds_dwordx4 v207, s[46:47] offset:1024\n"
    "v_mfma_f32_16x16x32_bf16 v[28:31], v[96:99], a[176:179], 0\n"
    "ds_read_b128 v[96:99], %[rbase] offset:32768\n"
    "s_waitcnt lgkmcnt(3)\n"
    "v_mfma_f32_16x16x32_bf16 v[16:19], v[104:107], a[132:135], v[16:19]\n"
    "v_mfma_f32_16x16x32_bf16 v[20:23], v[104:107], a[148:151], v[20:23]\n"
    "global_load_lds_dwordx4 v207, s[46:47] offset:2048\n"
    "v_mfma_f32_16x16x32_bf16 v[24:27], v[104:107], a[164:167], v[24:27]\n"
    "v_mfma_f32_16x16x32_bf16 v[28:31], v[104:107], a[180:183], v[28:31]\n"
    "ds_read_b128 v[104:107], %[rbase] offset:33792\n"
    "s_nop 0\n"
    "global_load_lds_dwordx4 v207, s[46:47] offset:3072\n"
    "s_waitcnt lgkmcnt(3)\n"
    "v_mfma_f32_16x16x32_bf16 v[16:19], v[112:115], a[136:139], v[16:19]\n"
    "s_add_u32 m0, %[wdst0], 98304\n"
    "v_mfma_f32_16x16x32_bf16 v[20:23], v[112:115], a[152:155], v[20:23]\n"
    "s_add_u32 s46, %[khlo], s53\n"
    "s_addc_u32 s47, %[khhi], 0\n"
    "v_mfma_f32_16x16x32_bf16 v[24:27], v[112:115], a[168:171], v[24:27]\n"
    "v_mfma_f32_16x16x32_bf16 v[28:31], v[112:115], a[184:187], v[28:31]\n"
    "s_nop 0\n"
    "ds_read_b128 v[112:115], %[rbase] offset:34816\n"
    "global_load_lds_dwordx4 v205, s[48:49] offset:0\n"
    "s_waitcnt lgkmcnt(3)\n"
    "v_mfma_f32_16x16x32_bf16 v[16:19], v[120:123], a[140:143], v[16:19]\n"
    "v_mfma_f32_16x16x32_bf16 v[20:23], v[120:123], a[156:159], v[20:23]\n"
    "v_mfma_f32_16x16x32_bf16 v[24:27], v[120:123], a[172:175], v[24:27]\n"
    "v_mfma_f32_16x16x32_bf16 v[28:31], v[120:123], a[188:191], v[28:31]\n"
    "ds_read_b128 v[120:123], %[rbase] offset:35840\n"
    "s_nop 7\n"
    "s_waitcnt lgkmcnt(3)\n"
    "v_mfma_f32_16x16x32_bf16 v[32:35], v[96:99], a[128:131], 0\n"
    "v_exp_f32 v0, v0\n"
    "v_mfma_f32_16x16x32_bf16 v[36:39], v[96:99], a[144:147], 0\n"
    "v_exp_f32 v1, v1\n"
    "v_mfma_f32_16x16x32_bf16 v[40:43], v[96:99], a[160:163], 0\n"
    "v_exp_f32 v2, v2\n"
    "v_mfma_f32_16x16x32_bf16 v[44:47], v[96:99], a[176:179], 0\n"
    "v_exp_f32 v3, v3\n"
    "s_nop 0\n"
    "global_load_lds_dwordx4 v205, s[48:49] offset:1024\n"
    "v_cvt_pk_bf16_f32 v64, v0, v1\n"
    "v_cvt_pk_bf16_f32 v65, v2, v3\n"
    "ds_read_b128 v[96:99], %[rbase] offset:36864\n"
    "s_waitcnt lgkmcnt(3)\n"
    "v_mfma_f32_16x16x32_bf16 v[32:35], v[104:107], a[132:135], v[32:35]\n"
    "v_exp_f32 v4, v4\n"
    "v_mfma_f32_16x16x32_bf16 v[36:39], v[104:107], a[148:151], v[36:39]\n"
    "v_exp_f32 v5, v5\n"
    "v_mfma_f32_16x16x32_bf16 v[40:43], v[104:107], a[164:167], v[40:43]\n"
    "v_exp_f32 v6, v6\n"
    "v_mfma_f32_16x16x32_bf16 v[44:47], v[104:107], a[180:183], v[44:47]\n"
    "v_exp_f32 v7, v7\n"
    "s_nop 0\n"
    "global_load_lds_dwordx4 v205, s[48:49] offset:2048\n"
    "v_cvt_pk_bf16_f32 v68, v4, v5\n"
    "v_cvt_pk_bf16_f32 v69, v6, v7\n"
    "ds_read_b128 v[104:107], %[rbase] offset:37888\n"
    "s_waitcnt lgkmcnt(3)\n"
    "v_mfma_f32_16x16x32_bf16 v[32:35], v[112:115], a[136:139], v[32:35]\n"
    "v_exp_f32 v8, v8\n"
    "v_mfma_f32_16x16x32_bf16 v[36:39], v[112:115], a[152:155], v[36:39]\n"
    "v_exp_f32 v9, v9\n"
    "v_mfma_f32_16x16x32_bf16 v[40:43], v[112:115], a[168:171], v[40:43]\n"
    "v_exp_f32 v10, v10\n"
    "v_mfma_f32_16x16x32_bf16 v[44:47], v[112:115], a[184:187], v[44:47]\n"
    "v_exp_f32 v11, v11\n"
    "s_nop 0\n"
    "global_load_lds_dwordx4 v205, s[48:49] offset:3072\n"
    "v_cvt_pk_bf16_f32 v72, v8, v9\n"
    "s_nop 0\n"
    "s_add_u32 m0, %[wdst0], 8192\n"
    "v_cvt_pk_bf16_f32 v73, v10, v11\n"
    "ds_read_b128 v[112:115], %[rbase] offset:38912\n"
    "s_waitcnt lgkmcnt(3)\n"
    "v_mfma_f32_16x16x32_bf16 v[32:35], v[120:123], a[140:143], v[32:35]\n"
    "v_exp_f32 v12, v12\n"
    "v_mfma_f32_16x16x32_bf16 v[36:39], v[120:123], a[156:159], v[36:39]\n"
    "v_exp_f32 v13, v13\n"
    "v_mfma_f32_16x16x32_bf16 v[40:43], v[120:123], a[172:175], v[40:43]\n"
    "v_exp_f32 v14, v14\n"
    "v_mfma_f32_16x16x32_bf16 v[44:47], v[120:123], a[188:191], v[44:47]\n"
    "v_exp_f32 v15, v15\n"
    "s_waitcnt vmcnt(8)\n"
    "s_barrier\n"
    "s_nop 0\n"
    "global_load_lds_dwordx4 %[lanev], s[48:49] offset:0\n"
    "v_cvt_pk_bf16_f32 v76, v12, v13\n"
    "s_nop 0\n"
    "s_add_u32 s40, s40, 1\n"
    "v_cvt_pk_bf16_f32 v77, v14, v15\n"
    "ds_read_b128 v[120:123], %[rbase] offset:39936\n"
    "s_waitcnt lgkmcnt(3)\n"
    "v_mfma_f32_16x16x32_bf16 v[48:51], v[96:99], a[128:131], 0\n"
    "v_exp_f32 v16, v16\n"
    "v_mfma_f32_16x16x32_bf16 v[52:55], v[96:99], a[144:147], 0\n"
    "v_exp_f32 v17, v17\n"
    "v_mfma_f32_16x16x32_bf16 v[56:59], v[96:99], a[160:163], 0\n"
    "v_exp_f32 v18, v18\n"
    "v_mfma_f32_16x16x32_bf16 v[60:63], v[96:99], a[176:179], 0\n"
    "v_exp_f32 v19, v19\n"
    "s_nop 0\n"
    "global_load_lds_dwordx4 %[lanev], s[48:49] offset:1024\n"
    "v_cvt_pk_bf16_f32 v66, v16, v17\n"
    "v_cvt_pk_bf16_f32 v67, v18, v19\n"
    "ds_read_b128 v[96:99], v204 offset:0\n"
    "s_waitcnt lgkmcnt(3)\n"
    "v_mfma_f32_16x16x32_bf16 v[48:51], v[104:107], a[132:135], v[48:51]\n"
    "v_exp_f32 v20, v20\n"
    "v_mfma_f32_16x16x32_bf16 v[52:55], v[104:107], a[148:151], v[52:55]\n"
    "v_exp_f32 v21, v21\n"
    "v_mfma_f32_16x16x32_bf16 v[56:59], v[104:107], a[164:167], v[56:59]\n"
    "ds_read_b128 v[100:103], v204 offset:8192\n"
    "v_exp_f32 v22, v22\n"
    "v_mfma_f32_16x16x32_bf16 v[60:63], v[104:107], a[180:183], v[60:63]\n"
    "v_exp_f32 v23, v23\n"
    "s_nop 0\n"
    "global_load_lds_dwordx4 %[lanev], s[48:49] offset:2048\n"
    "v_cvt_pk_bf16_f32 v70, v20, v21\n"
    "v_cvt_pk_bf16_f32 v71, v22, v23\n"
    "ds_read_b128 v[104:107], v204 offset:1024\n"
    "s_waitcnt lgkmcnt(4)\n"
    "v_mfma_f32_16x16x32_bf16 v[48:51], v[112:115], a[136:139], v[48:51]\n"
    "v_exp_f32 v24, v24\n"
    "v_mfma_f32_16x16x32_bf16 v[52:55], v[112:115], a[152:155], v[52:55]\n"
    "v_exp_f32 v25, v25\n"
    "v_mfma_f32_16x16x32_bf16 v[56:59], v[112:115], a[168:171], v[56:59]\n"
    "ds_read_b128 v[108:111], v204 offset:9216\n"
    "v_exp_f32 v26, v26\n"
    "v_mfma_f32_16x16x32_bf16 v[60:63], v[112:115], a[184:187], v[60:63]\n"
    "v_exp_f32 v27, v27\n"
    "s_nop 0\n"
    "global_load_lds_dwordx4 %[lanev], s[48:49] offset:3072\n"
    "v_cvt_pk_bf16_f32 v74, v24, v25\n"
    "s_nop 0\n"
    "s_add_u32 m0, %[wdst0], 0\n"
    "v_cvt_pk_bf16_f32 v75, v26, v27\n"
    "ds_read_b128 v[112:115], v204 offset:2048\n"
    "s_waitcnt lgkmcnt(5)\n"
    "v_mfma_f32_16x16x32_bf16 v[48:51], v[120:123], a[140:143], v[48:51]\n"
    "v_exp_f32 v28, v28\n"
    "v_mfma_f32_16x16x32_bf16 v[52:55], v[120:123], a[156:159], v[52:55]\n"
    "v_exp_f32 v29, v29\n"
    "v_mfma_f32_16x16x32_bf16 v[56:59], v[120:123], a[172:175], v[56:59]\n"
    "ds_read_b128 v[116:119], v204 offset:10240\n"
    "v_exp_f32 v30, v30\n"
    "v_mfma_f32_16x16x32_bf16 v[60:63], v[120:123], a[188:191], v[60:63]\n"
    "v_exp_f32 v31, v31\n"
    "s_nop 0\n"
    "global_load_lds_dwordx4 %[lane16], s[46:47] offset:0\n"
    "v_cvt_pk_bf16_f32 v78, v28, v29\n"
    "v_cvt_pk_bf16_f32 v79, v30, v31\n"
    "ds_read_b128 v[120:123], v204 offset:3072\n"
    "s_waitcnt lgkmcnt(3)\n"
    ".Lloop%=:\n"
    "s_bitcmp1_b32 %[special], s40\n"
    "s_cbranch_scc1 .Lqaf0%=\n"
    ".Lraf0%=:\n"
    "v_mfma_f32_16x16x32_bf16 v[0:3], v[96:99], a[128:131], 0\n"
    "v_exp_f32 v32, v32\n"
    "v_mfma_f32_16x16x32_bf16 v[4:7], v[96:99], a[144:147], 0\n"
    "v_exp_f32 v33, v33\n"
    "v_mfma_f32_16x16x32_bf16 v[8:11], v[96:99], a[160:163], 0\n"
    "ds_read_b128 v[124:127], v204 offset:11264\n"
    "v_cvt_pk_bf16_f32 v80, v32, v33\n"
    "v_mfma_f32_16x16x32_bf16 v[12:15], v[96:99], a[176:179], 0\n"
    "global_load_lds_dwordx4 %[lane16], s[46:47] offset:1024\n"
    "s_add_u32 s57, s40, 2\n"
    "v_mfma_f32_16x16x32_bf16 a[0:3], v[100:103], v[64:67], a[0:3]\n"
    "v_exp_f32 v34, v34\n"
    "v_mfma_f32_16x16x32_bf16 a[32:35], v[100:103], v[68:71], a[32:35]\n"
    "v_exp_f32 v35, v35\n"
    "v_mfma_f32_16x16x32_bf16 a[64:67], v[100:103], v[72:75], a[64:67]\n"
    "ds_read_b128 v[96:99], v204 offset:4096\n"
    "v_cvt_pk_bf16_f32 v81, v34, v35\n"
    "v_mfma_f32_16x16x32_bf16 a[96:99], v[100:103], v[76:79], a[96:99]\n"
    "s_min_u32 s57, s57, 16\n"
    "v_mfma_f32_16x16x32_bf16 v[0:3], v[104:107], a[132:135], v[0:3]\n"
    "v_exp_f32 v36, v36\n"
    "v_mfma_f32_16x16x32_bf16 v[4:7], v[104:107], a[148:151], v[4:7]\n"
    "v_exp_f32 v37, v37\n"
    "v_mfma_f32_16x16x32_bf16 v[8:11], v[104:107], a[164:167], v[8:11]\n"
    "ds_read_b128 v[100:103], v204 offset:12288\n"
    "v_cvt_pk_bf16_f32 v84, v36, v37\n"
    "v_mfma_f32_16x16x32_bf16 v[12:15], v[104:107], a[180:183], v[12:15]\n"
    "global_load_lds_dwordx4 %[lane16], s[46:47] offset:2048\n"
    "v_readlane_b32 s92, v218, 0\n"
    "v_mfma_f32_16x16x32_bf16 a[4:7], v[108:111], v[64:67], a[4:7]\n"
    "v_exp_f32 v38, v38\n"
    "v_mfma_f32_16x16x32_bf16 a[36:39], v[108:111], v[68:71], a[36:39]\n"
    "v_exp_f32 v39, v39\n"
    "v_mfma_f32_16x16x32_bf16 a[68:71], v[108:111], v[72:75], a[68:71]\n"
    "ds_read_b128 v[104:107], v204 offset:5120\n"
    "v_cvt_pk_bf16_f32 v85, v38, v39\n"
    "v_mfma_f32_16x16x32_bf16 a[100:103], v[108:111], v[76:79], a[100:103]\n"
    "s_cmp_lt_i32 s57, %[na]\n"
    "s_cselect_b32 s53, %[kt0], s45\n"
    "v_mfma_f32_16x16x32_bf16 v[128:131], v[132:135], v[64:67], v[128:131]\n"
    "s_add_i32 s53, s53, s57\n"
    "s_waitcnt lgkmcnt(3)\n"
    "v_mfma_f32_16x16x32_bf16 v[0:3], v[112:115], a[136:139], v[0:3]\n"
    "v_exp_f32 v40, v40\n"
    "v_mfma_f32_16x16x32_bf16 v[4:7], v[112:115], a[152:155], v[4:7]\n"
    "v_exp_f32 v41, v41\n"
    "v_mfma_f32_16x16x32_bf16 v[8:11], v[112:115], a[168:171], v[8:11]\n"
    "ds_read_b128 v[108:111], v204 offset:13312\n"
    "v_cvt_pk_bf16_f32 v88, v40, v41\n"
    "v_mfma_f32_16x16x32_bf16 v[12:15], v[112:115], a[184:187], v[12:15]\n"
    "global_load_lds_dwordx4 %[lane16], s[46:47] offset:3072\n"
    "s_add_u32 m0, %[wdst0], 40960\n"
    "v_mfma_f32_16x16x32_bf16 a[8:11], v[116:119], v[64:67], a[8:11]\n"
    "v_exp_f32 v42, v42\n"
    "v_mfma_f32_16x16x32_bf16 a[40:43], v[116:119], v[68:71], a[40:43]\n"
    "v_exp_f32 v43, v43\n"
    "v_mfma_f32_16x16x32_bf16 a[72:75], v[116:119], v[72:75], a[72:75]\n"
    "ds_read_b128 v[112:115], v204 offset:6144\n"
    "v_cvt_pk_bf16_f32 v89, v42, v43\n"
    "v_mfma_f32_16x16x32_bf16 a[104:107], v[116:119], v[76:79], a[104:107]\n"
    "s_lshl_b32 s53, s53, 14\n"
    "v_mfma_f32_16x16x32_bf16 v[0:3], v[120:123], a[140:143], v[0:3]\n"
    "v_exp_f32 v44, v44\n"
    "v_mfma_f32_16x16x32_bf16 v[4:7], v[120:123], a[156:159], v[4:7]\n"
    "v_exp_f32 v45, v45\n"
    "v_mfma_f32_16x16x32_bf16 v[8:11], v[120:123], a[172:175], v[8:11]\n"
    "ds_read_b128 v[116:119], v204 offset:14336\n"
    "v_cvt_pk_bf16_f32 v92, v44, v45\n"
    "v_mfma_f32_16x16x32_bf16 v[12:15], v[120:123], a[188:191], v[12:15]\n"
    "s_waitcnt vmcnt(8)\n"
    "s_barrier\n"
    "v_mfma_f32_16x16x32_bf16 a[12:15], v[124:127], v[64:67], a[12:15]\n"
    "v_exp_f32 v46, v46\n"
    "v_mfma_f32_16x16x32_bf16 a[44:47], v[124:127], v[68:71], a[44:47]\n"
    "v_exp_f32 v47, v47\n"
    "v_mfma_f32_16x16x32_bf16 a[76:79], v[124:127], v[72:75], a[76:79]\n"
    "ds_read_b128 v[120:123], v204 offset:7168\n"
    "v_cvt_pk_bf16_f32 v93, v46, v47\n"
    "v_mfma_f32_16x16x32_bf16 a[108:111], v[124:127], v[76:79], a[108:111]\n"
    "global_load_lds_dwordx4 v207, s[48:49] offset:0\n"
    "v_readlane_b32 s93, v218, 1\n"
    "v_mfma_f32_16x16x32_bf16 v[128:131], v[136:139], v[68:71], v[128:131]\n"
    "s_waitcnt lgkmcnt(3)\n"
    "v_mfma_f32_16x16x32_bf16 v[16:19], v[96:99], a[128:131], 0\n"
    "v_exp_f32 v48, v48\n"
    "v_mfma_f32_16x16x32_bf16 v[20:23], v[96:99], a[144:147], 0\n"
    "v_exp_f32 v49, v49\n"
    "v_mfma_f32_16x16x32_bf16 v[24:27], v[96:99], a[160:163], 0\n"
    "ds_read_b128 v[124:127], v204 offset:15360\n"
    "v_cvt_pk_bf16_f32 v82, v48, v49\n"
    "v_mfma_f32_16x16x32_bf16 v[28:31], v[96:99], a[176:179], 0\n"
    "v_readlane_b32 s52, v218, 5\n"
    "v_readlane_b32 s38, v218, 4\n"
    "v_mfma_f32_16x16x32_bf16 a[16:19], v[100:103], v[64:67], a[16:19]\n"
    "v_exp_f32 v50, v50\n"
    "v_mfma_f32_16x16x32_bf16 a[48:51], v[100:103], v[68:71], a[48:51]\n"
    "v_exp_f32 v51, v51\n"
    "v_mfma_f32_16x16x32_bf16 a[80:83], v[100:103], v[72:75], a[80:83]\n"
    "ds_read_b128 v[96:99], v204 offset:32768\n"
    "v_cvt_pk_bf16_f32 v83, v50, v51\n"
    "v_mfma_f32_16x16x32_bf16 a[112:115], v[100:103], v[76:79], a[112:115]\n"
    "global_load_lds_dwordx4 v207, s[48:49] offset:1024\n"
    "v_lshrrev_b32 v214, 8, %[lane16]\n"
    "v_mfma_f32_16x16x32_bf16 v[16:19], v[104:107], a[132:135], v[16:19]\n"
    "v_exp_f32 v52, v52\n"
    "v_mfma_f32_16x16x32_bf16 v[20:23], v[104:107], a[148:151], v[20:23]\n"
    "v_exp_f32 v53, v53\n"
    "v_mfma_f32_16x16x32_bf16 v[24:27], v[104:107], a[164:167], v[24:27]\n"
    "ds_read_b128 v[100:103], v204 offset:40960\n"
    "v_cvt_pk_bf16_f32 v86, v52, v53\n"
    "v_mfma_f32_16x16x32_bf16 v[28:31], v[104:107], a[180:183], v[28:31]\n"
    "v_bfe_u32 v215, %[lane16], 4, 4\n"
    "v_mfma_f32_16x16x32_bf16 a[20:23], v[108:111], v[64:67], a[20:23]\n"
    "v_exp_f32 v54, v54\n"
    "v_mfma_f32_16x16x32_bf16 a[52:55], v[108:111], v[68:71], a[52:55]\n"
    "v_exp_f32 v55, v55\n"
    "v_mfma_f32_16x16x32_bf16 a[84:87], v[108:111], v[72:75], a[84:87]\n"
    "ds_read_b128 v[104:107], v204 offset:33792\n"
    "v_cvt_pk_bf16_f32 v87, v54, v55\n"
    "v_mfma_f32_16x16x32_bf16 a[116:119], v[108:111], v[76:79], a[116:119]\n"
    "global_load_lds_dwordx4 v207, s[48:49] offset:2048\n"
    "v_mul_u32_u24 v214, s52, v214\n"
    "v_mfma_f32_16x16x32_bf16 v[128:131], v[140:143], v[72:75], v[128:131]\n"
    "s_waitcnt lgkmcnt(3)\n"
    "v_mfma_f32_16x16x32_bf16 v[16:19], v[112:115], a[136:139], v[16:19]\n"
    "v_exp_f32 v56, v56\n"
    "v_mfma_f32_16x16x32_bf16 v[20:23], v[112:115], a[152:155], v[20:23]\n"
    "v_exp_f32 v57, v57\n"
    "v_mfma_f32_16x16x32_bf16 v[24:27], v[112:115], a[168:171], v[24:27]\n"
    "ds_read_b128 v[108:111], v204 offset:41984\n"
    "v_cvt_pk_bf16_f32 v90, v56, v57\n"
    "v_mfma_f32_16x16x32_bf16 v[28:31], v[112:115], a[184:187], v[28:31]\n"
    "v_readlane_b32 s52, v218, 3\n"
    "v_lshl_add_u32 v214, v215, 3, v214\n"
    "v_mfma_f32_16x16x32_bf16 a[24:27], v[116:119], v[64:67], a[24:27]\n"
    "v_exp_f32 v58, v58\n"
    "v_mfma_f32_16x16x32_bf16 a[56:59], v[116:119], v[68:71], a[56:59]\n"
    "v_exp_f32 v59, v59\n"
    "v_mfma_f32_16x16x32_bf16 a[88:91], v[116:119], v[72:75], a[88:91]\n"
    "ds_read_b128 v[112:115], v204 offset:34816\n"
    "v_cvt_pk_bf16_f32 v91, v58, v59\n"
    "v_mfma_f32_16x16x32_bf16 a[120:123], v[116:119], v[76:79], a[120:123]\n"
    "global_load_lds_dwordx4 v207, s[48:49] offset:3072\n"
    "s_add_u32 m0, %[wdst0], 32768\n"
    "v_mfma_f32_16x16x32_bf16 v[16:19], v[120:123], a[140:143], v[16:19]\n"
    "v_exp_f32 v60, v60\n"
    "v_mfma_f32_16x16x32_bf16 v[20:23], v[120:123], a[156:159], v[20:23]\n"
    "v_exp_f32 v61, v61\n"
    "v_mfma_f32_16x16x32_bf16 v[24:27], v[120:123], a[172:175], v[24:27]\n"
    "ds_read_b128 v[116:119], v204 offset:43008\n"
    "v_cvt_pk_bf16_f32 v94, v60, v61\n"
    "v_mfma_f32_16x16x32_bf16 v[28:31], v[120:123], a[188:191], v[28:31]\n"
    "s_add_u32 s48, %[khlo], s53\n"
    "s_addc_u32 s49, %[khhi], 0\n"
    "v_mfma_f32_16x16x32_bf16 a[28:31], v[124:127], v[64:67], a[28:31]\n"
    "v_exp_f32 v62, v62\n"
    "v_mfma_f32_16x16x32_bf16 a[60:63], v[124:127], v[68:71], a[60:63]\n"
    "v_exp_f32 v63, v63\n"
    "v_mfma_f32_16x16x32_bf16 a[92:95], v[124:127], v[72:75], a[92:95]\n"
    "ds_read_b128 v[120:123], v204 offset:35840\n"
    "v_cvt_pk_bf16_f32 v95, v62, v63\n"
    "v_mfma_f32_16x16x32_bf16 a[124:127], v[124:127], v[76:79], a[124:127]\n"
    "global_load_lds_dwordx4 v205, s[46:47] offset:0\n"
    "s_waitcnt lgkmcnt(3)\n"
    "v_mfma_f32_16x16x32_bf16 v[128:131], v[144:147], v[76:79], v[128:131]\n"
    "v_add_u32 v215, s38, v214\n"
    "v_mfma_f32_16x16x32_bf16 v[32:35], v[96:99], a[128:131], 0\n"
    "v_exp_f32 v0, v0\n"
    "v_mfma_f32_16x16x32_bf16 v[36:39], v[96:99], a[144:147], 0\n"
    "v_exp_f32 v1, v1\n"
    "v_mfma_f32_16x16x32_bf16 v[40:43], v[96:99], a[160:163], 0\n"
    "ds_read_b128 v[124:127], v204 offset:44032\n"
    "v_cvt_pk_bf16_f32 v64, v0, v1\n"
    "v_mfma_f32_16x16x32_bf16 v[44:47], v[96:99], a[176:179], 0\n"
    "global_load_lds_dwordx4 v205, s[46:47] offset:1024\n"
    "s_add_u32 s40, s40, 1\n"
    "v_mfma_f32_16x16x32_bf16 a[0:3], v[100:103], v[80:83], a[0:3]\n"
    "v_exp_f32 v2, v2\n"
    "v_mfma_f32_16x16x32_bf16 a[32:35], v[100:103], v[84:87], a[32:35]\n"
    "v_exp_f32 v3, v3\n"
    "v_mfma_f32_16x16x32_bf16 a[64:67], v[100:103], v[88:91], a[64:67]\n"
    "ds_read_b128 v[96:99], v204 offset:36864\n"
    "v_cvt_pk_bf16_f32 v65, v2, v3\n"
    "v_mfma_f32_16x16x32_bf16 a[96:99], v[100:103], v[92:95], a[96:99]\n"
    "v_readlane_b32 s63, v218, 2\n"
    "v_add_u32 v216, s52, v214\n"
    "v_mfma_f32_16x16x32_bf16 v[32:35], v[104:107], a[132:135], v[32:35]\n"
    "v_exp_f32 v4, v4\n"
    "v_mfma_f32_16x16x32_bf16 v[36:39], v[104:107], a[148:151], v[36:39]\n"
    "v_exp_f32 v5, v5\n"
    "v_mfma_f32_16x16x32_bf16 v[40:43], v[104:107], a[164:167], v[40:43]\n"
    "ds_read_b128 v[100:103], v204 offset:45056\n"
    "v_cvt_pk_bf16_f32 v68, v4, v5\n"
    "v_mfma_f32_16x16x32_bf16 v[44:47], v[104:107], a[180:183], v[44:47]\n"
    "global_load_lds_dwordx4 v205, s[46:47] offset:2048\n"
    "v_add_u32 v217, s52, v215\n"
    "v_mfma_f32_16x16x32_bf16 a[4:7], v[108:111], v[80:83], a[4:7]\n"
    "v_exp_f32 v6, v6\n"
    "v_mfma_f32_16x16x32_bf16 a[36:39], v[108:111], v[84:87], a[36:39]\n"
    "v_exp_f32 v7, v7\n"
    "v_mfma_f32_16x16x32_bf16 a[68:71], v[108:111], v[88:91], a[68:71]\n"
    "ds_read_b128 v[104:107], v204 offset:37888\n"
    "v_cvt_pk_bf16_f32 v69, v6, v7\n"
    "v_mfma_f32_16x16x32_bf16 a[100:103], v[108:111], v[92:95], a[100:103]\n"
    "v_mfma_f32_16x16x32_bf16 v[128:131], v[132:135], v[80:83], v[128:131]\n"
    "s_waitcnt lgkmcnt(3)\n"
    "v_mfma_f32_16x16x32_bf16 v[32:35], v[112:115], a[136:139], v[32:35]\n"
    "v_exp_f32 v8, v8\n"
    "v_mfma_f32_16x16x32_bf16 v[36:39], v[112:115], a[152:155], v[36:39]\n"
    "v_exp_f32 v9, v9\n"
    "v_mfma_f32_16x16x32_bf16 v[40:43], v[112:115], a[168:171], v[40:43]\n"
    "ds_read_b128 v[108:111], v204 offset:46080\n"
    "v_cvt_pk_bf16_f32 v72, v8, v9\n"
    "v_mfma_f32_16x16x32_bf16 v[44:47], v[112:115], a[184:187], v[44:47]\n"
    "global_load_lds_dwordx4 v205, s[46:47] offset:3072\n"
    "s_add_u32 m0, %[wdst0], 73728\n"
    "v_mfma_f32_16x16x32_bf16 a[8:11], v[116:119], v[80:83], a[8:11]\n"
    "v_exp_f32 v10, v10\n"
    "v_mfma_f32_16x16x32_bf16 a[40:43], v[116:119], v[84:87], a[40:43]\n"
    "v_exp_f32 v11, v11\n"
    "v_mfma_f32_16x16x32_bf16 a[72:75], v[116:119], v[88:91], a[72:75]\n"
    "ds_read_b128 v[112:115], v204 offset:38912\n"
    "v_cvt_pk_bf16_f32 v73, v10, v11\n"
    "v_mfma_f32_16x16x32_bf16 a[104:107], v[116:119], v[92:95], a[104:107]\n"
    "v_mfma_f32_16x16x32_bf16 v[32:35], v[120:123], a[140:143], v[32:35]\n"
    "v_exp_f32 v12, v12\n"
    "v_mfma_f32_16x16x32_bf16 v[36:39], v[120:123], a[156:159], v[36:39]\n"
    "v_exp_f32 v13, v13\n"
    "v_mfma_f32_16x16x32_bf16 v[40:43], v[120:123], a[172:175], v[40:43]\n"
    "ds_read_b128 v[116:119], v204 offset:47104\n"
    "v_cvt_pk_bf16_f32 v76, v12, v13\n"
    "v_mfma_f32_16x16x32_bf16 v[44:47], v[120:123], a[188:191], v[44:47]\n"
    "s_waitcnt vmcnt(8)\n"
    "s_barrier\n"
    "v_mfma_f32_16x16x32_bf16 a[12:15], v[124:127], v[80:83], a[12:15]\n"
    "v_exp_f32 v14, v14\n"
    "v_mfma_f32_16x16x32_bf16 a[44:47], v[124:127], v[84:87], a[44:47]\n"
    "v_exp_f32 v15, v15\n"
    "v_mfma_f32_16x16x32_bf16 a[76:79], v[124:127], v[88:91], a[76:79]\n"
    "ds_read_b128 v[120:123], v204 offset:39936\n"
    "v_cvt_pk_bf16_f32 v77, v14, v15\n"
    "v_mfma_f32_16x16x32_bf16 a[108:111], v[124:127], v[92:95], a[108:111]\n"
    "global_load_lds_dwordx4 %[lanev], s[46:47] offset:0\n"
    "global_load_lds_dwordx4 %[lanev], s[46:47] offset:1024\n"
    "v_mfma_f32_16x16x32_bf16 v[128:131], v[136:139], v[84:87], v[128:131]\n"
    "global_load_lds_dwordx4 %[lanev], s[46:47] offset:2048\n"
    "s_waitcnt lgkmcnt(3)\n"
    "v_mfma_f32_16x16x32_bf16 v[48:51], v[96:99], a[128:131], 0\n"
    "v_exp_f32 v16, v16\n"
    "v_mfma_f32_16x16x32_bf16 v[52:55], v[96:99], a[144:147], 0\n"
    "v_exp_f32 v17, v17\n"
    "v_mfma_f32_16x16x32_bf16 v[56:59], v[96:99], a[160:163], 0\n"
    "ds_read_b128 v[124:127], v204 offset:48128\n"
    "v_cvt_pk_bf16_f32 v66, v16, v17\n"
    "v_mfma_f32_16x16x32_bf16 v[60:63], v[96:99], a[176:179], 0\n"
    "global_load_lds_dwordx4 %[lanev], s[46:47] offset:3072\n"
    "s_add_u32 m0, %[wdst0], 65536\n"
    "v_mfma_f32_16x16x32_bf16 a[16:19], v[100:103], v[80:83], a[16:19]\n"
    "v_exp_f32 v18, v18\n"
    "v_mfma_f32_16x16x32_bf16 a[48:51], v[100:103], v[84:87], a[48:51]\n"
    "v_exp_f32 v19, v19\n"
    "v_mfma_f32_16x16x32_bf16 a[80:83], v[100:103], v[88:91], a[80:83]\n"
    "ds_read_b128 v[96:99], %[rbase] offset:0\n"
    "v_cvt_pk_bf16_f32 v67, v18, v19\n"
    "v_mfma_f32_16x16x32_bf16 a[112:115], v[100:103], v[92:95], a[112:115]\n"
    "global_load_lds_dwordx4 %[lane16], s[48:49] offset:0\n"
    "global_load_lds_dwordx4 %[lane16], s[48:49] offset:1024\n"
    "v_mfma_f32_16x16x32_bf16 v[48:51], v[104:107], a[132:135], v[48:51]\n"
    "v_exp_f32 v20, v20\n"
    "v_mfma_f32_16x16x32_bf16 v[52:55], v[104:107], a[148:151], v[52:55]\n"
    "v_exp_f32 v21, v21\n"
    "v_mfma_f32_16x16x32_bf16 v[56:59], v[104:107], a[164:167], v[56:59]\n"
    "ds_read_b128 v[100:103], %[rbase] offset:8192\n"
    "v_cvt_pk_bf16_f32 v70, v20, v21\n"
    "v_mfma_f32_16x16x32_bf16 v[60:63], v[104:107], a[180:183], v[60:63]\n"
    "global_load_lds_dwordx4 %[lane16], s[48:49] offset:2048\n"
    "global_load_lds_dwordx4 %[lane16], s[48:49] offset:3072\n"
    "v_mfma_f32_16x16x32_bf16 a[20:23], v[108:111], v[80:83], a[20:23]\n"
    "v_exp_f32 v22, v22\n"
    "v_mfma_f32_16x16x32_bf16 a[52:55], v[108:111], v[84:87], a[52:55]\n"
    "v_exp_f32 v23, v23\n"
    "v_mfma_f32_16x16x32_bf16 a[84:87], v[108:111], v[88:91], a[84:87]\n"
    "ds_read_b128 v[104:107], %[rbase] offset:1024\n"
    "v_cvt_pk_bf16_f32 v71, v22, v23\n"
    "v_mfma_f32_16x16x32_bf16 a[116:119], v[108:111], v[92:95], a[116:119]\n"
    "global_load_dwordx2 v[148:149], v214, s[92:93] nt\n"
    "global_load_dwordx2 v[150:151], v215, s[92:93] nt\n"
    "v_mfma_f32_16x16x32_bf16 v[128:131], v[140:143], v[88:91], v[128:131]\n"
    "global_load_dwordx2 v[152:153], v216, s[92:93] nt\n"
    "s_waitcnt lgkmcnt(3)\n"
    "v_mfma_f32_16x16x32_bf16 v[48:51], v[112:115], a[136:139], v[48:51]\n"
    "v_exp_f32 v24, v24\n"
    "v_mfma_f32_16x16x32_bf16 v[52:55], v[112:115], a[152:155], v[52:55]\n"
    "v_exp_f32 v25, v25\n"
    "v_mfma_f32_16x16x32_bf16 v[56:59], v[112:115], a[168:171], v[56:59]\n"
    "ds_read_b128 v[108:111], %[rbase] offset:9216\n"
    "v_cvt_pk_bf16_f32 v74, v24, v25\n"
    "v_mfma_f32_16x16x32_bf16 v[60:63], v[112:115], a[184:187], v[60:63]\n"
    "global_load_dwordx2 v[154:155], v217, s[92:93] nt\n"
    "v_mfma_f32_16x16x32_bf16 a[24:27], v[116:119], v[80:83], a[24:27]\n"
    "v_exp_f32 v26, v26\n"
    "v_mfma_f32_16x16x32_bf16 a[56:59], v[116:119], v[84:87], a[56:59]\n"
    "v_exp_f32 v27, v27\n"
    "v_mfma_f32_16x16x32_bf16 a[88:91], v[116:119], v[88:91], a[88:91]\n"
    "ds_read_b128 v[112:115], %[rbase] offset:2048\n"
    "v_cvt_pk_bf16_f32 v75, v26, v27\n"
    "v_mfma_f32_16x16x32_bf16 a[120:123], v[116:119], v[92:95], a[120:123]\n"
    "s_add_u32 s92, s92, s63\n"
    "s_addc_u32 s93, s93, 0\n"
    "v_mfma_f32_16x16x32_bf16 v[48:51], v[120:123], a[140:143], v[48:51]\n"
    "v_exp_f32 v28, v28\n"
    "v_mfma_f32_16x16x32_bf16 v[52:55], v[120:123], a[156:159], v[52:55]\n"
    "v_exp_f32 v29, v29\n"
    "v_mfma_f32_16x16x32_bf16 v[56:59], v[120:123], a[172:175], v[56:59]\n"
    "ds_read_b128 v[116:119], %[rbase] offset:10240\n"
    "v_cvt_pk_bf16_f32 v78, v28, v29\n"
    "v_mfma_f32_16x16x32_bf16 v[60:63], v[120:123], a[188:191], v[60:63]\n"
    "global_load_dwordx2 v[156:157], v214, s[92:93] nt\n"
    "global_load_dwordx2 v[158:159], v215, s[92:93] nt\n"
    "v_mfma_f32_16x16x32_bf16 a[28:31], v[124:127], v[80:83], a[28:31]\n"
    "v_exp_f32 v30, v30\n"
    "v_mfma_f32_16x16x32_bf16 a[60:63], v[124:127], v[84:87], a[60:63]\n"
    "v_exp_f32 v31, v31\n"
    "v_mfma_f32_16x16x32_bf16 a[92:95], v[124:127], v[88:91], a[92:95]\n"
    "ds_read_b128 v[120:123], %[rbase] offset:3072\n"
    "v_cvt_pk_bf16_f32 v79, v30, v31\n"
    "v_mfma_f32_16x16x32_bf16 a[124:127], v[124:127], v[92:95], a[124:127]\n"
    "global_load_dwordx2 v[160:161], v216, s[92:93] nt\n"
    "s_waitcnt lgkmcnt(3)\n"
    "v_mfma_f32_16x16x32_bf16 v[128:131], v[144:147], v[92:95], v[128:131]\n"
    "s_bitcmp1_b32 %[special], s40\n"
    "s_cbranch_scc1 .Lqaf2%=\n"
    ".Lraf2%=:\n"
    "v_mfma_f32_16x16x32_bf16 v[0:3], v[96:99], a[128:131], 0\n"
    "v_exp_f32 v32, v32\n"
    "v_mfma_f32_16x16x32_bf16 v[4:7], v[96:99], a[144:147], 0\n"
    "v_exp_f32 v33, v33\n"
    "v_mfma_f32_16x16x32_bf16 v[8:11], v[96:99], a[160:163], 0\n"
    "ds_read_b128 v[124:127], %[rbase] offset:11264\n"
    "v_cvt_pk_bf16_f32 v80, v32, v33\n"
    "v_mfma_f32_16x16x32_bf16 v[12:15], v[96:99], a[176:179], 0\n"
    "s_add_u32 m0, %[wdst0], 106496\n"
    "s_add_u32 s57, s40, 2\n"
    "v_mfma_f32_16x16x32_bf16 a[0:3], v[100:103], v[64:67], a[0:3]\n"
    "v_exp_f32 v34, v34\n"
    "v_mfma_f32_16x16x32_bf16 a[32:35], v[100:103], v[68:71], a[32:35]\n"
    "v_exp_f32 v35, v35\n"
    "v_mfma_f32_16x16x32_bf16 a[64:67], v[100:103], v[72:75], a[64:67]\n"
    "ds_read_b128 v[96:99], %[rbase] offset:4096\n"
    "v_cvt_pk_bf16_f32 v81, v34, v35\n"
    "v_mfma_f32_16x16x32_bf16 a[96:99], v[100:103], v[76:79], a[96:99]\n"
    "global_load_dwordx2 v[162:163], v217, s[92:93] nt\n"
    "s_min_u32 s57, s57, 16\n"
    "v_mfma_f32_16x16x32_bf16 v[0:3], v[104:107], a[132:135], v[0:3]\n"
    "v_exp_f32 v36, v36\n"
    "v_mfma_f32_16x16x32_bf16 v[4:7], v[104:107], a[148:151], v[4:7]\n"
    "v_exp_f32 v37, v37\n"
    "v_mfma_f32_16x16x32_bf16 v[8:11], v[104:107], a[164:167], v[8:11]\n"
    "ds_read_b128 v[100:103], %[rbase] offset:12288\n"
    "v_cvt_pk_bf16_f32 v84, v36, v37\n"
    "v_mfma_f32_16x16x32_bf16 v[12:15], v[104:107], a[180:183], v[12:15]\n"
    "s_cmp_lt_i32 s57, %[na]\n"
    "s_cselect_b32 s53, %[kt0], s45\n"
    "v_mfma_f32_16x16x32_bf16 a[4:7], v[108:111], v[64:67], a[4:7]\n"
    "v_exp_f32 v38, v38\n"
    "v_mfma_f32_16x16x32_bf16 a[36:39], v[108:111], v[68:71], a[36:39]\n"
    "v_exp_f32 v39, v39\n"
    "v_mfma_f32_16x16x32_bf16 a[68:71], v[108:111], v[72:75], a[68:71]\n"
    "ds_read_b128 v[104:107], %[rbase] offset:5120\n"
    "v_cvt_pk_bf16_f32 v85, v38, v39\n"
    "v_mfma_f32_16x16x32_bf16 a[100:103], v[108:111], v[76:79], a[100:103]\n"
    "s_add_i32 s53, s53, s57\n"
    "s_lshl_b32 s53, s53, 14\n"
    "v_mfma_f32_16x16x32_bf16 v[128:131], v[132:135], v[64:67], v[128:131]\n"
    "s_waitcnt lgkmcnt(3)\n"
    "v_mfma_f32_16x16x32_bf16 v[0:3], v[112:115], a[136:139], v[0:3]\n"
    "v_exp_f32 v40, v40\n"
    "v_mfma_f32_16x16x32_bf16 v[4:7], v[112:115], a[152:155], v[4:7]\n"
    "v_exp_f32 v41, v41\n"
    "v_mfma_f32_16x16x32_bf16 v[8:11], v[112:115], a[168:171], v[8:11]\n"
    "ds_read_b128 v[108:111], %[rbase] offset:13312\n"
    "v_cvt_pk_bf16_f32 v88, v40, v41\n"
    "v_mfma_f32_16x16x32_bf16 v[12:15], v[112:115], a[184:187], v[12:15]\n"
    "v_mfma_f32_16x16x32_bf16 a[8:11], v[116:119], v[64:67], a[8:11]\n"
    "v_exp_f32 v42, v42\n"
    "v_mfma_f32_16x16x32_bf16 a[40:43], v[116:119], v[68:71], a[40:43]\n"
    "v_exp_f32 v43, v43\n"
    "v_mfma_f32_16x16x32_bf16 a[72:75], v[116:119], v[72:75], a[72:75]\n"
    "ds_read_b128 v[112:115], %[rbase] offset:6144\n"
    "v_cvt_pk_bf16_f32 v89, v42, v43\n"
    "v_mfma_f32_16x16x32_bf16 a[104:107], v[116:119], v[76:79], a[104:107]\n"
    "v_mfma_f32_16x16x32_bf16 v[0:3], v[120:123], a[140:143], v[0:3]\n"
    "v_exp_f32 v44, v44\n"
    "v_mfma_f32_16x16x32_bf16 v[4:7], v[120:123], a[156:159], v[4:7]\n"
    "v_exp_f32 v45, v45\n"
    "v_mfma_f32_16x16x32_bf16 v[8:11], v[120:123], a[172:175], v[8:11]\n"
    "ds_read_b128 v[116:119], %[rbase] offset:14336\n"
    "v_cvt_pk_bf16_f32 v92, v44, v45\n"
    "v_mfma_f32_16x16x32_bf16 v[12:15], v[120:123], a[188:191], v[12:15]\n"
    "s_waitcnt vmcnt(16)\n"
    "s_barrier\n"
    "v_mfma_f32_16x16x32_bf16 a[12:15], v[124:127], v[64:67], a[12:15]\n"
    "v_exp_f32 v46, v46\n"
    "v_mfma_f32_16x16x32_bf16 a[44:47], v[124:127], v[68:71], a[44:47]\n"
    "v_exp_f32 v47, v47\n"
    "v_mfma_f32_16x16x32_bf16 a[76:79], v[124:127], v[72:75], a[76:79]\n"
    "ds_read_b128 v[120:123], %[rbase] offset:7168\n"
    "v_cvt_pk_bf16_f32 v93, v46, v47\n"
    "v_mfma_f32_16x16x32_bf16 a[108:111], v[124:127], v[76:79], a[108:111]\n"
    "global_load_lds_dwordx4 v207, s[46:47] offset:0\n"
    "v_mfma_f32_16x16x32_bf16 v[128:131], v[136:139], v[68:71], v[128:131]\n"
    "s_waitcnt lgkmcnt(3)\n"
    "v_mfma_f32_16x16x32_bf16 v[16:19], v[96:99], a[128:131], 0\n"
    "v_exp_f32 v48, v48\n"
    "v_mfma_f32_16x16x32_bf16 v[20:23], v[96:99], a[144:147], 0\n"
    "v_exp_f32 v49, v49\n"
    "v_mfma_f32_16x16x32_bf16 v[24:27], v[96:99], a[160:163], 0\n"
    "ds_read_b128 v[124:127], %[rbase] offset:15360\n"
    "v_cvt_pk_bf16_f32 v82, v48, v49\n"
    "v_mfma_f32_16x16x32_bf16 v[28:31], v[96:99], a[176:179], 0\n"
    "v_mfma_f32_16x16x32_bf16 a[16:19], v[100:103], v[64:67], a[16:19]\n"
    "v_exp_f32 v50, v50\n"
    "v_mfma_f32_16x16x32_bf16 a[48:51], v[100:103], v[68:71], a[48:51]\n"
    "v_exp_f32 v51, v51\n"
    "v_mfma_f32_16x16x32_bf16 a[80:83], v[100:103], v[72:75], a[80:83]\n"
    "ds_read_b128 v[96:99], %[rbase] offset:32768\n"
    "v_cvt_pk_bf16_f32 v83, v50, v51\n"
    "v_mfma_f32_16x16x32_bf16 a[112:115], v[100:103], v[76:79], a[112:115]\n"
    "global_load_lds_dwordx4 v207, s[46:47] offset:1024\n"
    "v_mfma_f32_16x16x32_bf16 v[16:19], v[104:107], a[132:135], v[16:19]\n"
    "v_exp_f32 v52, v52\n"
    "v_mfma_f32_16x16x32_bf16 v[20:23], v[104:107], a[148:151], v[20:23]\n"
    "v_exp_f32 v53, v53\n"
    "v_mfma_f32_16x16x32_bf16 v[24:27], v[104:107], a[164:167], v[24:27]\n"
    "ds_read_b128 v[100:103], %[rbase] offset:40960\n"
    "v_cvt_pk_bf16_f32 v86, v52, v53\n"
    "v_mfma_f32_16x16x32_bf16 v[28:31], v[104:107], a[180:183], v[28:31]\n"
    "v_mfma_f32_16x16x32_bf16 a[20:23], v[108:111], v[64:67], a[20:23]\n"
    "v_exp_f32 v54, v54\n"
    "v_mfma_f32_16x16x32_bf16 a[52:55], v[108:111], v[68:71], a[52:55]\n"
    "v_exp_f32 v55, v55\n"
    "v_mfma_f32_16x16x32_bf16 a[84:87], v[108:111], v[72:75], a[84:87]\n"
    "ds_read_b128 v[104:107], %[rbase] offset:33792\n"
    "v_cvt_pk_bf16_f32 v87, v54, v55\n"
    "v_mfma_f32_16x16x32_bf16 a[116:119], v[108:111], v[76:79], a[116:119]\n"
    "global_load_lds_dwordx4 v207, s[46:47] offset:2048\n"
    ".Lk2_p3:\n\ts_cbranch_execz .Lk2_p7\n"
    "v_mfma_f32_16x16x32_bf16 v[128:131], v[140:143], v[72:75], v[128:131]\n"
    "s_waitcnt lgkmcnt(3)\n"
    "v_mfma_f32_16x16x32_bf16 v[16:19], v[112:115], a[136:139], v[16:19]\n"
    "v_exp_f32 v56, v56\n"
    "v_mfma_f32_16x16x32_bf16 v[20:23], v[112:115], a[152:155], v[20:23]\n"
    "v_exp_f32 v57, v57\n"
    "v_mfma_f32_16x16x32_bf16 v[24:27], v[112:115], a[168:171], v[24:27]\n"
    "ds_read_b128 v[108:111], %[rbase] offset:41984\n"
    "v_cvt_pk_bf16_f32 v90, v56, v57\n"
    "v_mfma_f32_16x16x32_bf16 v[28:31], v[112:115], a[184:187], v[28:31]\n"
    "v_mfma_f32_16x16x32_bf16 a[24:27], v[116:119], v[64:67], a[24:27]\n"
    "v_exp_f32 v58, v58\n"
    "v_mfma_f32_16x16x32_bf16 a[56:59], v[116:119], v[68:71], a[56:59]\n"
    "v_exp_f32 v59, v59\n"
    "v_mfma_f32_16x16x32_bf16 a[88:91], v[116:119], v[72:75], a[88:91]\n"
    "ds_read_b128 v[112:115], %[rbase] offset:34816\n"
    "v_cvt_pk_bf16_f32 v91, v58, v59\n"
    "v_mfma_f32_16x16x32_bf16 a[120:123], v[116:119], v[76:79], a[120:123]\n"
    "global_load_lds_dwordx4 v207, s[46:47] offset:3072\n"
    "s_add_u32 m0, %[wdst0], 98304\n"
    "v_mfma_f32_16x16x32_bf16 v[16:19], v[120:123], a[140:143], v[16:19]\n"
    "v_exp_f32 v60, v60\n"
    "v_mfma_f32_16x16x32_bf16 v[20:23], v[120:123], a[156:159], v[20:23]\n"
    "v_exp_f32 v61, v61\n"
    "v_mfma_f32_16x16x32_bf16 v[24:27], v[120:123], a[172:175], v[24:27]\n"
    "ds_read_b128 v[116:119], %[rbase] offset:43008\n"
    "v_cvt_pk_bf16_f32 v94, v60, v61\n"
    "v_mfma_f32_16x16x32_bf16 v[28:31], v[120:123], a[188:191], v[28:31]\n"
    "s_add_u32 s46, %[khlo], s53\n"
    "s_addc_u32 s47, %[khhi], 0\n"
    "v_mfma_f32_16x16x32_bf16 a[28:31], v[124:127], v[64:67], a[28:31]\n"
    "v_exp_f32 v62, v62\n"
    "v_mfma_f32_16x16x32_bf16 a[60:63], v[124:127], v[68:71], a[60:63]\n"
    "v_exp_f32 v63, v63\n"
    "v_mfma_f32_16x16x32_bf16 a[92:95], v[124:127], v[72:75], a[92:95]\n"
    "ds_read_b128 v[120:123], %[rbase] offset:35840\n"
    "v_cvt_pk_bf16_f32 v95, v62, v63\n"
    "v_mfma_f32_16x16x32_bf16 a[124:127], v[124:127], v[76:79], a[124:127]\n"
    "global_load_lds_dwordx4 v205, s[48:49] offset:0\n"
    "s_waitcnt lgkmcnt(3)\n"
    "v_mfma_f32_16x16x32_bf16 v[128:131], v[144:147], v[76:79], v[128:131]\n"
    "v_mfma_f32_16x16x32_bf16 v[32:35], v[96:99], a[128:131], 0\n"
    "v_exp_f32 v0, v0\n"
    "v_mfma_f32_16x16x32_bf16 v[36:39], v[96:99], a[144:147], 0\n"
    "v_exp_f32 v1, v1\n"
    "v_mfma_f32_16x16x32_bf16 v[40:43], v[96:99], a[160:163], 0\n"
    "ds_read_b128 v[124:127], %[rbase] offset:44032\n"
    "v_cvt_pk_bf16_f32 v64, v0, v1\n"
    "v_mfma_f32_16x16x32_bf16 v[44:47], v[96:99], a[176:179], 0\n"
    "global_load_lds_dwordx4 v205, s[48:49] offset:1024\n"
    "v_mfma_f32_16x16x32_bf16 a[0:3], v[100:103], v[80:83], a[0:3]\n"
    "v_exp_f32 v2, v2\n"
    "v_mfma_f32_16x16x32_bf16 a[32:35], v[100:103], v[84:87], a[32:35]\n"
    "v_exp_f32 v3, v3\n"
    "v_mfma_f32_16x16x32_bf16 a[64:67], v[100:103], v[88:91], a[64:67]\n"
    "ds_read_b128 v[96:99], %[rbase] offset:36864\n"
    "v_cvt_pk_bf16_f32 v65, v2, v3\n"
    "v_mfma_f32_16x16x32_bf16 a[96:99], v[100:103], v[92:95], a[96:99]\n"
    "s_add_u32 s54, %[wslo], %[cw]\n"
    "s_addc_u32 s55, %[wshi], 0\n"
    "v_mfma_f32_16x16x32_bf16 v[32:35], v[104:107], a[132:135], v[32:35]\n"
    "v_exp_f32 v4, v4\n"
    "v_mfma_f32_16x16x32_bf16 v[36:39], v[104:107], a[148:151], v[36:39]\n"
    "v_exp_f32 v5, v5\n"
    "v_mfma_f32_16x16x32_bf16 v[40:43], v[104:107], a[164:167], v[40:43]\n"
    "ds_read_b128 v[100:103], %[rbase] offset:45056\n"
    "v_cvt_pk_bf16_f32 v68, v4, v5\n"
    "v_mfma_f32_16x16x32_bf16 v[44:47], v[104:107], a[180:183], v[44:47]\n"
    "global_load_lds_dwordx4 v205, s[48:49] offset:2048\n"
    "v_mfma_f32_16x16x32_bf16 a[4:7], v[108:111], v[80:83], a[4:7]\n"
    "v_exp_f32 v6, v6\n"
    "v_mfma_f32_16x16x32_bf16 a[36:39], v[108:111], v[84:87], a[36:39]\n"
    "v_exp_f32 v7, v7\n"
    "v_mfma_f32_16x16x32_bf16 a[68:71], v[108:111], v[88:91], a[68:71]\n"
    "ds_read_b128 v[104:107], %[rbase] offset:37888\n"
    "v_cvt_pk_bf16_f32 v69, v6, v7\n"
    "v_mfma_f32_16x16x32_bf16 a[100:103], v[108:111], v[92:95], a[100:103]\n"
    "v_mfma_f32_16x16x32_bf16 v[128:131], v[132:135], v[80:83], v[128:131]\n"
    "s_waitcnt lgkmcnt(3)\n"
    "v_mfma_f32_16x16x32_bf16 v[32:35], v[112:115], a[136:139], v[32:35]\n"
    "v_exp_f32 v8, v8\n"
    "v_mfma_f32_16x16x32_bf16 v[36:39], v[112:115], a[152:155], v[36:39]\n"
    "v_exp_f32 v9, v9\n"
    "v_mfma_f32_16x16x32_bf16 v[40:43], v[112:115], a[168:171], v[40:43]\n"
    "ds_read_b128 v[108:111], %[rbase] offset:46080\n"
    "v_cvt_pk_bf16_f32 v72, v8, v9\n"
    "v_mfma_f32_16x16x32_bf16 v[44:47], v[112:115], a[184:187], v[44:47]\n"
    "global_load_lds_dwordx4 v205, s[48:49] offset:3072\n"
    "s_add_u32 m0, %[wdst0], 8192\n"
    "v_mfma_f32_16x16x32_bf16 a[8:11], v[116:119], v[80:83], a[8:11]\n"
    "v_exp_f32 v10, v10\n"
    "v_mfma_f32_16x16x32_bf16 a[40:43], v[116:119], v[84:87], a[40:43]\n"
    "v_exp_f32 v11, v11\n"
    "v_mfma_f32_16x16x32_bf16 a[72:75], v[116:119], v[88:91], a[72:75]\n"
    "ds_read_b128 v[112:115], %[rbase] offset:38912\n"
    "v_cvt_pk_bf16_f32 v73, v10, v11\n"
    "v_mfma_f32_16x16x32_bf16 a[104:107], v[116:119], v[92:95], a[104:107]\n"
    "v_mfma_f32_16x16x32_bf16 v[32:35], v[120:123], a[140:143], v[32:35]\n"
    "v_exp_f32 v12, v12\n"
    "v_mfma_f32_16x16x32_bf16 v[36:39], v[120:123], a[156:159], v[36:39]\n"
    "v_exp_f32 v13, v13\n"
    "v_mfma_f32_16x16x32_bf16 v[40:43], v[120:123], a[172:175], v[40:43]\n"
    "ds_read_b128 v[116:119], %[rbase] offset:47104\n"
    "v_cvt_pk_bf16_f32 v76, v12, v13\n"
    "v_mfma_f32_16x16x32_bf16 v[44:47], v[120:123], a[188:191], v[44:47]\n"
    "s_waitcnt vmcnt(16)\n"
    "s_barrier\n"
    "v_mfma_f32_16x16x32_bf16 a[12:15], v[124:127], v[80:83], a[12:15]\n"
    "v_exp_f32 v14, v14\n"
    "v_mfma_f32_16x16x32_bf16 a[44:47], v[124:127], v[84:87], a[44:47]\n"
    "v_exp_f32 v15, v15\n"
    "v_mfma_f32_16x16x32_bf16 a[76:79], v[124:127], v[88:91], a[76:79]\n"
    "ds_read_b128 v[120:123], %[rbase] offset:39936\n"
    "v_cvt_pk_bf16_f32 v77, v14, v15\n"
    "v_mfma_f32_16x16x32_bf16 a[108:111], v[124:127], v[92:95], a[108:111]\n"
    "global_load_lds_dwordx4 %[lanev], s[48:49] offset:0\n"
    "s_add_u32 s40, s40, 1\n"
    "v_mfma_f32_16x16x32_bf16 v[128:131], v[136:139], v[84:87], v[128:131]\n"
    "global_store_dword %[rfo], %[cnan], s[54:55] sc1\n"
    "s_waitcnt lgkmcnt(3)\n"
    "v_mfma_f32_16x16x32_bf16 v[48:51], v[96:99], a[128:131], 0\n"
    "v_exp_f32 v16, v16\n"
    "v_mfma_f32_16x16x32_bf16 v[52:55], v[96:99], a[144:147], 0\n"
    "v_exp_f32 v17, v17\n"
    "v_mfma_f32_16x16x32_bf16 v[56:59], v[96:99], a[160:163], 0\n"
    "ds_read_b128 v[124:127], %[rbase] offset:48128\n"
    "v_cvt_pk_bf16_f32 v66, v16, v17\n"
    "v_mfma_f32_16x16x32_bf16 v[60:63], v[96:99], a[176:179], 0\n"
    "v_add_u32 %[rfo], 0xfffe0000, %[rfo]\n"
    "v_mfma_f32_16x16x32_bf16 a[16:19], v[100:103], v[80:83], a[16:19]\n"
    "v_exp_f32 v18, v18\n"
    "v_mfma_f32_16x16x32_bf16 a[48:51], v[100:103], v[84:87], a[48:51]\n"
    "v_exp_f32 v19, v19\n"
    "v_mfma_f32_16x16x32_bf16 a[80:83], v[100:103], v[88:91], a[80:83]\n"
    "ds_read_b128 v[96:99], v204 offset:0\n"
    "v_cvt_pk_bf16_f32 v67, v18, v19\n"
    "v_mfma_f32_16x16x32_bf16 a[112:115], v[100:103], v[92:95], a[112:115]\n"
    "global_load_lds_dwordx4 %[lanev], s[48:49] offset:1024\n"
    "v_mfma_f32_16x16x32_bf16 v[48:51], v[104:107], a[132:135], v[48:51]\n"
    "v_exp_f32 v20, v20\n"
    "v_mfma_f32_16x16x32_bf16 v[52:55], v[104:107], a[148:151], v[52:55]\n"
    "v_exp_f32 v21, v21\n"
    "v_mfma_f32_16x16x32_bf16 v[56:59], v[104:107], a[164:167], v[56:59]\n"
    "ds_read_b128 v[100:103], v204 offset:8192\n"
    "v_cvt_pk_bf16_f32 v70, v20, v21\n"
    "v_mfma_f32_16x16x32_bf16 v[60:63], v[104:107], a[180:183], v[60:63]\n"
    "v_mfma_f32_16x16x32_bf16 a[20:23], v[108:111], v[80:83], a[20:23]\n"
    "v_exp_f32 v22, v22\n"
    "v_mfma_f32_16x16x32_bf16 a[52:55], v[108:111], v[84:87], a[52:55]\n"
    "v_exp_f32 v23, v23\n"
    "v_mfma_f32_16x16x32_bf16 a[84:87], v[108:111], v[88:91], a[84:87]\n"
    "ds_read_b128 v[104:107], v204 offset:1024\n"
    "v_cvt_pk_bf16_f32 v71, v22, v23\n"
    "v_mfma_f32_16x16x32_bf16 a[116:119], v[108:111], v[92:95], a[116:119]\n"
    "global_load_lds_dwordx4 %[lanev], s[48:49] offset:2048\n"
    "v_mfma_f32_16x16x32_bf16 v[128:131], v[140:143], v[88:91], v[128:131]\n"
    "s_waitcnt lgkmcnt(3)\n"
    "v_mfma_f32_16x16x32_bf16 v[48:51], v[112:115], a[136:139], v[48:51]\n"
    "v_exp_f32 v24, v24\n"
    "v_mfma_f32_16x16x32_bf16 v[52:55], v[112:115], a[152:155], v[52:55]\n"
    "v_exp_f32 v25, v25\n"
    "v_mfma_f32_16x16x32_bf16 v[56:59], v[112:115], a[168:171], v[56:59]\n"
    "ds_read_b128 v[108:111], v204 offset:9216\n"
    "v_cvt_pk_bf16_f32 v74, v24, v25\n"
    "v_mfma_f32_16x16x32_bf16 v[60:63], v[112:115], a[184:187], v[60:63]\n"
    "v_mfma_f32_16x16x32_bf16 a[24:27], v[116:119], v[80:83], a[24:27]\n"
    "v_exp_f32 v26, v26\n"
    "v_mfma_f32_16x16x32_bf16 a[56:59], v[116:119], v[84:87], a[56:59]\n"
    "v_exp_f32 v27, v27\n"
    "v_mfma_f32_16x16x32_bf16 a[88:91], v[116:119], v[88:91], a[88:91]\n"
    "ds_read_b128 v[112:115], v204 offset:2048\n"
    "v_cvt_pk_bf16_f32 v75, v26, v27\n"
    "v_mfma_f32_16x16x32_bf16 a[120:123], v[116:119], v[92:95], a[120:123]\n"
    "global_load_lds_dwordx4 %[lanev], s[48:49] offset:3072\n"
    "s_add_u32 m0, %[wdst0], 0\n"
    "v_mfma_f32_16x16x32_bf16 v[48:51], v[120:123], a[140:143], v[48:51]\n"
    "v_exp_f32 v28, v28\n"
    "v_mfma_f32_16x16x32_bf16 v[52:55], v[120:123], a[156:159], v[52:55]\n"
    "v_exp_f32 v29, v29\n"
    "v_mfma_f32_16x16x32_bf16 v[56:59], v[120:123], a[172:175], v[56:59]\n"
    "ds_read_b128 v[116:119], v204 offset:10240\n"
    "v_cvt_pk_bf16_f32 v78, v28, v29\n"
    "v_mfma_f32_16x16x32_bf16 v[60:63], v[120:123], a[188:191], v[60:63]\n"
    "v_mfma_f32_16x16x32_bf16 a[28:31], v[124:127], v[80:83], a[28:31]\n"
    "v_exp_f32 v30, v30\n"
    "v_mfma_f32_16x16x32_bf16 a[60:63], v[124:127], v[84:87], a[60:63]\n"
    "v_exp_f32 v31, v31\n"
    "v_mfma_f32_16x16x32_bf16 a[92:95], v[124:127], v[88:91], a[92:95]\n"
    "ds_read_b128 v[120:123], v204 offset:3072\n"
    "v_cvt_pk_bf16_f32 v79, v30, v31\n"
    "v_mfma_f32_16x16x32_bf16 a[124:127], v[124:127], v[92:95], a[124:127]\n"
    "global_load_lds_dwordx4 %[lane16], s[46:47] offset:0\n"
    "s_waitcnt lgkmcnt(3)\n"
    "v_mfma_f32_16x16x32_bf16 v[128:131], v[144:147], v[92:95], v[128:131]\n"
    "s_cmp_eq_u32 s40, 15\n"
    "s_cbranch_scc1 .Ldia%=\n"
    "s_bitcmp1_b32 %[special], s40\n"
    "s_cbranch_scc1 .Lqaf4%=\n"
    ".Lraf4%=:\n"
    "v_mfma_f32_16x16x32_bf16 v[0:3], v[96:99], a[128:131], 0\n"
    "v_exp_f32 v32, v32\n"
    "v_mfma_f32_16x16x32_bf16 v[4:7], v[96:99], a[144:147], 0\n"
    "v_exp_f32 v33, v33\n"
    "v_mfma_f32_16x16x32_bf16 v[8:11], v[96:99], a[160:163], 0\n"
    "ds_read_b128 v[124:127], v204 offset:11264\n"
    "v_cvt_pk_bf16_f32 v80, v32, v33\n"
    "v_mfma_f32_16x16x32_bf16 v[12:15], v[96:99], a[176:179], 0\n"
    "global_load_lds_dwordx4 %[lane16], s[46:47] offset:1024\n"
    "s_add_u32 s57, s40, 2\n"
    "v_mfma_f32_16x16x32_bf16 a[0:3], v[100:103], v[64:67], a[0:3]\n"
    "v_exp_f32 v34, v34\n"
    "v_mfma_f32_16x16x32_bf16 a[32:35], v[100:103], v[68:71], a[32:35]\n"
    "v_exp_f32 v35, v35\n"
    "v_mfma_f32_16x16x32_bf16 a[64:67], v[100:103], v[72:75], a[64:67]\n"
    "ds_read_b128 v[96:99], v204 offset:4096\n"
    "v_cvt_pk_bf16_f32 v81, v34, v35\n"
    "v_mfma_f32_16x16x32_bf16 a[96:99], v[100:103], v[76:79], a[96:99]\n"
    "s_min_u32 s57, s57, 16\n"
    "v_mfma_f32_16x16x32_bf16 v[0:3], v[104:107], a[132:135], v[0:3]\n"
    "v_exp_f32 v36, v36\n"
    "v_mfma_f32_16x16x32_bf16 v[4:7], v[104:107], a[148:151], v[4:7]\n"
    "v_exp_f32 v37, v37\n"
    "v_mfma_f32_16x16x32_bf16 v[8:11], v[104:107], a[164:167], v[8:11]\n"
    "ds_read_b128 v[100:103], v204 offset:12288\n"
    "v_cvt_pk_bf16_f32 v84, v36, v37\n"
    "v_mfma_f32_16x16x32_bf16 v[12:15], v[104:107], a[180:183], v[12:15]\n"
    "global_load_lds_dwordx4 %[lane16], s[46:47] offset:2048\n"
    "v_mfma_f32_16x16x32_bf16 a[4:7], v[108:111], v[64:67], a[4:7]\n"
    "v_exp_f32 v38, v38\n"
    "v_mfma_f32_16x16x32_bf16 a[36:39], v[108:111], v[68:71], a[36:39]\n"
    "v_exp_f32 v39, v39\n"
    "v_mfma_f32_16x16x32_bf16 a[68:71], v[108:111], v[72:75], a[68:71]\n"
    "ds_read_b128 v[104:107], v204 offset:5120\n"
    "v_cvt_pk_bf16_f32 v85, v38, v39\n"
    "v_mfma_f32_16x16x32_bf16 a[100:103], v[108:111], v[76:79], a[100:103]\n"
    "s_cmp_lt_i32 s57, %[na]\n"
    "s_cselect_b32 s53, %[kt0], s45\n"
    "v_mfma_f32_16x16x32_bf16 v[128:131], v[132:135], v[64:67], v[128:131]\n"
    "s_add_i32 s53, s53, s57\n"
    "s_waitcnt lgkmcnt(3)\n"
    "v_mfma_f32_16x16x32_bf16 v[0:3], v[112:115], a[136:139], v[0:3]\n"
    "v_exp_f32 v40, v40\n"
    "v_mfma_f32_16x16x32_bf16 v[4:7], v[112:115], a[152:155], v[4:7]\n"
    "v_exp_f32 v41, v41\n"
    "v_mfma_f32_16x16x32_bf16 v[8:11], v[112:115], a[168:171], v[8:11]\n"
    "ds_read_b128 v[108:111], v204 offset:13312\n"
    "v_cvt_pk_bf16_f32 v88, v40, v41\n"
    "v_mfma_f32_16x16x32_bf16 v[12:15], v[112:115], a[184:187], v[12:15]\n"
    "global_load_lds_dwordx4 %[lane16], s[46:47] offset:3072\n"
    "s_add_u32 m0, %[wdst0], 40960\n"
    "v_mfma_f32_16x16x32_bf16 a[8:11], v[116:119], v[64:67], a[8:11]\n"
    "v_exp_f32 v42, v42\n"
    "v_mfma_f32_16x16x32_bf16 a[40:43], v[116:119], v[68:71], a[40:43]\n"
    "v_exp_f32 v43, v43\n"
    "v_mfma_f32_16x16x32_bf16 a[72:75], v[116:119], v[72:75], a[72:75]\n"
    "ds_read_b128 v[112:115], v204 offset:6144\n"
    "v_cvt_pk_bf16_f32 v89, v42, v43\n"
    "v_mfma_f32_16x16x32_bf16 a[104:107], v[116:119], v[76:79], a[104:107]\n"
    "s_lshl_b32 s53, s53, 14\n"
    "v_mfma_f32_16x16x32_bf16 v[0:3], v[120:123], a[140:143], v[0:3]\n"
    "v_exp_f32 v44, v44\n"
    "v_mfma_f32_16x16x32_bf16 v[4:7], v[120:123], a[156:159], v[4:7]\n"
    "v_exp_f32 v45, v45\n"
    "v_mfma_f32_16x16x32_bf16 v[8:11], v[120:123], a[172:175], v[8:11]\n"
    "ds_read_b128 v[116:119], v204 offset:14336\n"
    "v_cvt_pk_bf16_f32 v92, v44, v45\n"
    "v_mfma_f32_16x16x32_bf16 v[12:15], v[120:123], a[188:191], v[12:15]\n"
    "s_waitcnt vmcnt(9)\n"
    "s_barrier\n"
    "v_mfma_f32_16x16x32_bf16 a[12:15], v[124:127], v[64:67], a[12:15]\n"
    "v_exp_f32 v46, v46\n"
    "v_mfma_f32_16x16x32_bf16 a[44:47], v[124:127], v[68:71], a[44:47]\n"
    "v_exp_f32 v47, v47\n"
    "v_mfma_f32_16x16x32_bf16 a[76:79], v[124:127], v[72:75], a[76:79]\n"
    "ds_read_b128 v[120:123], v204 offset:7168\n"
    "v_cvt_pk_bf16_f32 v93, v46, v47\n"
    "v_mfma_f32_16x16x32_bf16 a[108:111], v[124:127], v[76:79], a[108:111]\n"
    "global_load_lds_dwordx4 v207, s[48:49] offset:0\n"
    "v_mfma_f32_16x16x32_bf16 v[128:131], v[136:139], v[68:71], v[128:131]\n"
    "s_waitcnt lgkmcnt(3)\n"
    "v_mfma_f32_16x16x32_bf16 v[16:19], v[96:99], a[128:131], 0\n"
    "v_exp_f32 v48, v48\n"
    "v_mfma_f32_16x16x32_bf16 v[20:23], v[96:99], a[144:147], 0\n"
    "v_exp_f32 v49, v49\n"
    "v_mfma_f32_16x16x32_bf16 v[24:27], v[96:99], a[160:163], 0\n"
    "ds_read_b128 v[124:127], v204 offset:15360\n"
    "v_cvt_pk_bf16_f32 v82, v48, v49\n"
    "v_mfma_f32_16x16x32_bf16 v[28:31], v[96:99], a[176:179], 0\n"
    "v_mfma_f32_16x16x32_bf16 a[16:19], v[100:103], v[64:67], a[16:19]\n"
    "v_exp_f32 v50, v50\n"
    "v_mfma_f32_16x16x32_bf16 a[48:51], v[100:103], v[68:71], a[48:51]\n"
    "v_exp_f32 v51, v51\n"
    "v_mfma_f32_16x16x32_bf16 a[80:83], v[100:103], v[72:75], a[80:83]\n"
    "ds_read_b128 v[96:99], v204 offset:32768\n"
    "v_cvt_pk_bf16_f32 v83, v50, v51\n"
    "v_mfma_f32_16x16x32_bf16 a[112:115], v[100:103], v[76:79], a[112:115]\n"
    "global_load_lds_dwordx4 v207, s[48:49] offset:1024\n"
    "v_mfma_f32_16x16x32_bf16 v[16:19], v[104:107], a[132:135], v[16:19]\n"
    "v_exp_f32 v52, v52\n"
    "v_mfma_f32_16x16x32_bf16 v[20:23], v[104:107], a[148:151], v[20:23]\n"
    "v_exp_f32 v53, v53\n"
    "v_mfma_f32_16x16x32_bf16 v[24:27], v[104:107], a[164:167], v[24:27]\n"
    "ds_read_b128 v[100:103], v204 offset:40960\n"
    "v_cvt_pk_bf16_f32 v86, v52, v53\n"
    "v_mfma_f32_16x16x32_bf16 v[28:31], v[104:107], a[180:183], v[28:31]\n"
    "v_mfma_f32_16x16x32_bf16 a[20:23], v[108:111], v[64:67], a[20:23]\n"
    "v_exp_f32 v54, v54\n"
    "v_mfma_f32_16x16x32_bf16 a[52:55], v[108:111], v[68:71], a[52:55]\n"
    "v_exp_f32 v55, v55\n"
    "v_mfma_f32_16x16x32_bf16 a[84:87], v[108:111], v[72:75], a[84:87]\n"
    "ds_read_b128 v[104:107], v204 offset:33792\n"
    "v_cvt_pk_bf16_f32 v87, v54, v55\n"
    "v_mfma_f32_16x16x32_bf16 a[116:119], v[108:111], v[76:79], a[116:119]\n"
    "global_load_lds_dwordx4 v207, s[48:49] offset:2048\n"
    "v_mfma_f32_16x16x32_bf16 v[128:131], v[140:143], v[72:75], v[128:131]\n"
    "s_waitcnt lgkmcnt(3)\n"
    "v_mfma_f32_16x16x32_bf16 v[16:19], v[112:115], a[136:139], v[16:19]\n"
    "v_exp_f32 v56, v56\n"
    "v_mfma_f32_16x16x32_bf16 v[20:23], v[112:115], a[152:155], v[20:23]\n"
    "v_exp_f32 v57, v57\n"
    "v_mfma_f32_16x16x32_bf16 v[24:27], v[112:115], a[168:171], v[24:27]\n"
    "ds_read_b128 v[108:111], v204 offset:41984\n"
    "v_cvt_pk_bf16_f32 v90, v56, v57\n"
    "v_mfma_f32_16x16x32_bf16 v[28:31], v[112:115], a[184:187], v[28:31]\n"
    "v_mfma_f32_16x16x32_bf16 a[24:27], v[116:119], v[64:67], a[24:27]\n"
    "v_exp_f32 v58, v58\n"
    "v_mfma_f32_16x16x32_bf16 a[56:59], v[116:119], v[68:71], a[56:59]\n"
    "v_exp_f32 v59, v59\n"
    "v_mfma_f32_16x16x32_bf16 a[88:91], v[116:119], v[72:75], a[88:91]\n"
    "ds_read_b128 v[112:115], v204 offset:34816\n"
    "v_cvt_pk_bf16_f32 v91, v58, v59\n"
    "v_mfma_f32_16x16x32_bf16 a[120:123], v[116:119], v[76:79], a[120:123]\n"
    "global_load_lds_dwordx4 v207, s[48:49] offset:3072\n"
    "s_add_u32 m0, %[wdst0], 32768\n"
    "v_mfma_f32_16x16x32_bf16 v[16:19], v[120:123], a[140:143], v[16:19]\n"
    "v_exp_f32 v60, v60\n"
    "v_mfma_f32_16x16x32_bf16 v[20:23], v[120:123], a[156:159], v[20:23]\n"
    "v_exp_f32 v61, v61\n"
    "v_mfma_f32_16x16x32_bf16 v[24:27], v[120:123], a[172:175], v[24:27]\n"
    "ds_read_b128 v[116:119], v204 offset:43008\n"
    "v_cvt_pk_bf16_f32 v94, v60, v61\n"
    "v_mfma_f32_16x16x32_bf16 v[28:31], v[120:123], a[188:191], v[28:31]\n"
    "s_add_u32 s48, %[khlo], s53\n"
    "s_addc_u32 s49, %[khhi], 0\n"
    "v_mfma_f32_16x16x32_bf16 a[28:31], v[124:127], v[64:67], a[28:31]\n"
    "v_exp_f32 v62, v62\n"
    "v_mfma_f32_16x16x32_bf16 a[60:63], v[124:127], v[68:71], a[60:63]\n"
    "v_exp_f32 v63, v63\n"
    "v_mfma_f32_16x16x32_bf16 a[92:95], v[124:127], v[72:75], a[92:95]\n"
    "ds_read_b128 v[120:123], v204 offset:35840\n"
    "v_cvt_pk_bf16_f32 v95, v62, v63\n"
    "v_mfma_f32_16x16x32_bf16 a[124:127], v[124:127], v[76:79], a[124:127]\n"
    "global_load_lds_dwordx4 v205, s[46:47] offset:0\n"
    "s_waitcnt lgkmcnt(3)\n"
    "v_mfma_f32_16x16x32_bf16 v[128:131], v[144:147], v[76:79], v[128:131]\n"
    "v_mfma_f32_16x16x32_bf16 v[32:35], v[96:99], a[128:131], 0\n"
    "v_exp_f32 v0, v0\n"
    "v_mfma_f32_16x16x32_bf16 v[36:39], v[96:99], a[144:147], 0\n"
    "v_exp_f32 v1, v1\n"
    "v_mfma_f32_16x16x32_bf16 v[40:43], v[96:99], a[160:163], 0\n"
    "ds_read_b128 v[124:127], v204 offset:44032\n"
    "v_cvt_pk_bf16_f32 v64, v0, v1\n"
    "v_mfma_f32_16x16x32_bf16 v[44:47], v[96:99], a[176:179], 0\n"
    "global_load_lds_dwordx4 v205, s[46:47] offset:1024\n"
    "s_waitcnt vmcnt(23)\n"
    "v_mfma_f32_16x16x32_bf16 a[0:3], v[100:103], v[80:83], a[0:3]\n"
    "v_exp_f32 v2, v2\n"
    "v_mfma_f32_16x16x32_bf16 a[32:35], v[100:103], v[84:87], a[32:35]\n"
    "v_exp_f32 v3, v3\n"
    "v_mfma_f32_16x16x32_bf16 a[64:67], v[100:103], v[88:91], a[64:67]\n"
    "ds_read_b128 v[96:99], v204 offset:36864\n"
    "v_cvt_pk_bf16_f32 v65, v2, v3\n"
    "v_mfma_f32_16x16x32_bf16 a[96:99], v[100:103], v[92:95], a[96:99]\n"
    "v_readlane_b32 s58, v218, 8\n"
    "v_perm_b32 v164, v150, v148, s60\n"
    "v_mfma_f32_16x16x32_bf16 v[32:35], v[104:107], a[132:135], v[32:35]\n"
    "v_exp_f32 v4, v4\n"
    "v_mfma_f32_16x16x32_bf16 v[36:39], v[104:107], a[148:151], v[36:39]\n"
    "v_exp_f32 v5, v5\n"
    "v_mfma_f32_16x16x32_bf16 v[40:43], v[104:107], a[164:167], v[40:43]\n"
    "ds_read_b128 v[100:103], v204 offset:45056\n"
    "v_cvt_pk_bf16_f32 v68, v4, v5\n"
    "v_mfma_f32_16x16x32_bf16 v[44:47], v[104:107], a[180:183], v[44:47]\n"
    "global_load_lds_dwordx4 v205, s[46:47] offset:2048\n"
    "v_perm_b32 v165, v154, v152, s60\n"
    "v_mfma_f32_16x16x32_bf16 a[4:7], v[108:111], v[80:83], a[4:7]\n"
    "v_exp_f32 v6, v6\n"
    "v_mfma_f32_16x16x32_bf16 a[36:39], v[108:111], v[84:87], a[36:39]\n"
    "v_exp_f32 v7, v7\n"
    "v_mfma_f32_16x16x32_bf16 a[68:71], v[108:111], v[88:91], a[68:71]\n"
    "ds_read_b128 v[104:107], v204 offset:37888\n"
    "v_cvt_pk_bf16_f32 v69, v6, v7\n"
    "v_mfma_f32_16x16x32_bf16 a[100:103], v[108:111], v[92:95], a[100:103]\n"
    "v_perm_b32 v166, v158, v156, s60\n"
    "v_perm_b32 v167, v162, v160, s60\n"
    "v_mfma_f32_16x16x32_bf16 v[128:131], v[132:135], v[80:83], v[128:131]\n"
    "s_waitcnt lgkmcnt(3)\n"
    "v_mfma_f32_16x16x32_bf16 v[32:35], v[112:115], a[136:139], v[32:35]\n"
    "v_exp_f32 v8, v8\n"
    "v_mfma_f32_16x16x32_bf16 v[36:39], v[112:115], a[152:155], v[36:39]\n"
    "v_exp_f32 v9, v9\n"
    "v_mfma_f32_16x16x32_bf16 v[40:43], v[112:115], a[168:171], v[40:43]\n"
    "ds_read_b128 v[108:111], v204 offset:46080\n"
    "v_cvt_pk_bf16_f32 v72, v8, v9\n"
    "v_mfma_f32_16x16x32_bf16 v[44:47], v[112:115], a[184:187], v[44:47]\n"
    "global_load_lds_dwordx4 v205, s[46:47] offset:3072\n"
    "s_add_u32 m0, %[wdst0], 73728\n"
    "v_mfma_f32_16x16x32_bf16 a[8:11], v[116:119], v[80:83], a[8:11]\n"
    "v_exp_f32 v10, v10\n"
    "v_mfma_f32_16x16x32_bf16 a[40:43], v[116:119], v[84:87], a[40:43]\n"
    "v_exp_f32 v11, v11\n"
    "v_mfma_f32_16x16x32_bf16 a[72:75], v[116:119], v[88:91], a[72:75]\n"
    "ds_read_b128 v[112:115], v204 offset:38912\n"
    "v_cvt_pk_bf16_f32 v73, v10, v11\n"
    "v_mfma_f32_16x16x32_bf16 a[104:107], v[116:119], v[92:95], a[104:107]\n"
    "s_add_u32 s58, %[wslo], s58\n"
    "s_addc_u32 s59, %[wshi], 0\n"
    "v_mfma_f32_16x16x32_bf16 v[32:35], v[120:123], a[140:143], v[32:35]\n"
    "v_exp_f32 v12, v12\n"
    "v_mfma_f32_16x16x32_bf16 v[36:39], v[120:123], a[156:159], v[36:39]\n"
    "v_exp_f32 v13, v13\n"
    "v_mfma_f32_16x16x32_bf16 v[40:43], v[120:123], a[172:175], v[40:43]\n"
    "ds_read_b128 v[116:119], v204 offset:47104\n"
    "v_cvt_pk_bf16_f32 v76, v12, v13\n"
    "v_mfma_f32_16x16x32_bf16 v[44:47], v[120:123], a[188:191], v[44:47]\n"
    "s_waitcnt vmcnt(8)\n"
    "s_barrier\n"
    "v_mfma_f32_16x16x32_bf16 a[12:15], v[124:127], v[80:83], a[12:15]\n"
    "v_exp_f32 v14, v14\n"
    "v_mfma_f32_16x16x32_bf16 a[44:47], v[124:127], v[84:87], a[44:47]\n"
    "v_exp_f32 v15, v15\n"
    "v_mfma_f32_16x16x32_bf16 a[76:79], v[124:127], v[88:91], a[76:79]\n"
    "ds_read_b128 v[120:123], v204 offset:39936\n"
    "v_cvt_pk_bf16_f32 v77, v14, v15\n"
    "v_mfma_f32_16x16x32_bf16 a[108:111], v[124:127], v[92:95], a[108:111]\n"
    "global_load_lds_dwordx4 %[lanev], s[46:47] offset:0\n"
    "s_add_u32 s40, s40, 1\n"
    "v_mfma_f32_16x16x32_bf16 v[128:131], v[136:139], v[84:87], v[128:131]\n"
    "global_store_dwordx4 %[lane16], v[164:167], s[58:59] sc1\n"
    "s_waitcnt lgkmcnt(3)\n"
    "v_mfma_f32_16x16x32_bf16 v[48:51], v[96:99], a[128:131], 0\n"
    "v_exp_f32 v16, v16\n"
    "v_mfma_f32_16x16x32_bf16 v[52:55], v[96:99], a[144:147], 0\n"
    "v_exp_f32 v17, v17\n"
    "v_mfma_f32_16x16x32_bf16 v[56:59], v[96:99], a[160:163], 0\n"
    "ds_read_b128 v[124:127], v204 offset:48128\n"
    "v_cvt_pk_bf16_f32 v66, v16, v17\n"
    "v_mfma_f32_16x16x32_bf16 v[60:63], v[96:99], a[176:179], 0\n"
    "v_readlane_b32 s54, v218, 9\n"
    "v_perm_b32 v168, v150, v148, s61\n"
    "v_mfma_f32_16x16x32_bf16 a[16:19], v[100:103], v[80:83], a[16:19]\n"
    "v_exp_f32 v18, v18\n"
    "v_mfma_f32_16x16x32_bf16 a[48:51], v[100:103], v[84:87], a[48:51]\n"
    "v_exp_f32 v19, v19\n"
    "v_mfma_f32_16x16x32_bf16 a[80:83], v[100:103], v[88:91], a[80:83]\n"
    "ds_read_b128 v[96:99], %[rbase] offset:0\n"
    "v_cvt_pk_bf16_f32 v67, v18, v19\n"
    "v_mfma_f32_16x16x32_bf16 a[112:115], v[100:103], v[92:95], a[112:115]\n"
    "global_load_lds_dwordx4 %[lanev], s[46:47] offset:1024\n"
    "v_perm_b32 v169, v154, v152, s61\n"
    "v_mfma_f32_16x16x32_bf16 v[48:51], v[104:107], a[132:135], v[48:51]\n"
    "v_exp_f32 v20, v20\n"
    "v_mfma_f32_16x16x32_bf16 v[52:55], v[104:107], a[148:151], v[52:55]\n"
    "v_exp_f32 v21, v21\n"
    "v_mfma_f32_16x16x32_bf16 v[56:59], v[104:107], a[164:167], v[56:59]\n"
    "ds_read_b128 v[100:103], %[rbase] offset:8192\n"
    "v_cvt_pk_bf16_f32 v70, v20, v21\n"
    "v_mfma_f32_16x16x32_bf16 v[60:63], v[104:107], a[180:183], v[60:63]\n"
    "v_perm_b32 v170, v158, v156, s61\n"
    "v_perm_b32 v171, v162, v160, s61\n"
    "v_mfma_f32_16x16x32_bf16 a[20:23], v[108:111], v[80:83], a[20:23]\n"
    "v_exp_f32 v22, v22\n"
    "v_mfma_f32_16x16x32_bf16 a[52:55], v[108:111], v[84:87], a[52:55]\n"
    "v_exp_f32 v23, v23\n"
    "v_mfma_f32_16x16x32_bf16 a[84:87], v[108:111], v[88:91], a[84:87]\n"
    "ds_read_b128 v[104:107], %[rbase] offset:1024\n"
    "v_cvt_pk_bf16_f32 v71, v22, v23\n"
    "v_mfma_f32_16x16x32_bf16 a[116:119], v[108:111], v[92:95], a[116:119]\n"
    "global_load_lds_dwordx4 %[lanev], s[46:47] offset:2048\n"
    "v_mfma_f32_16x16x32_bf16 v[128:131], v[140:143], v[88:91], v[128:131]\n"
    "s_waitcnt lgkmcnt(3)\n"
    "v_mfma_f32_16x16x32_bf16 v[48:51], v[112:115], a[136:139], v[48:51]\n"
    "v_exp_f32 v24, v24\n"
    "v_mfma_f32_16x16x32_bf16 v[52:55], v[112:115], a[152:155], v[52:55]\n"
    "v_exp_f32 v25, v25\n"
    "v_mfma_f32_16x16x32_bf16 v[56:59], v[112:115], a[168:171], v[56:59]\n"
    "ds_read_b128 v[108:111], %[rbase] offset:9216\n"
    "v_cvt_pk_bf16_f32 v74, v24, v25\n"
    "v_mfma_f32_16x16x32_bf16 v[60:63], v[112:115], a[184:187], v[60:63]\n"
    "s_add_u32 s54, %[wslo], s54\n"
    "s_addc_u32 s55, %[wshi], 0\n"
    "v_mfma_f32_16x16x32_bf16 a[24:27], v[116:119], v[80:83], a[24:27]\n"
    "v_exp_f32 v26, v26\n"
    "v_mfma_f32_16x16x32_bf16 a[56:59], v[116:119], v[84:87], a[56:59]\n"
    "v_exp_f32 v27, v27\n"
    "v_mfma_f32_16x16x32_bf16 a[88:91], v[116:119], v[88:91], a[88:91]\n"
    "ds_read_b128 v[112:115], %[rbase] offset:2048\n"
    "v_cvt_pk_bf16_f32 v75, v26, v27\n"
    "v_mfma_f32_16x16x32_bf16 a[120:123], v[116:119], v[92:95], a[120:123]\n"
    "global_load_lds_dwordx4 %[lanev], s[46:47] offset:3072\n"
    "s_add_u32 m0, %[wdst0], 65536\n"
    "v_mfma_f32_16x16x32_bf16 v[48:51], v[120:123], a[140:143], v[48:51]\n"
    "v_exp_f32 v28, v28\n"
    "v_mfma_f32_16x16x32_bf16 v[52:55], v[120:123], a[156:159], v[52:55]\n"
    "v_exp_f32 v29, v29\n"
    "v_mfma_f32_16x16x32_bf16 v[56:59], v[120:123], a[172:175], v[56:59]\n"
    "ds_read_b128 v[116:119], %[rbase] offset:10240\n"
    "v_cvt_pk_bf16_f32 v78, v28, v29\n"
    "v_mfma_f32_16x16x32_bf16 v[60:63], v[120:123], a[188:191], v[60:63]\n"
    "global_store_dwordx4 %[lane16], v[168:171], s[54:55] sc1\n"
    "v_mfma_f32_16x16x32_bf16 a[28:31], v[124:127], v[80:83], a[28:31]\n"
    "v_exp_f32 v30, v30\n"
    "v_mfma_f32_16x16x32_bf16 a[60:63], v[124:127], v[84:87], a[60:63]\n"
    "v_exp_f32 v31, v31\n"
    "v_mfma_f32_16x16x32_bf16 a[92:95], v[124:127], v[88:91], a[92:95]\n"
    "ds_read_b128 v[120:123], %[rbase] offset:3072\n"
    "v_cvt_pk_bf16_f32 v79, v30, v31\n"
    "v_mfma_f32_16x16x32_bf16 a[124:127], v[124:127], v[92:95], a[124:127]\n"
    "global_load_lds_dwordx4 %[lane16], s[48:49] offset:0\n"
    "s_waitcnt lgkmcnt(3)\n"
    "v_mfma_f32_16x16x32_bf16 v[128:131], v[144:147], v[92:95], v[128:131]\n"
    "s_bitcmp1_b32 %[special], s40\n"
    "s_cbranch_scc1 .Lqaf6%=\n"
    ".Lraf6%=:\n"
    "v_mfma_f32_16x16x32_bf16 v[0:3], v[96:99], a[128:131], 0\n"
    "v_exp_f32 v32, v32\n"
    "v_mfma_f32_16x16x32_bf16 v[4:7], v[96:99], a[144:147], 0\n"
    "v_exp_f32 v33, v33\n"
    "v_mfma_f32_16x16x32_bf16 v[8:11], v[96:99], a[160:163], 0\n"
    "ds_read_b128 v[124:127], %[rbase] offset:11264\n"
    "v_cvt_pk_bf16_f32 v80, v32, v33\n"
    "v_mfma_f32_16x16x32_bf16 v[12:15], v[96:99], a[176:179], 0\n"
    "global_load_lds_dwordx4 %[lane16], s[48:49] offset:1024\n"
    "s_add_u32 s57, s40, 2\n"
    "v_mfma_f32_16x16x32_bf16 a[0:3], v[100:103], v[64:67], a[0:3]\n"
    "v_exp_f32 v34, v34\n"
    "v_mfma_f32_16x16x32_bf16 a[32:35], v[100:103], v[68:71], a[32:35]\n"
    "v_exp_f32 v35, v35\n"
    "v_mfma_f32_16x16x32_bf16 a[64:67], v[100:103], v[72:75], a[64:67]\n"
    "ds_read_b128 v[96:99], %[rbase] offset:4096\n"
    "v_cvt_pk_bf16_f32 v81, v34, v35\n"
    "v_mfma_f32_16x16x32_bf16 a[96:99], v[100:103], v[76:79], a[96:99]\n"
    "s_min_u32 s57, s57, 16\n"
    "v_mfma_f32_16x16x32_bf16 v[0:3], v[104:107], a[132:135], v[0:3]\n"
    "v_exp_f32 v36, v36\n"
    "v_mfma_f32_16x16x32_bf16 v[4:7], v[104:107], a[148:151], v[4:7]\n"
    "v_exp_f32 v37, v37\n"
    "v_mfma_f32_16x16x32_bf16 v[8:11], v[104:107], a[164:167], v[8:11]\n"
    "ds_read_b128 v[100:103], %[rbase] offset:12288\n"
    "v_cvt_pk_bf16_f32 v84, v36, v37\n"
    "v_mfma_f32_16x16x32_bf16 v[12:15], v[104:107], a[180:183], v[12:15]\n"
    "global_load_lds_dwordx4 %[lane16], s[48:49] offset:2048\n"
    "v_readlane_b32 s58, v218, 10\n"
    "v_mfma_f32_16x16x32_bf16 a[4:7], v[108:111], v[64:67], a[4:7]\n"
    "v_exp_f32 v38, v38\n"
    "v_mfma_f32_16x16x32_bf16 a[36:39], v[108:111], v[68:71], a[36:39]\n"
    "v_exp_f32 v39, v39\n"
    "v_mfma_f32_16x16x32_bf16 a[68:71], v[108:111], v[72:75], a[68:71]\n"
    "ds_read_b128 v[104:107], %[rbase] offset:5120\n"
    "v_cvt_pk_bf16_f32 v85, v38, v39\n"
    "v_mfma_f32_16x16x32_bf16 a[100:103], v[108:111], v[76:79], a[100:103]\n"
    "s_cmp_lt_i32 s57, %[na]\n"
    "s_cselect_b32 s53, %[kt0], s45\n"
    "v_mfma_f32_16x16x32_bf16 v[128:131], v[132:135], v[64:67], v[128:131]\n"
    "s_add_i32 s53, s53, s57\n"
    "s_waitcnt lgkmcnt(3)\n"
    "v_mfma_f32_16x16x32_bf16 v[0:3], v[112:115], a[136:139], v[0:3]\n"
    "v_exp_f32 v40, v40\n"
    "v_mfma_f32_16x16x32_bf16 v[4:7], v[112:115], a[152:155], v[4:7]\n"
    "v_exp_f32 v41, v41\n"
    "v_mfma_f32_16x16x32_bf16 v[8:11], v[112:115], a[168:171], v[8:11]\n"
    "ds_read_b128 v[108:111], %[rbase] offset:13312\n"
    "v_cvt_pk_bf16_f32 v88, v40, v41\n"
    "v_mfma_f32_16x16x32_bf16 v[12:15], v[112:115], a[184:187], v[12:15]\n"
    "global_load_lds_dwordx4 %[lane16], s[48:49] offset:3072\n"
    "s_add_u32 m0, %[wdst0], 106496\n"
    "v_mfma_f32_16x16x32_bf16 a[8:11], v[116:119], v[64:67], a[8:11]\n"
    "v_exp_f32 v42, v42\n"
    "v_mfma_f32_16x16x32_bf16 a[40:43], v[116:119], v[68:71], a[40:43]\n"
    "v_exp_f32 v43, v43\n"
    "v_mfma_f32_16x16x32_bf16 a[72:75], v[116:119], v[72:75], a[72:75]\n"
    "ds_read_b128 v[112:115], %[rbase] offset:6144\n"
    "v_cvt_pk_bf16_f32 v89, v42, v43\n"
    "v_mfma_f32_16x16x32_bf16 a[104:107], v[116:119], v[76:79], a[104:107]\n"
    "s_lshl_b32 s53, s53, 14\n"
    "v_perm_b32 v164, v151, v149, s60\n"
    "v_mfma_f32_16x16x32_bf16 v[0:3], v[120:123], a[140:143], v[0:3]\n"
    "v_exp_f32 v44, v44\n"
    "v_mfma_f32_16x16x32_bf16 v[4:7], v[120:123], a[156:159], v[4:7]\n"
    "v_exp_f32 v45, v45\n"
    "v_mfma_f32_16x16x32_bf16 v[8:11], v[120:123], a[172:175], v[8:11]\n"
    "ds_read_b128 v[116:119], %[rbase] offset:14336\n"
    "v_cvt_pk_bf16_f32 v92, v44, v45\n"
    "v_mfma_f32_16x16x32_bf16 v[12:15], v[120:123], a[188:191], v[12:15]\n"
    "s_waitcnt vmcnt(10)\n"
    "s_barrier\n"
    "v_mfma_f32_16x16x32_bf16 a[12:15], v[124:127], v[64:67], a[12:15]\n"
    "v_exp_f32 v46, v46\n"
    "v_mfma_f32_16x16x32_bf16 a[44:47], v[124:127], v[68:71], a[44:47]\n"
    "v_exp_f32 v47, v47\n"
    ".Lk2_p4:\n\ts_cbranch_execz .Lk2_p8\n"
    "v_mfma_f32_16x16x32_bf16 a[76:79], v[124:127], v[72:75], a[76:79]\n"
    "ds_read_b128 v[120:123], %[rbase] offset:7168\n"
    "v_cvt_pk_bf16_f32 v93, v46, v47\n"
    "v_mfma_f32_16x16x32_bf16 a[108:111], v[124:127], v[76:79], a[108:111]\n"
    "global_load_lds_dwordx4 v207, s[46:47] offset:0\n"
    "v_perm_b32 v165, v155, v153, s60\n"
    "v_mfma_f32_16x16x32_bf16 v[128:131], v[136:139], v[68:71], v[128:131]\n"
    "v_perm_b32 v166, v159, v157, s60\n"
    "s_waitcnt lgkmcnt(3)\n"
    "v_mfma_f32_16x16x32_bf16 v[16:19], v[96:99], a[128:131], 0\n"
    "v_exp_f32 v48, v48\n"
    "v_mfma_f32_16x16x32_bf16 v[20:23], v[96:99], a[144:147], 0\n"
    "v_exp_f32 v49, v49\n"
    "v_mfma_f32_16x16x32_bf16 v[24:27], v[96:99], a[160:163], 0\n"
    "ds_read_b128 v[124:127], %[rbase] offset:15360\n"
    "v_cvt_pk_bf16_f32 v82, v48, v49\n"
    "v_mfma_f32_16x16x32_bf16 v[28:31], v[96:99], a[176:179], 0\n"
    "v_perm_b32 v167, v163, v161, s60\n"
    "v_mfma_f32_16x16x32_bf16 a[16:19], v[100:103], v[64:67], a[16:19]\n"
    "v_exp_f32 v50, v50\n"
    "v_mfma_f32_16x16x32_bf16 a[48:51], v[100:103], v[68:71], a[48:51]\n"
    "v_exp_f32 v51, v51\n"
    "v_mfma_f32_16x16x32_bf16 a[80:83], v[100:103], v[72:75], a[80:83]\n"
    "ds_read_b128 v[96:99], %[rbase] offset:32768\n"
    "v_cvt_pk_bf16_f32 v83, v50, v51\n"
    "v_mfma_f32_16x16x32_bf16 a[112:115], v[100:103], v[76:79], a[112:115]\n"
    "global_load_lds_dwordx4 v207, s[46:47] offset:1024\n"
    "v_mfma_f32_16x16x32_bf16 v[16:19], v[104:107], a[132:135], v[16:19]\n"
    "v_exp_f32 v52, v52\n"
    "v_mfma_f32_16x16x32_bf16 v[20:23], v[104:107], a[148:151], v[20:23]\n"
    "v_exp_f32 v53, v53\n"
    "v_mfma_f32_16x16x32_bf16 v[24:27], v[104:107], a[164:167], v[24:27]\n"
    "ds_read_b128 v[100:103], %[rbase] offset:40960\n"
    "v_cvt_pk_bf16_f32 v86, v52, v53\n"
    "v_mfma_f32_16x16x32_bf16 v[28:31], v[104:107], a[180:183], v[28:31]\n"
    "s_add_u32 s58, %[wslo], s58\n"
    "s_addc_u32 s59, %[wshi], 0\n"
    "v_mfma_f32_16x16x32_bf16 a[20:23], v[108:111], v[64:67], a[20:23]\n"
    "v_exp_f32 v54, v54\n"
    "v_mfma_f32_16x16x32_bf16 a[52:55], v[108:111], v[68:71], a[52:55]\n"
    "v_exp_f32 v55, v55\n"
    "v_mfma_f32_16x16x32_bf16 a[84:87], v[108:111], v[72:75], a[84:87]\n"
    "ds_read_b128 v[104:107], %[rbase] offset:33792\n"
    "v_cvt_pk_bf16_f32 v87, v54, v55\n"
    "v_mfma_f32_16x16x32_bf16 a[116:119], v[108:111], v[76:79], a[116:119]\n"
    "global_load_lds_dwordx4 v207, s[46:47] offset:2048\n"
    "global_store_dwordx4 %[lane16], v[164:167], s[58:59] sc1\n"
    "v_mfma_f32_16x16x32_bf16 v[128:131], v[140:143], v[72:75], v[128:131]\n"
    "s_waitcnt lgkmcnt(3)\n"
    "v_mfma_f32_16x16x32_bf16 v[16:19], v[112:115], a[136:139], v[16:19]\n"
    "v_exp_f32 v56, v56\n"
    "v_mfma_f32_16x16x32_bf16 v[20:23], v[112:115], a[152:155], v[20:23]\n"
    "v_exp_f32 v57, v57\n"
    "v_mfma_f32_16x16x32_bf16 v[24:27], v[112:115], a[168:171], v[24:27]\n"
    "ds_read_b128 v[108:111], %[rbase] offset:41984\n"
    "v_cvt_pk_bf16_f32 v90, v56, v57\n"
    "v_mfma_f32_16x16x32_bf16 v[28:31], v[112:115], a[184:187], v[28:31]\n"
    "v_mfma_f32_16x16x32_bf16 a[24:27], v[116:119], v[64:67], a[24:27]\n"
    "v_exp_f32 v58, v58\n"
    "v_mfma_f32_16x16x32_bf16 a[56:59], v[116:119], v[68:71], a[56:59]\n"
    "v_exp_f32 v59, v59\n"
    "v_mfma_f32_16x16x32_bf16 a[88:91], v[116:119], v[72:75], a[88:91]\n"
    "ds_read_b128 v[112:115], %[rbase] offset:34816\n"
    "v_cvt_pk_bf16_f32 v91, v58, v59\n"
    "v_mfma_f32_16x16x32_bf16 a[120:123], v[116:119], v[76:79], a[120:123]\n"
    "global_load_lds_dwordx4 v207, s[46:47] offset:3072\n"
    "s_add_u32 m0, %[wdst0], 98304\n"
    "v_mfma_f32_16x16x32_bf16 v[16:19], v[120:123], a[140:143], v[16:19]\n"
    "v_exp_f32 v60, v60\n"
    "v_mfma_f32_16x16x32_bf16 v[20:23], v[120:123], a[156:159], v[20:23]\n"
    "v_exp_f32 v61, v61\n"
    "v_mfma_f32_16x16x32_bf16 v[24:27], v[120:123], a[172:175], v[24:27]\n"
    "ds_read_b128 v[116:119], %[rbase] offset:43008\n"
    "v_cvt_pk_bf16_f32 v94, v60, v61\n"
    "v_mfma_f32_16x16x32_bf16 v[28:31], v[120:123], a[188:191], v[28:31]\n"
    "s_add_u32 s46, %[khlo], s53\n"
    "s_addc_u32 s47, %[khhi], 0\n"
    "v_mfma_f32_16x16x32_bf16 a[28:31], v[124:127], v[64:67], a[28:31]\n"
    "v_exp_f32 v62, v62\n"
    "v_mfma_f32_16x16x32_bf16 a[60:63], v[124:127], v[68:71], a[60:63]\n"
    "v_exp_f32 v63, v63\n"
    "v_mfma_f32_16x16x32_bf16 a[92:95], v[124:127], v[72:75], a[92:95]\n"
    "ds_read_b128 v[120:123], %[rbase] offset:35840\n"
    "v_cvt_pk_bf16_f32 v95, v62, v63\n"
    "v_mfma_f32_16x16x32_bf16 a[124:127], v[124:127], v[76:79], a[124:127]\n"
    "global_load_lds_dwordx4 v205, s[48:49] offset:0\n"
    "s_waitcnt lgkmcnt(3)\n"
    "v_mfma_f32_16x16x32_bf16 v[128:131], v[144:147], v[76:79], v[128:131]\n"
    "v_mfma_f32_16x16x32_bf16 v[32:35], v[96:99], a[128:131], 0\n"
    "v_exp_f32 v0, v0\n"
    "v_mfma_f32_16x16x32_bf16 v[36:39], v[96:99], a[144:147], 0\n"
    "v_exp_f32 v1, v1\n"
    "v_mfma_f32_16x16x32_bf16 v[40:43], v[96:99], a[160:163], 0\n"
    "ds_read_b128 v[124:127], %[rbase] offset:44032\n"
    "v_cvt_pk_bf16_f32 v64, v0, v1\n"
    "v_mfma_f32_16x16x32_bf16 v[44:47], v[96:99], a[176:179], 0\n"
    "global_load_lds_dwordx4 v205, s[48:49] offset:1024\n"
    "v_readlane_b32 s58, v218, 11\n"
    "v_mfma_f32_16x16x32_bf16 a[0:3], v[100:103], v[80:83], a[0:3]\n"
    "v_exp_f32 v2, v2\n"
    "v_mfma_f32_16x16x32_bf16 a[32:35], v[100:103], v[84:87], a[32:35]\n"
    "v_exp_f32 v3, v3\n"
    "v_mfma_f32_16x16x32_bf16 a[64:67], v[100:103], v[88:91], a[64:67]\n"
    "ds_read_b128 v[96:99], %[rbase] offset:36864\n"
    "v_cvt_pk_bf16_f32 v65, v2, v3\n"
    "v_mfma_f32_16x16x32_bf16 a[96:99], v[100:103], v[92:95], a[96:99]\n"
    "v_perm_b32 v164, v151, v149, s61\n"
    "v_perm_b32 v165, v155, v153, s61\n"
    "v_mfma_f32_16x16x32_bf16 v[32:35], v[104:107], a[132:135], v[32:35]\n"
    "v_exp_f32 v4, v4\n"
    "v_mfma_f32_16x16x32_bf16 v[36:39], v[104:107], a[148:151], v[36:39]\n"
    "v_exp_f32 v5, v5\n"
    "v_mfma_f32_16x16x32_bf16 v[40:43], v[104:107], a[164:167], v[40:43]\n"
    "ds_read_b128 v[100:103], %[rbase] offset:45056\n"
    "v_cvt_pk_bf16_f32 v68, v4, v5\n"
    "v_mfma_f32_16x16x32_bf16 v[44:47], v[104:107], a[180:183], v[44:47]\n"
    "global_load_lds_dwordx4 v205, s[48:49] offset:2048\n"
    "v_perm_b32 v166, v159, v157, s61\n"
    "v_mfma_f32_16x16x32_bf16 a[4:7], v[108:111], v[80:83], a[4:7]\n"
    "v_exp_f32 v6, v6\n"
    "v_mfma_f32_16x16x32_bf16 a[36:39], v[108:111], v[84:87], a[36:39]\n"
    "v_exp_f32 v7, v7\n"
    "v_mfma_f32_16x16x32_bf16 a[68:71], v[108:111], v[88:91], a[68:71]\n"
    "ds_read_b128 v[104:107], %[rbase] offset:37888\n"
    "v_cvt_pk_bf16_f32 v69, v6, v7\n"
    "v_mfma_f32_16x16x32_bf16 a[100:103], v[108:111], v[92:95], a[100:103]\n"
    "v_perm_b32 v167, v163, v161, s61\n"
    "v_mfma_f32_16x16x32_bf16 v[128:131], v[132:135], v[80:83], v[128:131]\n"
    "s_waitcnt lgkmcnt(3)\n"
    "v_mfma_f32_16x16x32_bf16 v[32:35], v[112:115], a[136:139], v[32:35]\n"
    "v_exp_f32 v8, v8\n"
    "v_mfma_f32_16x16x32_bf16 v[36:39], v[112:115], a[152:155], v[36:39]\n"
    "v_exp_f32 v9, v9\n"
    "v_mfma_f32_16x16x32_bf16 v[40:43], v[112:115], a[168:171], v[40:43]\n"
    "ds_read_b128 v[108:111], %[rbase] offset:46080\n"
    "v_cvt_pk_bf16_f32 v72, v8, v9\n"
    "v_mfma_f32_16x16x32_bf16 v[44:47], v[112:115], a[184:187], v[44:47]\n"
    "global_load_lds_dwordx4 v205, s[48:49] offset:3072\n"
    "s_add_u32 m0, %[wdst0], 8192\n"
    "v_mfma_f32_16x16x32_bf16 a[8:11], v[116:119], v[80:83], a[8:11]\n"
    "v_exp_f32 v10, v10\n"
    "v_mfma_f32_16x16x32_bf16 a[40:43], v[116:119], v[84:87], a[40:43]\n"
    "v_exp_f32 v11, v11\n"
    "v_mfma_f32_16x16x32_bf16 a[72:75], v[116:119], v[88:91], a[72:75]\n"
    "ds_read_b128 v[112:115], %[rbase] offset:38912\n"
    "v_cvt_pk_bf16_f32 v73, v10, v11\n"
    "v_mfma_f32_16x16x32_bf16 a[104:107], v[116:119], v[92:95], a[104:107]\n"
    "s_add_u32 s58, %[wslo], s58\n"
    "s_addc_u32 s59, %[wshi], 0\n"
    "v_mfma_f32_16x16x32_bf16 v[32:35], v[120:123], a[140:143], v[32:35]\n"
    "v_exp_f32 v12, v12\n"
    "v_mfma_f32_16x16x32_bf16 v[36:39], v[120:123], a[156:159], v[36:39]\n"
    "v_exp_f32 v13, v13\n"
    "v_mfma_f32_16x16x32_bf16 v[40:43], v[120:123], a[172:175], v[40:43]\n"
    "ds_read_b128 v[116:119], %[rbase] offset:47104\n"
    "v_cvt_pk_bf16_f32 v76, v12, v13\n"
    "v_mfma_f32_16x16x32_bf16 v[44:47], v[120:123], a[188:191], v[44:47]\n"
    "s_waitcnt vmcnt(9)\n"
    "s_barrier\n"
    "v_mfma_f32_16x16x32_bf16 a[12:15], v[124:127], v[80:83], a[12:15]\n"
    "v_exp_f32 v14, v14\n"
    "v_mfma_f32_16x16x32_bf16 a[44:47], v[124:127], v[84:87], a[44:47]\n"
    "v_exp_f32 v15, v15\n"
    "v_mfma_f32_16x16x32_bf16 a[76:79], v[124:127], v[88:91], a[76:79]\n"
    "ds_read_b128 v[120:123], %[rbase] offset:39936\n"
    "v_cvt_pk_bf16_f32 v77, v14, v15\n"
    "v_mfma_f32_16x16x32_bf16 a[108:111], v[124:127], v[92:95], a[108:111]\n"
    "global_load_lds_dwordx4 %[lanev], s[48:49] offset:0\n"
    "s_add_u32 s40, s40, 1\n"
    "v_mfma_f32_16x16x32_bf16 v[128:131], v[136:139], v[84:87], v[128:131]\n"
    "global_store_dwordx4 %[lane16], v[164:167], s[58:59] sc1\n"
    "s_waitcnt lgkmcnt(3)\n"
    "v_mfma_f32_16x16x32_bf16 v[48:51], v[96:99], a[128:131], 0\n"
    "v_exp_f32 v16, v16\n"
    "v_mfma_f32_16x16x32_bf16 v[52:55], v[96:99], a[144:147], 0\n"
    "v_exp_f32 v17, v17\n"
    "v_mfma_f32_16x16x32_bf16 v[56:59], v[96:99], a[160:163], 0\n"
    "ds_read_b128 v[124:127], %[rbase] offset:48128\n"
    "v_cvt_pk_bf16_f32 v66, v16, v17\n"
    "v_mfma_f32_16x16x32_bf16 v[60:63], v[96:99], a[176:179], 0\n"
    "v_mov_b32 v218, v219\n"
    "v_mov_b32 v219, v220\n"
    "v_mfma_f32_16x16x32_bf16 a[16:19], v[100:103], v[80:83], a[16:19]\n"
    "v_exp_f32 v18, v18\n"
    "v_mfma_f32_16x16x32_bf16 a[48:51], v[100:103], v[84:87], a[48:51]\n"
    "v_exp_f32 v19, v19\n"
    "v_mfma_f32_16x16x32_bf16 a[80:83], v[100:103], v[88:91], a[80:83]\n"
    "ds_read_b128 v[96:99], v204 offset:0\n"
    "v_cvt_pk_bf16_f32 v67, v18, v19\n"
    "v_mfma_f32_16x16x32_bf16 a[112:115], v[100:103], v[92:95], a[112:115]\n"
    "global_load_lds_dwordx4 %[lanev], s[48:49] offset:1024\n"
    "v_mov_b32 v220, v221\n"
    "v_mfma_f32_16x16x32_bf16 v[48:51], v[104:107], a[132:135], v[48:51]\n"
    "v_exp_f32 v20, v20\n"
    "v_mfma_f32_16x16x32_bf16 v[52:55], v[104:107], a[148:151], v[52:55]\n"
    "v_exp_f32 v21, v21\n"
    "v_mfma_f32_16x16x32_bf16 v[56:59], v[104:107], a[164:167], v[56:59]\n"
    "ds_read_b128 v[100:103], v204 offset:8192\n"
    "v_cvt_pk_bf16_f32 v70, v20, v21\n"
    "v_mfma_f32_16x16x32_bf16 v[60:63], v[104:107], a[180:183], v[60:63]\n"
    "v_mfma_f32_16x16x32_bf16 a[20:23], v[108:111], v[80:83], a[20:23]\n"
    "v_exp_f32 v22, v22\n"
    "v_mfma_f32_16x16x32_bf16 a[52:55], v[108:111], v[84:87], a[52:55]\n"
    "v_exp_f32 v23, v23\n"
    "v_mfma_f32_16x16x32_bf16 a[84:87], v[108:111], v[88:91], a[84:87]\n"
    "ds_read_b128 v[104:107], v204 offset:1024\n"
    "v_cvt_pk_bf16_f32 v71, v22, v23\n"
    "v_mfma_f32_16x16x32_bf16 a[116:119], v[108:111], v[92:95], a[116:119]\n"
    "global_load_lds_dwordx4 %[lanev], s[48:49] offset:2048\n"
    "v_mfma_f32_16x16x32_bf16 v[128:131], v[140:143], v[88:91], v[128:131]\n"
    "s_waitcnt lgkmcnt(3)\n"
    "v_mfma_f32_16x16x32_bf16 v[48:51], v[112:115], a[136:139], v[48:51]\n"
    "v_exp_f32 v24, v24\n"
    "v_mfma_f32_16x16x32_bf16 v[52:55], v[112:115], a[152:155], v[52:55]\n"
    "v_exp_f32 v25, v25\n"
    "v_mfma_f32_16x16x32_bf16 v[56:59], v[112:115], a[168:171], v[56:59]\n"
    "ds_read_b128 v[108:111], v204 offset:9216\n"
    "v_cvt_pk_bf16_f32 v74, v24, v25\n"
    "v_mfma_f32_16x16x32_bf16 v[60:63], v[112:115], a[184:187], v[60:63]\n"
    "v_mfma_f32_16x16x32_bf16 a[24:27], v[116:119], v[80:83], a[24:27]\n"
    "v_exp_f32 v26, v26\n"
    "v_mfma_f32_16x16x32_bf16 a[56:59], v[116:119], v[84:87], a[56:59]\n"
    "v_exp_f32 v27, v27\n"
    "v_mfma_f32_16x16x32_bf16 a[88:91], v[116:119], v[88:91], a[88:91]\n"
    "ds_read_b128 v[112:115], v204 offset:2048\n"
    "v_cvt_pk_bf16_f32 v75, v26, v27\n"
    "v_mfma_f32_16x16x32_bf16 a[120:123], v[116:119], v[92:95], a[120:123]\n"
    "global_load_lds_dwordx4 %[lanev], s[48:49] offset:3072\n"
    "s_add_u32 m0, %[wdst0], 0\n"
    "v_mfma_f32_16x16x32_bf16 v[48:51], v[120:123], a[140:143], v[48:51]\n"
    "v_exp_f32 v28, v28\n"
    "v_mfma_f32_16x16x32_bf16 v[52:55], v[120:123], a[156:159], v[52:55]\n"
    "v_exp_f32 v29, v29\n"
    "v_mfma_f32_16x16x32_bf16 v[56:59], v[120:123], a[172:175], v[56:59]\n"
    "ds_read_b128 v[116:119], v204 offset:10240\n"
    "v_cvt_pk_bf16_f32 v78, v28, v29\n"
    "v_mfma_f32_16x16x32_bf16 v[60:63], v[120:123], a[188:191], v[60:63]\n"
    "v_mfma_f32_16x16x32_bf16 a[28:31], v[124:127], v[80:83], a[28:31]\n"
    "v_exp_f32 v30, v30\n"
    "v_mfma_f32_16x16x32_bf16 a[60:63], v[124:127], v[84:87], a[60:63]\n"
    "v_exp_f32 v31, v31\n"
    "v_mfma_f32_16x16x32_bf16 a[92:95], v[124:127], v[88:91], a[92:95]\n"
    "ds_read_b128 v[120:123], v204 offset:3072\n"
    "v_cvt_pk_bf16_f32 v79, v30, v31\n"
    "v_mfma_f32_16x16x32_bf16 a[124:127], v[124:127], v[92:95], a[124:127]\n"
    "global_load_lds_dwordx4 %[lane16], s[46:47] offset:0\n"
    "s_waitcnt lgkmcnt(3)\n"
    "v_mfma_f32_16x16x32_bf16 v[128:131], v[144:147], v[92:95], v[128:131]\n"
    "s_branch .Lloop%=\n"
    ".Ldia%=:\n"
    ".Lk2_p5:\n\ts_cbranch_execz .Lk2_p9\n"
    "s_bitcmp1_b32 %[special], s40\n"
    "s_cbranch_scc1 .Lqa30%=\n"
    ".Lra30%=:\n"
    "v_mfma_f32_16x16x32_bf16 v[0:3], v[96:99], a[128:131], v[222:225]\n"
    "v_exp_f32 v32, v32\n"
    "v_mfma_f32_16x16x32_bf16 v[4:7], v[96:99], a[144:147], 0\n"
    "v_exp_f32 v33, v33\n"
    "v_mfma_f32_16x16x32_bf16 v[8:11], v[96:99], a[160:163], 0\n"
    "ds_read_b128 v[124:127], v204 offset:11264\n"
    "v_cvt_pk_bf16_f32 v80, v32, v33\n"
    "v_mfma_f32_16x16x32_bf16 v[12:15], v[96:99], a[176:179], 0\n"
    "global_load_lds_dwordx4 %[lane16], s[46:47] offset:1024\n"
    "v_add_u32 v234, 0xfffe0000, %[qlds]\n"
    "v_mfma_f32_16x16x32_bf16 a[0:3], v[100:103], v[64:67], a[0:3]\n"
    "v_exp_f32 v34, v34\n"
    "v_mfma_f32_16x16x32_bf16 a[32:35], v[100:103], v[68:71], a[32:35]\n"
    "v_exp_f32 v35, v35\n"
    "v_mfma_f32_16x16x32_bf16 a[64:67], v[100:103], v[72:75], a[64:67]\n"
    "ds_read_b128 v[96:99], v204 offset:4096\n"
    "v_cvt_pk_bf16_f32 v81, v34, v35\n"
    "v_mfma_f32_16x16x32_bf16 a[96:99], v[100:103], v[76:79], a[96:99]\n"
    "v_add_u32 v235, 0xffff0000, %[qlds]\n"
    "v_mfma_f32_16x16x32_bf16 v[0:3], v[104:107], a[132:135], v[0:3]\n"
    "v_exp_f32 v36, v36\n"
    "v_mfma_f32_16x16x32_bf16 v[4:7], v[104:107], a[148:151], v[4:7]\n"
    "v_exp_f32 v37, v37\n"
    "v_mfma_f32_16x16x32_bf16 v[8:11], v[104:107], a[164:167], v[8:11]\n"
    "ds_read_b128 v[100:103], v204 offset:12288\n"
    "v_cvt_pk_bf16_f32 v84, v36, v37\n"
    "v_mfma_f32_16x16x32_bf16 v[12:15], v[104:107], a[180:183], v[12:15]\n"
    "global_load_lds_dwordx4 %[lane16], s[46:47] offset:2048\n"
    "v_mfma_f32_16x16x32_bf16 a[4:7], v[108:111], v[64:67], a[4:7]\n"
    "v_exp_f32 v38, v38\n"
    "v_mfma_f32_16x16x32_bf16 a[36:39], v[108:111], v[68:71], a[36:39]\n"
    "v_exp_f32 v39, v39\n"
    "v_mfma_f32_16x16x32_bf16 a[68:71], v[108:111], v[72:75], a[68:71]\n"
    "ds_read_b128 v[104:107], v204 offset:5120\n"
    "v_cvt_pk_bf16_f32 v85, v38, v39\n"
    "v_mfma_f32_16x16x32_bf16 a[100:103], v[108:111], v[76:79], a[100:103]\n"
    "v_mfma_f32_16x16x32_bf16 v[128:131], v[132:135], v[64:67], v[128:131]\n"
    "s_waitcnt lgkmcnt(3)\n"
    "v_mfma_f32_16x16x32_bf16 v[0:3], v[112:115], a[136:139], v[0:3]\n"
    "v_exp_f32 v40, v40\n"
    "v_mfma_f32_16x16x32_bf16 v[4:7], v[112:115], a[152:155], v[4:7]\n"
    "v_exp_f32 v41, v41\n"
    "v_mfma_f32_16x16x32_bf16 v[8:11], v[112:115], a[168:171], v[8:11]\n"
    "ds_read_b128 v[108:111], v204 offset:13312\n"
    "v_cvt_pk_bf16_f32 v88, v40, v41\n"
    "v_mfma_f32_16x16x32_bf16 v[12:15], v[112:115], a[184:187], v[12:15]\n"
    "global_load_lds_dwordx4 %[lane16], s[46:47] offset:3072\n"
    "s_add_u32 m0, %[wdst0], 40960\n"
    "v_mfma_f32_16x16x32_bf16 a[8:11], v[116:119], v[64:67], a[8:11]\n"
    "v_exp_f32 v42, v42\n"
    "v_mfma_f32_16x16x32_bf16 a[40:43], v[116:119], v[68:71], a[40:43]\n"
    "v_exp_f32 v43, v43\n"
    "v_mfma_f32_16x16x32_bf16 a[72:75], v[116:119], v[72:75], a[72:75]\n"
    "ds_read_b128 v[112:115], v204 offset:6144\n"
    "v_cvt_pk_bf16_f32 v89, v42, v43\n"
    "v_mfma_f32_16x16x32_bf16 a[104:107], v[116:119], v[76:79], a[104:107]\n"
    "v_mfma_f32_16x16x32_bf16 v[0:3], v[120:123], a[140:143], v[0:3]\n"
    "v_exp_f32 v44, v44\n"
    "v_mfma_f32_16x16x32_bf16 v[4:7], v[120:123], a[156:159], v[4:7]\n"
    "v_exp_f32 v45, v45\n"
    "v_mfma_f32_16x16x32_bf16 v[8:11], v[120:123], a[172:175], v[8:11]\n"
    "ds_read_b128 v[116:119], v204 offset:14336\n"
    "v_cvt_pk_bf16_f32 v92, v44, v45\n"
    "v_mfma_f32_16x16x32_bf16 v[12:15], v[120:123], a[188:191], v[12:15]\n"
    "s_waitcnt vmcnt(9)\n"
    "s_barrier\n"
    "v_mfma_f32_16x16x32_bf16 a[12:15], v[124:127], v[64:67], a[12:15]\n"
    "v_exp_f32 v46, v46\n"
    "v_mfma_f32_16x16x32_bf16 a[44:47], v[124:127], v[68:71], a[44:47]\n"
    "v_exp_f32 v47, v47\n"
    "v_mfma_f32_16x16x32_bf16 a[76:79], v[124:127], v[72:75], a[76:79]\n"
    "ds_read_b128 v[120:123], v204 offset:7168\n"
    "v_cvt_pk_bf16_f32 v93, v46, v47\n"
    "v_mfma_f32_16x16x32_bf16 a[108:111], v[124:127], v[76:79], a[108:111]\n"
    "global_load_lds_dwordx4 v207, s[48:49] offset:0\n"
    "v_mfma_f32_16x16x32_bf16 v[128:131], v[136:139], v[68:71], v[128:131]\n"
    "s_waitcnt lgkmcnt(3)\n"
    "v_mfma_f32_16x16x32_bf16 v[16:19], v[96:99], a[128:131], v[226:229]\n"
    "v_exp_f32 v48, v48\n"
    "v_mfma_f32_16x16x32_bf16 v[20:23], v[96:99], a[144:147], 0\n"
    "v_exp_f32 v49, v49\n"
    "v_mfma_f32_16x16x32_bf16 v[24:27], v[96:99], a[160:163], 0\n"
    "ds_read_b128 v[124:127], v204 offset:15360\n"
    "v_cvt_pk_bf16_f32 v82, v48, v49\n"
    "v_mfma_f32_16x16x32_bf16 v[28:31], v[96:99], a[176:179], 0\n"
    "v_mfma_f32_16x16x32_bf16 a[16:19], v[100:103], v[64:67], a[16:19]\n"
    "v_exp_f32 v50, v50\n"
    "v_mfma_f32_16x16x32_bf16 a[48:51], v[100:103], v[68:71], a[48:51]\n"
    "v_exp_f32 v51, v51\n"
    "v_mfma_f32_16x16x32_bf16 a[80:83], v[100:103], v[72:75], a[80:83]\n"
    "ds_read_b128 v[96:99], v204 offset:32768\n"
    "v_cvt_pk_bf16_f32 v83, v50, v51\n"
    "v_mfma_f32_16x16x32_bf16 a[112:115], v[100:103], v[76:79], a[112:115]\n"
    "global_load_lds_dwordx4 v207, s[48:49] offset:1024\n"
    "v_mfma_f32_16x16x32_bf16 v[16:19], v[104:107], a[132:135], v[16:19]\n"
    "v_exp_f32 v52, v52\n"
    "v_mfma_f32_16x16x32_bf16 v[20:23], v[104:107], a[148:151], v[20:23]\n"
    "v_exp_f32 v53, v53\n"
    "v_mfma_f32_16x16x32_bf16 v[24:27], v[104:107], a[164:167], v[24:27]\n"
    "ds_read_b128 v[100:103], v204 offset:40960\n"
    "v_cvt_pk_bf16_f32 v86, v52, v53\n"
    "v_mfma_f32_16x16x32_bf16 v[28:31], v[104:107], a[180:183], v[28:31]\n"
    "v_mfma_f32_16x16x32_bf16 a[20:23], v[108:111], v[64:67], a[20:23]\n"
    "v_exp_f32 v54, v54\n"
    "v_mfma_f32_16x16x32_bf16 a[52:55], v[108:111], v[68:71], a[52:55]\n"
    "v_exp_f32 v55, v55\n"
    "v_mfma_f32_16x16x32_bf16 a[84:87], v[108:111], v[72:75], a[84:87]\n"
    "ds_read_b128 v[104:107], v204 offset:33792\n"
    "v_cvt_pk_bf16_f32 v87, v54, v55\n"
    "v_mfma_f32_16x16x32_bf16 a[116:119], v[108:111], v[76:79], a[116:119]\n"
    "global_load_lds_dwordx4 v207, s[48:49] offset:2048\n"
    "v_mfma_f32_16x16x32_bf16 v[128:131], v[140:143], v[72:75], v[128:131]\n"
    "s_waitcnt lgkmcnt(3)\n"
    "v_mfma_f32_16x16x32_bf16 v[16:19], v[112:115], a[136:139], v[16:19]\n"
    "v_exp_f32 v56, v56\n"
    "v_mfma_f32_16x16x32_bf16 v[20:23], v[112:115], a[152:155], v[20:23]\n"
    "v_exp_f32 v57, v57\n"
    "v_mfma_f32_16x16x32_bf16 v[24:27], v[112:115], a[168:171], v[24:27]\n"
    "ds_read_b128 v[108:111], v204 offset:41984\n"
    "v_cvt_pk_bf16_f32 v90, v56, v57\n"
    "v_mfma_f32_16x16x32_bf16 v[28:31], v[112:115], a[184:187], v[28:31]\n"
    "v_mfma_f32_16x16x32_bf16 a[24:27], v[116:119], v[64:67], a[24:27]\n"
    "v_exp_f32 v58, v58\n"
    "v_mfma_f32_16x16x32_bf16 a[56:59], v[116:119], v[68:71], a[56:59]\n"
    "v_exp_f32 v59, v59\n"
    "v_mfma_f32_16x16x32_bf16 a[88:91], v[116:119], v[72:75], a[88:91]\n"
    "ds_read_b128 v[112:115], v204 offset:34816\n"
    "v_cvt_pk_bf16_f32 v91, v58, v59\n"
    "v_mfma_f32_16x16x32_bf16 a[120:123], v[116:119], v[76:79], a[120:123]\n"
    "global_load_lds_dwordx4 v207, s[48:49] offset:3072\n"
    "s_add_u32 m0, %[wdst0], 32768\n"
    "v_mfma_f32_16x16x32_bf16 v[16:19], v[120:123], a[140:143], v[16:19]\n"
    "v_exp_f32 v60, v60\n"
    "v_mfma_f32_16x16x32_bf16 v[20:23], v[120:123], a[156:159], v[20:23]\n"
    "v_exp_f32 v61, v61\n"
    "v_mfma_f32_16x16x32_bf16 v[24:27], v[120:123], a[172:175], v[24:27]\n"
    "ds_read_b128 v[116:119], v204 offset:43008\n"
    "v_cvt_pk_bf16_f32 v94, v60, v61\n"
    "v_mfma_f32_16x16x32_bf16 v[28:31], v[120:123], a[188:191], v[28:31]\n"
    "v_mfma_f32_16x16x32_bf16 a[28:31], v[124:127], v[64:67], a[28:31]\n"
    "v_exp_f32 v62, v62\n"
    "v_mfma_f32_16x16x32_bf16 a[60:63], v[124:127], v[68:71], a[60:63]\n"
    "v_exp_f32 v63, v63\n"
    "v_mfma_f32_16x16x32_bf16 a[92:95], v[124:127], v[72:75], a[92:95]\n"
    "ds_read_b128 v[120:123], v204 offset:35840\n"
    "v_cvt_pk_bf16_f32 v95, v62, v63\n"
    "v_mfma_f32_16x16x32_bf16 a[124:127], v[124:127], v[76:79], a[124:127]\n"
    "global_load_lds_dwordx4 v205, s[46:47] offset:0\n"
    "s_waitcnt lgkmcnt(3)\n"
    "v_mfma_f32_16x16x32_bf16 v[128:131], v[144:147], v[76:79], v[128:131]\n"
    "s_waitcnt lgkmcnt(6)\n"
    "v_mfma_f32_16x16x32_bf16 v[36:39], v[96:99], a[144:147], v[222:225]\n"
    "global_load_lds_dwordx4 v205, s[46:47] offset:1024\n"
    "s_add_u32 s40, s40, 1\n"
    "v_mfma_f32_16x16x32_bf16 v[40:43], v[96:99], a[160:163], 0\n"
    "v_exp_f32 v0, v0\n"
    "v_mfma_f32_16x16x32_bf16 v[44:47], v[96:99], a[176:179], 0\n"
    "ds_read_b128 v[124:127], v204 offset:44032\n"
    "s_waitcnt lgkmcnt(6)\n"
    "v_mfma_f32_16x16x32_bf16 a[0:3], v[100:103], v[80:83], a[0:3]\n"
    "v_exp_f32 v1, v1\n"
    "v_mfma_f32_16x16x32_bf16 a[32:35], v[100:103], v[84:87], a[32:35]\n"
    "v_exp_f32 v2, v2\n"
    "v_mfma_f32_16x16x32_bf16 a[64:67], v[100:103], v[88:91], a[64:67]\n"
    "v_exp_f32 v3, v3\n"
    "v_cvt_pk_bf16_f32 v64, v0, v1\n"
    "v_mfma_f32_16x16x32_bf16 a[96:99], v[100:103], v[92:95], a[96:99]\n"
    "ds_read_b128 v[96:99], v204 offset:36864\n"
    "s_waitcnt lgkmcnt(6)\n"
    "v_mfma_f32_16x16x32_bf16 v[36:39], v[104:107], a[148:151], v[36:39]\n"
    "global_load_lds_dwordx4 v205, s[46:47] offset:2048\n"
    "v_exp_f32 v4, v4\n"
    "v_mfma_f32_16x16x32_bf16 v[40:43], v[104:107], a[164:167], v[40:43]\n"
    "v_exp_f32 v5, v5\n"
    "v_cvt_pk_bf16_f32 v65, v2, v3\n"
    "v_mfma_f32_16x16x32_bf16 v[44:47], v[104:107], a[180:183], v[44:47]\n"
    "ds_read_b128 v[100:103], v204 offset:45056\n"
    "s_waitcnt lgkmcnt(6)\n"
    "v_mfma_f32_16x16x32_bf16 a[4:7], v[108:111], v[80:83], a[4:7]\n"
    "v_exp_f32 v6, v6\n"
    "v_mfma_f32_16x16x32_bf16 a[36:39], v[108:111], v[84:87], a[36:39]\n"
    "v_exp_f32 v7, v7\n"
    "v_cvt_pk_bf16_f32 v68, v4, v5\n"
    "v_mfma_f32_16x16x32_bf16 a[68:71], v[108:111], v[88:91], a[68:71]\n"
    "v_exp_f32 v8, v8\n"
    "v_mfma_f32_16x16x32_bf16 a[100:103], v[108:111], v[92:95], a[100:103]\n"
    "ds_read_b128 v[104:107], v204 offset:37888\n"
    "v_exp_f32 v9, v9\n"
    "v_mfma_f32_16x16x32_bf16 v[128:131], v[132:135], v[80:83], v[128:131]\n"
    "global_load_lds_dwordx4 v205, s[46:47] offset:3072\n"
    "s_add_u32 m0, %[wdst0], 73728\n"
    "s_waitcnt lgkmcnt(6)\n"
    "v_mfma_f32_16x16x32_bf16 v[36:39], v[112:115], a[152:155], v[36:39]\n"
    "v_cvt_pk_bf16_f32 v69, v6, v7\n"
    "v_exp_f32 v10, v10\n"
    "v_mfma_f32_16x16x32_bf16 v[40:43], v[112:115], a[168:171], v[40:43]\n"
    "v_exp_f32 v11, v11\n"
    "v_cvt_pk_bf16_f32 v72, v8, v9\n"
    "v_mfma_f32_16x16x32_bf16 v[44:47], v[112:115], a[184:187], v[44:47]\n"
    "ds_read_b128 v[108:111], v204 offset:46080\n"
    "s_waitcnt lgkmcnt(6)\n"
    "v_mfma_f32_16x16x32_bf16 a[8:11], v[116:119], v[80:83], a[8:11]\n"
    "v_exp_f32 v12, v12\n"
    "v_mfma_f32_16x16x32_bf16 a[40:43], v[116:119], v[84:87], a[40:43]\n"
    "v_exp_f32 v13, v13\n"
    "v_cvt_pk_bf16_f32 v73, v10, v11\n"
    "v_mfma_f32_16x16x32_bf16 a[72:75], v[116:119], v[88:91], a[72:75]\n"
    "v_exp_f32 v14, v14\n"
    "v_mfma_f32_16x16x32_bf16 a[104:107], v[116:119], v[92:95], a[104:107]\n"
    "ds_read_b128 v[112:115], v204 offset:38912\n"
    "s_waitcnt lgkmcnt(6)\n"
    "v_mfma_f32_16x16x32_bf16 v[36:39], v[120:123], a[156:159], v[36:39]\n"
    "v_exp_f32 v15, v15\n"
    "v_cvt_pk_bf16_f32 v76, v12, v13\n"
    "v_mfma_f32_16x16x32_bf16 v[40:43], v[120:123], a[172:175], v[40:43]\n"
    "v_exp_f32 v16, v16\n"
    "v_mfma_f32_16x16x32_bf16 v[44:47], v[120:123], a[188:191], v[44:47]\n"
    "ds_read_b128 v[116:119], v204 offset:47104\n"
    "s_waitcnt lgkmcnt(6)\n"
    "s_waitcnt vmcnt(8)\n"
    "s_barrier\n"
    "v_mfma_f32_16x16x32_bf16 a[12:15], v[124:127], v[80:83], a[12:15]\n"
    "global_load_lds_dwordx4 %[lanev], s[46:47] offset:0\n"
    "v_exp_f32 v17, v17\n"
    "v_mfma_f32_16x16x32_bf16 a[44:47], v[124:127], v[84:87], a[44:47]\n"
    "v_cvt_pk_bf16_f32 v77, v14, v15\n"
    "v_exp_f32 v18, v18\n"
    "v_mfma_f32_16x16x32_bf16 a[76:79], v[124:127], v[88:91], a[76:79]\n"
    "v_exp_f32 v19, v19\n"
    "v_cvt_pk_bf16_f32 v66, v16, v17\n"
    "v_mfma_f32_16x16x32_bf16 a[108:111], v[124:127], v[92:95], a[108:111]\n"
    "ds_read_b128 v[120:123], v204 offset:39936\n"
    "global_load_lds_dwordx4 %[lanev], s[46:47] offset:1024\n"
    "v_mfma_f32_16x16x32_bf16 v[128:131], v[136:139], v[84:87], v[128:131]\n"
    "v_exp_f32 v20, v20\n"
    "s_waitcnt lgkmcnt(6)\n"
    "v_mfma_f32_16x16x32_bf16 v[52:55], v[96:99], a[144:147], v[226:229]\n"
    "v_exp_f32 v21, v21\n"
    "v_cvt_pk_bf16_f32 v67, v18, v19\n"
    "v_mfma_f32_16x16x32_bf16 v[56:59], v[96:99], a[160:163], 0\n"
    "global_load_lds_dwordx4 %[lanev], s[46:47] offset:2048\n"
    "v_exp_f32 v22, v22\n"
    "v_mfma_f32_16x16x32_bf16 v[60:63], v[96:99], a[176:179], 0\n"
    "ds_read_b128 v[124:127], v204 offset:48128\n"
    "s_waitcnt lgkmcnt(6)\n"
    "v_mfma_f32_16x16x32_bf16 a[16:19], v[100:103], v[80:83], a[16:19]\n"
    "v_exp_f32 v23, v23\n"
    "v_cvt_pk_bf16_f32 v70, v20, v21\n"
    "v_mfma_f32_16x16x32_bf16 a[48:51], v[100:103], v[84:87], a[48:51]\n"
    "global_load_lds_dwordx4 %[lanev], s[46:47] offset:3072\n"
    "v_exp_f32 v24, v24\n"
    "v_mfma_f32_16x16x32_bf16 a[80:83], v[100:103], v[88:91], a[80:83]\n"
    "v_exp_f32 v25, v25\n"
    "v_cvt_pk_bf16_f32 v71, v22, v23\n"
    "v_mfma_f32_16x16x32_bf16 a[112:115], v[100:103], v[92:95], a[112:115]\n"
    "ds_read_b128 v[96:99], %[rbase] offset:0\n"
    "s_waitcnt lgkmcnt(6)\n"
    "v_mfma_f32_16x16x32_bf16 v[52:55], v[104:107], a[148:151], v[52:55]\n"
    "v_exp_f32 v26, v26\n"
    "v_mfma_f32_16x16x32_bf16 v[56:59], v[104:107], a[164:167], v[56:59]\n"
    "v_exp_f32 v27, v27\n"
    "v_cvt_pk_bf16_f32 v74, v24, v25\n"
    "v_mfma_f32_16x16x32_bf16 v[60:63], v[104:107], a[180:183], v[60:63]\n"
    "ds_read_b128 v[100:103], %[rbase] offset:8192\n"
    "s_waitcnt lgkmcnt(6)\n"
    "v_mfma_f32_16x16x32_bf16 a[20:23], v[108:111], v[80:83], a[20:23]\n"
    "v_exp_f32 v28, v28\n"
    "v_mfma_f32_16x16x32_bf16 a[52:55], v[108:111], v[84:87], a[52:55]\n"
    "v_exp_f32 v29, v29\n"
    "v_cvt_pk_bf16_f32 v75, v26, v27\n"
    "v_mfma_f32_16x16x32_bf16 a[84:87], v[108:111], v[88:91], a[84:87]\n"
    "v_exp_f32 v30, v30\n"
    "v_mfma_f32_16x16x32_bf16 a[116:119], v[108:111], v[92:95], a[116:119]\n"
    "ds_read_b128 v[104:107], %[rbase] offset:1024\n"
    "v_exp_f32 v31, v31\n"
    "v_mfma_f32_16x16x32_bf16 v[128:131], v[140:143], v[88:91], v[128:131]\n"
    "v_cvt_pk_bf16_f32 v78, v28, v29\n"
    "s_nop 0\n"
    "s_waitcnt lgkmcnt(6)\n"
    "v_mfma_f32_16x16x32_bf16 v[52:55], v[112:115], a[152:155], v[52:55]\n"
    "v_cvt_pk_bf16_f32 v79, v30, v31\n"
    "s_waitcnt vmcnt(29)\n"
    "v_readlane_b32 s58, v218, 8\n"
    "v_mfma_f32_16x16x32_bf16 v[56:59], v[112:115], a[168:171], v[56:59]\n"
    "v_perm_b32 v164, v150, v148, s60\n"
    "v_perm_b32 v165, v154, v152, s60\n"
    "v_perm_b32 v166, v158, v156, s60\n"
    "v_mfma_f32_16x16x32_bf16 v[60:63], v[112:115], a[184:187], v[60:63]\n"
    "ds_read_b128 v[108:111], %[rbase] offset:9216\n"
    "v_perm_b32 v167, v162, v160, s60\n"
    "s_waitcnt lgkmcnt(6)\n"
    "v_mfma_f32_16x16x32_bf16 a[24:27], v[116:119], v[80:83], a[24:27]\n"
    "s_nop 1\n"
    "s_add_u32 s58, %[wslo], s58\n"
    "s_addc_u32 s59, %[wshi], 0\n"
    "v_mfma_f32_16x16x32_bf16 a[56:59], v[116:119], v[84:87], a[56:59]\n"
    "global_store_dwordx4 %[lane16], v[164:167], s[58:59] sc1\n"
    "v_mfma_f32_16x16x32_bf16 a[88:91], v[116:119], v[88:91], a[88:91]\n"
    "v_mfma_f32_16x16x32_bf16 a[120:123], v[116:119], v[92:95], a[120:123]\n"
    "ds_read_b128 v[112:115], %[rbase] offset:2048\n"
    "s_waitcnt lgkmcnt(6)\n"
    "v_mfma_f32_16x16x32_bf16 v[52:55], v[120:123], a[156:159], v[52:55]\n"
    "v_mfma_f32_16x16x32_bf16 v[56:59], v[120:123], a[172:175], v[56:59]\n"
    "v_mfma_f32_16x16x32_bf16 v[60:63], v[120:123], a[188:191], v[60:63]\n"
    "ds_read_b128 v[116:119], %[rbase] offset:10240\n"
    "s_waitcnt lgkmcnt(6)\n"
    "v_mfma_f32_16x16x32_bf16 a[28:31], v[124:127], v[80:83], a[28:31]\n"
    "v_mfma_f32_16x16x32_bf16 a[60:63], v[124:127], v[84:87], a[60:63]\n"
    "v_mfma_f32_16x16x32_bf16 a[92:95], v[124:127], v[88:91], a[92:95]\n"
    "v_mfma_f32_16x16x32_bf16 a[124:127], v[124:127], v[92:95], a[124:127]\n"
    "ds_read_b128 v[120:123], %[rbase] offset:3072\n"
    "v_mfma_f32_16x16x32_bf16 v[128:131], v[144:147], v[92:95], v[128:131]\n"
    "s_bitcmp1_b32 %[special], s40\n"
    "s_cbranch_scc1 .Lqa32%=\n"
    ".Lra32%=:\n"
    "s_waitcnt lgkmcnt(6)\n"
    "v_mfma_f32_16x16x32_bf16 v[8:11], v[96:99], a[160:163], v[222:225]\n"
    "ds_write_b128 %[lsa], v[230:233]\n"
    "s_add_u32 m0, %[wdst0], 106496\n"
    "v_mfma_f32_16x16x32_bf16 v[12:15], v[96:99], a[176:179], 0\n"
    "ds_read_b128 v[124:127], %[rbase] offset:11264\n"
    "s_waitcnt lgkmcnt(7)\n"
    "v_mfma_f32_16x16x32_bf16 a[0:3], v[100:103], v[64:67], a[0:3]\n"
    "v_exp_f32 v36, v36\n"
    "v_mfma_f32_16x16x32_bf16 a[32:35], v[100:103], v[68:71], a[32:35]\n"
    "v_exp_f32 v37, v37\n"
    "v_mfma_f32_16x16x32_bf16 a[64:67], v[100:103], v[72:75], a[64:67]\n"
    "v_exp_f32 v38, v38\n"
    "v_mfma_f32_16x16x32_bf16 a[96:99], v[100:103], v[76:79], a[96:99]\n"
    "ds_read_b128 v[96:99], %[rbase] offset:4096\n"
    "s_waitcnt lgkmcnt(7)\n"
    "v_mfma_f32_16x16x32_bf16 v[8:11], v[104:107], a[164:167], v[8:11]\n"
    "v_exp_f32 v39, v39\n"
    "v_cvt_pk_bf16_f32 v84, v36, v37\n"
    "v_mfma_f32_16x16x32_bf16 v[12:15], v[104:107], a[180:183], v[12:15]\n"
    "ds_read_b128 v[100:103], %[rbase] offset:12288\n"
    "s_waitcnt lgkmcnt(7)\n"
    "v_mfma_f32_16x16x32_bf16 a[4:7], v[108:111], v[64:67], a[4:7]\n"
    "v_exp_f32 v40, v40\n"
    "v_mfma_f32_16x16x32_bf16 a[36:39], v[108:111], v[68:71], a[36:39]\n"
    "v_exp_f32 v41, v41\n"
    "v_cvt_pk_bf16_f32 v85, v38, v39\n"
    "v_mfma_f32_16x16x32_bf16 a[68:71], v[108:111], v[72:75], a[68:71]\n"
    "v_exp_f32 v42, v42\n"
    "v_mfma_f32_16x16x32_bf16 a[100:103], v[108:111], v[76:79], a[100:103]\n"
    "ds_read_b128 v[104:107], %[rbase] offset:5120\n"
    "v_exp_f32 v43, v43\n"
    "v_mfma_f32_16x16x32_bf16 v[128:131], v[132:135], v[64:67], v[128:131]\n"
    "v_cvt_pk_bf16_f32 v88, v40, v41\n"
    "s_waitcnt lgkmcnt(7)\n"
    "v_mfma_f32_16x16x32_bf16 v[8:11], v[112:115], a[168:171], v[8:11]\n"
    "v_exp_f32 v44, v44\n"
    ".Lk2_p6:\n\ts_cbranch_execz .Lk2_p10\n"
    "v_mfma_f32_16x16x32_bf16 v[12:15], v[112:115], a[184:187], v[12:15]\n"
    "ds_read_b128 v[108:111], %[rbase] offset:13312\n"
    "s_waitcnt lgkmcnt(7)\n"
    "v_mfma_f32_16x16x32_bf16 a[8:11], v[116:119], v[64:67], a[8:11]\n"
    "v_exp_f32 v45, v45\n"
    "v_cvt_pk_bf16_f32 v89, v42, v43\n"
    "v_mfma_f32_16x16x32_bf16 a[40:43], v[116:119], v[68:71], a[40:43]\n"
    "v_exp_f32 v46, v46\n"
    "v_mfma_f32_16x16x32_bf16 a[72:75], v[116:119], v[72:75], a[72:75]\n"
    "v_exp_f32 v47, v47\n"
    "v_cvt_pk_bf16_f32 v92, v44, v45\n"
    "v_mfma_f32_16x16x32_bf16 a[104:107], v[116:119], v[76:79], a[104:107]\n"
    "ds_read_b128 v[112:115], %[rbase] offset:6144\n"
    "s_waitcnt lgkmcnt(7)\n"
    "v_mfma_f32_16x16x32_bf16 v[8:11], v[120:123], a[172:175], v[8:11]\n"
    "v_exp_f32 v52, v52\n"
    "v_mfma_f32_16x16x32_bf16 v[12:15], v[120:123], a[188:191], v[12:15]\n"
    "ds_read_b128 v[116:119], %[rbase] offset:14336\n"
    "s_waitcnt lgkmcnt(6)\n"
    "s_waitcnt vmcnt(5) lgkmcnt(0)\n"
    "s_barrier\n"
    "v_mfma_f32_16x16x32_bf16 a[12:15], v[124:127], v[64:67], a[12:15]\n"
    "global_load_lds_dwordx4 v207, s[46:47] offset:0\n"
    "v_exp_f32 v53, v53\n"
    "v_mfma_f32_16x16x32_bf16 a[44:47], v[124:127], v[68:71], a[44:47]\n"
    "v_cvt_pk_bf16_f32 v93, v46, v47\n"
    "v_exp_f32 v54, v54\n"
    "v_mfma_f32_16x16x32_bf16 a[76:79], v[124:127], v[72:75], a[76:79]\n"
    "v_exp_f32 v55, v55\n"
    "v_cvt_pk_bf16_f32 v86, v52, v53\n"
    "v_mfma_f32_16x16x32_bf16 a[108:111], v[124:127], v[76:79], a[108:111]\n"
    "ds_read_b128 v[120:123], %[rbase] offset:7168\n"
    "global_load_lds_dwordx4 v207, s[46:47] offset:1024\n"
    "v_mfma_f32_16x16x32_bf16 v[128:131], v[136:139], v[68:71], v[128:131]\n"
    "v_exp_f32 v56, v56\n"
    "s_waitcnt lgkmcnt(6)\n"
    "v_mfma_f32_16x16x32_bf16 v[24:27], v[96:99], a[160:163], v[226:229]\n"
    "v_exp_f32 v57, v57\n"
    "v_cvt_pk_bf16_f32 v87, v54, v55\n"
    "v_mfma_f32_16x16x32_bf16 v[28:31], v[96:99], a[176:179], 0\n"
    "ds_read_b128 v[124:127], %[rbase] offset:15360\n"
    "global_load_lds_dwordx4 v207, s[46:47] offset:2048\n"
    "s_waitcnt lgkmcnt(6)\n"
    "v_mfma_f32_16x16x32_bf16 a[16:19], v[100:103], v[64:67], a[16:19]\n"
    "v_exp_f32 v58, v58\n"
    "v_mfma_f32_16x16x32_bf16 a[48:51], v[100:103], v[68:71], a[48:51]\n"
    "v_exp_f32 v59, v59\n"
    "v_cvt_pk_bf16_f32 v90, v56, v57\n"
    "v_mfma_f32_16x16x32_bf16 a[80:83], v[100:103], v[72:75], a[80:83]\n"
    "global_load_lds_dwordx4 v207, s[46:47] offset:3072\n"
    "v_exp_f32 v60, v60\n"
    "v_mfma_f32_16x16x32_bf16 a[112:115], v[100:103], v[76:79], a[112:115]\n"
    "ds_read_b128 v[96:99], %[rbase] offset:32768\n"
    "s_waitcnt lgkmcnt(6)\n"
    "v_mfma_f32_16x16x32_bf16 v[24:27], v[104:107], a[164:167], v[24:27]\n"
    "v_exp_f32 v61, v61\n"
    "v_cvt_pk_bf16_f32 v91, v58, v59\n"
    "v_mfma_f32_16x16x32_bf16 v[28:31], v[104:107], a[180:183], v[28:31]\n"
    "ds_read_b128 v[100:103], %[rbase] offset:40960\n"
    "s_waitcnt lgkmcnt(6)\n"
    "v_mfma_f32_16x16x32_bf16 a[20:23], v[108:111], v[64:67], a[20:23]\n"
    "v_exp_f32 v62, v62\n"
    "v_mfma_f32_16x16x32_bf16 a[52:55], v[108:111], v[68:71], a[52:55]\n"
    "v_exp_f32 v63, v63\n"
    "v_cvt_pk_bf16_f32 v94, v60, v61\n"
    "v_mfma_f32_16x16x32_bf16 a[84:87], v[108:111], v[72:75], a[84:87]\n"
    "s_nop 0\n"
    "v_cvt_pk_bf16_f32 v95, v62, v63\n"
    "v_readlane_b32 s54, v218, 9\n"
    "v_mfma_f32_16x16x32_bf16 a[116:119], v[108:111], v[76:79], a[116:119]\n"
    "ds_read_b128 v[104:107], %[rbase] offset:33792\n"
    "v_perm_b32 v168, v150, v148, s61\n"
    "v_perm_b32 v169, v154, v152, s61\n"
    "v_mfma_f32_16x16x32_bf16 v[128:131], v[140:143], v[72:75], v[128:131]\n"
    "v_perm_b32 v170, v158, v156, s61\n"
    "v_perm_b32 v171, v162, v160, s61\n"
    "s_waitcnt lgkmcnt(6)\n"
    "v_mfma_f32_16x16x32_bf16 v[24:27], v[112:115], a[168:171], v[24:27]\n"
    "s_nop 1\n"
    "s_add_u32 s54, %[wslo], s54\n"
    "s_addc_u32 s55, %[wshi], 0\n"
    "v_mfma_f32_16x16x32_bf16 v[28:31], v[112:115], a[184:187], v[28:31]\n"
    "ds_read_b128 v[108:111], %[rbase] offset:41984\n"
    "global_store_dwordx4 %[lane16], v[168:171], s[54:55] sc1\n"
    "s_waitcnt lgkmcnt(6)\n"
    "v_mfma_f32_16x16x32_bf16 a[24:27], v[116:119], v[64:67], a[24:27]\n"
    "v_readlane_b32 s58, v218, 10\n"
    "v_perm_b32 v164, v151, v149, s60\n"
    "v_perm_b32 v165, v155, v153, s60\n"
    "v_mfma_f32_16x16x32_bf16 a[56:59], v[116:119], v[68:71], a[56:59]\n"
    "v_perm_b32 v166, v159, v157, s60\n"
    "v_perm_b32 v167, v163, v161, s60\n"
    "s_nop 1\n"
    "v_mfma_f32_16x16x32_bf16 a[88:91], v[116:119], v[72:75], a[88:91]\n"
    "s_add_u32 s58, %[wslo], s58\n"
    "s_addc_u32 s59, %[wshi], 0\n"
    "global_store_dwordx4 %[lane16], v[164:167], s[58:59] sc1\n"
    "v_mfma_f32_16x16x32_bf16 a[120:123], v[116:119], v[76:79], a[120:123]\n"
    "ds_read_b128 v[112:115], %[rbase] offset:34816\n"
    "v_readlane_b32 s54, v218, 11\n"
    "s_waitcnt lgkmcnt(6)\n"
    "v_mfma_f32_16x16x32_bf16 v[24:27], v[120:123], a[172:175], v[24:27]\n"
    "v_perm_b32 v168, v151, v149, s61\n"
    "v_perm_b32 v169, v155, v153, s61\n"
    "v_perm_b32 v170, v159, v157, s61\n"
    "v_mfma_f32_16x16x32_bf16 v[28:31], v[120:123], a[188:191], v[28:31]\n"
    "ds_read_b128 v[116:119], %[rbase] offset:43008\n"
    "v_perm_b32 v171, v163, v161, s61\n"
    "s_waitcnt lgkmcnt(6)\n"
    "v_mfma_f32_16x16x32_bf16 a[28:31], v[124:127], v[64:67], a[28:31]\n"
    "s_nop 1\n"
    "s_add_u32 s54, %[wslo], s54\n"
    "s_addc_u32 s55, %[wshi], 0\n"
    "v_mfma_f32_16x16x32_bf16 a[60:63], v[124:127], v[68:71], a[60:63]\n"
    "global_store_dwordx4 %[lane16], v[168:171], s[54:55] sc1\n"
    "v_mfma_f32_16x16x32_bf16 a[92:95], v[124:127], v[72:75], a[92:95]\n"
    "v_mfma_f32_16x16x32_bf16 a[124:127], v[124:127], v[76:79], a[124:127]\n"
    "ds_read_b128 v[120:123], %[rbase] offset:35840\n"
    "v_mfma_f32_16x16x32_bf16 v[128:131], v[144:147], v[76:79], v[128:131]\n"
    "s_cmp_lt_i32 %[na], 50\n"
    "s_cbranch_scc1 .Ld0%=\n"
    "s_waitcnt lgkmcnt(6)\n"
    "v_mfma_f32_16x16x32_bf16 v[44:47], v[96:99], a[176:179], v[222:225]\n"
    "ds_read_b128 v[124:127], %[rbase] offset:44032\n"
    "s_waitcnt lgkmcnt(6)\n"
    "v_mfma_f32_16x16x32_bf16 a[32:35], v[100:103], v[84:87], a[32:35]\n"
    "v_exp_f32 v8, v8\n"
    "ds_read_b128 v[230:233], %[lsa] offset:2048\n"
    "v_mfma_f32_16x16x32_bf16 a[64:67], v[100:103], v[88:91], a[64:67]\n"
    "v_exp_f32 v9, v9\n"
    "ds_read_b128 v[148:151], %[qlds] offset:0\n"
    "v_mfma_f32_16x16x32_bf16 a[96:99], v[100:103], v[92:95], a[96:99]\n"
    "ds_read_b128 v[96:99], %[rbase] offset:36864\n"
    "s_waitcnt lgkmcnt(8)\n"
    "v_mfma_f32_16x16x32_bf16 v[44:47], v[104:107], a[180:183], v[44:47]\n"
    "ds_read_b128 v[100:103], %[rbase] offset:45056\n"
    "s_waitcnt lgkmcnt(8)\n"
    "v_mfma_f32_16x16x32_bf16 a[36:39], v[108:111], v[84:87], a[36:39]\n"
    "v_exp_f32 v10, v10\n"
    "ds_read_b128 v[152:155], %[qlds] offset:1024\n"
    "v_mfma_f32_16x16x32_bf16 a[68:71], v[108:111], v[88:91], a[68:71]\n"
    "v_exp_f32 v11, v11\n"
    "ds_read_b128 v[156:159], %[qlds] offset:2048\n"
    "v_mfma_f32_16x16x32_bf16 a[100:103], v[108:111], v[92:95], a[100:103]\n"
    "ds_read_b128 v[104:107], %[rbase] offset:37888\n"
    "v_cvt_pk_bf16_f32 v72, v8, v9\n"
    "s_waitcnt lgkmcnt(10)\n"
    "v_mfma_f32_16x16x32_bf16 v[44:47], v[112:115], a[184:187], v[44:47]\n"
    "ds_read_b128 v[108:111], %[rbase] offset:46080\n"
    "ds_read_b128 v[160:163], %[qlds] offset:3072\n"
    "s_waitcnt lgkmcnt(11)\n"
    "v_mfma_f32_16x16x32_bf16 a[40:43], v[116:119], v[84:87], a[40:43]\n"
    "v_exp_f32 v12, v12\n"
    "ds_read_b128 v[0:3], %[qlds] offset:4096\n"
    "v_mfma_f32_16x16x32_bf16 a[72:75], v[116:119], v[88:91], a[72:75]\n"
    "v_exp_f32 v13, v13\n"
    "ds_read_b128 v[4:7], %[qlds] offset:5120\n"
    "v_mfma_f32_16x16x32_bf16 a[104:107], v[116:119], v[92:95], a[104:107]\n"
    "ds_read_b128 v[112:115], %[rbase] offset:38912\n"
    "v_cvt_pk_bf16_f32 v73, v10, v11\n"
    "s_waitcnt lgkmcnt(13)\n"
    "v_mfma_f32_16x16x32_bf16 v[44:47], v[120:123], a[188:191], v[44:47]\n"
    "ds_read_b128 v[116:119], %[rbase] offset:47104\n"
    "s_waitcnt lgkmcnt(13)\n"
    "s_waitcnt vmcnt(8)\n"
    "s_barrier\n"
    "v_mfma_f32_16x16x32_bf16 a[44:47], v[124:127], v[84:87], a[44:47]\n"
    "ds_read_b128 v[16:19], %[qlds] offset:6144\n"
    "v_exp_f32 v14, v14\n"
    "ds_read_b128 v[20:23], %[qlds] offset:7168\n"
    "v_mfma_f32_16x16x32_bf16 a[76:79], v[124:127], v[88:91], a[76:79]\n"
    "v_exp_f32 v15, v15\n"
    "v_cvt_pk_bf16_f32 v76, v12, v13\n"
    "v_mfma_f32_16x16x32_bf16 a[108:111], v[124:127], v[92:95], a[108:111]\n"
    "ds_read_b128 v[120:123], %[rbase] offset:39936\n"
    "v_exp_f32 v24, v24\n"
    "v_mfma_f32_16x16x32_bf16 v[128:131], v[136:139], v[84:87], v[128:131]\n"
    "v_exp_f32 v25, v25\n"
    "s_waitcnt lgkmcnt(13)\n"
    "v_mfma_f32_16x16x32_bf16 v[60:63], v[96:99], a[176:179], v[226:229]\n"
    "ds_read_b128 v[124:127], %[rbase] offset:48128\n"
    "v_cvt_pk_bf16_f32 v77, v14, v15\n"
    "s_waitcnt lgkmcnt(13)\n"
    "v_mfma_f32_16x16x32_bf16 a[48:51], v[100:103], v[84:87], a[48:51]\n"
    "v_exp_f32 v26, v26\n"
    "v_mfma_f32_16x16x32_bf16 a[80:83], v[100:103], v[88:91], a[80:83]\n"
    "v_exp_f32 v27, v27\n"
    "v_cvt_pk_bf16_f32 v74, v24, v25\n"
    "v_mfma_f32_16x16x32_bf16 a[112:115], v[100:103], v[92:95], a[112:115]\n"
    "v_exp_f32 v28, v28\n"
    "s_waitcnt lgkmcnt(10)\n"
    "v_mfma_f32_16x16x32_bf16 v[60:63], v[104:107], a[180:183], v[60:63]\n"
    "ds_read_b128 v[100:103], v204 offset:8192\n"
    "s_waitcnt lgkmcnt(10)\n"
    "v_mfma_f32_16x16x32_bf16 a[52:55], v[108:111], v[84:87], a[52:55]\n"
    "v_exp_f32 v29, v29\n"
    "v_cvt_pk_bf16_f32 v75, v26, v27\n"
    "v_mfma_f32_16x16x32_bf16 a[84:87], v[108:111], v[88:91], a[84:87]\n"
    "v_exp_f32 v30, v30\n"
    "v_mfma_f32_16x16x32_bf16 a[116:119], v[108:111], v[92:95], a[116:119]\n"
    "v_exp_f32 v31, v31\n"
    "v_cvt_pk_bf16_f32 v78, v28, v29\n"
    "v_mfma_f32_16x16x32_bf16 v[128:131], v[140:143], v[88:91], v[128:131]\n"
    "s_nop 0\n"
    "v_cvt_pk_bf16_f32 v79, v30, v31\n"
    "s_waitcnt lgkmcnt(6)\n"
    "v_mfma_f32_16x16x32_bf16 v[60:63], v[112:115], a[184:187], v[60:63]\n"
    "ds_read_b128 v[108:111], v204 offset:9216\n"
    "s_waitcnt lgkmcnt(15)\n"
    "s_waitcnt lgkmcnt(6)\n"
    "v_mfma_f32_16x16x32_bf16 a[56:59], v[116:119], v[84:87], a[56:59]\n"
    "v_add_f32 v230, v230, v128\n"
    "s_add_u32 s54, %[cxlo], 0\n"
    "s_addc_u32 s55, %[cxhi], 0\n"
    "v_mfma_f32_16x16x32_bf16 a[88:91], v[116:119], v[88:91], a[88:91]\n"
    "v_mfma_f32_16x16x32_bf16 a[120:123], v[116:119], v[92:95], a[120:123]\n"
    "s_waitcnt lgkmcnt(3)\n"
    "v_mfma_f32_16x16x32_bf16 v[60:63], v[120:123], a[188:191], v[60:63]\n"
    "ds_read_b128 v[116:119], v204 offset:10240\n"
    "s_waitcnt lgkmcnt(3)\n"
    "v_mfma_f32_16x16x32_bf16 a[60:63], v[124:127], v[84:87], a[60:63]\n"
    "v_mfma_f32_16x16x32_bf16 a[92:95], v[124:127], v[88:91], a[92:95]\n"
    "v_mfma_f32_16x16x32_bf16 a[124:127], v[124:127], v[92:95], a[124:127]\n"
    "v_mfma_f32_16x16x32_bf16 v[128:131], v[144:147], v[92:95], v[128:131]\n"
    "v_accvgpr_read_b32 v164, a0\n"
    "v_accvgpr_read_b32 v165, a1\n"
    "v_accvgpr_read_b32 v166, a2\n"
    "v_accvgpr_read_b32 v167, a3\n"
    "v_rcp_f32 v206, v230\n"
    "v_accvgpr_read_b32 v168, a4\n"
    "v_accvgpr_read_b32 v169, a5\n"
    "v_accvgpr_read_b32 v170, a6\n"
    "v_accvgpr_read_b32 v171, a7\n"
    "s_waitcnt lgkmcnt(15)\n"
    "v_pk_add_f32 v[164:165], v[164:165], v[148:149]\n"
    "v_pk_add_f32 v[166:167], v[166:167], v[150:151]\n"
    "v_pk_add_f32 v[168:169], v[168:169], v[152:153]\n"
    "v_pk_add_f32 v[170:171], v[170:171], v[154:155]\n"
    "v_pk_mul_f32 v[164:165], v[164:165], v[206:207] op_sel_hi:[1,0]\n"
    "v_pk_mul_f32 v[166:167], v[166:167], v[206:207] op_sel_hi:[1,0]\n"
    "v_pk_mul_f32 v[168:169], v[168:169], v[206:207] op_sel_hi:[1,0]\n"
    "v_pk_mul_f32 v[170:171], v[170:171], v[206:207] op_sel_hi:[1,0]\n"
    "ds_read_b128 v[32:35], %[qlds] offset:8192\n"
    "ds_read_b128 v[36:39], %[qlds] offset:9216\n"
    "v_cvt_pk_bf16_f32 v64, v164, v165\n"
    "v_accvgpr_read_b32 v172, a8\n"
    "v_accvgpr_read_b32 v173, a9\n"
    "v_cvt_pk_bf16_f32 v65, v166, v167\n"
    "v_accvgpr_read_b32 v174, a10\n"
    "v_accvgpr_read_b32 v175, a11\n"
    "v_cvt_pk_bf16_f32 v66, v168, v169\n"
    "v_accvgpr_read_b32 v176, a12\n"
    "v_accvgpr_read_b32 v177, a13\n"
    "v_cvt_pk_bf16_f32 v67, v170, v171\n"
    "v_accvgpr_read_b32 v178, a14\n"
    "v_accvgpr_read_b32 v179, a15\n"
    "global_store_dwordx4 %[lane16], v[64:67], s[54:55] offset:0 sc1\n"
    "s_waitcnt lgkmcnt(13)\n"
    "v_pk_add_f32 v[172:173], v[172:173], v[156:157]\n"
    "v_pk_add_f32 v[174:175], v[174:175], v[158:159]\n"
    "v_pk_add_f32 v[176:177], v[176:177], v[160:161]\n"
    "v_pk_add_f32 v[178:179], v[178:179], v[162:163]\n"
    "v_pk_mul_f32 v[172:173], v[172:173], v[206:207] op_sel_hi:[1,0]\n"
    "v_pk_mul_f32 v[174:175], v[174:175], v[206:207] op_sel_hi:[1,0]\n"
    "v_pk_mul_f32 v[176:177], v[176:177], v[206:207] op_sel_hi:[1,0]\n"
    "v_pk_mul_f32 v[178:179], v[178:179], v[206:207] op_sel_hi:[1,0]\n"
    "ds_read_b128 v[48:51], %[qlds] offset:10240\n"
    "ds_read_b128 v[52:55], %[qlds] offset:11264\n"
    "v_cvt_pk_bf16_f32 v80, v172, v173\n"
    "v_accvgpr_read_b32 v180, a16\n"
    "v_accvgpr_read_b32 v181, a17\n"
    "v_cvt_pk_bf16_f32 v81, v174, v175\n"
    "v_accvgpr_read_b32 v182, a18\n"
    "v_accvgpr_read_b32 v183, a19\n"
    "v_cvt_pk_bf16_f32 v82, v176, v177\n"
    "v_accvgpr_read_b32 v184, a20\n"
    "v_accvgpr_read_b32 v185, a21\n"
    "v_cvt_pk_bf16_f32 v83, v178, v179\n"
    "v_accvgpr_read_b32 v186, a22\n"
    "v_accvgpr_read_b32 v187, a23\n"
    "global_store_dwordx4 %[lane16], v[80:83], s[54:55] offset:1024 sc1\n"
    "s_waitcnt lgkmcnt(13)\n"
    "v_pk_add_f32 v[180:181], v[180:181], v[0:1]\n"
    "v_pk_add_f32 v[182:183], v[182:183], v[2:3]\n"
    "v_pk_add_f32 v[184:185], v[184:185], v[4:5]\n"
    "v_pk_add_f32 v[186:187], v[186:187], v[6:7]\n"
    "v_pk_mul_f32 v[180:181], v[180:181], v[206:207] op_sel_hi:[1,0]\n"
    "v_pk_mul_f32 v[182:183], v[182:183], v[206:207] op_sel_hi:[1,0]\n"
    "v_pk_mul_f32 v[184:185], v[184:185], v[206:207] op_sel_hi:[1,0]\n"
    "v_pk_mul_f32 v[186:187], v[186:187], v[206:207] op_sel_hi:[1,0]\n"
    "ds_read_b128 v[148:151], %[qlds] offset:12288\n"
    "ds_read_b128 v[152:155], %[qlds] offset:13312\n"
    "v_cvt_pk_bf16_f32 v68, v180, v181\n"
    "v_accvgpr_read_b32 v188, a24\n"
    "v_accvgpr_read_b32 v189, a25\n"
    "v_cvt_pk_bf16_f32 v69, v182, v183\n"
    "v_accvgpr_read_b32 v190, a26\n"
    "v_accvgpr_read_b32 v191, a27\n"
    "v_cvt_pk_bf16_f32 v70, v184, v185\n"
    "v_accvgpr_read_b32 v192, a28\n"
    "v_accvgpr_read_b32 v193, a29\n"
    "v_cvt_pk_bf16_f32 v71, v186, v187\n"
    "v_accvgpr_read_b32 v194, a30\n"
    "v_accvgpr_read_b32 v195, a31\n"
    "global_store_dwordx4 %[lane16], v[68:71], s[54:55] offset:2048 sc1\n"
    "s_waitcnt lgkmcnt(11)\n"
    "v_pk_add_f32 v[188:189], v[188:189], v[16:17]\n"
    "v_pk_add_f32 v[190:191], v[190:191], v[18:19]\n"
    "v_pk_add_f32 v[192:193], v[192:193], v[20:21]\n"
    "v_pk_add_f32 v[194:195], v[194:195], v[22:23]\n"
    "v_pk_mul_f32 v[188:189], v[188:189], v[206:207] op_sel_hi:[1,0]\n"
    "v_pk_mul_f32 v[190:191], v[190:191], v[206:207] op_sel_hi:[1,0]\n"
    "v_pk_mul_f32 v[192:193], v[192:193], v[206:207] op_sel_hi:[1,0]\n"
    "v_pk_mul_f32 v[194:195], v[194:195], v[206:207] op_sel_hi:[1,0]\n"
    "ds_read_b128 v[156:159], %[qlds] offset:14336\n"
    "ds_read_b128 v[160:163], %[qlds] offset:15360\n"
    "v_cvt_pk_bf16_f32 v200, v188, v189\n"
    "v_cvt_pk_bf16_f32 v201, v190, v191\n"
    "v_cvt_pk_bf16_f32 v202, v192, v193\n"
    "v_cvt_pk_bf16_f32 v203, v194, v195\n"
    "global_store_dwordx4 %[lane16], v[200:203], s[54:55] offset:3072 sc1\n"
    "ds_read_b128 v[124:127], v204 offset:11264\n"
    "s_waitcnt lgkmcnt(11)\n"
    "v_mfma_f32_16x16x32_bf16 a[64:67], v[100:103], v[72:75], a[64:67]\n"
    "s_waitcnt lgkmcnt(15)\n"
    "v_add_f32 v231, v231, v129\n"
    "s_add_u32 s54, %[cxlo], 131072\n"
    "s_addc_u32 s55, %[cxhi], 0\n"
    "v_mfma_f32_16x16x32_bf16 a[96:99], v[100:103], v[76:79], a[96:99]\n"
    "ds_read_b128 v[100:103], v204 offset:12288\n"
    "v_exp_f32 v44, v44\n"
    "s_waitcnt lgkmcnt(11)\n"
    "v_mfma_f32_16x16x32_bf16 a[68:71], v[108:111], v[72:75], a[68:71]\n"
    "v_exp_f32 v45, v45\n"
    "v_exp_f32 v46, v46\n"
    "v_mfma_f32_16x16x32_bf16 a[100:103], v[108:111], v[76:79], a[100:103]\n"
    "ds_read_b128 v[108:111], v204 offset:13312\n"
    "v_exp_f32 v47, v47\n"
    "s_waitcnt lgkmcnt(11)\n"
    "v_mfma_f32_16x16x32_bf16 a[72:75], v[116:119], v[72:75], a[72:75]\n"
    "v_cvt_pk_bf16_f32 v92, v44, v45\n"
    "v_exp_f32 v60, v60\n"
    "v_mfma_f32_16x16x32_bf16 a[104:107], v[116:119], v[76:79], a[104:107]\n"
    "ds_read_b128 v[116:119], v204 offset:14336\n"
    "s_waitcnt lgkmcnt(3)\n"
    "s_waitcnt vmcnt(7) lgkmcnt(0)\n"
    "s_barrier\n"
    "v_mfma_f32_16x16x32_bf16 a[76:79], v[124:127], v[72:75], a[76:79]\n"
    "v_exp_f32 v61, v61\n"
    "v_cvt_pk_bf16_f32 v93, v46, v47\n"
    "v_mfma_f32_16x16x32_bf16 a[108:111], v[124:127], v[76:79], a[108:111]\n"
    "ds_read_b128 v[124:127], v204 offset:15360\n"
    "v_exp_f32 v62, v62\n"
    "s_waitcnt lgkmcnt(3)\n"
    "v_mfma_f32_16x16x32_bf16 a[80:83], v[100:103], v[72:75], a[80:83]\n"
    "v_exp_f32 v63, v63\n"
    "v_cvt_pk_bf16_f32 v94, v60, v61\n"
    "s_nop 0\n"
    "v_mfma_f32_16x16x32_bf16 a[112:115], v[100:103], v[76:79], a[112:115]\n"
    "ds_read_b128 v[100:103], v204 offset:40960\n"
    "v_cvt_pk_bf16_f32 v95, v62, v63\n"
    "s_waitcnt lgkmcnt(3)\n"
    "v_mfma_f32_16x16x32_bf16 a[84:87], v[108:111], v[72:75], a[84:87]\n"
    "s_add_u32 s58, %[wslo], %[cw]\n"
    "s_addc_u32 s59, %[wshi], 0\n"
    "v_mfma_f32_16x16x32_bf16 a[116:119], v[108:111], v[76:79], a[116:119]\n"
    "v_add_u32 v214, 0x20000, %[lane16]\n"
    "s_getpc_b64 s[92:93]\n"
    "v_mfma_f32_16x16x32_bf16 v[128:131], v[140:143], v[72:75], v[128:131]\n"
    "ds_read_b128 v[108:111], v204 offset:41984\n"
    "s_waitcnt lgkmcnt(3)\n"
    "v_mfma_f32_16x16x32_bf16 a[88:91], v[116:119], v[72:75], a[88:91]\n"
    "global_load_dwordx4 v[218:221], v214, s[58:59] sc1\n"
    "global_load_dword %[cto], %[cto], s[92:93]\n"
    "v_mfma_f32_16x16x32_bf16 a[120:123], v[116:119], v[76:79], a[120:123]\n"
    "ds_read_b128 v[116:119], v204 offset:43008\n"
    "s_waitcnt lgkmcnt(3)\n"
    "v_mfma_f32_16x16x32_bf16 a[92:95], v[124:127], v[72:75], a[92:95]\n"
    "ds_read_b128 v[0:3], v235 offset:32768\n"
    "ds_read_b128 v[4:7], v235 offset:33792\n"
    "v_mfma_f32_16x16x32_bf16 a[124:127], v[124:127], v[76:79], a[124:127]\n"
    "ds_read_b128 v[16:19], v235 offset:34816\n"
    "ds_read_b128 v[20:23], v235 offset:35840\n"
    "v_mfma_f32_16x16x32_bf16 v[128:131], v[144:147], v[76:79], v[128:131]\n"
    "v_accvgpr_read_b32 v164, a32\n"
    "v_accvgpr_read_b32 v165, a33\n"
    "v_accvgpr_read_b32 v166, a34\n"
    "v_accvgpr_read_b32 v167, a35\n"
    "v_rcp_f32 v206, v231\n"
    "v_accvgpr_read_b32 v168, a36\n"
    "v_accvgpr_read_b32 v169, a37\n"
    "v_accvgpr_read_b32 v170, a38\n"
    "v_accvgpr_read_b32 v171, a39\n"
    "s_waitcnt lgkmcnt(15)\n"
    "v_pk_add_f32 v[164:165], v[164:165], v[32:33]\n"
    "v_pk_add_f32 v[166:167], v[166:167], v[34:35]\n"
    "v_pk_add_f32 v[168:169], v[168:169], v[36:37]\n"
    "v_pk_add_f32 v[170:171], v[170:171], v[38:39]\n"
    "v_pk_mul_f32 v[164:165], v[164:165], v[206:207] op_sel_hi:[1,0]\n"
    "v_pk_mul_f32 v[166:167], v[166:167], v[206:207] op_sel_hi:[1,0]\n"
    "v_pk_mul_f32 v[168:169], v[168:169], v[206:207] op_sel_hi:[1,0]\n"
    "v_pk_mul_f32 v[170:171], v[170:171], v[206:207] op_sel_hi:[1,0]\n"
    "v_cvt_pk_bf16_f32 v84, v164, v165\n"
    "v_accvgpr_read_b32 v172, a40\n"
    "v_accvgpr_read_b32 v173, a41\n"
    "v_cvt_pk_bf16_f32 v85, v166, v167\n"
    "v_accvgpr_read_b32 v174, a42\n"
    "v_accvgpr_read_b32 v175, a43\n"
    "v_cvt_pk_bf16_f32 v86, v168, v169\n"
    "v_accvgpr_read_b32 v176, a44\n"
    "v_accvgpr_read_b32 v177, a45\n"
    "v_cvt_pk_bf16_f32 v87, v170, v171\n"
    "v_accvgpr_read_b32 v178, a46\n"
    "v_accvgpr_read_b32 v179, a47\n"
    "global_store_dwordx4 %[lane16], v[84:87], s[54:55] offset:0 sc1\n"
    "s_waitcnt lgkmcnt(15)\n"
    "v_pk_add_f32 v[172:173], v[172:173], v[48:49]\n"
    "v_pk_add_f32 v[174:175], v[174:175], v[50:51]\n"
    "v_pk_add_f32 v[176:177], v[176:177], v[52:53]\n"
    "v_pk_add_f32 v[178:179], v[178:179], v[54:55]\n"
    "v_pk_mul_f32 v[172:173], v[172:173], v[206:207] op_sel_hi:[1,0]\n"
    "v_pk_mul_f32 v[174:175], v[174:175], v[206:207] op_sel_hi:[1,0]\n"
    "v_pk_mul_f32 v[176:177], v[176:177], v[206:207] op_sel_hi:[1,0]\n"
    "v_pk_mul_f32 v[178:179], v[178:179], v[206:207] op_sel_hi:[1,0]\n"
    "v_cvt_pk_bf16_f32 v222, v172, v173\n"
    "v_accvgpr_read_b32 v180, a48\n"
    "v_accvgpr_read_b32 v181, a49\n"
    "v_cvt_pk_bf16_f32 v223, v174, v175\n"
    "v_accvgpr_read_b32 v182, a50\n"
    "v_accvgpr_read_b32 v183, a51\n"
    "v_cvt_pk_bf16_f32 v224, v176, v177\n"
    "v_accvgpr_read_b32 v184, a52\n"
    "v_accvgpr_read_b32 v185, a53\n"
    "v_cvt_pk_bf16_f32 v225, v178, v179\n"
    "v_accvgpr_read_b32 v186, a54\n"
    "v_accvgpr_read_b32 v187, a55\n"
    "global_store_dwordx4 %[lane16], v[222:225], s[54:55] offset:1024 sc1\n"
    "s_waitcnt lgkmcnt(14)\n"
    "v_pk_add_f32 v[180:181], v[180:181], v[148:149]\n"
    "v_pk_add_f32 v[182:183], v[182:183], v[150:151]\n"
    "v_pk_add_f32 v[184:185], v[184:185], v[152:153]\n"
    "v_pk_add_f32 v[186:187], v[186:187], v[154:155]\n"
    "v_pk_mul_f32 v[180:181], v[180:181], v[206:207] op_sel_hi:[1,0]\n"
    "v_pk_mul_f32 v[182:183], v[182:183], v[206:207] op_sel_hi:[1,0]\n"
    "v_pk_mul_f32 v[184:185], v[184:185], v[206:207] op_sel_hi:[1,0]\n"
    "v_pk_mul_f32 v[186:187], v[186:187], v[206:207] op_sel_hi:[1,0]\n"
    "v_cvt_pk_bf16_f32 v226, v180, v181\n"
    "v_accvgpr_read_b32 v188, a56\n"
    "v_accvgpr_read_b32 v189, a57\n"
    "v_cvt_pk_bf16_f32 v227, v182, v183\n"
    "v_accvgpr_read_b32 v190, a58\n"
    "v_accvgpr_read_b32 v191, a59\n"
    "v_cvt_pk_bf16_f32 v228, v184, v185\n"
    "v_accvgpr_read_b32 v192, a60\n"
    "v_accvgpr_read_b32 v193, a61\n"
    "v_cvt_pk_bf16_f32 v229, v186, v187\n"
    "v_accvgpr_read_b32 v194, a62\n"
    "v_accvgpr_read_b32 v195, a63\n"
    "global_store_dwordx4 %[lane16], v[226:229], s[54:55] offset:2048 sc1\n"
    "s_waitcnt lgkmcnt(12)\n"
    "v_pk_add_f32 v[188:189], v[188:189], v[156:157]\n"
    "v_pk_add_f32 v[190:191], v[190:191], v[158:159]\n"
    "v_pk_add_f32 v[192:193], v[192:193], v[160:161]\n"
    "v_pk_add_f32 v[194:195], v[194:195], v[162:163]\n"
    "v_pk_mul_f32 v[188:189], v[188:189], v[206:207] op_sel_hi:[1,0]\n"
    "v_pk_mul_f32 v[190:191], v[190:191], v[206:207] op_sel_hi:[1,0]\n"
    "v_pk_mul_f32 v[192:193], v[192:193], v[206:207] op_sel_hi:[1,0]\n"
    "v_pk_mul_f32 v[194:195], v[194:195], v[206:207] op_sel_hi:[1,0]\n"
    "v_cvt_pk_bf16_f32 v96, v188, v189\n"
    "v_cvt_pk_bf16_f32 v97, v190, v191\n"
    "v_cvt_pk_bf16_f32 v98, v192, v193\n"
    "v_cvt_pk_bf16_f32 v99, v194, v195\n"
    "global_store_dwordx4 %[lane16], v[96:99], s[54:55] offset:3072 sc1\n"
    "ds_read_b128 v[32:35], v235 offset:36864\n"
    "ds_read_b128 v[36:39], v235 offset:37888\n"
    "ds_read_b128 v[48:51], v235 offset:38912\n"
    "ds_read_b128 v[52:55], v235 offset:39936\n"
    "ds_read_b128 v[124:127], v204 offset:44032\n"
    "s_waitcnt lgkmcnt(11)\n"
    "v_mfma_f32_16x16x32_bf16 a[96:99], v[100:103], v[92:95], a[96:99]\n"
    "ds_read_b128 v[100:103], v204 offset:45056\n"
    "s_waitcnt lgkmcnt(15)\n"
    "v_add_f32 v232, v232, v130\n"
    "s_waitcnt lgkmcnt(11)\n"
    "v_mfma_f32_16x16x32_bf16 a[100:103], v[108:111], v[92:95], a[100:103]\n"
    "ds_read_b128 v[108:111], v204 offset:46080\n"
    "s_add_u32 s54, %[cxlo], 262144\n"
    "s_addc_u32 s55, %[cxhi], 0\n"
    "s_waitcnt lgkmcnt(11)\n"
    "v_mfma_f32_16x16x32_bf16 a[104:107], v[116:119], v[92:95], a[104:107]\n"
    "ds_read_b128 v[116:119], v204 offset:47104\n"
    "s_waitcnt lgkmcnt(3)\n"
    "v_mfma_f32_16x16x32_bf16 a[108:111], v[124:127], v[92:95], a[108:111]\n"
    "ds_read_b128 v[124:127], v204 offset:48128\n"
    "s_waitcnt lgkmcnt(3)\n"
    "v_mfma_f32_16x16x32_bf16 a[112:115], v[100:103], v[92:95], a[112:115]\n"
    "s_waitcnt lgkmcnt(2)\n"
    "v_mfma_f32_16x16x32_bf16 a[116:119], v[108:111], v[92:95], a[116:119]\n"
    "s_waitcnt lgkmcnt(1)\n"
    "v_mfma_f32_16x16x32_bf16 a[120:123], v[116:119], v[92:95], a[120:123]\n"
    "s_waitcnt lgkmcnt(0)\n"
    "v_mfma_f32_16x16x32_bf16 a[124:127], v[124:127], v[92:95], a[124:127]\n"
    "v_mfma_f32_16x16x32_bf16 v[128:131], v[144:147], v[92:95], v[128:131]\n"
    "v_accvgpr_read_b32 v164, a64\n"
    "v_accvgpr_read_b32 v165, a65\n"
    "v_accvgpr_read_b32 v166, a66\n"
    "v_accvgpr_read_b32 v167, a67\n"
    "v_rcp_f32 v206, v232\n"
    "v_accvgpr_read_b32 v168, a68\n"
    "v_accvgpr_read_b32 v169, a69\n"
    "v_accvgpr_read_b32 v170, a70\n"
    "v_accvgpr_read_b32 v171, a71\n"
    "s_waitcnt lgkmcnt(11)\n"
    "v_pk_add_f32 v[164:165], v[164:165], v[0:1]\n"
    "v_pk_add_f32 v[166:167], v[166:167], v[2:3]\n"
    "v_pk_add_f32 v[168:169], v[168:169], v[4:5]\n"
    "v_pk_add_f32 v[170:171], v[170:171], v[6:7]\n"
    "v_pk_mul_f32 v[164:165], v[164:165], v[206:207] op_sel_hi:[1,0]\n"
    "v_pk_mul_f32 v[166:167], v[166:167], v[206:207] op_sel_hi:[1,0]\n"
    "v_pk_mul_f32 v[168:169], v[168:169], v[206:207] op_sel_hi:[1,0]\n"
    "v_pk_mul_f32 v[170:171], v[170:171], v[206:207] op_sel_hi:[1,0]\n"
    "ds_read_b128 v[148:151], v234 offset:0\n"
    "ds_read_b128 v[152:155], v234 offset:1024\n"
    "v_cvt_pk_bf16_f32 v104, v164, v165\n"
    "v_accvgpr_read_b32 v172, a72\n"
    "v_accvgpr_read_b32 v173, a73\n"
    "v_cvt_pk_bf16_f32 v105, v166, v167\n"
    "v_accvgpr_read_b32 v174, a74\n"
    "v_accvgpr_read_b32 v175, a75\n"
    "v_cvt_pk_bf16_f32 v106, v168, v169\n"
    "v_accvgpr_read_b32 v176, a76\n"
    "v_accvgpr_read_b32 v177, a77\n"
    "v_cvt_pk_bf16_f32 v107, v170, v171\n"
    "v_accvgpr_read_b32 v178, a78\n"
    "v_accvgpr_read_b32 v179, a79\n"
    "global_store_dwordx4 %[lane16], v[104:107], s[54:55] offset:0 sc1\n"
    "s_waitcnt lgkmcnt(11)\n"
    "v_pk_add_f32 v[172:173], v[172:173], v[16:17]\n"
    "v_pk_add_f32 v[174:175], v[174:175], v[18:19]\n"
    "v_pk_add_f32 v[176:177], v[176:177], v[20:21]\n"
    "v_pk_add_f32 v[178:179], v[178:179], v[22:23]\n"
    "v_pk_mul_f32 v[172:173], v[172:173], v[206:207] op_sel_hi:[1,0]\n"
    "v_pk_mul_f32 v[174:175], v[174:175], v[206:207] op_sel_hi:[1,0]\n"
    "v_pk_mul_f32 v[176:177], v[176:177], v[206:207] op_sel_hi:[1,0]\n"
    "v_pk_mul_f32 v[178:179], v[178:179], v[206:207] op_sel_hi:[1,0]\n"
    "ds_read_b128 v[156:159], v234 offset:2048\n"
    "ds_read_b128 v[160:163], v234 offset:3072\n"
    "v_cvt_pk_bf16_f32 v112, v172, v173\n"
    "v_accvgpr_read_b32 v180, a80\n"
    "v_accvgpr_read_b32 v181, a81\n"
    "v_cvt_pk_bf16_f32 v113, v174, v175\n"
    "v_accvgpr_read_b32 v182, a82\n"
    "v_accvgpr_read_b32 v183, a83\n"
    "v_cvt_pk_bf16_f32 v114, v176, v177\n"
    "v_accvgpr_read_b32 v184, a84\n"
    "v_accvgpr_read_b32 v185, a85\n"
    "v_cvt_pk_bf16_f32 v115, v178, v179\n"
    "v_accvgpr_read_b32 v186, a86\n"
    "v_accvgpr_read_b32 v187, a87\n"
    "global_store_dwordx4 %[lane16], v[112:115], s[54:55] offset:1024 sc1\n"
    "s_waitcnt lgkmcnt(11)\n"
    "v_pk_add_f32 v[180:181], v[180:181], v[32:33]\n"
    "v_pk_add_f32 v[182:183], v[182:183], v[34:35]\n"
    "v_pk_add_f32 v[184:185], v[184:185], v[36:37]\n"
    "v_pk_add_f32 v[186:187], v[186:187], v[38:39]\n"
    "v_pk_mul_f32 v[180:181], v[180:181], v[206:207] op_sel_hi:[1,0]\n"
    "v_pk_mul_f32 v[182:183], v[182:183], v[206:207] op_sel_hi:[1,0]\n"
    "v_pk_mul_f32 v[184:185], v[184:185], v[206:207] op_sel_hi:[1,0]\n"
    "v_pk_mul_f32 v[186:187], v[186:187], v[206:207] op_sel_hi:[1,0]\n"
    "ds_read_b128 v[0:3], v234 offset:4096\n"
    "ds_read_b128 v[4:7], v234 offset:5120\n"
    "v_cvt_pk_bf16_f32 v120, v180, v181\n"
    "v_accvgpr_read_b32 v188, a88\n"
    "v_accvgpr_read_b32 v189, a89\n"
    "v_cvt_pk_bf16_f32 v121, v182, v183\n"
    "v_accvgpr_read_b32 v190, a90\n"
    "v_accvgpr_read_b32 v191, a91\n"
    "v_cvt_pk_bf16_f32 v122, v184, v185\n"
    "v_accvgpr_read_b32 v192, a92\n"
    "v_accvgpr_read_b32 v193, a93\n"
    "v_cvt_pk_bf16_f32 v123, v186, v187\n"
    "v_accvgpr_read_b32 v194, a94\n"
    "v_accvgpr_read_b32 v195, a95\n"
    "global_store_dwordx4 %[lane16], v[120:123], s[54:55] offset:2048 sc1\n"
    "s_waitcnt lgkmcnt(11)\n"
    "v_pk_add_f32 v[188:189], v[188:189], v[48:49]\n"
    "v_pk_add_f32 v[190:191], v[190:191], v[50:51]\n"
    "v_pk_add_f32 v[192:193], v[192:193], v[52:53]\n"
    "v_pk_add_f32 v[194:195], v[194:195], v[54:55]\n"
    "v_pk_mul_f32 v[188:189], v[188:189], v[206:207] op_sel_hi:[1,0]\n"
    "v_pk_mul_f32 v[190:191], v[190:191], v[206:207] op_sel_hi:[1,0]\n"
    "v_pk_mul_f32 v[192:193], v[192:193], v[206:207] op_sel_hi:[1,0]\n"
    "v_pk_mul_f32 v[194:195], v[194:195], v[206:207] op_sel_hi:[1,0]\n"
    "ds_read_b128 v[16:19], v234 offset:6144\n"
    "ds_read_b128 v[20:23], v234 offset:7168\n"
    "v_cvt_pk_bf16_f32 v88, v188, v189\n"
    "v_cvt_pk_bf16_f32 v89, v190, v191\n"
    "v_cvt_pk_bf16_f32 v90, v192, v193\n"
    "v_cvt_pk_bf16_f32 v91, v194, v195\n"
    "global_store_dwordx4 %[lane16], v[88:91], s[54:55] offset:3072 sc1\n"
    "s_waitcnt lgkmcnt(15)\n"
    "v_add_f32 v233, v233, v131\n"
    "s_add_u32 s54, %[cxlo], 393216\n"
    "s_addc_u32 s55, %[cxhi], 0\n"
    "v_accvgpr_read_b32 v164, a96\n"
    "v_accvgpr_read_b32 v165, a97\n"
    "v_accvgpr_read_b32 v166, a98\n"
    "v_accvgpr_read_b32 v167, a99\n"
    "v_rcp_f32 v206, v233\n"
    "v_accvgpr_read_b32 v168, a100\n"
    "v_accvgpr_read_b32 v169, a101\n"
    "v_accvgpr_read_b32 v170, a102\n"
    "v_accvgpr_read_b32 v171, a103\n"
    "s_waitcnt lgkmcnt(6)\n"
    "v_pk_add_f32 v[164:165], v[164:165], v[148:149]\n"
    "v_pk_add_f32 v[166:167], v[166:167], v[150:151]\n"
    "v_pk_add_f32 v[168:169], v[168:169], v[152:153]\n"
    "v_pk_add_f32 v[170:171], v[170:171], v[154:155]\n"
    "v_pk_mul_f32 v[164:165], v[164:165], v[206:207] op_sel_hi:[1,0]\n"
    "v_pk_mul_f32 v[166:167], v[166:167], v[206:207] op_sel_hi:[1,0]\n"
    "v_pk_mul_f32 v[168:169], v[168:169], v[206:207] op_sel_hi:[1,0]\n"
    "v_pk_mul_f32 v[170:171], v[170:171], v[206:207] op_sel_hi:[1,0]\n"
    "v_cvt_pk_bf16_f32 v164, v164, v165\n"
    "v_accvgpr_read_b32 v172, a104\n"
    "v_accvgpr_read_b32 v173, a105\n"
    "v_cvt_pk_bf16_f32 v165, v166, v167\n"
    "v_accvgpr_read_b32 v174, a106\n"
    "v_accvgpr_read_b32 v175, a107\n"
    "v_cvt_pk_bf16_f32 v166, v168, v169\n"
    "v_accvgpr_read_b32 v176, a108\n"
    "v_accvgpr_read_b32 v177, a109\n"
    "v_cvt_pk_bf16_f32 v167, v170, v171\n"
    "v_accvgpr_read_b32 v178, a110\n"
    "v_accvgpr_read_b32 v179, a111\n"
    "global_store_dwordx4 %[lane16], v[164:167], s[54:55] offset:0 sc1\n"
    "s_waitcnt lgkmcnt(4)\n"
    "v_pk_add_f32 v[172:173], v[172:173], v[156:157]\n"
    "v_pk_add_f32 v[174:175], v[174:175], v[158:159]\n"
    "v_pk_add_f32 v[176:177], v[176:177], v[160:161]\n"
    "v_pk_add_f32 v[178:179], v[178:179], v[162:163]\n"
    "v_pk_mul_f32 v[172:173], v[172:173], v[206:207] op_sel_hi:[1,0]\n"
    "v_pk_mul_f32 v[174:175], v[174:175], v[206:207] op_sel_hi:[1,0]\n"
    "v_pk_mul_f32 v[176:177], v[176:177], v[206:207] op_sel_hi:[1,0]\n"
    "v_pk_mul_f32 v[178:179], v[178:179], v[206:207] op_sel_hi:[1,0]\n"
    "v_cvt_pk_bf16_f32 v172, v172, v173\n"
    "v_accvgpr_read_b32 v180, a112\n"
    "v_accvgpr_read_b32 v181, a113\n"
    "v_cvt_pk_bf16_f32 v173, v174, v175\n"
    "v_accvgpr_read_b32 v182, a114\n"
    "v_accvgpr_read_b32 v183, a115\n"
    "v_cvt_pk_bf16_f32 v174, v176, v177\n"
    "v_accvgpr_read_b32 v184, a116\n"
    "v_accvgpr_read_b32 v185, a117\n"
    "v_cvt_pk_bf16_f32 v175, v178, v179\n"
    "v_accvgpr_read_b32 v186, a118\n"
    "v_accvgpr_read_b32 v187, a119\n"
    "global_store_dwordx4 %[lane16], v[172:175], s[54:55] offset:1024 sc1\n"
    "s_waitcnt lgkmcnt(2)\n"
    "v_pk_add_f32 v[180:181], v[180:181], v[0:1]\n"
    "v_pk_add_f32 v[182:183], v[182:183], v[2:3]\n"
    "v_pk_add_f32 v[184:185], v[184:185], v[4:5]\n"
    "v_pk_add_f32 v[186:187], v[186:187], v[6:7]\n"
    "v_pk_mul_f32 v[180:181], v[180:181], v[206:207] op_sel_hi:[1,0]\n"
    "v_pk_mul_f32 v[182:183], v[182:183], v[206:207] op_sel_hi:[1,0]\n"
    "v_pk_mul_f32 v[184:185], v[184:185], v[206:207] op_sel_hi:[1,0]\n"
    "v_pk_mul_f32 v[186:187], v[186:187], v[206:207] op_sel_hi:[1,0]\n"
    "v_cvt_pk_bf16_f32 v180, v180, v181\n"
    "v_accvgpr_read_b32 v188, a120\n"
    "v_accvgpr_read_b32 v189, a121\n"
    "v_cvt_pk_bf16_f32 v181, v182, v183\n"
    "v_accvgpr_read_b32 v190, a122\n"
    "v_accvgpr_read_b32 v191, a123\n"
    "v_cvt_pk_bf16_f32 v182, v184, v185\n"
    "v_accvgpr_read_b32 v192, a124\n"
    "v_accvgpr_read_b32 v193, a125\n"
    "v_cvt_pk_bf16_f32 v183, v186, v187\n"
    "v_accvgpr_read_b32 v194, a126\n"
    "v_accvgpr_read_b32 v195, a127\n"
    "global_store_dwordx4 %[lane16], v[180:183], s[54:55] offset:2048 sc1\n"
    "s_waitcnt lgkmcnt(0)\n"
    "v_pk_add_f32 v[188:189], v[188:189], v[16:17]\n"
    "v_pk_add_f32 v[190:191], v[190:191], v[18:19]\n"
    "v_pk_add_f32 v[192:193], v[192:193], v[20:21]\n"
    "v_pk_add_f32 v[194:195], v[194:195], v[22:23]\n"
    "v_pk_mul_f32 v[188:189], v[188:189], v[206:207] op_sel_hi:[1,0]\n"
    "v_pk_mul_f32 v[190:191], v[190:191], v[206:207] op_sel_hi:[1,0]\n"
    "v_pk_mul_f32 v[192:193], v[192:193], v[206:207] op_sel_hi:[1,0]\n"
    "v_pk_mul_f32 v[194:195], v[194:195], v[206:207] op_sel_hi:[1,0]\n"
    "v_cvt_pk_bf16_f32 v188, v188, v189\n"
    "v_cvt_pk_bf16_f32 v189, v190, v191\n"
    "v_cvt_pk_bf16_f32 v190, v192, v193\n"
    "v_cvt_pk_bf16_f32 v191, v194, v195\n"
    "global_store_dwordx4 %[lane16], v[188:191], s[54:55] offset:3072 sc1\n"
    ".Lk2_p7:\n\ts_cbranch_execz .Lk2_p11\n"
    "v_mfma_f32_16x16x32_bf16 v[196:199], v[132:135], v[64:67], 0\n"
    "v_accvgpr_write_b32 a128, v64\n"
    "v_accvgpr_write_b32 a129, v65\n"
    "v_accvgpr_write_b32 a130, v66\n"
    "v_accvgpr_write_b32 a131, v67\n"
    "v_mfma_f32_16x16x32_bf16 v[196:199], v[132:135], v[80:83], v[196:199]\n"
    "v_accvgpr_write_b32 a132, v80\n"
    "v_accvgpr_write_b32 a133, v81\n"
    "v_accvgpr_write_b32 a134, v82\n"
    "v_accvgpr_write_b32 a135, v83\n"
    "v_mfma_f32_16x16x32_bf16 v[196:199], v[132:135], v[68:71], v[196:199]\n"
    "v_accvgpr_write_b32 a136, v68\n"
    "v_accvgpr_write_b32 a137, v69\n"
    "v_accvgpr_write_b32 a138, v70\n"
    "v_accvgpr_write_b32 a139, v71\n"
    "v_mfma_f32_16x16x32_bf16 v[196:199], v[132:135], v[200:203], v[196:199]\n"
    "v_accvgpr_write_b32 a140, v200\n"
    "v_accvgpr_write_b32 a141, v201\n"
    "v_accvgpr_write_b32 a142, v202\n"
    "v_accvgpr_write_b32 a143, v203\n"
    "v_mfma_f32_16x16x32_bf16 v[196:199], v[132:135], v[84:87], v[196:199]\n"
    "v_accvgpr_write_b32 a144, v84\n"
    "v_accvgpr_write_b32 a145, v85\n"
    "v_accvgpr_write_b32 a146, v86\n"
    "v_accvgpr_write_b32 a147, v87\n"
    "v_mfma_f32_16x16x32_bf16 v[196:199], v[132:135], v[222:225], v[196:199]\n"
    "v_accvgpr_write_b32 a148, v222\n"
    "v_accvgpr_write_b32 a149, v223\n"
    "v_accvgpr_write_b32 a150, v224\n"
    "v_accvgpr_write_b32 a151, v225\n"
    "v_mfma_f32_16x16x32_bf16 v[196:199], v[132:135], v[226:229], v[196:199]\n"
    "v_accvgpr_write_b32 a152, v226\n"
    "v_accvgpr_write_b32 a153, v227\n"
    "v_accvgpr_write_b32 a154, v228\n"
    "v_accvgpr_write_b32 a155, v229\n"
    "v_mfma_f32_16x16x32_bf16 v[196:199], v[132:135], v[96:99], v[196:199]\n"
    "v_accvgpr_write_b32 a156, v96\n"
    "v_accvgpr_write_b32 a157, v97\n"
    "v_accvgpr_write_b32 a158, v98\n"
    "v_accvgpr_write_b32 a159, v99\n"
    "v_mfma_f32_16x16x32_bf16 v[196:199], v[132:135], v[104:107], v[196:199]\n"
    "v_accvgpr_write_b32 a160, v104\n"
    "v_accvgpr_write_b32 a161, v105\n"
    "v_accvgpr_write_b32 a162, v106\n"
    "v_accvgpr_write_b32 a163, v107\n"
    "v_mfma_f32_16x16x32_bf16 v[196:199], v[132:135], v[112:115], v[196:199]\n"
    "v_accvgpr_write_b32 a164, v112\n"
    "v_accvgpr_write_b32 a165, v113\n"
    "v_accvgpr_write_b32 a166, v114\n"
    "v_accvgpr_write_b32 a167, v115\n"
    "v_mfma_f32_16x16x32_bf16 v[196:199], v[132:135], v[120:123], v[196:199]\n"
    "v_accvgpr_write_b32 a168, v120\n"
    "v_accvgpr_write_b32 a169, v121\n"
    "v_accvgpr_write_b32 a170, v122\n"
    "v_accvgpr_write_b32 a171, v123\n"
    "v_mfma_f32_16x16x32_bf16 v[196:199], v[132:135], v[88:91], v[196:199]\n"
    "v_accvgpr_write_b32 a172, v88\n"
    "v_accvgpr_write_b32 a173, v89\n"
    "v_accvgpr_write_b32 a174, v90\n"
    "v_accvgpr_write_b32 a175, v91\n"
    "v_mfma_f32_16x16x32_bf16 v[196:199], v[132:135], v[164:167], v[196:199]\n"
    "v_accvgpr_write_b32 a176, v164\n"
    "v_accvgpr_write_b32 a177, v165\n"
    "v_accvgpr_write_b32 a178, v166\n"
    "v_accvgpr_write_b32 a179, v167\n"
    "v_mfma_f32_16x16x32_bf16 v[196:199], v[132:135], v[172:175], v[196:199]\n"
    "v_accvgpr_write_b32 a180, v172\n"
    "v_accvgpr_write_b32 a181, v173\n"
    "v_accvgpr_write_b32 a182, v174\n"
    "v_accvgpr_write_b32 a183, v175\n"
    "v_mfma_f32_16x16x32_bf16 v[196:199], v[132:135], v[180:183], v[196:199]\n"
    "v_accvgpr_write_b32 a184, v180\n"
    "v_accvgpr_write_b32 a185, v181\n"
    "v_accvgpr_write_b32 a186, v182\n"
    "v_accvgpr_write_b32 a187, v183\n"
    "v_mfma_f32_16x16x32_bf16 v[196:199], v[132:135], v[188:191], v[196:199]\n"
    "v_accvgpr_write_b32 a188, v188\n"
    "v_accvgpr_write_b32 a189, v189\n"
    "v_accvgpr_write_b32 a190, v190\n"
    "v_accvgpr_write_b32 a191, v191\n"
    "v_min3_f32 v1, v230, v231, v232\n"
    "v_add_f32 v2, v230, v231\n"
    "v_add_f32 v3, v232, v233\n"
    "v_min_f32 v1, v1, v233\n"
    "v_add_f32 v0, v2, v3\n"
    "s_mov_b32 s62, %[psmax]\n"
    "s_mov_b32 s57, %[psmin]\n"
    "v_cmp_nge_f32 s[58:59], s62, v0\n"
    "v_cmp_nle_f32 vcc, s57, v1\n"
    "s_nop 1\n"
    "s_or_b64 s[58:59], s[58:59], vcc\n"
    "v_cmp_class_f32 vcc, v196, %[cnan]\n"
    "s_waitcnt vmcnt(13)\n"
    "s_nop 1\n"
    "s_or_b64 s[58:59], s[58:59], vcc\n"
    "s_cselect_b32 %[flag], 1, 0\n"
    "s_branch .Lend%=\n"
    ".Ld0%=:\n"
    ".Lk2_p8:\n\ts_cbranch_execz .Lk2_p12\n"
    "s_waitcnt lgkmcnt(6)\n"
    "v_mfma_f32_16x16x32_bf16 v[44:47], v[96:99], a[176:179], v[222:225]\n"
    "ds_read_b128 v[124:127], %[rbase] offset:44032\n"
    "v_exp_f32 v8, v8\n"
    "s_waitcnt lgkmcnt(6)\n"
    "v_mfma_f32_16x16x32_bf16 a[32:35], v[100:103], v[84:87], a[32:35]\n"
    "ds_write_b128 v235, a[192:195] offset:32768\n"
    "v_mfma_f32_16x16x32_bf16 a[64:67], v[100:103], v[88:91], a[64:67]\n"
    "v_exp_f32 v9, v9\n"
    "v_mfma_f32_16x16x32_bf16 a[96:99], v[100:103], v[92:95], a[96:99]\n"
    "ds_read_b128 v[96:99], %[rbase] offset:36864\n"
    "s_waitcnt lgkmcnt(7)\n"
    "v_mfma_f32_16x16x32_bf16 v[44:47], v[104:107], a[180:183], v[44:47]\n"
    "ds_read_b128 v[100:103], %[rbase] offset:45056\n"
    "s_waitcnt lgkmcnt(7)\n"
    "v_mfma_f32_16x16x32_bf16 a[36:39], v[108:111], v[84:87], a[36:39]\n"
    "ds_write_b128 v235, a[196:199] offset:33792\n"
    "v_mfma_f32_16x16x32_bf16 a[68:71], v[108:111], v[88:91], a[68:71]\n"
    "v_exp_f32 v10, v10\n"
    "v_mfma_f32_16x16x32_bf16 a[100:103], v[108:111], v[92:95], a[100:103]\n"
    "ds_read_b128 v[104:107], %[rbase] offset:37888\n"
    "s_waitcnt lgkmcnt(8)\n"
    "v_mfma_f32_16x16x32_bf16 v[44:47], v[112:115], a[184:187], v[44:47]\n"
    "ds_read_b128 v[108:111], %[rbase] offset:46080\n"
    "s_waitcnt lgkmcnt(8)\n"
    "v_mfma_f32_16x16x32_bf16 a[40:43], v[116:119], v[84:87], a[40:43]\n"
    "ds_write_b128 v235, a[200:203] offset:34816\n"
    "v_mfma_f32_16x16x32_bf16 a[72:75], v[116:119], v[88:91], a[72:75]\n"
    "v_exp_f32 v11, v11\n"
    "v_mfma_f32_16x16x32_bf16 a[104:107], v[116:119], v[92:95], a[104:107]\n"
    "ds_read_b128 v[112:115], %[rbase] offset:38912\n"
    "s_waitcnt lgkmcnt(9)\n"
    "v_mfma_f32_16x16x32_bf16 v[44:47], v[120:123], a[188:191], v[44:47]\n"
    "ds_read_b128 v[116:119], %[rbase] offset:47104\n"
    "s_waitcnt lgkmcnt(9)\n"
    "s_waitcnt vmcnt(8)\n"
    "s_barrier\n"
    "v_mfma_f32_16x16x32_bf16 a[44:47], v[124:127], v[84:87], a[44:47]\n"
    "ds_write_b128 v235, a[204:207] offset:35840\n"
    "v_cvt_pk_bf16_f32 v72, v8, v9\n"
    "ds_write_b128 v235, a[208:211] offset:36864\n"
    "v_exp_f32 v12, v12\n"
    "ds_write_b128 v235, a[212:215] offset:37888\n"
    "v_exp_f32 v13, v13\n"
    "ds_write_b128 v235, a[216:219] offset:38912\n"
    "v_cvt_pk_bf16_f32 v73, v10, v11\n"
    "ds_write_b128 v235, a[220:223] offset:39936\n"
    "v_exp_f32 v14, v14\n"
    "v_mfma_f32_16x16x32_bf16 a[76:79], v[124:127], v[88:91], a[76:79]\n"
    "v_exp_f32 v15, v15\n"
    "v_mfma_f32_16x16x32_bf16 a[108:111], v[124:127], v[92:95], a[108:111]\n"
    "ds_read_b128 v[120:123], %[rbase] offset:39936\n"
    "ds_write_b128 v234, a[224:227] offset:0\n"
    "v_mfma_f32_16x16x32_bf16 v[128:131], v[136:139], v[84:87], v[128:131]\n"
    "v_cvt_pk_bf16_f32 v76, v12, v13\n"
    "s_waitcnt lgkmcnt(14)\n"
    "v_mfma_f32_16x16x32_bf16 v[60:63], v[96:99], a[176:179], v[226:229]\n"
    "ds_read_b128 v[124:127], %[rbase] offset:48128\n"
    "s_waitcnt lgkmcnt(14)\n"
    "v_mfma_f32_16x16x32_bf16 a[48:51], v[100:103], v[84:87], a[48:51]\n"
    "ds_write_b128 v234, a[228:231] offset:1024\n"
    "v_mfma_f32_16x16x32_bf16 a[80:83], v[100:103], v[88:91], a[80:83]\n"
    "v_exp_f32 v24, v24\n"
    "v_mfma_f32_16x16x32_bf16 a[112:115], v[100:103], v[92:95], a[112:115]\n"
    "ds_write_b128 v234, a[232:235] offset:2048\n"
    "s_waitcnt lgkmcnt(14)\n"
    "v_mfma_f32_16x16x32_bf16 v[60:63], v[104:107], a[180:183], v[60:63]\n"
    "ds_read_b128 v[100:103], v204 offset:8192\n"
    "v_exp_f32 v25, v25\n"
    "s_waitcnt lgkmcnt(14)\n"
    "v_mfma_f32_16x16x32_bf16 a[52:55], v[108:111], v[84:87], a[52:55]\n"
    "ds_write_b128 v234, a[236:239] offset:3072\n"
    "v_cvt_pk_bf16_f32 v77, v14, v15\n"
    "v_mfma_f32_16x16x32_bf16 a[84:87], v[108:111], v[88:91], a[84:87]\n"
    "ds_write_b128 v234, a[240:243] offset:4096\n"
    "v_mfma_f32_16x16x32_bf16 a[116:119], v[108:111], v[92:95], a[116:119]\n"
    "v_exp_f32 v26, v26\n"
    "v_mfma_f32_16x16x32_bf16 v[128:131], v[140:143], v[88:91], v[128:131]\n"
    "ds_write_b128 v234, a[244:247] offset:5120\n"
    "s_waitcnt lgkmcnt(15)\n"
    "v_mfma_f32_16x16x32_bf16 v[60:63], v[112:115], a[184:187], v[60:63]\n"
    "ds_read_b128 v[108:111], v204 offset:9216\n"
    "v_exp_f32 v27, v27\n"
    "s_waitcnt lgkmcnt(15)\n"
    "v_mfma_f32_16x16x32_bf16 a[56:59], v[116:119], v[84:87], a[56:59]\n"
    "ds_write_b128 v234, a[248:251] offset:6144\n"
    "v_cvt_pk_bf16_f32 v74, v24, v25\n"
    "v_mfma_f32_16x16x32_bf16 a[88:91], v[116:119], v[88:91], a[88:91]\n"
    "ds_write_b128 v234, a[252:255] offset:7168\n"
    "v_mfma_f32_16x16x32_bf16 a[120:123], v[116:119], v[92:95], a[120:123]\n"
    "v_exp_f32 v28, v28\n"
    "s_waitcnt lgkmcnt(11)\n"
    "v_mfma_f32_16x16x32_bf16 v[60:63], v[120:123], a[188:191], v[60:63]\n"
    "ds_read_b128 v[116:119], v204 offset:10240\n"
    "v_exp_f32 v29, v29\n"
    "s_waitcnt lgkmcnt(10)\n"
    "v_mfma_f32_16x16x32_bf16 a[60:63], v[124:127], v[84:87], a[60:63]\n"
    "v_cvt_pk_bf16_f32 v75, v26, v27\n"
    "v_exp_f32 v30, v30\n"
    "v_mfma_f32_16x16x32_bf16 a[92:95], v[124:127], v[88:91], a[92:95]\n"
    "v_exp_f32 v31, v31\n"
    "v_cvt_pk_bf16_f32 v78, v28, v29\n"
    "s_nop 0\n"
    "v_mfma_f32_16x16x32_bf16 a[124:127], v[124:127], v[92:95], a[124:127]\n"
    "v_cvt_pk_bf16_f32 v79, v30, v31\n"
    "v_mov_b32 v230, v128\n"
    "s_add_u32 s54, %[cxlo], 0\n"
    "s_addc_u32 s55, %[cxhi], 0\n"
    "v_mfma_f32_16x16x32_bf16 v[128:131], v[144:147], v[92:95], v[128:131]\n"
    "v_accvgpr_read_b32 v164, a0\n"
    "v_accvgpr_read_b32 v165, a1\n"
    "v_accvgpr_read_b32 v166, a2\n"
    "v_accvgpr_read_b32 v167, a3\n"
    "v_rcp_f32 v206, v230\n"
    "v_accvgpr_read_b32 v168, a4\n"
    "v_accvgpr_read_b32 v169, a5\n"
    "v_accvgpr_read_b32 v170, a6\n"
    "v_accvgpr_read_b32 v171, a7\n"
    "v_pk_mul_f32 v[164:165], v[164:165], v[206:207] op_sel_hi:[1,0]\n"
    "v_pk_mul_f32 v[166:167], v[166:167], v[206:207] op_sel_hi:[1,0]\n"
    "v_pk_mul_f32 v[168:169], v[168:169], v[206:207] op_sel_hi:[1,0]\n"
    "v_pk_mul_f32 v[170:171], v[170:171], v[206:207] op_sel_hi:[1,0]\n"
    "v_cvt_pk_bf16_f32 v64, v164, v165\n"
    "v_accvgpr_read_b32 v172, a8\n"
    "v_accvgpr_read_b32 v173, a9\n"
    "v_cvt_pk_bf16_f32 v65, v166, v167\n"
    "v_accvgpr_read_b32 v174, a10\n"
    "v_accvgpr_read_b32 v175, a11\n"
    "v_cvt_pk_bf16_f32 v66, v168, v169\n"
    "v_accvgpr_read_b32 v176, a12\n"
    "v_accvgpr_read_b32 v177, a13\n"
    "v_cvt_pk_bf16_f32 v67, v170, v171\n"
    "v_accvgpr_read_b32 v178, a14\n"
    "v_accvgpr_read_b32 v179, a15\n"
    "global_store_dwordx4 %[lane16], v[64:67], s[54:55] offset:0 sc1\n"
    "v_pk_mul_f32 v[172:173], v[172:173], v[206:207] op_sel_hi:[1,0]\n"
    "v_pk_mul_f32 v[174:175], v[174:175], v[206:207] op_sel_hi:[1,0]\n"
    "v_pk_mul_f32 v[176:177], v[176:177], v[206:207] op_sel_hi:[1,0]\n"
    "v_pk_mul_f32 v[178:179], v[178:179], v[206:207] op_sel_hi:[1,0]\n"
    "v_cvt_pk_bf16_f32 v80, v172, v173\n"
    "v_accvgpr_read_b32 v180, a16\n"
    "v_accvgpr_read_b32 v181, a17\n"
    "v_cvt_pk_bf16_f32 v81, v174, v175\n"
    "v_accvgpr_read_b32 v182, a18\n"
    "v_accvgpr_read_b32 v183, a19\n"
    "v_cvt_pk_bf16_f32 v82, v176, v177\n"
    "v_accvgpr_read_b32 v184, a20\n"
    "v_accvgpr_read_b32 v185, a21\n"
    "v_cvt_pk_bf16_f32 v83, v178, v179\n"
    "v_accvgpr_read_b32 v186, a22\n"
    "v_accvgpr_read_b32 v187, a23\n"
    "global_store_dwordx4 %[lane16], v[80:83], s[54:55] offset:1024 sc1\n"
    "v_pk_mul_f32 v[180:181], v[180:181], v[206:207] op_sel_hi:[1,0]\n"
    "v_pk_mul_f32 v[182:183], v[182:183], v[206:207] op_sel_hi:[1,0]\n"
    "v_pk_mul_f32 v[184:185], v[184:185], v[206:207] op_sel_hi:[1,0]\n"
    "v_pk_mul_f32 v[186:187], v[186:187], v[206:207] op_sel_hi:[1,0]\n"
    "v_cvt_pk_bf16_f32 v68, v180, v181\n"
    "v_accvgpr_read_b32 v188, a24\n"
    "v_accvgpr_read_b32 v189, a25\n"
    "v_cvt_pk_bf16_f32 v69, v182, v183\n"
    "v_accvgpr_read_b32 v190, a26\n"
    "v_accvgpr_read_b32 v191, a27\n"
    "v_cvt_pk_bf16_f32 v70, v184, v185\n"
    "v_accvgpr_read_b32 v192, a28\n"
    "v_accvgpr_read_b32 v193, a29\n"
    "v_cvt_pk_bf16_f32 v71, v186, v187\n"
    "v_accvgpr_read_b32 v194, a30\n"
    "v_accvgpr_read_b32 v195, a31\n"
    "global_store_dwordx4 %[lane16], v[68:71], s[54:55] offset:2048 sc1\n"
    "v_pk_mul_f32 v[188:189], v[188:189], v[206:207] op_sel_hi:[1,0]\n"
    "v_pk_mul_f32 v[190:191], v[190:191], v[206:207] op_sel_hi:[1,0]\n"
    "v_pk_mul_f32 v[192:193], v[192:193], v[206:207] op_sel_hi:[1,0]\n"
    "v_pk_mul_f32 v[194:195], v[194:195], v[206:207] op_sel_hi:[1,0]\n"
    "v_cvt_pk_bf16_f32 v200, v188, v189\n"
    "v_cvt_pk_bf16_f32 v201, v190, v191\n"
    "v_cvt_pk_bf16_f32 v202, v192, v193\n"
    "v_cvt_pk_bf16_f32 v203, v194, v195\n"
    "global_store_dwordx4 %[lane16], v[200:203], s[54:55] offset:3072 sc1\n"
    "ds_read_b128 v[124:127], v204 offset:11264\n"
    "s_waitcnt lgkmcnt(8)\n"
    "v_mfma_f32_16x16x32_bf16 a[64:67], v[100:103], v[72:75], a[64:67]\n"
    "v_mov_b32 v231, v129\n"
    "s_add_u32 s54, %[cxlo], 131072\n"
    "s_addc_u32 s55, %[cxhi], 0\n"
    "v_mfma_f32_16x16x32_bf16 a[96:99], v[100:103], v[76:79], a[96:99]\n"
    "ds_read_b128 v[100:103], v204 offset:12288\n"
    "v_exp_f32 v44, v44\n"
    "s_waitcnt lgkmcnt(5)\n"
    "v_mfma_f32_16x16x32_bf16 a[68:71], v[108:111], v[72:75], a[68:71]\n"
    "v_exp_f32 v45, v45\n"
    "v_exp_f32 v46, v46\n"
    "v_mfma_f32_16x16x32_bf16 a[100:103], v[108:111], v[76:79], a[100:103]\n"
    "ds_read_b128 v[108:111], v204 offset:13312\n"
    "v_exp_f32 v47, v47\n"
    "s_waitcnt lgkmcnt(3)\n"
    "v_mfma_f32_16x16x32_bf16 a[72:75], v[116:119], v[72:75], a[72:75]\n"
    "v_cvt_pk_bf16_f32 v92, v44, v45\n"
    "v_exp_f32 v60, v60\n"
    "v_mfma_f32_16x16x32_bf16 a[104:107], v[116:119], v[76:79], a[104:107]\n"
    "ds_read_b128 v[116:119], v204 offset:14336\n"
    "s_waitcnt lgkmcnt(3)\n"
    "s_waitcnt vmcnt(7) lgkmcnt(0)\n"
    "s_barrier\n"
    "v_mfma_f32_16x16x32_bf16 a[76:79], v[124:127], v[72:75], a[76:79]\n"
    "v_exp_f32 v61, v61\n"
    "v_cvt_pk_bf16_f32 v93, v46, v47\n"
    "v_mfma_f32_16x16x32_bf16 a[108:111], v[124:127], v[76:79], a[108:111]\n"
    "ds_read_b128 v[124:127], v204 offset:15360\n"
    "v_exp_f32 v62, v62\n"
    "s_waitcnt lgkmcnt(3)\n"
    "v_mfma_f32_16x16x32_bf16 a[80:83], v[100:103], v[72:75], a[80:83]\n"
    "v_exp_f32 v63, v63\n"
    "v_cvt_pk_bf16_f32 v94, v60, v61\n"
    "s_nop 0\n"
    "v_mfma_f32_16x16x32_bf16 a[112:115], v[100:103], v[76:79], a[112:115]\n"
    "ds_read_b128 v[100:103], v204 offset:40960\n"
    "v_cvt_pk_bf16_f32 v95, v62, v63\n"
    "s_waitcnt lgkmcnt(3)\n"
    "v_mfma_f32_16x16x32_bf16 a[84:87], v[108:111], v[72:75], a[84:87]\n"
    "s_add_u32 s58, %[wslo], %[cw]\n"
    "s_addc_u32 s59, %[wshi], 0\n"
    "v_mfma_f32_16x16x32_bf16 a[116:119], v[108:111], v[76:79], a[116:119]\n"
    "v_add_u32 v214, 0x20000, %[lane16]\n"
    "s_getpc_b64 s[92:93]\n"
    ".Lk2_tch:\n"
    "v_mfma_f32_16x16x32_bf16 v[128:131], v[140:143], v[72:75], v[128:131]\n"
    "ds_read_b128 v[108:111], v204 offset:41984\n"
    "s_waitcnt lgkmcnt(3)\n"
    "v_mfma_f32_16x16x32_bf16 a[88:91], v[116:119], v[72:75], a[88:91]\n"
    "global_load_dwordx4 v[218:221], v214, s[58:59] sc1\n"
    "global_load_dword %[cto], %[cto], s[92:93]\n"
    "v_mfma_f32_16x16x32_bf16 a[120:123], v[116:119], v[76:79], a[120:123]\n"
    "ds_read_b128 v[116:119], v204 offset:43008\n"
    "s_waitcnt lgkmcnt(3)\n"
    "v_mfma_f32_16x16x32_bf16 a[92:95], v[124:127], v[72:75], a[92:95]\n"
    "v_mfma_f32_16x16x32_bf16 a[124:127], v[124:127], v[76:79], a[124:127]\n"
    "v_mfma_f32_16x16x32_bf16 v[128:131], v[144:147], v[76:79], v[128:131]\n"
    "v_accvgpr_read_b32 v164, a32\n"
    "v_accvgpr_read_b32 v165, a33\n"
    "v_accvgpr_read_b32 v166, a34\n"
    "v_accvgpr_read_b32 v167, a35\n"
    "v_rcp_f32 v206, v231\n"
    "v_accvgpr_read_b32 v168, a36\n"
    "v_accvgpr_read_b32 v169, a37\n"
    "v_accvgpr_read_b32 v170, a38\n"
    "v_accvgpr_read_b32 v171, a39\n"
    "v_pk_mul_f32 v[164:165], v[164:165], v[206:207] op_sel_hi:[1,0]\n"
    "v_pk_mul_f32 v[166:167], v[166:167], v[206:207] op_sel_hi:[1,0]\n"
    "v_pk_mul_f32 v[168:169], v[168:169], v[206:207] op_sel_hi:[1,0]\n"
    "v_pk_mul_f32 v[170:171], v[170:171], v[206:207] op_sel_hi:[1,0]\n"
    "v_cvt_pk_bf16_f32 v84, v164, v165\n"
    "v_accvgpr_read_b32 v172, a40\n"
    "v_accvgpr_read_b32 v173, a41\n"
    "v_cvt_pk_bf16_f32 v85, v166, v167\n"
    "v_accvgpr_read_b32 v174, a42\n"
    "v_accvgpr_read_b32 v175, a43\n"
    "v_cvt_pk_bf16_f32 v86, v168, v169\n"
    "v_accvgpr_read_b32 v176, a44\n"
    "v_accvgpr_read_b32 v177, a45\n"
    "v_cvt_pk_bf16_f32 v87, v170, v171\n"
    "v_accvgpr_read_b32 v178, a46\n"
    "v_accvgpr_read_b32 v179, a47\n"
    "global_store_dwordx4 %[lane16], v[84:87], s[54:55] offset:0 sc1\n"
    "v_pk_mul_f32 v[172:173], v[172:173], v[206:207] op_sel_hi:[1,0]\n"
    "v_pk_mul_f32 v[174:175], v[174:175], v[206:207] op_sel_hi:[1,0]\n"
    "v_pk_mul_f32 v[176:177], v[176:177], v[206:207] op_sel_hi:[1,0]\n"
    "v_pk_mul_f32 v[178:179], v[178:179], v[206:207] op_sel_hi:[1,0]\n"
    "v_cvt_pk_bf16_f32 v222, v172, v173\n"
    "v_accvgpr_read_b32 v180, a48\n"
    "v_accvgpr_read_b32 v181, a49\n"
    "v_cvt_pk_bf16_f32 v223, v174, v175\n"
    "v_accvgpr_read_b32 v182, a50\n"
    "v_accvgpr_read_b32 v183, a51\n"
    "v_cvt_pk_bf16_f32 v224, v176, v177\n"
    "v_accvgpr_read_b32 v184, a52\n"
    "v_accvgpr_read_b32 v185, a53\n"
    "v_cvt_pk_bf16_f32 v225, v178, v179\n"
    "v_accvgpr_read_b32 v186, a54\n"
    "v_accvgpr_read_b32 v187, a55\n"
    "global_store_dwordx4 %[lane16], v[222:225], s[54:55] offset:1024 sc1\n"
    "v_pk_mul_f32 v[180:181], v[180:181], v[206:207] op_sel_hi:[1,0]\n"
    "v_pk_mul_f32 v[182:183], v[182:183], v[206:207] op_sel_hi:[1,0]\n"
    "v_pk_mul_f32 v[184:185], v[184:185], v[206:207] op_sel_hi:[1,0]\n"
    "v_pk_mul_f32 v[186:187], v[186:187], v[206:207] op_sel_hi:[1,0]\n"
    "v_cvt_pk_bf16_f32 v226, v180, v181\n"
    "v_accvgpr_read_b32 v188, a56\n"
    "v_accvgpr_read_b32 v189, a57\n"
    "v_cvt_pk_bf16_f32 v227, v182, v183\n"
    "v_accvgpr_read_b32 v190, a58\n"
    "v_accvgpr_read_b32 v191, a59\n"
    "v_cvt_pk_bf16_f32 v228, v184, v185\n"
    "v_accvgpr_read_b32 v192, a60\n"
    "v_accvgpr_read_b32 v193, a61\n"
    "v_cvt_pk_bf16_f32 v229, v186, v187\n"
    "v_accvgpr_read_b32 v194, a62\n"
    "v_accvgpr_read_b32 v195, a63\n"
    "global_store_dwordx4 %[lane16], v[226:229], s[54:55] offset:2048 sc1\n"
    "v_pk_mul_f32 v[188:189], v[188:189], v[206:207] op_sel_hi:[1,0]\n"
    "v_pk_mul_f32 v[190:191], v[190:191], v[206:207] op_sel_hi:[1,0]\n"
    "v_pk_mul_f32 v[192:193], v[192:193], v[206:207] op_sel_hi:[1,0]\n"
    "v_pk_mul_f32 v[194:195], v[194:195], v[206:207] op_sel_hi:[1,0]\n"
    "v_cvt_pk_bf16_f32 v96, v188, v189\n"
    "v_cvt_pk_bf16_f32 v97, v190, v191\n"
    "v_cvt_pk_bf16_f32 v98, v192, v193\n"
    "v_cvt_pk_bf16_f32 v99, v194, v195\n"
    "global_store_dwordx4 %[lane16], v[96:99], s[54:55] offset:3072 sc1\n"
    "ds_read_b128 v[124:127], v204 offset:44032\n"
    "s_waitcnt lgkmcnt(3)\n"
    "v_mfma_f32_16x16x32_bf16 a[96:99], v[100:103], v[92:95], a[96:99]\n"
    "ds_read_b128 v[100:103], v204 offset:45056\n"
    "v_mov_b32 v232, v130\n"
    "s_waitcnt lgkmcnt(3)\n"
    "v_mfma_f32_16x16x32_bf16 a[100:103], v[108:111], v[92:95], a[100:103]\n"
    "ds_read_b128 v[108:111], v204 offset:46080\n"
    "s_waitcnt lgkmcnt(3)\n"
    "v_mfma_f32_16x16x32_bf16 a[104:107], v[116:119], v[92:95], a[104:107]\n"
    "ds_read_b128 v[116:119], v204 offset:47104\n"
    "s_add_u32 s54, %[cxlo], 262144\n"
    "s_addc_u32 s55, %[cxhi], 0\n"
    "s_waitcnt lgkmcnt(3)\n"
    "v_mfma_f32_16x16x32_bf16 a[108:111], v[124:127], v[92:95], a[108:111]\n"
    "ds_read_b128 v[124:127], v204 offset:48128\n"
    "s_waitcnt lgkmcnt(3)\n"
    "v_mfma_f32_16x16x32_bf16 a[112:115], v[100:103], v[92:95], a[112:115]\n"
    "s_waitcnt lgkmcnt(2)\n"
    "v_mfma_f32_16x16x32_bf16 a[116:119], v[108:111], v[92:95], a[116:119]\n"
    "s_waitcnt lgkmcnt(1)\n"
    "v_mfma_f32_16x16x32_bf16 a[120:123], v[116:119], v[92:95], a[120:123]\n"
    "s_waitcnt lgkmcnt(0)\n"
    "v_mfma_f32_16x16x32_bf16 a[124:127], v[124:127], v[92:95], a[124:127]\n"
    "v_mfma_f32_16x16x32_bf16 v[128:131], v[144:147], v[92:95], v[128:131]\n"
    "v_accvgpr_read_b32 v164, a64\n"
    "v_accvgpr_read_b32 v165, a65\n"
    "v_accvgpr_read_b32 v166, a66\n"
    "v_accvgpr_read_b32 v167, a67\n"
    "v_rcp_f32 v206, v232\n"
    "v_accvgpr_read_b32 v168, a68\n"
    "v_accvgpr_read_b32 v169, a69\n"
    "v_accvgpr_read_b32 v170, a70\n"
    "v_accvgpr_read_b32 v171, a71\n"
    "v_pk_mul_f32 v[164:165], v[164:165], v[206:207] op_sel_hi:[1,0]\n"
    "v_pk_mul_f32 v[166:167], v[166:167], v[206:207] op_sel_hi:[1,0]\n"
    "v_pk_mul_f32 v[168:169], v[168:169], v[206:207] op_sel_hi:[1,0]\n"
    "v_pk_mul_f32 v[170:171], v[170:171], v[206:207] op_sel_hi:[1,0]\n"
    "v_cvt_pk_bf16_f32 v104, v164, v165\n"
    "v_accvgpr_read_b32 v172, a72\n"
    "v_accvgpr_read_b32 v173, a73\n"
    "v_cvt_pk_bf16_f32 v105, v166, v167\n"
    "v_accvgpr_read_b32 v174, a74\n"
    "v_accvgpr_read_b32 v175, a75\n"
    "v_cvt_pk_bf16_f32 v106, v168, v169\n"
    "v_accvgpr_read_b32 v176, a76\n"
    "v_accvgpr_read_b32 v177, a77\n"
    "v_cvt_pk_bf16_f32 v107, v170, v171\n"
    "v_accvgpr_read_b32 v178, a78\n"
    "v_accvgpr_read_b32 v179, a79\n"
    "global_store_dwordx4 %[lane16], v[104:107], s[54:55] offset:0 sc1\n"
    "v_pk_mul_f32 v[172:173], v[172:173], v[206:207] op_sel_hi:[1,0]\n"
    "v_pk_mul_f32 v[174:175], v[174:175], v[206:207] op_sel_hi:[1,0]\n"
    "v_pk_mul_f32 v[176:177], v[176:177], v[206:207] op_sel_hi:[1,0]\n"
    "v_pk_mul_f32 v[178:179], v[178:179], v[206:207] op_sel_hi:[1,0]\n"
    "v_cvt_pk_bf16_f32 v112, v172, v173\n"
    "v_accvgpr_read_b32 v180, a80\n"
    "v_accvgpr_read_b32 v181, a81\n"
    "v_cvt_pk_bf16_f32 v113, v174, v175\n"
    "v_accvgpr_read_b32 v182, a82\n"
    "v_accvgpr_read_b32 v183, a83\n"
    "v_cvt_pk_bf16_f32 v114, v176, v177\n"
    "v_accvgpr_read_b32 v184, a84\n"
    "v_accvgpr_read_b32 v185, a85\n"
    "v_cvt_pk_bf16_f32 v115, v178, v179\n"
    "v_accvgpr_read_b32 v186, a86\n"
    "v_accvgpr_read_b32 v187, a87\n"
    "global_store_dwordx4 %[lane16], v[112:115], s[54:55] offset:1024 sc1\n"
    "v_pk_mul_f32 v[180:181], v[180:181], v[206:207] op_sel_hi:[1,0]\n"
    "v_pk_mul_f32 v[182:183], v[182:183], v[206:207] op_sel_hi:[1,0]\n"
    "v_pk_mul_f32 v[184:185], v[184:185], v[206:207] op_sel_hi:[1,0]\n"
    "v_pk_mul_f32 v[186:187], v[186:187], v[206:207] op_sel_hi:[1,0]\n"
    "v_cvt_pk_bf16_f32 v120, v180, v181\n"
    "v_accvgpr_read_b32 v188, a88\n"
    "v_accvgpr_read_b32 v189, a89\n"
    "v_cvt_pk_bf16_f32 v121, v182, v183\n"
    "v_accvgpr_read_b32 v190, a90\n"
    "v_accvgpr_read_b32 v191, a91\n"
    "v_cvt_pk_bf16_f32 v122, v184, v185\n"
    "v_accvgpr_read_b32 v192, a92\n"
    "v_accvgpr_read_b32 v193, a93\n"
    "v_cvt_pk_bf16_f32 v123, v186, v187\n"
    "v_accvgpr_read_b32 v194, a94\n"
    "v_accvgpr_read_b32 v195, a95\n"
    "global_store_dwordx4 %[lane16], v[120:123], s[54:55] offset:2048 sc1\n"
    "v_pk_mul_f32 v[188:189], v[188:189], v[206:207] op_sel_hi:[1,0]\n"
    "v_pk_mul_f32 v[190:191], v[190:191], v[206:207] op_sel_hi:[1,0]\n"
    "v_pk_mul_f32 v[192:193], v[192:193], v[206:207] op_sel_hi:[1,0]\n"
    "v_pk_mul_f32 v[194:195], v[194:195], v[206:207] op_sel_hi:[1,0]\n"
    "v_cvt_pk_bf16_f32 v88, v188, v189\n"
    "v_cvt_pk_bf16_f32 v89, v190, v191\n"
    "v_cvt_pk_bf16_f32 v90, v192, v193\n"
    "v_cvt_pk_bf16_f32 v91, v194, v195\n"
    "global_store_dwordx4 %[lane16], v[88:91], s[54:55] offset:3072 sc1\n"
    "v_mov_b32 v233, v131\n"
    "s_add_u32 s54, %[cxlo], 393216\n"
    "s_addc_u32 s55, %[cxhi], 0\n"
    "v_accvgpr_read_b32 v164, a96\n"
    "v_accvgpr_read_b32 v165, a97\n"
    "v_accvgpr_read_b32 v166, a98\n"
    "v_accvgpr_read_b32 v167, a99\n"
    "v_rcp_f32 v206, v233\n"
    "v_accvgpr_read_b32 v168, a100\n"
    "v_accvgpr_read_b32 v169, a101\n"
    "v_accvgpr_read_b32 v170, a102\n"
    "v_accvgpr_read_b32 v171, a103\n"
    "v_pk_mul_f32 v[164:165], v[164:165], v[206:207] op_sel_hi:[1,0]\n"
    "v_pk_mul_f32 v[166:167], v[166:167], v[206:207] op_sel_hi:[1,0]\n"
    "v_pk_mul_f32 v[168:169], v[168:169], v[206:207] op_sel_hi:[1,0]\n"
    "v_pk_mul_f32 v[170:171], v[170:171], v[206:207] op_sel_hi:[1,0]\n"
    "v_cvt_pk_bf16_f32 v164, v164, v165\n"
    "v_accvgpr_read_b32 v172, a104\n"
    "v_accvgpr_read_b32 v173, a105\n"
    "v_cvt_pk_bf16_f32 v165, v166, v167\n"
    "v_accvgpr_read_b32 v174, a106\n"
    "v_accvgpr_read_b32 v175, a107\n"
    "v_cvt_pk_bf16_f32 v166, v168, v169\n"
    "v_accvgpr_read_b32 v176, a108\n"
    "v_accvgpr_read_b32 v177, a109\n"
    "v_cvt_pk_bf16_f32 v167, v170, v171\n"
    "v_accvgpr_read_b32 v178, a110\n"
    "v_accvgpr_read_b32 v179, a111\n"
    "global_store_dwordx4 %[lane16], v[164:167], s[54:55] offset:0 sc1\n"
    "v_pk_mul_f32 v[172:173], v[172:173], v[206:207] op_sel_hi:[1,0]\n"
    "v_pk_mul_f32 v[174:175], v[174:175], v[206:207] op_sel_hi:[1,0]\n"
    "v_pk_mul_f32 v[176:177], v[176:177], v[206:207] op_sel_hi:[1,0]\n"
    "v_pk_mul_f32 v[178:179], v[178:179], v[206:207] op_sel_hi:[1,0]\n"
    "v_cvt_pk_bf16_f32 v172, v172, v173\n"
    "v_accvgpr_read_b32 v180, a112\n"
    "v_accvgpr_read_b32 v181, a113\n"
    "v_cvt_pk_bf16_f32 v173, v174, v175\n"
    "v_accvgpr_read_b32 v182, a114\n"
    "v_accvgpr_read_b32 v183, a115\n"
    "v_cvt_pk_bf16_f32 v174, v176, v177\n"
    "v_accvgpr_read_b32 v184, a116\n"
    "v_accvgpr_read_b32 v185, a117\n"
    "v_cvt_pk_bf16_f32 v175, v178, v179\n"
    "v_accvgpr_read_b32 v186, a118\n"
    "v_accvgpr_read_b32 v187, a119\n"
    "global_store_dwordx4 %[lane16], v[172:175], s[54:55] offset:1024 sc1\n"
    "v_pk_mul_f32 v[180:181], v[180:181], v[206:207] op_sel_hi:[1,0]\n"
    "v_pk_mul_f32 v[182:183], v[182:183], v[206:207] op_sel_hi:[1,0]\n"
    "v_pk_mul_f32 v[184:185], v[184:185], v[206:207] op_sel_hi:[1,0]\n"
    "v_pk_mul_f32 v[186:187], v[186:187], v[206:207] op_sel_hi:[1,0]\n"
    "v_cvt_pk_bf16_f32 v180, v180, v181\n"
    "v_accvgpr_read_b32 v188, a120\n"
    "v_accvgpr_read_b32 v189, a121\n"
    "v_cvt_pk_bf16_f32 v181, v182, v183\n"
    "v_accvgpr_read_b32 v190, a122\n"
    "v_accvgpr_read_b32 v191, a123\n"
    "v_cvt_pk_bf16_f32 v182, v184, v185\n"
    "v_accvgpr_read_b32 v192, a124\n"
    "v_accvgpr_read_b32 v193, a125\n"
    "v_cvt_pk_bf16_f32 v183, v186, v187\n"
    "v_accvgpr_read_b32 v194, a126\n"
    "v_accvgpr_read_b32 v195, a127\n"
    "global_store_dwordx4 %[lane16], v[180:183], s[54:55] offset:2048 sc1\n"
    "v_pk_mul_f32 v[188:189], v[188:189], v[206:207] op_sel_hi:[1,0]\n"
    "v_pk_mul_f32 v[190:191], v[190:191], v[206:207] op_sel_hi:[1,0]\n"
    "v_pk_mul_f32 v[192:193], v[192:193], v[206:207] op_sel_hi:[1,0]\n"
    "v_pk_mul_f32 v[194:195], v[194:195], v[206:207] op_sel_hi:[1,0]\n"
    "v_cvt_pk_bf16_f32 v188, v188, v189\n"
    "v_cvt_pk_bf16_f32 v189, v190, v191\n"
    "v_cvt_pk_bf16_f32 v190, v192, v193\n"
    "v_cvt_pk_bf16_f32 v191, v194, v195\n"
    "global_store_dwordx4 %[lane16], v[188:191], s[54:55] offset:3072 sc1\n"
    ".Lk2_p9:\n\ts_cbranch_execz .Lk2_p13\n"
    "v_mfma_f32_16x16x32_bf16 v[196:199], v[132:135], v[64:67], 0\n"
    "v_accvgpr_write_b32 a128, v64\n"
    "v_accvgpr_write_b32 a129, v65\n"
    "v_accvgpr_write_b32 a130, v66\n"
    "v_accvgpr_write_b32 a131, v67\n"
    "v_mfma_f32_16x16x32_bf16 v[196:199], v[132:135], v[80:83], v[196:199]\n"
    "v_accvgpr_write_b32 a132, v80\n"
    "v_accvgpr_write_b32 a133, v81\n"
    "v_accvgpr_write_b32 a134, v82\n"
    "v_accvgpr_write_b32 a135, v83\n"
    "v_mfma_f32_16x16x32_bf16 v[196:199], v[132:135], v[68:71], v[196:199]\n"
    "v_accvgpr_write_b32 a136, v68\n"
    "v_accvgpr_write_b32 a137, v69\n"
    "v_accvgpr_write_b32 a138, v70\n"
    "v_accvgpr_write_b32 a139, v71\n"
    "v_mfma_f32_16x16x32_bf16 v[196:199], v[132:135], v[200:203], v[196:199]\n"
    "v_accvgpr_write_b32 a140, v200\n"
    "v_accvgpr_write_b32 a141, v201\n"
    "v_accvgpr_write_b32 a142, v202\n"
    "v_accvgpr_write_b32 a143, v203\n"
    "v_mfma_f32_16x16x32_bf16 v[196:199], v[132:135], v[84:87], v[196:199]\n"
    "v_accvgpr_write_b32 a144, v84\n"
    "v_accvgpr_write_b32 a145, v85\n"
    "v_accvgpr_write_b32 a146, v86\n"
    "v_accvgpr_write_b32 a147, v87\n"
    "v_mfma_f32_16x16x32_bf16 v[196:199], v[132:135], v[222:225], v[196:199]\n"
    "v_accvgpr_write_b32 a148, v222\n"
    "v_accvgpr_write_b32 a149, v223\n"
    "v_accvgpr_write_b32 a150, v224\n"
    "v_accvgpr_write_b32 a151, v225\n"
    "v_mfma_f32_16x16x32_bf16 v[196:199], v[132:135], v[226:229], v[196:199]\n"
    "v_accvgpr_write_b32 a152, v226\n"
    "v_accvgpr_write_b32 a153, v227\n"
    "v_accvgpr_write_b32 a154, v228\n"
    "v_accvgpr_write_b32 a155, v229\n"
    "v_mfma_f32_16x16x32_bf16 v[196:199], v[132:135], v[96:99], v[196:199]\n"
    "v_accvgpr_write_b32 a156, v96\n"
    "v_accvgpr_write_b32 a157, v97\n"
    "v_accvgpr_write_b32 a158, v98\n"
    "v_accvgpr_write_b32 a159, v99\n"
    "v_mfma_f32_16x16x32_bf16 v[196:199], v[132:135], v[104:107], v[196:199]\n"
    "v_accvgpr_write_b32 a160, v104\n"
    "v_accvgpr_write_b32 a161, v105\n"
    "v_accvgpr_write_b32 a162, v106\n"
    "v_accvgpr_write_b32 a163, v107\n"
    "v_mfma_f32_16x16x32_bf16 v[196:199], v[132:135], v[112:115], v[196:199]\n"
    "v_accvgpr_write_b32 a164, v112\n"
    "v_accvgpr_write_b32 a165, v113\n"
    "v_accvgpr_write_b32 a166, v114\n"
    "v_accvgpr_write_b32 a167, v115\n"
    "v_mfma_f32_16x16x32_bf16 v[196:199], v[132:135], v[120:123], v[196:199]\n"
    "v_accvgpr_write_b32 a168, v120\n"
    "v_accvgpr_write_b32 a169, v121\n"
    "v_accvgpr_write_b32 a170, v122\n"
    "v_accvgpr_write_b32 a171, v123\n"
    "v_mfma_f32_16x16x32_bf16 v[196:199], v[132:135], v[88:91], v[196:199]\n"
    "v_accvgpr_write_b32 a172, v88\n"
    "v_accvgpr_write_b32 a173, v89\n"
    "v_accvgpr_write_b32 a174, v90\n"
    "v_accvgpr_write_b32 a175, v91\n"
    "v_mfma_f32_16x16x32_bf16 v[196:199], v[132:135], v[164:167], v[196:199]\n"
    "v_accvgpr_write_b32 a176, v164\n"
    "v_accvgpr_write_b32 a177, v165\n"
    "v_accvgpr_write_b32 a178, v166\n"
    "v_accvgpr_write_b32 a179, v167\n"
    "v_mfma_f32_16x16x32_bf16 v[196:199], v[132:135], v[172:175], v[196:199]\n"
    "v_accvgpr_write_b32 a180, v172\n"
    "v_accvgpr_write_b32 a181, v173\n"
    "v_accvgpr_write_b32 a182, v174\n"
    "v_accvgpr_write_b32 a183, v175\n"
    "v_mfma_f32_16x16x32_bf16 v[196:199], v[132:135], v[180:183], v[196:199]\n"
    "v_accvgpr_write_b32 a184, v180\n"
    "v_accvgpr_write_b32 a185, v181\n"
    "v_accvgpr_write_b32 a186, v182\n"
    "v_accvgpr_write_b32 a187, v183\n"
    "v_mfma_f32_16x16x32_bf16 v[196:199], v[132:135], v[188:191], v[196:199]\n"
    "v_accvgpr_write_b32 a188, v188\n"
    "v_accvgpr_write_b32 a189, v189\n"
    "v_accvgpr_write_b32 a190, v190\n"
    "v_accvgpr_write_b32 a191, v191\n"
    "v_min3_f32 v1, v230, v231, v232\n"
    "v_add_f32 v2, v230, v231\n"
    "v_add_f32 v3, v232, v233\n"
    "v_min_f32 v1, v1, v233\n"
    "v_add_f32 v0, v2, v3\n"
    "s_mov_b32 s62, %[psmax]\n"
    "s_mov_b32 s57, %[psmin]\n"
    "v_cmp_nge_f32 s[58:59], s62, v0\n"
    "v_cmp_nle_f32 vcc, s57, v1\n"
    "s_nop 1\n"
    "s_or_b64 s[58:59], s[58:59], vcc\n"
    "v_cmp_class_f32 vcc, v196, %[cnan]\n"
    "s_waitcnt vmcnt(13)\n"
    "s_nop 1\n"
    "s_or_b64 s[58:59], s[58:59], vcc\n"
    "s_cselect_b32 %[flag], 1, 0\n"
    "s_branch .Lend%=\n"
    ".Lk2_p10:\n\ts_cbranch_execz .Lk2_pr\n"
    ".Lqaf0%=:\n"
    "s_call_b64 s[50:51], .Lpre%=\n"
    "s_branch .Lraf0%=\n"
    ".Lqaf2%=:\n"
    "s_call_b64 s[50:51], .Lpre%=\n"
    "s_branch .Lraf2%=\n"
    ".Lqaf4%=:\n"
    "s_call_b64 s[50:51], .Lpre%=\n"
    "s_branch .Lraf4%=\n"
    ".Lqaf6%=:\n"
    "s_call_b64 s[50:51], .Lpre%=\n"
    "s_branch .Lraf6%=\n"
    ".Lqa30%=:\n"
    "s_call_b64 s[50:51], .Lpre%=\n"
    "s_branch .Lra30%=\n"
    ".Lqa32%=:\n"
    "s_call_b64 s[50:51], .Lpre%=\n"
    "s_branch .Lra32%=\n"
    ".Lpre%=:\n"
    ".Lk2_p11:\n\ts_cbranch_execz .Lk2_pr\n"
    "s_cmp_gt_i32 s40, %[na]\n"
    "s_cbranch_scc1 .Lfin%=\n"
    "v_accvgpr_mov_b32 a128, a192\n"
    "v_accvgpr_mov_b32 a129, a193\n"
    "v_accvgpr_mov_b32 a130, a194\n"
    "v_accvgpr_mov_b32 a131, a195\n"
    "v_accvgpr_mov_b32 a132, a196\n"
    "v_accvgpr_mov_b32 a133, a197\n"
    "v_accvgpr_mov_b32 a134, a198\n"
    "v_accvgpr_mov_b32 a135, a199\n"
    "v_accvgpr_mov_b32 a136, a200\n"
    "v_accvgpr_mov_b32 a137, a201\n"
    "v_accvgpr_mov_b32 a138, a202\n"
    "v_accvgpr_mov_b32 a139, a203\n"
    "v_accvgpr_mov_b32 a140, a204\n"
    "v_accvgpr_mov_b32 a141, a205\n"
    "v_accvgpr_mov_b32 a142, a206\n"
    "v_accvgpr_mov_b32 a143, a207\n"
    "v_accvgpr_mov_b32 a144, a208\n"
    "v_accvgpr_mov_b32 a145, a209\n"
    "v_accvgpr_mov_b32 a146, a210\n"
    "v_accvgpr_mov_b32 a147, a211\n"
    "v_accvgpr_mov_b32 a148, a212\n"
    "v_accvgpr_mov_b32 a149, a213\n"
    "v_accvgpr_mov_b32 a150, a214\n"
    "v_accvgpr_mov_b32 a151, a215\n"
    "v_accvgpr_mov_b32 a152, a216\n"
    "v_accvgpr_mov_b32 a153, a217\n"
    "v_accvgpr_mov_b32 a154, a218\n"
    "v_accvgpr_mov_b32 a155, a219\n"
    "v_accvgpr_mov_b32 a156, a220\n"
    "v_accvgpr_mov_b32 a157, a221\n"
    "v_accvgpr_mov_b32 a158, a222\n"
    "v_accvgpr_mov_b32 a159, a223\n"
    "v_accvgpr_mov_b32 a160, a224\n"
    "v_accvgpr_mov_b32 a161, a225\n"
    "v_accvgpr_mov_b32 a162, a226\n"
    "v_accvgpr_mov_b32 a163, a227\n"
    "v_accvgpr_mov_b32 a164, a228\n"
    "v_accvgpr_mov_b32 a165, a229\n"
    "v_accvgpr_mov_b32 a166, a230\n"
    "v_accvgpr_mov_b32 a167, a231\n"
    "v_accvgpr_mov_b32 a168, a232\n"
    "v_accvgpr_mov_b32 a169, a233\n"
    "v_accvgpr_mov_b32 a170, a234\n"
    "v_accvgpr_mov_b32 a171, a235\n"
    "v_accvgpr_mov_b32 a172, a236\n"
    "v_accvgpr_mov_b32 a173, a237\n"
    "v_accvgpr_mov_b32 a174, a238\n"
    "v_accvgpr_mov_b32 a175, a239\n"
    "v_accvgpr_mov_b32 a176, a240\n"
    "v_accvgpr_mov_b32 a177, a241\n"
    "v_accvgpr_mov_b32 a178, a242\n"
    "v_accvgpr_mov_b32 a179, a243\n"
    "v_accvgpr_mov_b32 a180, a244\n"
    "v_accvgpr_mov_b32 a181, a245\n"
    "v_accvgpr_mov_b32 a182, a246\n"
    "v_accvgpr_mov_b32 a183, a247\n"
    "v_accvgpr_mov_b32 a184, a248\n"
    "v_accvgpr_mov_b32 a185, a249\n"
    "v_accvgpr_mov_b32 a186, a250\n"
    "v_accvgpr_mov_b32 a187, a251\n"
    "v_accvgpr_mov_b32 a188, a252\n"
    "v_accvgpr_mov_b32 a189, a253\n"
    "v_accvgpr_mov_b32 a190, a254\n"
    "v_accvgpr_mov_b32 a191, a255\n"
    "s_nop 1\n"
    "s_setpc_b64 s[50:51]\n"
    ".Lfin%=:\n"
    ".Lk2_p12:\n\ts_cbranch_execz .Lk2_pr\n"
    "s_nop 7\n"
    "v_mfma_f32_32x32x16_bf16 a[192:207], v[200:203], v[200:203], a[64:79]\n"
    "ds_write_b128 %[qlds], a[0:3] offset:0\n"
    "ds_write_b128 %[qlds], a[4:7] offset:1024\n"
    "v_mfma_f32_32x32x16_bf16 a[208:223], v[200:203], v[200:203], a[80:95]\n"
    "ds_write_b128 %[qlds], a[8:11] offset:2048\n"
    "ds_write_b128 %[qlds], a[12:15] offset:3072\n"
    "v_mfma_f32_32x32x16_bf16 a[224:239], v[200:203], v[200:203], a[96:111]\n"
    "ds_write_b128 %[qlds], a[32:35] offset:8192\n"
    "ds_write_b128 %[qlds], a[36:39] offset:9216\n"
    "v_mfma_f32_32x32x16_bf16 a[240:255], v[200:203], v[200:203], a[112:127]\n"
    "ds_write_b128 %[qlds], a[40:43] offset:10240\n"
    "ds_write_b128 %[qlds], a[44:47] offset:11264\n"
    "v_mfma_f32_32x32x16_bf16 a[64:79], v[200:203], v[200:203], 0\n"
    "ds_write_b128 %[qlds], a[16:19] offset:4096\n"
    "ds_write_b128 %[qlds], a[20:23] offset:5120\n"
    "v_mfma_f32_32x32x16_bf16 a[96:111], v[200:203], v[200:203], 0\n"
    "ds_write_b128 %[qlds], a[24:27] offset:6144\n"
    "ds_write_b128 %[qlds], a[28:31] offset:7168\n"
    "v_mfma_f32_32x32x16_bf16 a[0:15], v[200:203], v[200:203], 0\n"
    "v_mfma_f32_32x32x16_bf16 a[32:47], v[200:203], v[200:203], 0\n"
    "ds_write_b128 %[qlds], a[48:51] offset:12288\n"
    "ds_write_b128 %[qlds], a[52:55] offset:13312\n"
    "ds_write_b128 %[qlds], a[56:59] offset:14336\n"
    "ds_write_b128 %[qlds], a[60:63] offset:15360\n"
    "v_mov_b32 v230, v128\n"
    "v_mov_b32 v231, v129\n"
    "v_mov_b32 v232, v130\n"
    "v_mov_b32 v233, v131\n"
    "v_mfma_f32_32x32x16_bf16 a[80:95], v[200:203], v[200:203], 0\n"
    "v_mfma_f32_32x32x16_bf16 a[112:127], v[200:203], v[200:203], 0\n"
    "v_mov_b32 v128, 0\n"
    "v_mov_b32 v129, 0\n"
    "v_mov_b32 v130, 0\n"
    "v_mov_b32 v131, 0\n"
    "v_mfma_f32_32x32x16_bf16 a[16:31], v[200:203], v[200:203], 0\n"
    "v_mfma_f32_32x32x16_bf16 a[48:63], v[200:203], v[200:203], 0\n"
    "s_setpc_b64 s[50:51]\n"
    ".Lend%=:\n"
    ".Lk2_p13:\n\ts_cbranch_execz .Lk2_pr\n"
    : "+{a[128:131]}"(cq[0]), "+{a[132:135]}"(cq[1]), "+{a[136:139]}"(cq[2]), "+{a[140:143]}"(cq[3]), "+{a[144:147]}"(cq[4]), "+{a[148:151]}"(cq[5]), "+{a[152:155]}"(cq[6]), "+{a[156:159]}"(cq[7]), "+{a[160:163]}"(cq[8]), "+{a[164:167]}"(cq[9]), "+{a[168:171]}"(cq[10]), "+{a[172:175]}"(cq[11]), "+{a[176:179]}"(cq[12]), "+{a[180:183]}"(cq[13]), "+{a[184:187]}"(cq[14]), "+{a[188:191]}"(cq[15]), [flag] "=&s"(redo), "+{v[218:221]}"(rdesc), [rfo] "+v"(rfo), [cto] "+v"(cto)
    : [lane16] "v"(lane16), [lanev] "v"(lanev), [rbase] "v"(rbase), [cnan] "v"(0x207u), [qlds] "v"(ldsb + 131072u + (unsigned)r * 16384u + lane16),
      [dm] "v"(16 * r + (lane & 15) - 4 * q), [lsa] "v"(lsa), [qx] "v"(ldsb + 8192u + (unsigned)r * 32768u + lane16),
      [khlo] "s"((unsigned)(size_t)Kh), [khhi] "s"((unsigned)((size_t)Kh >> 32)), [special] "s"(special), [na] "s"(nA), [kt0] "s"(kt0), [wdst0] "s"(wdst0),
      [wslo] "s"((unsigned)(size_t)ws), [wshi] "s"((unsigned)((size_t)ws >> 32)), [cxlo] "s"((unsigned)(size_t)ctxa), [cxhi] "s"((unsigned)((size_t)ctxa >> 32)),
      [psmax] "i"(__builtin_bit_cast(unsigned, PSUM_MAX)), [psmin] "i"(__builtin_bit_cast(unsigned, PSUM_MIN)), [cw] "i"((unsigned)WS_CNT + C_W * 128u - 131072u)
    : "v0", "v1", "v2", "v3", "v4", "v5", "v6", "v7", "v8", "v9", "v10", "v11", "v12", "v13", "v14", "v15", "v16", "v17", "v18", "v19", "v20", "v21", "v22", "v23", "v24", "v25", "v26", "v27", "v28", "v29", "v30", "v31", "v32", "v33", "v34", "v35", "v36", "v37", "v38", "v39", "v40", "v41", "v42", "v43", "v44", "v45", "v46", "v47", "v48", "v49", "v50", "v51", "v52", "v53", "v54", "v55", "v56", "v57", "v58", "v59", "v60", "v61", "v62", "v63", "v64", "v65", "v66", "v67", "v68", "v69", "v70", "v71", "v72", "v73", "v74", "v75", "v76", "v77", "v78", "v79", "v80", "v81", "v82", "v83", "v84", "v85", "v86", "v87", "v88", "v89", "v90", "v91", "v92", "v93", "v94", "v95", "v96", "v97", "v98", "v99", "v100", "v101", "v102", "v103", "v104", "v105", "v106", "v107", "v108", "v109", "v110", "v111", "v112", "v113", "v114", "v115", "v116", "v117", "v118", "v119", "v120", "v121", "v122", "v123", "v124", "v125", "v126", "v127", "v128", "v129", "v130", "v131", "v132", "v133", "v134", "v135", "v136", "v137", "v138", "v139", "v140", "v141", "v142", "v143", "v144", "v145", "v146", "v147", "v148", "v149", "v150", "v151", "v152", "v153", "v154", "v155", "v156", "v157", "v158", "v159", "v160", "v161", "v162", "v163", "v164", "v165", "v166", "v167", "v168", "v169", "v170", "v171", "v172", "v173", "v174", "v175", "v176", "v177", "v178", "v179", "v180", "v181", "v182", "v183", "v184", "v185", "v186", "v187", "v188", "v189", "v190", "v191", "v192", "v193", "v194", "v195", "v196", "v197", "v198", "v199", "v200", "v201", "v202", "v203", "v204", "v205", "v206", "v207", "v208", "v209", "v210", "v211", "v212", "v213", "v214", "v215", "v216", "v217", "v222", "v223", "v224", "v225", "v226", "v227", "v228", "v229", "v230", "v231", "v232", "v233", "v234", "v235", "a0", "a1", "a2", "a3", "a4", "a5", "a6", "a7", "a8", "a9", "a10", "a11", "a12", "a13", "a14", "a15", "a16", "a17", "a18", "a19", "a20", "a21", "a22", "a23", "a24", "a25", "a26", "a27", "a28", "a29", "a30", "a31", "a32", "a33", "a34", "a35", "a36", "a37", "a38", "a39", "a40", "a41", "a42", "a43", "a44", "a45", "a46", "a47", "a48", "a49", "a50", "a51", "a52", "a53", "a54", "a55", "a56", "a57", "a58", "a59", "a60", "a61", "a62", "a63", "a64", "a65", "a66", "a67", "a68", "a69", "a70", "a71", "a72", "a73", "a74", "a75", "a76", "a77", "a78", "a79", "a80", "a81", "a82", "a83", "a84", "a85", "a86", "a87", "a88", "a89", "a90", "a91", "a92", "a93", "a94", "a95", "a96", "a97", "a98", "a99", "a100", "a101", "a102", "a103", "a104", "a105", "a106", "a107", "a108", "a109", "a110", "a111", "a112", "a113", "a114", "a115", "a116", "a117", "a118", "a119", "a120", "a121", "a122", "a123", "a124", "a125", "a126", "a127", "a192", "a193", "a194", "a195", "a196", "a197", "a198", "a199", "a200", "a201", "a202", "a203", "a204", "a205", "a206", "a207", "a208", "a209", "a210", "a211", "a212", "a213", "a214", "a215", "a216", "a217", "a218", "a219", "a220", "a221", "a222", "a223", "a224", "a225", "a226", "a227", "a228", "a229", "a230", "a231", "a232", "a233", "a234", "a235", "a236", "a237", "a238", "a239", "a240", "a241", "a242", "a243", "a244", "a245", "a246", "a247", "a248", "a249", "a250", "a251", "a252", "a253", "a254", "a255", "s36", "s37", "s38", "s39", "s40", "s41", "s42", "s43", "s44", "s45", "s46", "s47", "s48", "s49", "s50", "s51", "s52", "s53", "s54", "s55", "s56", "s57", "s58", "s59", "s60", "s61", "s62", "s63", "s84", "s85", "s86", "s87", "s88", "s89", "s90", "s91", "s92", "s93", "vcc", "scc", "memory");

#pragma unroll
  for (int i = 0; i < 16; ++i) asm volatile("" : "+a"(cq[i]));
#define CNT(i) (ws + WS_CNT + (size_t)(i) * 128)
#define W_CHECK(v_, i) do { const u32x4 f_ = __builtin_bit_cast(u32x4, v_); \
    if (__builtin_expect(__builtin_amdgcn_ballot_w64(min(min(f_[0], f_[1]), min(f_[2], f_[3])) == 0u) != 0ull, 0)) spin4(CNT(i), lane16); } while (0)
#define PUB0(i, o) asm volatile("s_mov_b64 exec, %3\n\tglobal_store_dword %0, %1, %2 sc1\n\ts_mov_b64 exec, -1" :: "v"(o), "v"(64u), "s"(CNT(i)), "s"(pm0) : "memory")
  unsigned long long pm0 = 1ull; PINS(pm0); pm0 >>= wid;
  int rid = bid;
  const int xg = (bid & 7) * 4 + (bid >> 6);
  const char *wo_s = ws + WS_WO + (size_t)((bid & 7) * 4 + ((bid >> 3) & 3)) * (468 * 1024) + wid * 4096, *dp;
  bf16x8 wt[9][4];
  W_CHECK(rdesc, C_W);
#define X_PUBLISH() do { PUB0(C_X + xg, (unsigned)((rid >> 3) & 7) * 4u); PUB0(C_W + 8, (unsigned)rid * 4u); } while (0)

  if (__builtin_expect(redo != 0u, 0)) {
    WAITV(0);
#pragma unroll 1
    for (int i = 0; i < 4; ++i) slow_tile(wq, ws, h, blk * 8 + r + 2 * i, lane, (char*)ctxa + i * 131072 + lane16);
  }
  { unsigned o_ = lane16;
    asm volatile("s_cmp_eq_u32 %16, 0\n\ts_cbranch_scc1 .Lk2_keep\n\ts_waitcnt vmcnt(0)\n\tglobal_load_dwordx4 %0, %17, %18 offset:0 sc1\n\tglobal_load_dwordx4 %1, %17, %18 offset:1024 sc1\n\tglobal_load_dwordx4 %2, %17, %18 offset:2048 sc1\n\tglobal_load_dwordx4 %3, %17, %18 offset:3072 sc1\n\tv_add_u32 %17, 0x20000, %17\n\tglobal_load_dwordx4 %4, %17, %18 offset:0 sc1\n\tglobal_load_dwordx4 %5, %17, %18 offset:1024 sc1\n\tglobal_load_dwordx4 %6, %17, %18 offset:2048 sc1\n\tglobal_load_dwordx4 %7, %17, %18 offset:3072 sc1\n\tv_add_u32 %17, 0x20000, %17\n\tglobal_load_dwordx4 %8, %17, %18 offset:0 sc1\n\tglobal_load_dwordx4 %9, %17, %18 offset:1024 sc1\n\tglobal_load_dwordx4 %10, %17, %18 offset:2048 sc1\n\tglobal_load_dwordx4 %11, %17, %18 offset:3072 sc1\n\tv_add_u32 %17, 0x20000, %17\n\tglobal_load_dwordx4 %12, %17, %18 offset:0 sc1\n\tglobal_load_dwordx4 %13, %17, %18 offset:1024 sc1\n\tglobal_load_dwordx4 %14, %17, %18 offset:2048 sc1\n\tglobal_load_dwordx4 %15, %17, %18 offset:3072 sc1\n\ts_waitcnt vmcnt(0)\n\t.Lk2_keep:" : "+a"(cq[0]), "+a"(cq[1]), "+a"(cq[2]), "+a"(cq[3]), "+a"(cq[4]), "+a"(cq[5]), "+a"(cq[6]), "+a"(cq[7]), "+a"(cq[8]), "+a"(cq[9]), "+a"(cq[10]), "+a"(cq[11]), "+a"(cq[12]), "+a"(cq[13]), "+a"(cq[14]), "+a"(cq[15]), "+s"(redo), "+v"(o_) : "s"(ctxa) : "scc", "memory"); }
#undef KT
#undef KSRC
  asm volatile(".Lk2_end:" ::: "memory");
  {
  dp = wo_s + 0; PINS(dp);
  GLDSV(wt[0][0], dp, lane16, 0);
  GLDSV(wt[0][1], dp, lane16, 1024);
  GLDSV(wt[0][2], dp, lane16, 2048);
  GLDSV(wt[0][3], dp, lane16, 3072);
  dp = wo_s + 16384; PINS(dp);
  GLDSV(wt[1][0], dp, lane16, 0);
  GLDSV(wt[1][1], dp, lane16, 1024);
  GLDSV(wt[1][2], dp, lane16, 2048);
  GLDSV(wt[1][3], dp, lane16, 3072);
  asm volatile("s_mov_b64 exec, 0\n\ts_cmp_eq_u32 %0, 0\n\ts_cbranch_scc1 .Lk3_p0\n\ts_cmp_eq_u32 %0, 1\n\ts_cbranch_scc1 .Lk3_p1\n\ts_cmp_eq_u32 %0, 2\n\ts_cbranch_scc1 .Lk3_p2\n\ts_cmp_eq_u32 %0, 3\n\ts_cbranch_scc1 .Lk3_p3\n\t.Lk3_pr:\n\ts_mov_b64 exec, -1" :: "s"(wid) : "scc", "memory");
  asm volatile("s_waitcnt vmcnt(8)\n\ts_barrier" : "+v"(cto) :: "memory");
  X_PUBLISH();
  dp = wo_s + 32768; PINS(dp);
  GLDSV(wt[2][0], dp, lane16, 0);
  GLDSV(wt[2][1], dp, lane16, 1024);
  GLDSV(wt[2][2], dp, lane16, 2048);
  GLDSV(wt[2][3], dp, lane16, 3072);
  dp = wo_s + 49152; PINS(dp);
  GLDSV(wt[3][0], dp, lane16, 0);
  GLDSV(wt[3][1], dp, lane16, 1024);
  GLDSV(wt[3][2], dp, lane16, 2048);
  GLDSV(wt[3][3], dp, lane16, 3072);
  dp = wo_s + 65536; PINS(dp);
  GLDSV(wt[4][0], dp, lane16, 0);
  GLDSV(wt[4][1], dp, lane16, 1024);
  GLDSV(wt[4][2], dp, lane16, 2048);
  GLDSV(wt[4][3], dp, lane16, 3072);
  asm volatile("" : "+v"(lane)); asm volatile("" : "+s"(wid), "+s"(bid), "+s"(rid), "+s"(ws), "+s"(ka));
  const u16 *x = (const u16*)ka[2], *b1 = (const u16*)ka[3], *b2 = (const u16*)ka[4]; u16* out = (u16*)ka[5];
  const int tid = wid * 64 + lane;
  const unsigned vo0 = lane16, vo1 = lane16 + 131072u, vo2 = lane16 + 262144u, vo3 = lane16 + 393216u;
  const char *a_o0 = ws + WS_CTX + (size_t)(((rid >> 5) * 16 + wid * 4) * 128 + (rid & 7) * 16) * 1024, *a_o1 = a_o0 + (((rid >> 3) + 1) & 3) * 4096, *a_o2 = a_o0 + (((rid >> 3) + 2) & 3) * 4096, *a_o3 = a_o0 + (((rid >> 3) + 3) & 3) * 4096, *wp0, *wp1;
  bf16x8 wf[18], wl[6], bq[16][4], st[20], pw;
  bid = ((((bid & 7) >> 2) * 16 + (bid >> 4)) << 3) + ((bid & 3) << 1) + ((bid >> 3) & 1);
  const int team = bid >> 3, xs = bid & 7, rteam = rid >> 3, nc = rteam & 3;
  const unsigned q16 = (unsigned)(lane >> 4) * 16u;
  const char* w_s = ws + WS_WO + (size_t)(xs * 4 + wid) * (468 * 1024);
  const unsigned wb0 = ldsb + (unsigned)lane * 16u, wb1 = wb0 + 3 * T_SLOT, wb2 = wb0 + 6 * T_SLOT, sr0 = wb0 + (unsigned)wid * 4096u, sr1 = sr0 + 65536u, sr2 = sr0 + 131072u;
  const unsigned b1_r = ldsb + T_B1 + (unsigned)(9 * wid + (wid & 1)) * 64u + q16, b1_l = ldsb + T_B1 + (unsigned)(9 * wid + ((wid & 1) ? 0 : 8)) * 64u + q16;
  u32x2 bw;
  const unsigned bt = tid < 144 ? (unsigned)tid : 0u;
  unsigned bs0 = (unsigned)xs * 1152u + bt * 8u;
#define B1_PUT() do { LAS float* bl = (LAS float*)((lds_p)smem + T_B1) + (bt >> 4) * 64u + (bt & 15u); \
    bl[0] = bf_lo(bw[0]); bl[16] = bf_hi(bw[0]); bl[32] = bf_lo(bw[1]); bl[48] = bf_hi(bw[1]); } while (0)
  char* p1b = ws + WS_P2 + (size_t)(((rteam & ~3) + wid) * 8 + (rid & 7)) * 73728 + (nc >> 1) * 4096;
  char* p2b = ws + WS_P2 + (size_t)bid * 73728 + (wid >> 1) * 4096;
  char *p1 = p1b + (nc & 1) * 40960, *p1_l = p1b + 32768 + (nc & 1) * 2048, *p2 = p2b + (wid & 1) * 40960, *p2_l = p2b + 32768 + (wid & 1) * 2048;
  const char *a_h0, *a_h1, *a_h2, *a_h3, *a_h4; unsigned vr0, vr1, vr2, vr3, lr0, lr1, lr2, lr3, fw0, fw1, fw2, fw3, fr0, fr1, fdl, fwl;
#define G1_SETUP() do { asm volatile("" : "+v"(lane)); asm volatile("" : "+s"(wid)); const unsigned l_ = (unsigned)lane * 16u, w_ = (unsigned)wid; \
    a_h0 = ws + WS_HN + (size_t)team * 73728; a_h1 = a_h0 + 4096; a_h2 = a_h0 + 8192; a_h3 = a_h0 + 12288; a_h4 = a_h0 + 16384; \
    vr0 = l_ + w_ * T_SLOT; vr1 = l_ + ((w_ + 1u) & 3u) * T_SLOT; vr2 = l_ + ((w_ + 2u) & 3u) * T_SLOT; vr3 = l_ + ((w_ + 3u) & 3u) * T_SLOT; \
    lr0 = vr0 + ldsb + T_F; lr1 = vr1 + ldsb + T_F; lr2 = vr2 + ldsb + T_F; lr3 = vr3 + ldsb + T_F; fr0 = ldsb + T_F + l_; fr1 = fr0 + 65536u; \
    fw0 = fr0 + w_ * 5120u; fw1 = fr0 + w_ * 4096u + ((w_ + 1u) & 3u) * 1024u; fw2 = fr0 + w_ * 4096u + ((w_ + 2u) & 3u) * 1024u; fw3 = fr0 + w_ * 4096u + ((w_ + 3u) & 3u) * 1024u; \
    fdl = 49152u + (w_ >> 1) * 4096u - w_ * 4096u + (w_ & 1u) * 8u; \
    PINS(a_h0); PINS(a_h1); PINS(a_h2); PINS(a_h3); PINS(a_h4); PINS(fdl); PIN(vr0); PIN(vr1); PIN(vr2); PIN(vr3); PIN(lr0); PIN(lr1); PIN(lr2); PIN(lr3); \
    PIN(fw0); PIN(fw1); PIN(fw2); PIN(fw3); PIN(fr0); PIN(fr1); } while (0)
  const int rrow = ((rid & 16) ? 15 - (rid >> 5) : rid >> 5) * 128 + (rid & 8) * 2 + (rid & 6) * 16 + (rid & 1) * 8;
  const int rtok = lane & 7, wh = (lane >> 3) & 1, sg = lane >> 4;
  const unsigned rin = (unsigned)(wid * 16 + (rid & 1) * 8 + rtok) * 16u, rt16 = (unsigned)(rid >> 1);
  const unsigned rob = (rt16 * 18u + 9u * wh) * 1024u + rin;
  const unsigned ro2b = (unsigned)WS_P2 + (unsigned)rteam * (8u * 73728u) + wh * 4096u + rin + (unsigned)sg * (2u * 73728u);
  const unsigned ro2 = ro2b + (rt16 & 3u) * 1024u, ro2l = ro2b + ((rt16 & 3u) >> 1) * 1024u + (rt16 & 1u) * 8u;
  const unsigned rcol2 = (unsigned)(320 * wh + 16 * wid + 4 * sg) * 2u, rc8 = (unsigned)(256 + 16 * wid + 4 * sg + 2 * wh) * 2u, rrow2 = (unsigned)(rrow + rtok) * (DM * 2);
  LAS float* red = (LAS float*)((lds_p)smem + T_RED);
  u32x2 rx[4], rb2[4]; unsigned rx8, rb8; f32x4 h4[5]; bf16x8 rp[9][2];
  unsigned xo4 = rrow2 + rcol2, xo8 = rrow2 + rc8;
  u32x4 se0, se1;
  { const unsigned k_ = 4u * (unsigned)(lane & 1) + (unsigned)((lane >> 2) & 3), o_ = (k_ & 1u) ? 0x3f800000u : 0x3f80u;
#pragma unroll
    for (int i = 0; i < 4; ++i) { const unsigned w_ = (k_ >> 1) == (unsigned)i ? o_ : 0u; se0[i] = (lane & 2) ? 0u : w_; se1[i] = (lane & 2) ? w_ : 0u; } }
  const unsigned fo = (unsigned)(lane & 7) * 4u, fq = fo, fx = (unsigned)(lane & 3) * 4u + (unsigned)(rid >> 1 & 16), fo32 = (unsigned)(lane & 31) * 4u;
  const unsigned long long pmfo = wid ? 0ull : 1ull << xs, pmfq = wid ? 0ull : 1ull << (rid & 7), pmfo32 = wid ? 0ull : 1ull << (rid & 31);
#define PUBLISH(i, fo) asm volatile("s_mov_b64 exec, %3\n\tglobal_store_dword %0, %1, %2 sc1\n\ts_mov_b64 exec, -1" :: "v"(fo), "v"(b1_r), "s"(CNT(i)), "s"(pm##fo) : "memory")
#define T_SETUP() do { PIN(se0); PIN(se1); PIN(fo); PIN(fo32); PIN(xo4); PIN(xo8); PIN(bs0); PINS(p1); PINS(p1_l); PIN(ro2); PIN(ro2l); PIN(b1_r); PINS(x); PINS(b1); PINS(b2); PINS(out); PIN(fx); PINS(a_o1); PINS(a_o2); PINS(a_o3); PINS(w_s); PIN(vo1); PIN(vo2); PIN(vo3); PIN(sr0); PIN(sr1); PIN(sr2); PIN(wb0); PIN(wb1); PIN(wb2); } while (0)
#define GL2(d, vo, sb, off) asm volatile("global_load_dwordx2 %0, %1, %2 offset:%3" : "=v"(d) : "v"(vo), "s"(sb), "i"(off) : "memory")
#define GL1(d, vo, sb) asm volatile("global_load_dword %0, %1, %2" : "=v"(d) : "v"(vo), "s"(sb) : "memory")
  unsigned bo4, bo8;
#define R_XLOAD(n) do { if ((n) == 5) { bo8 = lane16; asm volatile("" : "+v"(bo8)); bo8 = ((unsigned)rrow + ((bo8 >> 4) & 7u)) * (DM * 2); bo4 = xo4 - bo8; bo8 = xo8 - bo8; } if ((n) < 4) GL2(rx[(n) & 3], xo4, x, ((n) & 3) * 128); else if ((n) == 4) GL1(rx8, xo8, x); else if ((n) < 9) GL2(rb2[((n) - 5) & 3], bo4, b2, (((n) - 5) & 3) * 128); \
    else if ((n) == 9) GL1(rb8, bo8, b2); else GL2(bw, bs0, b1, 0); } while (0)
#define R_SEL(v, s1, c) (wh ? ((c) == 0 ? s1 : v[((c) - 1) >> 1 & 3][((c) - 1) & 1]) : ((c) == 8 ? s1 : v[(c) >> 1 & 3][(c) & 1]))
#define HA(c) h4[(c) >> 1][2 * ((c) & 1)]
#define HC(c) h4[(c) >> 1][2 * ((c) & 1) + 1]
#define R_XADD() do { asm volatile("" : "+v"(rx[0]), "+v"(rx[1]), "+v"(rx[2]), "+v"(rx[3]), "+v"(rx8), "+v"(bw)); \
    h4[4] = (f32x4){}; \
    _Pragma("unroll") for (int c = 0; c < 9; ++c) { const unsigned w_ = R_SEL(rx, rx8, c); HA(c) = bf_lo(w_); HC(c) = bf_hi(w_); } \
    _Pragma("unroll") for (int m = 0; m < 5; ++m) PIN(h4[m]); } while (0)
#define R_LN1() do { float sm = 0.f; _Pragma("unroll") for (int c = 0; c < 9; ++c) sm += HA(c) + HC(c);  \
    float mw = x8_sum(sm) * (1.0f / 144), sq = 0.f;  \
    _Pragma("unroll") for (int c = 0; c < 9; ++c) { const float d0 = HA(c) - mw, d1 = HC(c) - mw; sq += d0 * d0; sq += d1 * d1; } \
     sq = x8_sum(sq);  if (lane < 8) *(LAS f32x2*)(red + lane * 8 + wid * 2) = (f32x2){mw, sq}; } while (0)
#define R_LN3() do { const f32x4 s0_ = *(LAS const f32x4*)(red + rtok * 8), s1_ = *(LAS const f32x4*)(red + rtok * 8 + 4); \
    float mean = ((s0_[0] + s0_[2]) + (s1_[0] + s1_[2])) * 0.25f;  \
    const float e0 = s0_[0] - mean, e1 = s0_[2] - mean, e2 = s1_[0] - mean, e3 = s1_[2] - mean; \
    const float var = ((s0_[1] + s0_[3]) + (s1_[1] + s1_[3])) * (1.0f / DM) + ((e0 * e0 + e1 * e1) + (e2 * e2 + e3 * e3)) * 0.25f; \
    float rstd = __builtin_amdgcn_rsqf(var + 1e-5f);  \
    unsigned w_[9]; _Pragma("unroll") for (int c = 0; c < 9; ++c) w_[c] = cvtpk((HA(c) - mean) * rstd, (HC(c) - mean) * rstd);   \
    asm volatile("global_store_dword %0, %1, %2 sc1" :: "v"(rob + (unsigned)sg * 4u), "v"(w_[8]), "s"(ws + WS_HN + 8192) : "memory"); \
    _Pragma("unroll") for (int m = 0; m < 2; ++m) {        \
      const auto a_ = __builtin_amdgcn_permlane32_swap(w_[4 * m], w_[4 * m + 2], false, false), b_ = __builtin_amdgcn_permlane32_swap(w_[4 * m + 1], w_[4 * m + 3], false, false); \
      const auto c_ = __builtin_amdgcn_permlane16_swap(a_[0], b_[0], false, false), d_ = __builtin_amdgcn_permlane16_swap(a_[1], b_[1], false, false); \
      st16o<0>(ws + WS_HN + m * 4096, rob + (unsigned)sg * 1024u, (u32x4){c_[0], c_[1], d_[0], d_[1]}); } } while (0)
#define R_ADDB() do { asm volatile("" : "+v"(rb2[0]), "+v"(rb2[1]), "+v"(rb2[2]), "+v"(rb2[3]), "+v"(rb8), "+v"(h4[0]), "+v"(h4[1])); _Pragma("unroll") for (int c = 0; c < 9; ++c) { const unsigned bw = R_SEL(rb2, rb8, c); HA(c) += bf_lo(bw); HC(c) += bf_hi(bw); } \
    _Pragma("unroll") for (int m = 0; m < 5; ++m) PIN(h4[m]); } while (0)
#define R_OUT() do { unsigned o_[9]; _Pragma("unroll") for (int c = 0; c < 9; ++c) o_[c] = cvtpk(HA(c), HC(c)); \
    _Pragma("unroll") for (int m = 0; m < 4; ++m) \
      *(u32x2*)((char*)out + (xo4 + m * 128)) = wh ? (u32x2){o_[2 * m + 1], o_[2 * m + 2]} : (u32x2){o_[2 * m], o_[2 * m + 1]}; \
    *(unsigned*)((char*)out + xo8) = wh ? o_[0] : o_[8]; } while (0)
  f32x4 ac0[4], ac1[4], ac2[4], ac3[4], ac4[4], ac5[4], ac6[4];
#pragma unroll
  for (int i = 0; i < 16; ++i) bq[i & 3][i >> 2] = cq[i];
  bf16x8 hq[18][4], bb[9], wx[28]; unsigned pk[2][4], pv; float tq[4]; char* sp;
  T_SETUP();
  asm volatile("s_waitcnt vmcnt(16)" :  :: "memory");
  DSWV(sr0, 0, wt[0][0]);
  DSWV(sr0, 1024, wt[0][1]);
  DSWV(sr0, 2048, wt[0][2]);
  DSWV(sr0, 3072, wt[0][3]);
  asm volatile("s_waitcnt vmcnt(12)" :  :: "memory");
  DSWV(sr0, 16384, wt[1][0]);
  DSWV(sr0, 17408, wt[1][1]);
  DSWV(sr0, 18432, wt[1][2]);
  DSWV(sr0, 19456, wt[1][3]);
  poll_issue(pv, CNT(C_X + xg), fx);
  asm volatile("s_waitcnt lgkmcnt(0)\n\ts_barrier" :  :: "memory");
  DSR(wl[0], wb0, 0);
  DSR(wl[1], wb0, 1024);
  DSR(wl[2], wb0, 2048);
  DSR(wl[3], wb0, 3072);
  asm volatile("s_waitcnt lgkmcnt(2)" :  :: "memory");
  asm volatile(".Lk3_p0:\n\ts_cbranch_execz .Lk3_p4" ::: "memory");
  MF0(ac0[0], wl[0], bq[0][0]); SBAR();
  DSR(wl[4], wb0, 4096);
  MF0(ac0[1], wl[0], bq[0][1]); SBAR();
  MF0(ac0[2], wl[0], bq[0][2]); SBAR();
  asm volatile("s_waitcnt vmcnt(9)" :  :: "memory");
  MF0(ac0[3], wl[0], bq[0][3]); SBAR();
  DSWV(sr0, 32768, wt[2][0]);
  MF0(ac1[0], wl[1], bq[0][0]); SBAR();
  DSR(wl[5], wb0, 5120);
  MF0(ac1[1], wl[1], bq[0][1]); SBAR();
  DSWV(sr0, 33792, wt[2][1]);
  MF0(ac1[2], wl[1], bq[0][2]); SBAR();
  MF0(ac1[3], wl[1], bq[0][3]); SBAR();
  asm volatile("s_waitcnt lgkmcnt(4)" :  :: "memory");
  MF0(ac2[0], wl[2], bq[0][0]); SBAR();
  DSR(wl[0], wb0, 6144);
  MF0(ac2[1], wl[2], bq[0][1]); SBAR();
  DSWV(sr0, 34816, wt[2][2]);
  MF0(ac2[2], wl[2], bq[0][2]); SBAR();
  MF0(ac2[3], wl[2], bq[0][3]); SBAR();
  DSWV(sr0, 35840, wt[2][3]);
  MF0(ac3[0], wl[3], bq[0][0]); SBAR();
  DSR(wl[1], wb0, 7168);
  MF0(ac3[1], wl[3], bq[0][1]); SBAR();
  asm volatile("s_waitcnt vmcnt(5)" :  :: "memory");
  MF0(ac3[2], wl[3], bq[0][2]); SBAR();
  DSWV(sr0, 49152, wt[3][0]);
  MF0(ac3[3], wl[3], bq[0][3]); SBAR();
  asm volatile("s_waitcnt lgkmcnt(6)" :  :: "memory");
  MF0(ac4[0], wl[4], bq[0][0]); SBAR();
  DSR(wl[2], wb0, 8192);
  MF0(ac4[1], wl[4], bq[0][1]); SBAR();
  DSWV(sr0, 50176, wt[3][1]);
  MF0(ac4[2], wl[4], bq[0][2]); SBAR();
  MF0(ac4[3], wl[4], bq[0][3]); SBAR();
  DSWV(sr0, 51200, wt[3][2]);
  MF0(ac5[0], wl[5], bq[0][0]); SBAR();
  DSR(wl[3], wb0, 9216);
  MF0(ac5[1], wl[5], bq[0][1]); SBAR();
  DSWV(sr0, 52224, wt[3][3]);
  MF0(ac5[2], wl[5], bq[0][2]); SBAR();
  MF0(ac5[3], wl[5], bq[0][3]); SBAR();
  asm volatile("s_waitcnt lgkmcnt(6)" :  :: "memory");
  asm volatile("s_waitcnt vmcnt(1)" :  :: "memory");
  MF0(ac6[0], wl[0], bq[0][0]); SBAR();
  DSR(wl[4], wb0, 10240);
  MF0(ac6[1], wl[0], bq[0][1]); SBAR();
  asm volatile("s_waitcnt vmcnt(0)" : "+v"(pv) :: "memory");
  if (__builtin_expect(__builtin_amdgcn_ballot_w64(pv == 0u) != 0ull, 0)) spin_until(CNT(C_X + xg), fx);
  GLDSA(bq[4][0], a_o1, vo0, 0);
  GLDSA(bq[4][1], a_o1, vo1, 0);
  GLDSA(bq[4][2], a_o1, vo2, 0);
  GLDSA(bq[4][3], a_o1, vo3, 0);
  GLDSA(bq[5][0], a_o2, vo0, 0);
  GLDSA(bq[5][1], a_o2, vo1, 0);
  GLDSA(bq[5][2], a_o2, vo2, 0);
  GLDSA(bq[5][3], a_o2, vo3, 0);
  GLDSA(bq[6][0], a_o3, vo0, 0);
  GLDSA(bq[6][1], a_o3, vo1, 0);
  GLDSA(bq[6][2], a_o3, vo2, 0);
  GLDSA(bq[6][3], a_o3, vo3, 0);
  MF0(ac6[2], wl[0], bq[0][2]); SBAR();
  DSWV(sr1, 0, wt[4][0]);
  MF0(ac6[3], wl[0], bq[0][3]); SBAR();
  GLDSA(bq[7][0], a_o1, vo0, 1024);
  MF(ac0[0], wl[1], bq[1][0]); SBAR();
  DSR(wl[5], wb0, 11264);
  MF(ac0[1], wl[1], bq[1][1]); SBAR();
  DSWV(sr1, 1024, wt[4][1]);
  MF(ac0[2], wl[1], bq[1][2]); SBAR();
  MF(ac0[3], wl[1], bq[1][3]); SBAR();
  GLDSA(bq[7][1], a_o1, vo1, 1024);
  asm volatile("s_waitcnt lgkmcnt(5)" :  :: "memory");
  MF(ac1[0], wl[2], bq[1][0]); SBAR();
  DSR(wl[0], wb0, 12288);
  MF(ac1[1], wl[2], bq[1][1]); SBAR();
  DSWV(sr1, 2048, wt[4][2]);
  MF(ac1[2], wl[2], bq[1][2]); SBAR();
  MF(ac1[3], wl[2], bq[1][3]); SBAR();
  DSWV(sr1, 3072, wt[4][3]);
  MF(ac2[0], wl[3], bq[1][0]); SBAR();
  DSR(wl[1], wb0, 13312);
  GLDSA(bq[7][2], a_o1, vo2, 1024);
  MF(ac2[1], wl[3], bq[1][1]); SBAR();
  MF(ac2[2], wl[3], bq[1][2]); SBAR();
  MF(ac2[3], wl[3], bq[1][3]); SBAR();
  asm volatile("s_waitcnt lgkmcnt(5)" :  :: "memory");
  MF(ac3[0], wl[4], bq[1][0]); SBAR();
  DSR(wl[2], wb0, 14336);
  GLDSA(bq[7][3], a_o1, vo3, 1024);
  MF(ac3[1], wl[4], bq[1][1]); SBAR();
  MF(ac3[2], wl[4], bq[1][2]); SBAR();
  MF(ac3[3], wl[4], bq[1][3]); SBAR();
  MF(ac4[0], wl[5], bq[1][0]); SBAR();
  DSR(wl[3], wb0, 15360);
  GLDSA(bq[8][0], a_o2, vo0, 1024);
  MF(ac4[1], wl[5], bq[1][1]); SBAR();
  MF(ac4[2], wl[5], bq[1][2]); SBAR();
  MF(ac4[3], wl[5], bq[1][3]); SBAR();
  asm volatile("s_waitcnt lgkmcnt(2)" :  :: "memory");
  MF(ac5[0], wl[0], bq[1][0]); SBAR();
  DSR(wl[4], wb0, 16384);
  GLDSA(bq[8][1], a_o2, vo1, 1024);
  MF(ac5[1], wl[0], bq[1][1]); SBAR();
  MF(ac5[2], wl[0], bq[1][2]); SBAR();
  MF(ac5[3], wl[0], bq[1][3]); SBAR();
  MF(ac6[0], wl[1], bq[1][0]); SBAR();
  DSR(wl[5], wb0, 17408);
  GLDSA(bq[8][2], a_o2, vo2, 1024);
  MF(ac6[1], wl[1], bq[1][1]); SBAR();
  MF(ac6[2], wl[1], bq[1][2]); SBAR();
  MF(ac6[3], wl[1], bq[1][3]); SBAR();
  asm volatile("s_waitcnt lgkmcnt(2)" :  :: "memory");
  MF(ac0[0], wl[2], bq[2][0]); SBAR();
  DSR(wl[0], wb0, 18432);
  GLDSA(bq[8][3], a_o2, vo3, 1024);
  MF(ac0[1], wl[2], bq[2][1]); SBAR();
  GLDSA(bq[9][0], a_o3, vo0, 1024);
  MF(ac0[2], wl[2], bq[2][2]); SBAR();
  MF(ac0[3], wl[2], bq[2][3]); SBAR();
  MF(ac1[0], wl[3], bq[2][0]); SBAR();
  DSR(wl[1], wb0, 19456);
  MF(ac1[1], wl[3], bq[2][1]); SBAR();
  GLDSA(bq[9][1], a_o3, vo1, 1024);
  MF(ac1[2], wl[3], bq[2][2]); SBAR();
  MF(ac1[3], wl[3], bq[2][3]); SBAR();
  asm volatile("s_waitcnt lgkmcnt(2)" :  :: "memory");
  MF(ac2[0], wl[4], bq[2][0]); SBAR();
  DSR(wl[2], wb0, 20480);
  MF(ac2[1], wl[4], bq[2][1]); SBAR();
  GLDSA(bq[9][2], a_o3, vo2, 1024);
  MF(ac2[2], wl[4], bq[2][2]); SBAR();
  MF(ac2[3], wl[4], bq[2][3]); SBAR();
  MF(ac3[0], wl[5], bq[2][0]); SBAR();
  DSR(wl[3], wb0, 21504);
  MF(ac3[1], wl[5], bq[2][1]); SBAR();
  GLDSA(bq[9][3], a_o3, vo3, 1024);
  MF(ac3[2], wl[5], bq[2][2]); SBAR();
  MF(ac3[3], wl[5], bq[2][3]); SBAR();
  asm volatile("s_waitcnt lgkmcnt(2)" :  :: "memory");
  MF(ac4[0], wl[0], bq[2][0]); SBAR();
  DSR(wl[4], wb0, 22528);
  MF(ac4[1], wl[0], bq[2][1]); SBAR();
  GLDSA(bq[10][0], a_o1, vo0, 2048);
  MF(ac4[2], wl[0], bq[2][2]); SBAR();
  MF(ac4[3], wl[0], bq[2][3]); SBAR();
  MF(ac5[0], wl[1], bq[2][0]); SBAR();
  DSR(wl[5], wb0, 23552);
  MF(ac5[1], wl[1], bq[2][1]); SBAR();
  GLDSA(bq[10][1], a_o1, vo1, 2048);
  MF(ac5[2], wl[1], bq[2][2]); SBAR();
  MF(ac5[3], wl[1], bq[2][3]); SBAR();
  asm volatile("s_waitcnt lgkmcnt(2)" :  :: "memory");
  MF(ac6[0], wl[2], bq[2][0]); SBAR();
  DSR(wl[0], wb0, 24576);
  MF(ac6[1], wl[2], bq[2][1]); SBAR();
  GLDSA(bq[10][2], a_o1, vo2, 2048);
  MF(ac6[2], wl[2], bq[2][2]); SBAR();
  MF(ac6[3], wl[2], bq[2][3]); SBAR();
  MF(ac0[0], wl[3], bq[3][0]); SBAR();
  DSR(wl[1], wb0, 25600);
  MF(ac0[1], wl[3], bq[3][1]); SBAR();
  GLDSA(bq[10][3], a_o1, vo3, 2048);
  MF(ac0[2], wl[3], bq[3][2]); SBAR();
  MF(ac0[3], wl[3], bq[3][3]); SBAR();
  asm volatile("s_waitcnt lgkmcnt(2)" :  :: "memory");
  MF(ac1[0], wl[4], bq[3][0]); SBAR();
  DSR(wl[2], wb0, 26624);
  MF(ac1[1], wl[4], bq[3][1]); SBAR();
  GLDSA(bq[11][0], a_o2, vo0, 2048);
  MF(ac1[2], wl[4], bq[3][2]); SBAR();
  MF(ac1[3], wl[4], bq[3][3]); SBAR();
  MF(ac2[0], wl[5], bq[3][0]); SBAR();
  DSR(wl[3], wb0, 27648);
  MF(ac2[1], wl[5], bq[3][1]); SBAR();
  GLDSA(bq[11][1], a_o2, vo1, 2048);
  MF(ac2[2], wl[5], bq[3][2]); SBAR();
  MF(ac2[3], wl[5], bq[3][3]); SBAR();
  asm volatile("s_waitcnt lgkmcnt(2)" :  :: "memory");
  MF(ac3[0], wl[0], bq[3][0]); SBAR();
  DSR(wl[4], wb0, 28672);
  MF(ac3[1], wl[0], bq[3][1]); SBAR();
  GLDSA(bq[11][2], a_o2, vo2, 2048);
  MF(ac3[2], wl[0], bq[3][2]); SBAR();
  MF(ac3[3], wl[0], bq[3][3]); SBAR();
  MF(ac4[0], wl[1], bq[3][0]); SBAR();
  DSR(wl[5], wb0, 29696);
  MF(ac4[1], wl[1], bq[3][1]); SBAR();
  GLDSA(bq[11][3], a_o2, vo3, 2048);
  MF(ac4[2], wl[1], bq[3][2]); SBAR();
  asm volatile("s_waitcnt lgkmcnt(15)\n\ts_barrier" :  :: "memory");
  MF(ac4[3], wl[1], bq[3][3]); SBAR();
  asm volatile("s_waitcnt lgkmcnt(2)" :  :: "memory");
  MF(ac5[0], wl[2], bq[3][0]); SBAR();
  DSR(wl[0], wb0, 30720);
  MF(ac5[1], wl[2], bq[3][1]); SBAR();
  GLDSA(bq[12][0], a_o3, vo0, 2048);
  MF(ac5[2], wl[2], bq[3][2]); SBAR();
  MF(ac5[3], wl[2], bq[3][3]); SBAR();
  MF(ac6[0], wl[3], bq[3][0]); SBAR();
  DSR(wl[1], wb0, 31744);
  MF(ac6[1], wl[3], bq[3][1]); SBAR();
  GLDSA(bq[12][1], a_o3, vo1, 2048);
  MF(ac6[2], wl[3], bq[3][2]); SBAR();
  MF(ac6[3], wl[3], bq[3][3]); SBAR();
  asm volatile("s_waitcnt vmcnt(30)" :  :: "memory");
  asm volatile("s_waitcnt lgkmcnt(2)" :  :: "memory");
  MF(ac0[0], wl[4], bq[4][0]); SBAR();
  DSR(wl[2], wb0, 32768);
  MF(ac0[1], wl[4], bq[4][1]); SBAR();
  GLDSA(bq[12][2], a_o3, vo2, 2048);
  MF(ac0[2], wl[4], bq[4][2]); SBAR();
  MF(ac0[3], wl[4], bq[4][3]); SBAR();
  MF(ac1[0], wl[5], bq[4][0]); SBAR();
  DSR(wl[3], wb0, 33792);
  MF(ac1[1], wl[5], bq[4][1]); SBAR();
  GLDSA(bq[12][3], a_o3, vo3, 2048);
  MF(ac1[2], wl[5], bq[4][2]); SBAR();
  MF(ac1[3], wl[5], bq[4][3]); SBAR();
  asm volatile("s_waitcnt lgkmcnt(2)" :  :: "memory");
  MF(ac2[0], wl[0], bq[4][0]); SBAR();
  DSR(wl[4], wb0, 34816);
  MF(ac2[1], wl[0], bq[4][1]); SBAR();
  GLDSA(bq[13][0], a_o1, vo0, 3072);
  MF(ac2[2], wl[0], bq[4][2]); SBAR();
  MF(ac2[3], wl[0], bq[4][3]); SBAR();
  MF(ac3[0], wl[1], bq[4][0]); SBAR();
  DSR(wl[5], wb0, 35840);
  MF(ac3[1], wl[1], bq[4][1]); SBAR();
  GLDSA(bq[13][1], a_o1, vo1, 3072);
  MF(ac3[2], wl[1], bq[4][2]); SBAR();
  MF(ac3[3], wl[1], bq[4][3]); SBAR();
  asm volatile("s_waitcnt lgkmcnt(2)" :  :: "memory");
  MF(ac4[0], wl[2], bq[4][0]); SBAR();
  DSR(wl[0], wb0, 36864);
  MF(ac4[1], wl[2], bq[4][1]); SBAR();
  GLDSA(bq[13][2], a_o1, vo2, 3072);
  MF(ac4[2], wl[2], bq[4][2]); SBAR();
  MF(ac4[3], wl[2], bq[4][3]); SBAR();
  asm volatile("s_waitcnt vmcnt(31)" :  :: "memory");
  MF(ac0[0], wl[3], bq[5][0]); SBAR();
  DSR(wl[1], wb0, 37888);
  MF(ac0[1], wl[3], bq[5][1]); SBAR();
  GLDSA(bq[13][3], a_o1, vo3, 3072);
  MF(ac0[2], wl[3], bq[5][2]); SBAR();
  MF(ac0[3], wl[3], bq[5][3]); SBAR();
  asm volatile("s_waitcnt lgkmcnt(2)" :  :: "memory");
  MF(ac1[0], wl[4], bq[5][0]); SBAR();
  DSR(wl[2], wb0, 38912);
  MF(ac1[1], wl[4], bq[5][1]); SBAR();
  GLDSA(bq[14][0], a_o2, vo0, 3072);
  MF(ac1[2], wl[4], bq[5][2]); SBAR();
  MF(ac1[3], wl[4], bq[5][3]); SBAR();
  MF(ac2[0], wl[5], bq[5][0]); SBAR();
  DSR(wl[3], wb0, 39936);
  MF(ac2[1], wl[5], bq[5][1]); SBAR();
  GLDSA(bq[14][1], a_o2, vo1, 3072);
  MF(ac2[2], wl[5], bq[5][2]); SBAR();
  MF(ac2[3], wl[5], bq[5][3]); SBAR();
  asm volatile("s_waitcnt lgkmcnt(2)" :  :: "memory");
  MF(ac3[0], wl[0], bq[5][0]); SBAR();
  DSR(wl[4], wb0, 40960);
  MF(ac3[1], wl[0], bq[5][1]); SBAR();
  GLDSA(bq[14][2], a_o2, vo2, 3072);
  MF(ac3[2], wl[0], bq[5][2]); SBAR();
  MF(ac3[3], wl[0], bq[5][3]); SBAR();
  MF(ac4[0], wl[1], bq[5][0]); SBAR();
  DSR(wl[5], wb0, 41984);
  MF(ac4[1], wl[1], bq[5][1]); SBAR();
  GLDSA(bq[14][3], a_o2, vo3, 3072);
  MF(ac4[2], wl[1], bq[5][2]); SBAR();
  MF(ac4[3], wl[1], bq[5][3]); SBAR();
  asm volatile("s_waitcnt vmcnt(32)" :  :: "memory");
  asm volatile("s_waitcnt lgkmcnt(2)" :  :: "memory");
  MF(ac0[0], wl[2], bq[6][0]); SBAR();
  DSR(wl[0], wb0, 43008);
  MF(ac0[1], wl[2], bq[6][1]); SBAR();
  GLDSA(bq[15][0], a_o3, vo0, 3072);
  MF(ac0[2], wl[2], bq[6][2]); SBAR();
  MF(ac0[3], wl[2], bq[6][3]); SBAR();
  MF(ac1[0], wl[3], bq[6][0]); SBAR();
  DSR(wl[1], wb0, 44032);
  MF(ac1[1], wl[3], bq[6][1]); SBAR();
  GLDSA(bq[15][1], a_o3, vo1, 3072);
  MF(ac1[2], wl[3], bq[6][2]); SBAR();
  MF(ac1[3], wl[3], bq[6][3]); SBAR();
  asm volatile("s_waitcnt lgkmcnt(2)" :  :: "memory");
  MF(ac2[0], wl[4], bq[6][0]); SBAR();
  DSR(wl[2], wb0, 45056);
  MF(ac2[1], wl[4], bq[6][1]); SBAR();
  GLDSA(bq[15][2], a_o3, vo2, 3072);
  MF(ac2[2], wl[4], bq[6][2]); SBAR();
  MF(ac2[3], wl[4], bq[6][3]); SBAR();
  MF(ac3[0], wl[5], bq[6][0]); SBAR();
  DSR(wl[3], wb0, 46080);
  MF(ac3[1], wl[5], bq[6][1]); SBAR();
  GLDSA(bq[15][3], a_o3, vo3, 3072);
  MF(ac3[2], wl[5], bq[6][2]); SBAR();
  dp = wo_s + 81920; PINS(dp);
  MF(ac3[3], wl[5], bq[6][3]); SBAR();
  GLDSV(wt[5][0], dp, lane16, 0);
  asm volatile("s_waitcnt lgkmcnt(2)" :  :: "memory");
  MF(ac4[0], wl[0], bq[6][0]); SBAR();
  DSR(wl[4], wb0, 47104);
  MF(ac4[1], wl[0], bq[6][1]); SBAR();
  MF(ac4[2], wl[0], bq[6][2]); SBAR();
  MF(ac4[3], wl[0], bq[6][3]); SBAR();
  GLDSV(wt[5][1], dp, lane16, 1024);
  asm volatile("s_waitcnt vmcnt(34)" :  :: "memory");
  MF(ac0[0], wl[1], bq[7][0]); SBAR();
  DSR(wl[5], wb0, 48128);
  MF(ac0[1], wl[1], bq[7][1]); SBAR();
  MF(ac0[2], wl[1], bq[7][2]); SBAR();
  MF(ac0[3], wl[1], bq[7][3]); SBAR();
  GLDSV(wt[5][2], dp, lane16, 2048);
  asm volatile("s_waitcnt lgkmcnt(2)" :  :: "memory");
  MF(ac1[0], wl[2], bq[7][0]); SBAR();
  DSR(wl[0], wb0, 49152);
  MF(ac1[1], wl[2], bq[7][1]); SBAR();
  MF(ac1[2], wl[2], bq[7][2]); SBAR();
  MF(ac1[3], wl[2], bq[7][3]); SBAR();
  GLDSV(wt[5][3], dp, lane16, 3072);
  MF(ac2[0], wl[3], bq[7][0]); SBAR();
  DSR(wl[1], wb0, 50176);
  MF(ac2[1], wl[3], bq[7][1]); SBAR();
  dp = wo_s + 98304; PINS(dp);
  MF(ac2[2], wl[3], bq[7][2]); SBAR();
  GLDSV(wt[6][0], dp, lane16, 0);
  MF(ac2[3], wl[3], bq[7][3]); SBAR();
  asm volatile("s_waitcnt lgkmcnt(2)" :  :: "memory");
  MF(ac3[0], wl[4], bq[7][0]); SBAR();
  DSR(wl[2], wb0, 51200);
  MF(ac3[1], wl[4], bq[7][1]); SBAR();
  MF(ac3[2], wl[4], bq[7][2]); SBAR();
  GLDSV(wt[6][1], dp, lane16, 1024);
  MF(ac3[3], wl[4], bq[7][3]); SBAR();
  MF(ac4[0], wl[5], bq[7][0]); SBAR();
  DSR(wl[3], wb0, 52224);
  MF(ac4[1], wl[5], bq[7][1]); SBAR();
  MF(ac4[2], wl[5], bq[7][2]); SBAR();
  GLDSV(wt[6][2], dp, lane16, 2048);
  MF(ac4[3], wl[5], bq[7][3]); SBAR();
  asm volatile("s_waitcnt vmcnt(35)" :  :: "memory");
  asm volatile("s_waitcnt lgkmcnt(2)" :  :: "memory");
  MF(ac0[0], wl[0], bq[8][0]); SBAR();
  DSR(wl[4], wb0, 53248);
  MF(ac0[1], wl[0], bq[8][1]); SBAR();
  MF(ac0[2], wl[0], bq[8][2]); SBAR();
  GLDSV(wt[6][3], dp, lane16, 3072);
  MF(ac0[3], wl[0], bq[8][3]); SBAR();
  dp = wo_s + 114688; PINS(dp);
  MF(ac1[0], wl[1], bq[8][0]); SBAR();
  DSR(wl[5], wb0, 54272);
  GLDSV(wt[7][0], dp, lane16, 0);
  MF(ac1[1], wl[1], bq[8][1]); SBAR();
  MF(ac1[2], wl[1], bq[8][2]); SBAR();
  MF(ac1[3], wl[1], bq[8][3]); SBAR();
  asm volatile("s_waitcnt lgkmcnt(2)" :  :: "memory");
  MF(ac2[0], wl[2], bq[8][0]); SBAR();
  DSR(wl[0], wb1, 0);
  GLDSV(wt[7][1], dp, lane16, 1024);
  MF(ac2[1], wl[2], bq[8][1]); SBAR();
  MF(ac2[2], wl[2], bq[8][2]); SBAR();
  MF(ac2[3], wl[2], bq[8][3]); SBAR();
  MF(ac3[0], wl[3], bq[8][0]); SBAR();
  DSR(wl[1], wb1, 1024);
  GLDSV(wt[7][2], dp, lane16, 2048);
  MF(ac3[1], wl[3], bq[8][1]); SBAR();
  MF(ac3[2], wl[3], bq[8][2]); SBAR();
  MF(ac3[3], wl[3], bq[8][3]); SBAR();
  asm volatile("s_waitcnt lgkmcnt(2)" :  :: "memory");
  MF(ac4[0], wl[4], bq[8][0]); SBAR();
  DSR(wl[2], wb1, 2048);
  GLDSV(wt[7][3], dp, lane16, 3072);
  MF(ac4[1], wl[4], bq[8][1]); SBAR();
  dp = wo_s + 131072; PINS(dp);
  MF(ac4[2], wl[4], bq[8][2]); SBAR();
  GLDSV(wt[8][0], dp, lane16, 0);
  MF(ac4[3], wl[4], bq[8][3]); SBAR();
  asm volatile("s_waitcnt vmcnt(37)" :  :: "memory");
  MF(ac0[0], wl[5], bq[9][0]); SBAR();
  DSR(wl[3], wb1, 3072);
  MF(ac0[1], wl[5], bq[9][1]); SBAR();
  MF(ac0[2], wl[5], bq[9][2]); SBAR();
  GLDSV(wt[8][1], dp, lane16, 1024);
  MF(ac0[3], wl[5], bq[9][3]); SBAR();
  asm volatile("s_waitcnt lgkmcnt(2)" :  :: "memory");
  MF(ac1[0], wl[0], bq[9][0]); SBAR();
  DSR(wl[4], wb1, 4096);
  MF(ac1[1], wl[0], bq[9][1]); SBAR();
  MF(ac1[2], wl[0], bq[9][2]); SBAR();
  GLDSV(wt[8][2], dp, lane16, 2048);
  MF(ac1[3], wl[0], bq[9][3]); SBAR();
  MF(ac2[0], wl[1], bq[9][0]); SBAR();
  DSR(wl[5], wb1, 5120);
  MF(ac2[1], wl[1], bq[9][1]); SBAR();
  MF(ac2[2], wl[1], bq[9][2]); SBAR();
  GLDSV(wt[8][3], dp, lane16, 3072);
  MF(ac2[3], wl[1], bq[9][3]); SBAR();
  asm volatile("s_waitcnt lgkmcnt(2)" :  :: "memory");
  MF(ac3[0], wl[2], bq[9][0]); SBAR();
  DSR(wl[0], wb1, 6144);
  MF(ac3[1], wl[2], bq[9][1]); SBAR();
  MF(ac3[2], wl[2], bq[9][2]); SBAR();
  gld_sc1<0>(pw, CNT(C_W + 8), lane16);
  MF(ac3[3], wl[2], bq[9][3]); SBAR();
  R_XLOAD(0);
  MF(ac4[0], wl[3], bq[9][0]); SBAR();
  DSR(wl[1], wb1, 7168);
  MF(ac4[1], wl[3], bq[9][1]); SBAR();
  MF(ac4[2], wl[3], bq[9][2]); SBAR();
  R_XLOAD(1);
  MF(ac4[3], wl[3], bq[9][3]); SBAR();
  asm volatile("s_waitcnt vmcnt(39)" :  :: "memory");
  asm volatile("s_waitcnt lgkmcnt(2)" :  :: "memory");
  MF(ac0[0], wl[4], bq[10][0]); SBAR();
  DSR(wl[2], wb1, 8192);
  MF(ac0[1], wl[4], bq[10][1]); SBAR();
  R_XLOAD(2);
  MF(ac0[2], wl[4], bq[10][2]); SBAR();
  MF(ac0[3], wl[4], bq[10][3]); SBAR();
  MF(ac1[0], wl[5], bq[10][0]); SBAR();
  DSR(wl[3], wb1, 9216);
  MF(ac1[1], wl[5], bq[10][1]); SBAR();
  R_XLOAD(3);
  MF(ac1[2], wl[5], bq[10][2]); SBAR();
  MF(ac1[3], wl[5], bq[10][3]); SBAR();
  asm volatile("s_waitcnt lgkmcnt(2)" :  :: "memory");
  MF(ac2[0], wl[0], bq[10][0]); SBAR();
  DSR(wl[4], wb1, 10240);
  MF(ac2[1], wl[0], bq[10][1]); SBAR();
  R_XLOAD(4);
  MF(ac2[2], wl[0], bq[10][2]); SBAR();
  MF(ac2[3], wl[0], bq[10][3]); SBAR();
  MF(ac3[0], wl[1], bq[10][0]); SBAR();
  DSR(wl[5], wb1, 11264);
  MF(ac3[1], wl[1], bq[10][1]); SBAR();
  R_XLOAD(10);
  MF(ac3[2], wl[1], bq[10][2]); SBAR();
  MF(ac3[3], wl[1], bq[10][3]); SBAR();
  asm volatile("s_waitcnt lgkmcnt(2)" :  :: "memory");
  MF(ac4[0], wl[2], bq[10][0]); SBAR();
  DSR(wl[0], wb1, 12288);
  MF(ac4[1], wl[2], bq[10][1]); SBAR();
  MF(ac4[2], wl[2], bq[10][2]); SBAR();
  MF(ac4[3], wl[2], bq[10][3]); SBAR();
  asm volatile("s_waitcnt vmcnt(39)" :  :: "memory");
  MF(ac0[0], wl[3], bq[11][0]); SBAR();
  DSR(wl[1], wb1, 13312);
  MF(ac0[1], wl[3], bq[11][1]); SBAR();
  MF(ac0[2], wl[3], bq[11][2]); SBAR();
  MF(ac0[3], wl[3], bq[11][3]); SBAR();
  asm volatile("s_waitcnt lgkmcnt(2)" :  :: "memory");
  MF(ac1[0], wl[4], bq[11][0]); SBAR();
  DSR(wl[2], wb1, 14336);
  MF(ac1[1], wl[4], bq[11][1]); SBAR();
  MF(ac1[2], wl[4], bq[11][2]); SBAR();
  MF(ac1[3], wl[4], bq[11][3]); SBAR();
  MF(ac2[0], wl[5], bq[11][0]); SBAR();
  DSR(wl[3], wb1, 15360);
  asm volatile("s_waitcnt vmcnt(19)" :  :: "memory");
  MF(ac2[1], wl[5], bq[11][1]); SBAR();
  DSWV(sr1, 16384, wt[5][0]);
  MF(ac2[2], wl[5], bq[11][2]); SBAR();
  asm volatile("s_barrier" :  :: "memory");
  MF(ac2[3], wl[5], bq[11][3]); SBAR();
  asm volatile("s_waitcnt lgkmcnt(3)" :  :: "memory");
  MF(ac3[0], wl[0], bq[11][0]); SBAR();
  DSR(wl[4], wb1, 16384);
  MF(ac3[1], wl[0], bq[11][1]); SBAR();
  DSWV(sr1, 17408, wt[5][1]);
  MF(ac3[2], wl[0], bq[11][2]); SBAR();
  asm volatile("s_waitcnt vmcnt(6)" : "+v"(pw) :: "memory");
  W_CHECK(pw, C_W + 8);
  MF(ac3[3], wl[0], bq[11][3]); SBAR();
  DSWV(sr1, 18432, wt[5][2]);
  MF(ac4[0], wl[1], bq[11][0]); SBAR();
  DSR(wl[5], wb1, 17408);
  MF(ac4[1], wl[1], bq[11][1]); SBAR();
  DSWV(sr1, 19456, wt[5][3]);
  MF(ac4[2], wl[1], bq[11][2]); SBAR();
  dp = w_s + 368640; PINS(dp);
  MF(ac4[3], wl[1], bq[11][3]); SBAR();
  asm volatile("s_waitcnt vmcnt(35)" :  :: "memory");
  asm volatile("s_waitcnt lgkmcnt(6)" :  :: "memory");
  MF(ac0[0], wl[2], bq[12][0]); SBAR();
  DSR(wl[0], wb1, 18432);
  GLDSV(st[0], dp, lane16, 0);
  MF(ac0[1], wl[2], bq[12][1]); SBAR();
  MF(ac0[2], wl[2], bq[12][2]); SBAR();
  MF(ac0[3], wl[2], bq[12][3]); SBAR();
  asm volatile("s_waitcnt vmcnt(16)" :  :: "memory");
  MF(ac1[0], wl[3], bq[12][0]); SBAR();
  DSR(wl[1], wb1, 19456);
  MF(ac1[1], wl[3], bq[12][1]); SBAR();
  DSWV(sr1, 32768, wt[6][0]);
  asm volatile(".Lk3_p1:\n\ts_cbranch_execz .Lk3_p5" ::: "memory");
  MF(ac1[2], wl[3], bq[12][2]); SBAR();
  dp = w_s + 369664; PINS(dp);
  MF(ac1[3], wl[3], bq[12][3]); SBAR();
  GLDSV(st[1], dp, lane16, 0);
  asm volatile("s_waitcnt lgkmcnt(4)" :  :: "memory");
  MF(ac2[0], wl[4], bq[12][0]); SBAR();
  DSR(wl[2], wb1, 20480);
  MF(ac2[1], wl[4], bq[12][1]); SBAR();
  DSWV(sr1, 33792, wt[6][1]);
  MF(ac2[2], wl[4], bq[12][2]); SBAR();
  MF(ac2[3], wl[4], bq[12][3]); SBAR();
  DSWV(sr1, 34816, wt[6][2]);
  MF(ac3[0], wl[5], bq[12][0]); SBAR();
  DSR(wl[3], wb1, 21504);
  MF(ac3[1], wl[5], bq[12][1]); SBAR();
  DSWV(sr1, 35840, wt[6][3]);
  MF(ac3[2], wl[5], bq[12][2]); SBAR();
  dp = w_s + 370688; PINS(dp);
  MF(ac3[3], wl[5], bq[12][3]); SBAR();
  GLDSV(st[2], dp, lane16, 0);
  asm volatile("s_waitcnt lgkmcnt(6)" :  :: "memory");
  MF(ac4[0], wl[0], bq[12][0]); SBAR();
  DSR(wl[4], wb1, 22528);
  MF(ac4[1], wl[0], bq[12][1]); SBAR();
  asm volatile("s_waitcnt vmcnt(14)" :  :: "memory");
  MF(ac4[2], wl[0], bq[12][2]); SBAR();
  DSWV(sr1, 49152, wt[7][0]);
  MF(ac4[3], wl[0], bq[12][3]); SBAR();
  asm volatile("s_waitcnt vmcnt(34)" :  :: "memory");
  MF(ac0[0], wl[1], bq[13][0]); SBAR();
  DSR(wl[5], wb1, 23552);
  MF(ac0[1], wl[1], bq[13][1]); SBAR();
  DSWV(sr1, 50176, wt[7][1]);
  MF(ac0[2], wl[1], bq[13][2]); SBAR();
  asm volatile("s_waitcnt lgkmcnt(12)\n\ts_barrier" :  :: "memory");
  MF(ac0[3], wl[1], bq[13][3]); SBAR();
  asm volatile("s_waitcnt lgkmcnt(5)" :  :: "memory");
  MF(ac1[0], wl[2], bq[13][0]); SBAR();
  DSR(wl[0], wb1, 24576);
  MF(ac1[1], wl[2], bq[13][1]); SBAR();
  DSWV(sr1, 51200, wt[7][2]);
  MF(ac1[2], wl[2], bq[13][2]); SBAR();
  dp = w_s + 371712; PINS(dp);
  MF(ac1[3], wl[2], bq[13][3]); SBAR();
  DSWV(sr1, 52224, wt[7][3]);
  MF(ac2[0], wl[3], bq[13][0]); SBAR();
  DSR(wl[1], wb1, 25600);
  GLDSV(st[3], dp, lane16, 0);
  MF(ac2[1], wl[3], bq[13][1]); SBAR();
  MF(ac2[2], wl[3], bq[13][2]); SBAR();
  MF(ac2[3], wl[3], bq[13][3]); SBAR();
  asm volatile("s_waitcnt lgkmcnt(5)" :  :: "memory");
  asm volatile("s_waitcnt vmcnt(11)" :  :: "memory");
  MF(ac3[0], wl[4], bq[13][0]); SBAR();
  DSR(wl[2], wb1, 26624);
  MF(ac3[1], wl[4], bq[13][1]); SBAR();
  DSWV(sr2, 0, wt[8][0]);
  MF(ac3[2], wl[4], bq[13][2]); SBAR();
  dp = w_s + 372736; PINS(dp);
  MF(ac3[3], wl[4], bq[13][3]); SBAR();
  DSWV(sr2, 1024, wt[8][1]);
  MF(ac4[0], wl[5], bq[13][0]); SBAR();
  DSR(wl[3], wb1, 27648);
  GLDSV(st[4], dp, lane16, 0);
  MF(ac4[1], wl[5], bq[13][1]); SBAR();
  DSWV(sr2, 2048, wt[8][2]);
  MF(ac4[2], wl[5], bq[13][2]); SBAR();
  MF(ac4[3], wl[5], bq[13][3]); SBAR();
  asm volatile("s_waitcnt vmcnt(32)" :  :: "memory");
  asm volatile("s_waitcnt lgkmcnt(5)" :  :: "memory");
  MF(ac0[0], wl[0], bq[14][0]); SBAR();
  DSR(wl[4], wb1, 28672);
  MF(ac0[1], wl[0], bq[14][1]); SBAR();
  DSWV(sr2, 3072, wt[8][3]);
  MF(ac0[2], wl[0], bq[14][2]); SBAR();
  dp = w_s + 373760; PINS(dp);
  MF(ac0[3], wl[0], bq[14][3]); SBAR();
  GLDSV(st[5], dp, lane16, 0);
  MF(ac1[0], wl[1], bq[14][0]); SBAR();
  DSR(wl[5], wb1, 29696);
  MF(ac1[1], wl[1], bq[14][1]); SBAR();
  MF(ac1[2], wl[1], bq[14][2]); SBAR();
  MF(ac1[3], wl[1], bq[14][3]); SBAR();
  asm volatile("s_waitcnt lgkmcnt(4)" :  :: "memory");
  MF(ac2[0], wl[2], bq[14][0]); SBAR();
  DSR(wl[0], wb1, 30720);
  MF(ac2[1], wl[2], bq[14][1]); SBAR();
  dp = w_s + 374784; PINS(dp);
  MF(ac2[2], wl[2], bq[14][2]); SBAR();
  GLDSV(st[6], dp, lane16, 0);
  MF(ac2[3], wl[2], bq[14][3]); SBAR();
  MF(ac3[0], wl[3], bq[14][0]); SBAR();
  DSR(wl[1], wb1, 31744);
  MF(ac3[1], wl[3], bq[14][1]); SBAR();
  MF(ac3[2], wl[3], bq[14][2]); SBAR();
  asm volatile("s_barrier" :  :: "memory");
  MF(ac3[3], wl[3], bq[14][3]); SBAR();
  asm volatile("s_waitcnt lgkmcnt(2)" :  :: "memory");
  MF(ac4[0], wl[4], bq[14][0]); SBAR();
  DSR(wl[2], wb1, 32768);
  MF(ac4[1], wl[4], bq[14][1]); SBAR();
  dp = w_s + 405504; PINS(dp);
  MF(ac4[2], wl[4], bq[14][2]); SBAR();
  GLDSV(st[7], dp, lane16, 0);
  MF(ac4[3], wl[4], bq[14][3]); SBAR();
  asm volatile("s_waitcnt vmcnt(31)" :  :: "memory");
  MF(ac0[0], wl[5], bq[15][0]); SBAR();
  DSR(wl[3], wb1, 33792);
  MF(ac0[1], wl[5], bq[15][1]); SBAR();
  MF(ac0[2], wl[5], bq[15][2]); SBAR();
  MF(ac0[3], wl[5], bq[15][3]); SBAR();
  asm volatile("s_waitcnt lgkmcnt(2)" :  :: "memory");
  MF(ac1[0], wl[0], bq[15][0]); SBAR();
  DSR(wl[4], wb1, 34816);
  MF(ac1[1], wl[0], bq[15][1]); SBAR();
  dp = w_s + 406528; PINS(dp);
  MF(ac1[2], wl[0], bq[15][2]); SBAR();
  GLDSV(st[8], dp, lane16, 0);
  MF(ac1[3], wl[0], bq[15][3]); SBAR();
  MF(ac2[0], wl[1], bq[15][0]); SBAR();
  DSR(wl[5], wb1, 35840);
  MF(ac2[1], wl[1], bq[15][1]); SBAR();
  MF(ac2[2], wl[1], bq[15][2]); SBAR();
  MF(ac2[3], wl[1], bq[15][3]); SBAR();
  asm volatile("s_waitcnt lgkmcnt(2)" :  :: "memory");
  MF(ac3[0], wl[2], bq[15][0]); SBAR();
  DSR(wl[0], wb1, 36864);
  MF(ac3[1], wl[2], bq[15][1]); SBAR();
  dp = w_s + 407552; PINS(dp);
  MF(ac3[2], wl[2], bq[15][2]); SBAR();
  GLDSV(st[9], dp, lane16, 0);
  MF(ac3[3], wl[2], bq[15][3]); SBAR();
  MF(ac4[0], wl[3], bq[15][0]); SBAR();
  DSR(wl[1], wb1, 37888);
  MF(ac4[1], wl[3], bq[15][1]); SBAR();
  MF(ac4[2], wl[3], bq[15][2]); SBAR();
  MF(ac4[3], wl[3], bq[15][3]); SBAR();
  asm volatile("s_waitcnt lgkmcnt(2)" :  :: "memory");
  MF(ac5[0], wl[4], bq[4][0]); SBAR();
  DSR(wl[2], wb1, 38912);
  asm volatile("s_waitcnt vmcnt(9)" :  :: "memory");
  MF(ac5[1], wl[4], bq[4][1]); SBAR();
  sp = p1; PINS(sp);
  MF(ac5[2], wl[4], bq[4][2]); SBAR();
  DSWV(sr0, 0, st[0]);
  MF(ac5[3], wl[4], bq[4][3]); SBAR();
  dp = w_s + 408576; PINS(dp);
  MF(ac6[0], wl[5], bq[4][0]); SBAR();
  DSR(wl[3], wb1, 39936);
  GLDSV(st[10], dp, lane16, 0);
  MF(ac6[1], wl[5], bq[4][1]); SBAR();
  MF(ac6[2], wl[5], bq[4][2]); SBAR();
  asm volatile("s_waitcnt lgkmcnt(15)\n\ts_barrier" :  :: "memory");
  MF(ac6[3], wl[5], bq[4][3]); SBAR();
  pk[0][0] = cvtpk(ac0[0][0], ac0[0][1]); PIN(pk[0][0]);
  asm volatile("s_waitcnt lgkmcnt(3)" :  :: "memory");
  MF(ac5[0], wl[0], bq[5][0]); SBAR();
  DSR(wl[4], wb1, 40960);
  pk[0][1] = cvtpk(ac0[0][2], ac0[0][3]); PIN(pk[0][1]);
  MF(ac5[1], wl[0], bq[5][1]); SBAR();
  pk[0][2] = cvtpk(ac1[0][0], ac1[0][1]); PIN(pk[0][2]);
  pk[0][3] = cvtpk(ac1[0][2], ac1[0][3]); PIN(pk[0][3]);
  MF(ac5[2], wl[0], bq[5][2]); SBAR();
  st16n<0>(sp, lane16, (u32x4){pk[0][0], pk[0][1], pk[0][2], pk[0][3]});
  asm volatile("s_waitcnt vmcnt(10)" :  :: "memory");
  MF(ac5[3], wl[0], bq[5][3]); SBAR();
  DSWV(sr0, 1024, st[1]);
  MF(ac6[0], wl[1], bq[5][0]); SBAR();
  DSR(wl[5], wb1, 41984);
  MF(ac6[1], wl[1], bq[5][1]); SBAR();
  dp = w_s + 409600; PINS(dp);
  MF(ac6[2], wl[1], bq[5][2]); SBAR();
  pk[1][0] = cvtpk(ac0[1][0], ac0[1][1]); PIN(pk[1][0]);
  pk[1][1] = cvtpk(ac0[1][2], ac0[1][3]); PIN(pk[1][1]);
  MF(ac6[3], wl[1], bq[5][3]); SBAR();
  pk[1][2] = cvtpk(ac1[1][0], ac1[1][1]); PIN(pk[1][2]);
  asm volatile("s_waitcnt lgkmcnt(3)" :  :: "memory");
  MF(ac5[0], wl[2], bq[6][0]); SBAR();
  DSR(wl[0], wb1, 43008);
  pk[1][3] = cvtpk(ac1[1][2], ac1[1][3]); PIN(pk[1][3]);
  MF(ac5[1], wl[2], bq[6][1]); SBAR();
  st16n<1024>(sp, lane16, (u32x4){pk[1][0], pk[1][1], pk[1][2], pk[1][3]});
  asm volatile("s_waitcnt vmcnt(10)" :  :: "memory");
  MF(ac5[2], wl[2], bq[6][2]); SBAR();
  DSWV(sr0, 2048, st[2]);
  MF(ac5[3], wl[2], bq[6][3]); SBAR();
  GLDSV(st[11], dp, lane16, 0);
  MF(ac6[0], wl[3], bq[6][0]); SBAR();
  DSR(wl[1], wb1, 44032);
  MF(ac6[1], wl[3], bq[6][1]); SBAR();
  pk[0][0] = cvtpk(ac0[2][0], ac0[2][1]); PIN(pk[0][0]);
  pk[0][1] = cvtpk(ac0[2][2], ac0[2][3]); PIN(pk[0][1]);
  MF(ac6[2], wl[3], bq[6][2]); SBAR();
  pk[0][2] = cvtpk(ac1[2][0], ac1[2][1]); PIN(pk[0][2]);
  pk[0][3] = cvtpk(ac1[2][2], ac1[2][3]); PIN(pk[0][3]);
  MF(ac6[3], wl[3], bq[6][3]); SBAR();
  st16n<2048>(sp, lane16, (u32x4){pk[0][0], pk[0][1], pk[0][2], pk[0][3]});
  asm volatile("s_waitcnt lgkmcnt(3)" :  :: "memory");
  MF(ac5[0], wl[4], bq[7][0]); SBAR();
  DSR(wl[2], wb1, 45056);
  MF(ac5[1], wl[4], bq[7][1]); SBAR();
  dp = w_s + 410624; PINS(dp);
  MF(ac5[2], wl[4], bq[7][2]); SBAR();
  GLDSV(st[12], dp, lane16, 0);
  MF(ac5[3], wl[4], bq[7][3]); SBAR();
  pk[1][0] = cvtpk(ac0[3][0], ac0[3][1]); PIN(pk[1][0]);
  pk[1][1] = cvtpk(ac0[3][2], ac0[3][3]); PIN(pk[1][1]);
  MF(ac6[0], wl[5], bq[7][0]); SBAR();
  DSR(wl[3], wb1, 46080);
  pk[1][2] = cvtpk(ac1[3][0], ac1[3][1]); PIN(pk[1][2]);
  MF(ac6[1], wl[5], bq[7][1]); SBAR();
  pk[1][3] = cvtpk(ac1[3][2], ac1[3][3]); PIN(pk[1][3]);
  asm volatile("s_waitcnt vmcnt(12)" :  :: "memory");
  MF(ac6[2], wl[5], bq[7][2]); SBAR();
  st16n<3072>(sp, lane16, (u32x4){pk[1][0], pk[1][1], pk[1][2], pk[1][3]});
  MF(ac6[3], wl[5], bq[7][3]); SBAR();
  asm volatile("s_waitcnt lgkmcnt(2)" :  :: "memory");
  MF(ac5[0], wl[0], bq[8][0]); SBAR();
  DSR(wl[4], wb1, 47104);
  MF(ac5[1], wl[0], bq[8][1]); SBAR();
  DSWV(sr0, 3072, st[3]);
  MF(ac5[2], wl[0], bq[8][2]); SBAR();
  sp = p1 + 8192; PINS(sp);
  MF(ac5[3], wl[0], bq[8][3]); SBAR();
  pk[0][0] = cvtpk(ac2[0][0], ac2[0][1]); PIN(pk[0][0]);
  pk[0][1] = cvtpk(ac2[0][2], ac2[0][3]); PIN(pk[0][1]);
  MF(ac6[0], wl[1], bq[8][0]); SBAR();
  DSR(wl[5], wb1, 48128);
  pk[0][2] = cvtpk(ac3[0][0], ac3[0][1]); PIN(pk[0][2]);
  MF(ac6[1], wl[1], bq[8][1]); SBAR();
  pk[0][3] = cvtpk(ac3[0][2], ac3[0][3]); PIN(pk[0][3]);
  asm volatile("s_waitcnt vmcnt(12)" :  :: "memory");
  MF(ac6[2], wl[1], bq[8][2]); SBAR();
  asm volatile("s_barrier" :  :: "memory");
  MF(ac6[3], wl[1], bq[8][3]); SBAR();
  st16n<0>(sp, lane16, (u32x4){pk[0][0], pk[0][1], pk[0][2], pk[0][3]});
  asm volatile("s_waitcnt lgkmcnt(3)" :  :: "memory");
  MF(ac5[0], wl[2], bq[9][0]); SBAR();
  DSR(wl[0], wb1, 49152);
  MF(ac5[1], wl[2], bq[9][1]); SBAR();
  DSWV(sr0, 16384, st[4]);
  MF(ac5[2], wl[2], bq[9][2]); SBAR();
  dp = w_s + 411648; PINS(dp);
  MF(ac5[3], wl[2], bq[9][3]); SBAR();
  pk[1][0] = cvtpk(ac2[1][0], ac2[1][1]); PIN(pk[1][0]);
  pk[1][1] = cvtpk(ac2[1][2], ac2[1][3]); PIN(pk[1][1]);
  MF(ac6[0], wl[3], bq[9][0]); SBAR();
  DSR(wl[1], wb1, 50176);
  pk[1][2] = cvtpk(ac3[1][0], ac3[1][1]); PIN(pk[1][2]);
  MF(ac6[1], wl[3], bq[9][1]); SBAR();
  pk[1][3] = cvtpk(ac3[1][2], ac3[1][3]); PIN(pk[1][3]);
  asm volatile("s_waitcnt vmcnt(12)" :  :: "memory");
  MF(ac6[2], wl[3], bq[9][2]); SBAR();
  st16n<1024>(sp, lane16, (u32x4){pk[1][0], pk[1][1], pk[1][2], pk[1][3]});
  GLDSV(st[13], dp, lane16, 0);
  MF(ac6[3], wl[3], bq[9][3]); SBAR();
  asm volatile("s_waitcnt lgkmcnt(3)" :  :: "memory");
  MF(ac5[0], wl[4], bq[10][0]); SBAR();
  DSR(wl[2], wb1, 51200);
  MF(ac5[1], wl[4], bq[10][1]); SBAR();
  DSWV(sr0, 17408, st[5]);
  MF(ac5[2], wl[4], bq[10][2]); SBAR();
  pk[0][0] = cvtpk(ac2[2][0], ac2[2][1]); PIN(pk[0][0]);
  pk[0][1] = cvtpk(ac2[2][2], ac2[2][3]); PIN(pk[0][1]);
  MF(ac5[3], wl[4], bq[10][3]); SBAR();
  pk[0][2] = cvtpk(ac3[2][0], ac3[2][1]); PIN(pk[0][2]);
  pk[0][3] = cvtpk(ac3[2][2], ac3[2][3]); PIN(pk[0][3]);
  MF(ac6[0], wl[5], bq[10][0]); SBAR();
  DSR(wl[3], wb1, 52224);
  st16n<2048>(sp, lane16, (u32x4){pk[0][0], pk[0][1], pk[0][2], pk[0][3]});
  MF(ac6[1], wl[5], bq[10][1]); SBAR();
  asm volatile("s_waitcnt vmcnt(14)" :  :: "memory");
  MF(ac6[2], wl[5], bq[10][2]); SBAR();
  DSWV(sr0, 18432, st[6]);
  MF(ac6[3], wl[5], bq[10][3]); SBAR();
  asm volatile("s_waitcnt lgkmcnt(4)" :  :: "memory");
  MF(ac5[0], wl[0], bq[11][0]); SBAR();
  DSR(wl[4], wb1, 53248);
  pk[1][0] = cvtpk(ac2[3][0], ac2[3][1]); PIN(pk[1][0]);
  MF(ac5[1], wl[0], bq[11][1]); SBAR();
  pk[1][1] = cvtpk(ac2[3][2], ac2[3][3]); PIN(pk[1][1]);
  pk[1][2] = cvtpk(ac3[3][0], ac3[3][1]); PIN(pk[1][2]);
  MF(ac5[2], wl[0], bq[11][2]); SBAR();
  pk[1][3] = cvtpk(ac3[3][2], ac3[3][3]); PIN(pk[1][3]);
  asm volatile("s_waitcnt vmcnt(13)" :  :: "memory");
  MF(ac5[3], wl[0], bq[11][3]); SBAR();
  st16n<3072>(sp, lane16, (u32x4){pk[1][0], pk[1][1], pk[1][2], pk[1][3]});
  MF(ac6[0], wl[1], bq[11][0]); SBAR();
  DSR(wl[5], wb1, 54272);
  MF(ac6[1], wl[1], bq[11][1]); SBAR();
  DSWV(sr0, 19456, st[7]);
  MF(ac6[2], wl[1], bq[11][2]); SBAR();
  dp = w_s + 442368; PINS(dp);
  MF(ac6[3], wl[1], bq[11][3]); SBAR();
  GLDSV(st[14], dp, lane16, 0);
  asm volatile("s_waitcnt lgkmcnt(4)" :  :: "memory");
  MF(ac5[0], wl[2], bq[12][0]); SBAR();
  DSR(wl[0], wb2, 0);
  MF(ac5[1], wl[2], bq[12][1]); SBAR();
  sp = p1_l; PINS(sp);
  MF(ac5[2], wl[2], bq[12][2]); SBAR();
  pk[0][0] = cvtpk(ac4[0][0], ac4[0][1]); PIN(pk[0][0]);
  pk[0][1] = cvtpk(ac4[0][2], ac4[0][3]); PIN(pk[0][1]);
  MF(ac5[3], wl[2], bq[12][3]); SBAR();
  pk[1][0] = cvtpk(ac4[1][0], ac4[1][1]); PIN(pk[1][0]);
  pk[1][1] = cvtpk(ac4[1][2], ac4[1][3]); PIN(pk[1][1]);
  MF(ac6[0], wl[3], bq[12][0]); SBAR();
  DSR(wl[1], wb2, 1024);
  st16n<0>(sp, lane16, (u32x4){pk[0][0], pk[0][1], pk[1][0], pk[1][1]});
  MF(ac6[1], wl[3], bq[12][1]); SBAR();
  asm volatile("s_waitcnt vmcnt(15)" :  :: "memory");
  MF(ac6[2], wl[3], bq[12][2]); SBAR();
  asm volatile("s_waitcnt lgkmcnt(15)\n\ts_barrier" :  :: "memory");
  MF(ac6[3], wl[3], bq[12][3]); SBAR();
  asm volatile("s_waitcnt lgkmcnt(3)" :  :: "memory");
  MF(ac5[0], wl[4], bq[13][0]); SBAR();
  DSR(wl[2], wb2, 2048);
  pk[0][0] = cvtpk(ac4[2][0], ac4[2][1]); PIN(pk[0][0]);
  MF(ac5[1], wl[4], bq[13][1]); SBAR();
  pk[0][1] = cvtpk(ac4[2][2], ac4[2][3]); PIN(pk[0][1]);
  pk[1][0] = cvtpk(ac4[3][0], ac4[3][1]); PIN(pk[1][0]);
  MF(ac5[2], wl[4], bq[13][2]); SBAR();
  pk[1][1] = cvtpk(ac4[3][2], ac4[3][3]); PIN(pk[1][1]);
  MF(ac5[3], wl[4], bq[13][3]); SBAR();
  st16n<1024>(sp, lane16, (u32x4){pk[0][0], pk[0][1], pk[1][0], pk[1][1]});
  MF(ac6[0], wl[5], bq[13][0]); SBAR();
  DSR(wl[3], wb2, 3072);
  MF(ac6[1], wl[5], bq[13][1]); SBAR();
  DSWV(sr0, 32768, st[8]);
  MF(ac6[2], wl[5], bq[13][2]); SBAR();
  asm volatile("s_waitcnt vmcnt(15)" :  :: "memory");
  MF(ac6[3], wl[5], bq[13][3]); SBAR();
  asm volatile("s_waitcnt lgkmcnt(3)" :  :: "memory");
  MF(ac5[0], wl[0], bq[14][0]); SBAR();
  DSR(wl[4], wb2, 4096);
  MF(ac5[1], wl[0], bq[14][1]); SBAR();
  DSWV(sr0, 33792, st[9]);
  MF(ac5[2], wl[0], bq[14][2]); SBAR();
  dp = w_s + 443392; PINS(dp);
  MF(ac5[3], wl[0], bq[14][3]); SBAR();
  GLDSV(st[15], dp, lane16, 0);
  MF(ac6[0], wl[1], bq[14][0]); SBAR();
  DSR(wl[5], wb2, 5120);
  asm volatile("s_waitcnt vmcnt(15)" :  :: "memory");
  MF(ac6[1], wl[1], bq[14][1]); SBAR();
  DSWV(sr0, 34816, st[10]);
  MF(ac6[2], wl[1], bq[14][2]); SBAR();
  MF(ac6[3], wl[1], bq[14][3]); SBAR();
  asm volatile("s_waitcnt lgkmcnt(5)" :  :: "memory");
  MF(ac5[0], wl[2], bq[15][0]); SBAR();
  DSR(wl[0], wb2, 6144);
  MF(ac5[1], wl[2], bq[15][1]); SBAR();
  dp = w_s + 444416; PINS(dp);
  MF(ac5[2], wl[2], bq[15][2]); SBAR();
  GLDSV(st[16], dp, lane16, 0);
  MF(ac5[3], wl[2], bq[15][3]); SBAR();
  MF(ac6[0], wl[3], bq[15][0]); SBAR();
  DSR(wl[1], wb2, 7168);
  MF(ac6[1], wl[3], bq[15][1]); SBAR();
  MF(ac6[2], wl[3], bq[15][2]); SBAR();
  MF(ac6[3], wl[3], bq[15][3]); SBAR();
  asm volatile("s_waitcnt lgkmcnt(3)" :  :: "memory");
  MF0(ac0[0], wl[4], bq[0][0]); SBAR();
  DSR(wl[2], wb2, 8192);
  MF0(ac0[1], wl[4], bq[0][1]); SBAR();
  dp = w_s + 445440; PINS(dp);
  MF0(ac0[2], wl[4], bq[0][2]); SBAR();
  GLDSV(st[17], dp, lane16, 0);
  MF0(ac0[3], wl[4], bq[0][3]); SBAR();
  asm volatile("s_waitcnt vmcnt(14)" :  :: "memory");
  MF0(ac1[0], wl[5], bq[0][0]); SBAR();
  DSR(wl[3], wb2, 9216);
  MF0(ac1[1], wl[5], bq[0][1]); SBAR();
  DSWV(sr0, 35840, st[11]);
  MF0(ac1[2], wl[5], bq[0][2]); SBAR();
  MF0(ac1[3], wl[5], bq[0][3]); SBAR();
  asm volatile("s_waitcnt lgkmcnt(3)" :  :: "memory");
  MF(ac0[0], wl[0], bq[1][0]); SBAR();
  DSR(wl[4], wb2, 10240);
  MF(ac0[1], wl[0], bq[1][1]); SBAR();
  dp = w_s + 446464; PINS(dp);
  MF(ac0[2], wl[0], bq[1][2]); SBAR();
  GLDSV(st[18], dp, lane16, 0);
  asm volatile("s_waitcnt vmcnt(13)" :  :: "memory");
  MF(ac0[3], wl[0], bq[1][3]); SBAR();
  DSWV(sr0, 49152, st[12]);
  MF(ac1[0], wl[1], bq[1][0]); SBAR();
  DSR(wl[5], wb2, 11264);
  MF(ac1[1], wl[1], bq[1][1]); SBAR();
  MF(ac1[2], wl[1], bq[1][2]); SBAR();
  MF(ac1[3], wl[1], bq[1][3]); SBAR();
  asm volatile("s_waitcnt lgkmcnt(4)" :  :: "memory");
  MF(ac0[0], wl[2], bq[2][0]); SBAR();
  DSR(wl[0], wb2, 12288);
  MF(ac0[1], wl[2], bq[2][1]); SBAR();
  dp = w_s + 447488; PINS(dp);
  MF(ac0[2], wl[2], bq[2][2]); SBAR();
  GLDSV(st[19], dp, lane16, 0);
  MF(ac0[3], wl[2], bq[2][3]); SBAR();
  MF(ac1[0], wl[3], bq[2][0]); SBAR();
  DSR(wl[1], wb2, 13312);
  MF(ac1[1], wl[3], bq[2][1]); SBAR();
  MF(ac1[2], wl[3], bq[2][2]); SBAR();
  MF(ac1[3], wl[3], bq[2][3]); SBAR();
  asm volatile("s_waitcnt lgkmcnt(2)" :  :: "memory");
  MF(ac0[0], wl[4], bq[3][0]); SBAR();
  DSR(wl[2], wb2, 14336);
  MF(ac0[1], wl[4], bq[3][1]); SBAR();
  MF(ac0[2], wl[4], bq[3][2]); SBAR();
  MF(ac0[3], wl[4], bq[3][3]); SBAR();
  MF(ac1[0], wl[5], bq[3][0]); SBAR();
  DSR(wl[3], wb2, 15360);
  MF(ac1[1], wl[5], bq[3][1]); SBAR();
  sp = p1 + 16384; PINS(sp);
  MF(ac1[2], wl[5], bq[3][2]); SBAR();
  asm volatile("s_waitcnt vmcnt(5)\n\ts_barrier" :  :: "memory");
  MF(ac1[3], wl[5], bq[3][3]); SBAR();
  asm volatile("s_waitcnt lgkmcnt(2)" :  :: "memory");
  asm volatile("s_waitcnt vmcnt(10)" :  :: "memory");
  MF(ac0[0], wl[0], bq[4][0]); SBAR();
  DSR(wl[4], wb2, 16384);
  MF(ac0[1], wl[0], bq[4][1]); SBAR();
  PUBLISH(C_O + (rid >> 5) * 3 + 1, fo32);
  MF(ac0[2], wl[0], bq[4][2]); SBAR();
  DSWV(sr0, 50176, st[13]);
  MF(ac0[3], wl[0], bq[4][3]); SBAR();
  pk[0][0] = cvtpk(ac5[0][0], ac5[0][1]); PIN(pk[0][0]);
  pk[0][1] = cvtpk(ac5[0][2], ac5[0][3]); PIN(pk[0][1]);
  MF(ac1[0], wl[1], bq[4][0]); SBAR();
  DSR(wl[5], wb2, 17408);
  pk[0][2] = cvtpk(ac6[0][0], ac6[0][1]); PIN(pk[0][2]);
  MF(ac1[1], wl[1], bq[4][1]); SBAR();
  pk[0][3] = cvtpk(ac6[0][2], ac6[0][3]); PIN(pk[0][3]);
  MF(ac1[2], wl[1], bq[4][2]); SBAR();
  asm volatile("s_waitcnt lgkmcnt(15)\n\ts_barrier" :  :: "memory");
  MF(ac1[3], wl[1], bq[4][3]); SBAR();
  st16n<0>(sp, lane16, (u32x4){pk[0][0], pk[0][1], pk[0][2], pk[0][3]});
  asm volatile("s_waitcnt lgkmcnt(3)" :  :: "memory");
  MF(ac0[0], wl[2], bq[5][0]); SBAR();
  DSR(wl[0], wb2, 18432);
  MF(ac0[1], wl[2], bq[5][1]); SBAR();
  MF(ac0[2], wl[2], bq[5][2]); SBAR();
  MF(ac0[3], wl[2], bq[5][3]); SBAR();
  MF(ac1[0], wl[3], bq[5][0]); SBAR();
  DSR(wl[1], wb2, 19456);
  MF(ac1[1], wl[3], bq[5][1]); SBAR();
  MF(ac1[2], wl[3], bq[5][2]); SBAR();
  MF(ac1[3], wl[3], bq[5][3]); SBAR();
  pk[1][0] = cvtpk(ac5[1][0], ac5[1][1]); PIN(pk[1][0]);
  asm volatile("s_waitcnt lgkmcnt(2)" :  :: "memory");
  MF(ac0[0], wl[4], bq[6][0]); SBAR();
  DSR(wl[2], wb2, 20480);
  pk[1][1] = cvtpk(ac5[1][2], ac5[1][3]); PIN(pk[1][1]);
  MF(ac0[1], wl[4], bq[6][1]); SBAR();
  pk[1][2] = cvtpk(ac6[1][0], ac6[1][1]); PIN(pk[1][2]);
  pk[1][3] = cvtpk(ac6[1][2], ac6[1][3]); PIN(pk[1][3]);
  MF(ac0[2], wl[4], bq[6][2]); SBAR();
  st16n<1024>(sp, lane16, (u32x4){pk[1][0], pk[1][1], pk[1][2], pk[1][3]});
  asm volatile("s_waitcnt vmcnt(9)" :  :: "memory");
  MF(ac0[3], wl[4], bq[6][3]); SBAR();
  DSWV(sr0, 51200, st[14]);
  MF(ac1[0], wl[5], bq[6][0]); SBAR();
  DSR(wl[3], wb2, 21504);
  MF(ac1[1], wl[5], bq[6][1]); SBAR();
  wp0 = w_s + 151552; PINS(wp0);
  MF(ac1[2], wl[5], bq[6][2]); SBAR();
  wp1 = w_s + 159744; PINS(wp1);
  MF(ac1[3], wl[5], bq[6][3]); SBAR();
  GLDV(wf[0], wp0, lane16, -4096);
  asm volatile("s_waitcnt lgkmcnt(3)" :  :: "memory");
  MF(ac0[0], wl[0], bq[7][0]); SBAR();
  DSR(wl[4], wb2, 22528);
  MF(ac0[1], wl[0], bq[7][1]); SBAR();
  MF(ac0[2], wl[0], bq[7][2]); SBAR();
  pk[0][0] = cvtpk(ac5[2][0], ac5[2][1]); PIN(pk[0][0]);
  pk[0][1] = cvtpk(ac5[2][2], ac5[2][3]); PIN(pk[0][1]);
  MF(ac0[3], wl[0], bq[7][3]); SBAR();
  pk[0][2] = cvtpk(ac6[2][0], ac6[2][1]); PIN(pk[0][2]);
  pk[0][3] = cvtpk(ac6[2][2], ac6[2][3]); PIN(pk[0][3]);
  MF(ac1[0], wl[1], bq[7][0]); SBAR();
  DSR(wl[5], wb2, 23552);
  st16n<2048>(sp, lane16, (u32x4){pk[0][0], pk[0][1], pk[0][2], pk[0][3]});
  MF(ac1[1], wl[1], bq[7][1]); SBAR();
  GLDV(wf[1], wp0, lane16, -3072);
  MF(ac1[2], wl[1], bq[7][2]); SBAR();
  MF(ac1[3], wl[1], bq[7][3]); SBAR();
  asm volatile("s_waitcnt lgkmcnt(2)" :  :: "memory");
  MF(ac0[0], wl[2], bq[8][0]); SBAR();
  DSR(wl[0], wb2, 24576);
  MF(ac0[1], wl[2], bq[8][1]); SBAR();
  GLDV(wf[2], wp0, lane16, -2048);
  MF(ac0[2], wl[2], bq[8][2]); SBAR();
  MF(ac0[3], wl[2], bq[8][3]); SBAR();
  asm volatile("s_waitcnt vmcnt(10)" :  :: "memory");
  MF(ac1[0], wl[3], bq[8][0]); SBAR();
  DSR(wl[1], wb2, 25600);
  pk[1][0] = cvtpk(ac5[3][0], ac5[3][1]); PIN(pk[1][0]);
  MF(ac1[1], wl[3], bq[8][1]); SBAR();
  pk[1][1] = cvtpk(ac5[3][2], ac5[3][3]); PIN(pk[1][1]);
  pk[1][2] = cvtpk(ac6[3][0], ac6[3][1]); PIN(pk[1][2]);
  MF(ac1[2], wl[3], bq[8][2]); SBAR();
  GLDV(wf[3], wp0, lane16, -1024);
  pk[1][3] = cvtpk(ac6[3][2], ac6[3][3]); PIN(pk[1][3]);
  MF(ac1[3], wl[3], bq[8][3]); SBAR();
  st16n<3072>(sp, lane16, (u32x4){pk[1][0], pk[1][1], pk[1][2], pk[1][3]});
  asm volatile("s_waitcnt lgkmcnt(2)" :  :: "memory");
  MF(ac0[0], wl[4], bq[9][0]); SBAR();
  DSR(wl[2], wb2, 26624);
  MF(ac0[1], wl[4], bq[9][1]); SBAR();
  DSWV(sr0, 52224, st[15]);
  MF(ac0[2], wl[4], bq[9][2]); SBAR();
  GLDV(wf[4], wp0, lane16, 0);
  asm volatile("s_waitcnt vmcnt(12)" :  :: "memory");
  asm volatile(".Lk3_p2:\n\ts_cbranch_execz .Lk3_p6" ::: "memory");
  MF(ac0[3], wl[4], bq[9][3]); SBAR();
  DSWV(sr1, 0, st[16]);
  MF(ac1[0], wl[5], bq[9][0]); SBAR();
  DSR(wl[3], wb2, 27648);
  MF(ac1[1], wl[5], bq[9][1]); SBAR();
  MF(ac1[2], wl[5], bq[9][2]); SBAR();
  GLDV(wf[5], wp0, lane16, 1024);
  MF(ac1[3], wl[5], bq[9][3]); SBAR();
  asm volatile("s_waitcnt lgkmcnt(4)" :  :: "memory");
  MF(ac0[0], wl[0], bq[10][0]); SBAR();
  DSR(wl[4], wb2, 28672);
  MF(ac0[1], wl[0], bq[10][1]); SBAR();
  sp = p1 + 24576; PINS(sp);
  MF(ac0[2], wl[0], bq[10][2]); SBAR();
  GLDV(wf[6], wp0, lane16, 2048);
  asm volatile("s_waitcnt vmcnt(13)" :  :: "memory");
  MF(ac0[3], wl[0], bq[10][3]); SBAR();
  DSWV(sr1, 1024, st[17]);
  MF(ac1[0], wl[1], bq[10][0]); SBAR();
  DSR(wl[5], wb2, 29696);
  MF(ac1[1], wl[1], bq[10][1]); SBAR();
  MF(ac1[2], wl[1], bq[10][2]); SBAR();
  GLDV(wf[7], wp0, lane16, 3072);
  MF(ac1[3], wl[1], bq[10][3]); SBAR();
  asm volatile("s_waitcnt lgkmcnt(3)" :  :: "memory");
  MF(ac0[0], wl[2], bq[11][0]); SBAR();
  DSR(wl[0], wb2, 30720);
  MF(ac0[1], wl[2], bq[11][1]); SBAR();
  MF(ac0[2], wl[2], bq[11][2]); SBAR();
  GLDV(wf[8], wp1, lane16, -4096);
  asm volatile("s_waitcnt vmcnt(14)" :  :: "memory");
  MF(ac0[3], wl[2], bq[11][3]); SBAR();
  DSWV(sr1, 2048, st[18]);
  MF(ac1[0], wl[3], bq[11][0]); SBAR();
  DSR(wl[1], wb2, 31744);
  MF(ac1[1], wl[3], bq[11][1]); SBAR();
  MF(ac1[2], wl[3], bq[11][2]); SBAR();
  MF(ac1[3], wl[3], bq[11][3]); SBAR();
  asm volatile("s_waitcnt lgkmcnt(3)" :  :: "memory");
  MF(ac0[0], wl[4], bq[12][0]); SBAR();
  DSR(wl[2], wb2, 32768);
  MF(ac0[1], wl[4], bq[12][1]); SBAR();
  MF(ac0[2], wl[4], bq[12][2]); SBAR();
  asm volatile("s_waitcnt vmcnt(13)" :  :: "memory");
  MF(ac0[3], wl[4], bq[12][3]); SBAR();
  DSWV(sr1, 3072, st[19]);
  MF(ac1[0], wl[5], bq[12][0]); SBAR();
  DSR(wl[3], wb2, 33792);
  MF(ac1[1], wl[5], bq[12][1]); SBAR();
  MF(ac1[2], wl[5], bq[12][2]); SBAR();
  MF(ac1[3], wl[5], bq[12][3]); SBAR();
  asm volatile("s_waitcnt lgkmcnt(3)" :  :: "memory");
  MF(ac0[0], wl[0], bq[13][0]); SBAR();
  DSR(wl[4], wb2, 34816);
  MF(ac0[1], wl[0], bq[13][1]); SBAR();
  MF(ac0[2], wl[0], bq[13][2]); SBAR();
  MF(ac0[3], wl[0], bq[13][3]); SBAR();
  MF(ac1[0], wl[1], bq[13][0]); SBAR();
  DSR(wl[5], wb2, 35840);
  MF(ac1[1], wl[1], bq[13][1]); SBAR();
  MF(ac1[2], wl[1], bq[13][2]); SBAR();
  MF(ac1[3], wl[1], bq[13][3]); SBAR();
  asm volatile("s_waitcnt lgkmcnt(2)" :  :: "memory");
  MF(ac0[0], wl[2], bq[14][0]); SBAR();
  MF(ac0[1], wl[2], bq[14][1]); SBAR();
  MF(ac0[2], wl[2], bq[14][2]); SBAR();
  MF(ac0[3], wl[2], bq[14][3]); SBAR();
  MF(ac1[0], wl[3], bq[14][0]); SBAR();
  MF(ac1[1], wl[3], bq[14][1]); SBAR();
  MF(ac1[2], wl[3], bq[14][2]); SBAR();
  MF(ac1[3], wl[3], bq[14][3]); SBAR();
  asm volatile("s_waitcnt lgkmcnt(0)" :  :: "memory");
  MF(ac0[0], wl[4], bq[15][0]); SBAR();
  MF(ac0[1], wl[4], bq[15][1]); SBAR();
  MF(ac0[2], wl[4], bq[15][2]); SBAR();
  MF(ac0[3], wl[4], bq[15][3]); SBAR();
  MF(ac1[0], wl[5], bq[15][0]); SBAR();
  MF(ac1[1], wl[5], bq[15][1]); SBAR();
  poll_issue(pv, CNT(C_O + (rid >> 5) * 3 + 1), fo32);
  MF(ac1[2], wl[5], bq[15][2]); SBAR();
  MF(ac1[3], wl[5], bq[15][3]); SBAR();
  asm volatile("s_nop 7" ::: "memory");
  pk[0][0] = cvtpk(ac0[0][0], ac0[0][1]); PIN(pk[0][0]);
  pk[0][1] = cvtpk(ac0[0][2], ac0[0][3]); PIN(pk[0][1]);
  pk[0][2] = cvtpk(ac1[0][0], ac1[0][1]); PIN(pk[0][2]);
  pk[0][3] = cvtpk(ac1[0][2], ac1[0][3]); PIN(pk[0][3]);
  st16o<0>(sp, lane16, (u32x4){pk[0][0], pk[0][1], pk[0][2], pk[0][3]});
  pk[1][0] = cvtpk(ac0[1][0], ac0[1][1]); PIN(pk[1][0]);
  pk[1][1] = cvtpk(ac0[1][2], ac0[1][3]); PIN(pk[1][1]);
  pk[1][2] = cvtpk(ac1[1][0], ac1[1][1]); PIN(pk[1][2]);
  pk[1][3] = cvtpk(ac1[1][2], ac1[1][3]); PIN(pk[1][3]);
  st16o<1024>(sp, lane16, (u32x4){pk[1][0], pk[1][1], pk[1][2], pk[1][3]});
  pk[0][0] = cvtpk(ac0[2][0], ac0[2][1]); PIN(pk[0][0]);
  pk[0][1] = cvtpk(ac0[2][2], ac0[2][3]); PIN(pk[0][1]);
  pk[0][2] = cvtpk(ac1[2][0], ac1[2][1]); PIN(pk[0][2]);
  pk[0][3] = cvtpk(ac1[2][2], ac1[2][3]); PIN(pk[0][3]);
  st16o<2048>(sp, lane16, (u32x4){pk[0][0], pk[0][1], pk[0][2], pk[0][3]});
  pk[1][0] = cvtpk(ac0[3][0], ac0[3][1]); PIN(pk[1][0]);
  pk[1][1] = cvtpk(ac0[3][2], ac0[3][3]); PIN(pk[1][1]);
  pk[1][2] = cvtpk(ac1[3][0], ac1[3][1]); PIN(pk[1][2]);
  pk[1][3] = cvtpk(ac1[3][2], ac1[3][3]); PIN(pk[1][3]);
  st16o<3072>(sp, lane16, (u32x4){pk[1][0], pk[1][1], pk[1][2], pk[1][3]});
  GLDV(wf[9], wp1, lane16, -3072);
  GLDV(wf[10], wp1, lane16, -2048);
  GLDV(wf[11], wp1, lane16, -1024);
  GLDV(wf[12], wp1, lane16, 0);
  wp0 = w_s + 167936; PINS(wp0);
  GLDV(wf[13], wp1, lane16, 1024);
  GLDV(wf[14], wp1, lane16, 2048);
  GLDV(wf[15], wp1, lane16, 3072);
  GLDV(wf[16], wp0, lane16, -4096);
  asm volatile("s_waitcnt vmcnt(8)\n\ts_barrier" :  :: "memory");
  PUBLISH(C_O + (rid >> 5) * 3 + 1 + 1, fo32);
  asm volatile("s_waitcnt vmcnt(12)" : "+v"(pv) :: "memory");
  if (__builtin_expect(__builtin_amdgcn_ballot_w64(pv == 0u) != 0ull, 0)) spin_until(CNT(C_O + (rid >> 5) * 3 + 1), fo32);
  gld_sc1<0>(rp[0][0], ws + 0, ro2);
  gld_sc1<0>(rp[0][1], ws + 73728, ro2);
  gld_sc1<0>(rp[5][0], ws + 40960, ro2);
  gld_sc1<0>(rp[5][1], ws + 114688, ro2);
  gld_sc1<0>(rp[1][0], ws + 8192, ro2);
  gld_sc1<0>(rp[1][1], ws + 81920, ro2);
  gld_sc1<0>(rp[6][0], ws + 49152, ro2);
  gld_sc1<0>(rp[6][1], ws + 122880, ro2);
  gld2x8(rp[4][0], ws + 32768, ro2l);
  gld2x8(rp[4][1], ws + 106496, ro2l);
  R_XADD(); B1_PUT();
  asm volatile("s_nop 1" : "+v"(h4[0]), "+v"(h4[1]), "+v"(h4[2]), "+v"(h4[3]), "+v"(h4[4]));
  asm volatile("s_waitcnt vmcnt(10)" : "+v"(rp[0][0]), "+v"(rp[0][1]) :: "memory");
  MFV(h4[0], se0, rp[0][0]);
  MFV(h4[0], se0, rp[0][1]);
  asm volatile("s_waitcnt vmcnt(8)" : "+v"(rp[5][0]), "+v"(rp[5][1]) :: "memory");
  MFV(h4[2], se1, rp[5][0]);
  MFV(h4[2], se1, rp[5][1]);
  asm volatile("s_waitcnt vmcnt(6)" : "+v"(rp[1][0]), "+v"(rp[1][1]) :: "memory");
  MFV(h4[0], se1, rp[1][0]);
  MFV(h4[0], se1, rp[1][1]);
  asm volatile("s_waitcnt vmcnt(4)" : "+v"(rp[6][0]), "+v"(rp[6][1]) :: "memory");
  MFV(h4[3], se0, rp[6][0]);
  MFV(h4[3], se0, rp[6][1]);
  asm volatile("s_waitcnt vmcnt(0)" : "+v"(rp[4][0]), "+v"(rp[4][1]) :: "memory");
  MFV(h4[2], se0, rp[4][0]);
  MFV(h4[2], se0, rp[4][1]);
  spin_until(CNT(C_O + (rid >> 5) * 3 + 1 + 1), fo32);
  gld_sc1<0>(rp[2][0], ws + 16384, ro2);
  gld_sc1<0>(rp[2][1], ws + 90112, ro2);
  gld_sc1<0>(rp[7][0], ws + 57344, ro2);
  gld_sc1<0>(rp[7][1], ws + 131072, ro2);
  gld_sc1<0>(rp[3][0], ws + 24576, ro2);
  gld_sc1<0>(rp[3][1], ws + 98304, ro2);
  gld_sc1<0>(rp[8][0], ws + 65536, ro2);
  gld_sc1<0>(rp[8][1], ws + 139264, ro2);
  asm volatile("s_waitcnt vmcnt(6)" : "+v"(rp[2][0]), "+v"(rp[2][1]) :: "memory");
  MFV(h4[1], se0, rp[2][0]);
  MFV(h4[1], se0, rp[2][1]);
  asm volatile("s_waitcnt vmcnt(4)" : "+v"(rp[7][0]), "+v"(rp[7][1]) :: "memory");
  MFV(h4[3], se1, rp[7][0]);
  MFV(h4[3], se1, rp[7][1]);
  asm volatile("s_waitcnt vmcnt(2)" : "+v"(rp[3][0]), "+v"(rp[3][1]) :: "memory");
  MFV(h4[1], se1, rp[3][0]);
  MFV(h4[1], se1, rp[3][1]);
  asm volatile("s_waitcnt vmcnt(0)" : "+v"(rp[8][0]), "+v"(rp[8][1]) :: "memory");
  MFV(h4[4], se0, rp[8][0]);
  MFV(h4[4], se0, rp[8][1]);
  asm volatile("s_nop 7" : "+v"(h4[0]), "+v"(h4[1]), "+v"(h4[2]), "+v"(h4[3]), "+v"(h4[4]));
  R_LN1();
  asm volatile("s_waitcnt lgkmcnt(0)\n\ts_barrier" ::: "memory");
  R_LN3();
  dp = w_s + 164864; PINS(dp);
  GLDSA(wx[0], dp, lane16, 0);
  GLDSA(wx[1], dp, lane16, 1024);
  GLDSA(wx[2], dp, lane16, 2048);
  GLDSA(wx[3], dp, lane16, 3072);
  dp = w_s + 168960; PINS(dp);
  GLDSA(wx[4], dp, lane16, 0);
  GLDSA(wx[5], dp, lane16, 1024);
  GLDSA(wx[6], dp, lane16, 2048);
  GLDSA(wx[7], dp, lane16, 3072);
  dp = w_s + 173056; PINS(dp);
  GLDSA(wx[8], dp, lane16, 0);
  GLDSA(wx[9], dp, lane16, 1024);
  GLDSA(wx[10], dp, lane16, 2048);
  GLDSA(wx[11], dp, lane16, 3072);
  DSR(bb[0], b1_r, 0);
  DSR(bb[1], b1_r, 64);
  DSR(bb[2], b1_r, 128);
  DSR(bb[3], b1_r, 192);
  asm volatile("s_waitcnt vmcnt(12) lgkmcnt(0)\n\ts_barrier" :  :: "memory");
  PUBLISH(C_R + rteam, fq);
  dp = w_s + 177152; PINS(dp);
  GLDSA(wx[12], dp, lane16, 0);
  GLDSA(wx[13], dp, lane16, 1024);
  GLDSA(wx[14], dp, lane16, 2048);
  GLDSA(wx[15], dp, lane16, 3072);
  dp = w_s + 181248; PINS(dp);
  GLDSA(wx[16], dp, lane16, 0);
  GLDSA(wx[17], dp, lane16, 1024);
  GLDSA(wx[18], dp, lane16, 2048);
  GLDSA(wx[19], dp, lane16, 3072);
  dp = w_s + 185344; PINS(dp);
  GLDSA(wx[20], dp, lane16, 0);
  GLDSA(wx[21], dp, lane16, 1024);
  GLDSA(wx[22], dp, lane16, 2048);
  GLDSA(wx[23], dp, lane16, 3072);
  dp = w_s + 189440; PINS(dp);
  GLDSA(wx[24], dp, lane16, 0);
  GLDSA(wx[25], dp, lane16, 1024);
  GLDSA(wx[26], dp, lane16, 2048);
  GLDSA(wx[27], dp, lane16, 3072);
  G1_SETUP();
  spin_until(CNT(C_R + team), fo);
  GLDSA(hq[0][0], a_h0, vr0, 0);
  GLDSA(hq[0][1], a_h0, vr1, 0);
  GLDSA(hq[0][2], a_h0, vr2, 0);
  GLDSA(hq[0][3], a_h0, vr3, 0);
  GLDSA(hq[1][0], a_h0, vr0, 1024);
  GLDSA(hq[1][1], a_h0, vr1, 1024);
  GLDSA(hq[1][2], a_h0, vr2, 1024);
  GLDSA(hq[1][3], a_h0, vr3, 1024);
  GLDSA(hq[2][0], a_h0, vr0, 2048);
  GLDSA(hq[3][0], a_h0, vr0, 3072);
  GLDSA(hq[4][0], a_h1, vr0, 0);
  GLDSA(hq[5][0], a_h1, vr0, 1024);
  GLDSA(hq[6][0], a_h1, vr0, 2048);
  asm volatile("s_waitcnt vmcnt(9)" :  :: "memory");
  MFC(ac0[0], wf[0], hq[0][0], bb[0]); SBAR();
  MFC(ac0[1], wf[0], hq[0][1], bb[0]); SBAR();
  GLDSA(hq[7][0], a_h1, vr0, 3072);
  MFC(ac0[2], wf[0], hq[0][2], bb[0]); SBAR();
  MFC(ac0[3], wf[0], hq[0][3], bb[0]); SBAR();
  asm volatile("s_waitcnt vmcnt(5)" :  :: "memory");
  MFC(ac1[0], wf[1], hq[0][0], bb[1]); SBAR();
  DSWA(lr0, 2048, hq[2][0]);
  MFC(ac1[1], wf[1], hq[0][1], bb[1]); SBAR();
  asm volatile("s_waitcnt vmcnt(4)" :  :: "memory");
  MFC(ac1[2], wf[1], hq[0][2], bb[1]); SBAR();
  DSWA(lr0, 3072, hq[3][0]);
  MFC(ac1[3], wf[1], hq[0][3], bb[1]); SBAR();
  MFC(ac2[0], wf[2], hq[0][0], bb[2]); SBAR();
  MFC(ac2[1], wf[2], hq[0][1], bb[2]); SBAR();
  MFC(ac2[2], wf[2], hq[0][2], bb[2]); SBAR();
  MFC(ac2[3], wf[2], hq[0][3], bb[2]); SBAR();
  GLDSA(hq[8][0], a_h2, vr0, 0);
  asm volatile(".Lk3_p3:\n\ts_cbranch_execz .Lk3_p7" ::: "memory");
  MFC(ac3[0], wf[3], hq[0][0], bb[3]); SBAR();
  MFC(ac3[1], wf[3], hq[0][1], bb[3]); SBAR();
  wp1 = w_s + 176128; PINS(wp1);
  MFC(ac3[2], wf[3], hq[0][2], bb[3]); SBAR();
  MFC(ac3[3], wf[3], hq[0][3], bb[3]); SBAR();
  asm volatile("s_waitcnt vmcnt(7)" :  :: "memory");
  MF(ac0[0], wf[4], hq[1][0]); SBAR();
  MF(ac0[1], wf[4], hq[1][1]); SBAR();
  MF(ac0[2], wf[4], hq[1][2]); SBAR();
  MF(ac0[3], wf[4], hq[1][3]); SBAR();
  MF(ac1[0], wf[5], hq[1][0]); SBAR();
  MF(ac1[1], wf[5], hq[1][1]); SBAR();
  GLDSA(hq[9][0], a_h2, vr0, 1024);
  MF(ac1[2], wf[5], hq[1][2]); SBAR();
  asm volatile("s_waitcnt lgkmcnt(0)\n\ts_barrier" :  :: "memory");
  MF(ac1[3], wf[5], hq[1][3]); SBAR();
  DSRA(hq[2][1], lr1, 2048);
  DSRA(hq[2][2], lr2, 2048);
  MF(ac2[0], wf[6], hq[1][0]); SBAR();
  DSRA(hq[2][3], lr3, 2048);
  DSRA(hq[3][1], lr1, 3072);
  MF(ac2[1], wf[6], hq[1][1]); SBAR();
  DSRA(hq[3][2], lr2, 3072);
  DSRA(hq[3][3], lr3, 3072);
  MF(ac2[2], wf[6], hq[1][2]); SBAR();
  asm volatile("s_waitcnt vmcnt(5)" :  :: "memory");
  MF(ac2[3], wf[6], hq[1][3]); SBAR();
  DSWA(lr0, 4096, hq[4][0]);
  MF(ac3[0], wf[7], hq[1][0]); SBAR();
  asm volatile("s_waitcnt vmcnt(4)" :  :: "memory");
  MF(ac3[1], wf[7], hq[1][1]); SBAR();
  DSWA(lr0, 5120, hq[5][0]);
  MF(ac3[2], wf[7], hq[1][2]); SBAR();
  asm volatile("s_waitcnt vmcnt(3)" :  :: "memory");
  MF(ac3[3], wf[7], hq[1][3]); SBAR();
  GLDSA(hq[10][0], a_h2, vr0, 2048);
  asm volatile("s_waitcnt lgkmcnt(5)" :  :: "memory");
  MF(ac0[0], wf[8], hq[2][0]); SBAR();
  DSWA(lr0, 6144, hq[6][0]);
  MF(ac0[1], wf[8], hq[2][1]); SBAR();
  MF(ac0[2], wf[8], hq[2][2]); SBAR();
  MF(ac0[3], wf[8], hq[2][3]); SBAR();
  MF(ac1[0], wf[9], hq[2][0]); SBAR();
  MF(ac1[1], wf[9], hq[2][1]); SBAR();
  MF(ac1[2], wf[9], hq[2][2]); SBAR();
  MF(ac1[3], wf[9], hq[2][3]); SBAR();
  MF(ac2[0], wf[10], hq[2][0]); SBAR();
  MF(ac2[1], wf[10], hq[2][1]); SBAR();
  GLDSA(hq[11][0], a_h2, vr0, 3072);
  MF(ac2[2], wf[10], hq[2][2]); SBAR();
  MF(ac2[3], wf[10], hq[2][3]); SBAR();
  MF(ac3[0], wf[11], hq[2][0]); SBAR();
  MF(ac3[1], wf[11], hq[2][1]); SBAR();
  wp0 = w_s + 184320; PINS(wp0);
  MF(ac3[2], wf[11], hq[2][2]); SBAR();
  MF(ac3[3], wf[11], hq[2][3]); SBAR();
  asm volatile("s_waitcnt lgkmcnt(3)" :  :: "memory");
  MF(ac0[0], wf[12], hq[3][0]); SBAR();
  MF(ac0[1], wf[12], hq[3][1]); SBAR();
  MF(ac0[2], wf[12], hq[3][2]); SBAR();
  MF(ac0[3], wf[12], hq[3][3]); SBAR();
  GLDSA(hq[12][0], a_h3, vr0, 0);
  MF(ac1[0], wf[13], hq[3][0]); SBAR();
  MF(ac1[1], wf[13], hq[3][1]); SBAR();
  MF(ac1[2], wf[13], hq[3][2]); SBAR();
  asm volatile("s_waitcnt lgkmcnt(0)\n\ts_barrier" :  :: "memory");
  MF(ac1[3], wf[13], hq[3][3]); SBAR();
  DSRA(hq[4][1], lr1, 4096);
  DSRA(hq[4][2], lr2, 4096);
  MF(ac2[0], wf[14], hq[3][0]); SBAR();
  DSRA(hq[4][3], lr3, 4096);
  DSRA(hq[5][1], lr1, 5120);
  MF(ac2[1], wf[14], hq[3][1]); SBAR();
  DSRA(hq[5][2], lr2, 5120);
  DSRA(hq[5][3], lr3, 5120);
  MF(ac2[2], wf[14], hq[3][2]); SBAR();
  MF(ac2[3], wf[14], hq[3][3]); SBAR();
  MF(ac3[0], wf[15], hq[3][0]); SBAR();
  MF(ac3[1], wf[15], hq[3][1]); SBAR();
  GLDSA(hq[13][0], a_h3, vr0, 1024);
  MF(ac3[2], wf[15], hq[3][2]); SBAR();
  MF(ac3[3], wf[15], hq[3][3]); SBAR();
  DSRA(hq[6][1], lr1, 6144);
  DSRA(hq[6][2], lr2, 6144);
  asm volatile("s_waitcnt lgkmcnt(5)" :  :: "memory");
  MF(ac0[0], wf[16], hq[4][0]); SBAR();
  DSRA(hq[6][3], lr3, 6144);
  MF(ac0[1], wf[16], hq[4][1]); SBAR();
  MF(ac0[2], wf[16], hq[4][2]); SBAR();
  MF(ac0[3], wf[16], hq[4][3]); SBAR();
  MFX(ac1[0], wx[0], hq[4][0]); SBAR();
  MFX(ac1[1], wx[0], hq[4][1]); SBAR();
  MFX(ac1[2], wx[0], hq[4][2]); SBAR();
  MFX(ac1[3], wx[0], hq[4][3]); SBAR();
  GLDSA(hq[14][0], a_h3, vr0, 2048);
  MFX(ac2[0], wx[1], hq[4][0]); SBAR();
  MFX(ac2[1], wx[1], hq[4][1]); SBAR();
  asm volatile("s_waitcnt vmcnt(7)" :  :: "memory");
  MFX(ac2[2], wx[1], hq[4][2]); SBAR();
  DSWA(lr0, 7168, hq[7][0]);
  MFX(ac2[3], wx[1], hq[4][3]); SBAR();
  asm volatile("s_waitcnt vmcnt(6)" :  :: "memory");
  MFX(ac3[0], wx[2], hq[4][0]); SBAR();
  DSWA(lr0, 8192, hq[8][0]);
  MFX(ac3[1], wx[2], hq[4][1]); SBAR();
  wp1 = w_s + 192512; PINS(wp1);
  MFX(ac3[2], wx[2], hq[4][2]); SBAR();
  asm volatile("s_waitcnt vmcnt(5)" :  :: "memory");
  MFX(ac3[3], wx[2], hq[4][3]); SBAR();
  DSWA(lr0, 9216, hq[9][0]);
  asm volatile("s_waitcnt lgkmcnt(6)" :  :: "memory");
  MFX(ac0[0], wx[3], hq[5][0]); SBAR();
  asm volatile("s_waitcnt vmcnt(4)" :  :: "memory");
  MFX(ac0[1], wx[3], hq[5][1]); SBAR();
  GLDSA(hq[15][0], a_h3, vr0, 3072);
  MFX(ac0[2], wx[3], hq[5][2]); SBAR();
  DSWA(lr0, 10240, hq[10][0]);
  MFX(ac0[3], wx[3], hq[5][3]); SBAR();
  MFX(ac1[0], wx[4], hq[5][0]); SBAR();
  MFX(ac1[1], wx[4], hq[5][1]); SBAR();
  MFX(ac1[2], wx[4], hq[5][2]); SBAR();
  MFX(ac1[3], wx[4], hq[5][3]); SBAR();
  MFX(ac2[0], wx[5], hq[5][0]); SBAR();
  MFX(ac2[1], wx[5], hq[5][1]); SBAR();
  MFX(ac2[2], wx[5], hq[5][2]); SBAR();
  MFX(ac2[3], wx[5], hq[5][3]); SBAR();
  GLDSV(hq[16][0], a_h4, vr0, 0);
  MFX(ac3[0], wx[6], hq[5][0]); SBAR();
  MFX(ac3[1], wx[6], hq[5][1]); SBAR();
  MFX(ac3[2], wx[6], hq[5][2]); SBAR();
  MFX(ac3[3], wx[6], hq[5][3]); SBAR();
  asm volatile("s_waitcnt lgkmcnt(4)" :  :: "memory");
  MFX(ac0[0], wx[7], hq[6][0]); SBAR();
  MFX(ac0[1], wx[7], hq[6][1]); SBAR();
  MFX(ac0[2], wx[7], hq[6][2]); SBAR();
  MFX(ac0[3], wx[7], hq[6][3]); SBAR();
  MFX(ac1[0], wx[8], hq[6][0]); SBAR();
  MFX(ac1[1], wx[8], hq[6][1]); SBAR();
  GLDSV(hq[17][0], a_h4, vr0, 1024);
  MFX(ac1[2], wx[8], hq[6][2]); SBAR();
  asm volatile("s_waitcnt lgkmcnt(0)\n\ts_barrier" :  :: "memory");
  MFX(ac1[3], wx[8], hq[6][3]); SBAR();
  DSRA(hq[7][1], lr1, 7168);
  DSRA(hq[7][2], lr2, 7168);
  MFX(ac2[0], wx[9], hq[6][0]); SBAR();
  DSRA(hq[7][3], lr3, 7168);
  DSRA(hq[8][1], lr1, 8192);
  MFX(ac2[1], wx[9], hq[6][1]); SBAR();
  DSRA(hq[8][2], lr2, 8192);
  DSRA(hq[8][3], lr3, 8192);
  MFX(ac2[2], wx[9], hq[6][2]); SBAR();
  MFX(ac2[3], wx[9], hq[6][3]); SBAR();
  MFX(ac3[0], wx[10], hq[6][0]); SBAR();
  MFX(ac3[1], wx[10], hq[6][1]); SBAR();
  wp0 = w_s + 200704; PINS(wp0);
  MFX(ac3[2], wx[10], hq[6][2]); SBAR();
  MFX(ac3[3], wx[10], hq[6][3]); SBAR();
  DSRA(hq[9][1], lr1, 9216);
  DSRA(hq[9][2], lr2, 9216);
  asm volatile("s_waitcnt lgkmcnt(5)" :  :: "memory");
  MFX(ac0[0], wx[11], hq[7][0]); SBAR();
  GLDV(wf[9], wp1, lane16, 1024);
  DSRA(hq[9][3], lr3, 9216);
  MFX(ac0[1], wx[11], hq[7][1]); SBAR();
  MFX(ac0[2], wx[11], hq[7][2]); SBAR();
  MFX(ac0[3], wx[11], hq[7][3]); SBAR();
  MFX(ac1[0], wx[12], hq[7][0]); SBAR();
  GLDV(wf[10], wp1, lane16, 2048);
  MFX(ac1[1], wx[12], hq[7][1]); SBAR();
  MFX(ac1[2], wx[12], hq[7][2]); SBAR();
  MFX(ac1[3], wx[12], hq[7][3]); SBAR();
  MFX(ac2[0], wx[13], hq[7][0]); SBAR();
  GLDV(wf[11], wp1, lane16, 3072);
  MFX(ac2[1], wx[13], hq[7][1]); SBAR();
  MFX(ac2[2], wx[13], hq[7][2]); SBAR();
  MFX(ac2[3], wx[13], hq[7][3]); SBAR();
  MFX(ac3[0], wx[14], hq[7][0]); SBAR();
  GLDV(wf[12], wp0, lane16, -4096);
  MFX(ac3[1], wx[14], hq[7][1]); SBAR();
  MFX(ac3[2], wx[14], hq[7][2]); SBAR();
  MFX(ac3[3], wx[14], hq[7][3]); SBAR();
  DSRA(hq[10][1], lr1, 10240);
  DSRA(hq[10][2], lr2, 10240);
  asm volatile("s_waitcnt lgkmcnt(5)" :  :: "memory");
  MFX(ac0[0], wx[15], hq[8][0]); SBAR();
  GLDV(wf[13], wp0, lane16, -3072);
  DSRA(hq[10][3], lr3, 10240);
  MFX(ac0[1], wx[15], hq[8][1]); SBAR();
  MFX(ac0[2], wx[15], hq[8][2]); SBAR();
  MFX(ac0[3], wx[15], hq[8][3]); SBAR();
  MFX(ac1[0], wx[16], hq[8][0]); SBAR();
  GLDV(wf[14], wp0, lane16, -2048);
  MFX(ac1[1], wx[16], hq[8][1]); SBAR();
  MFX(ac1[2], wx[16], hq[8][2]); SBAR();
  MFX(ac1[3], wx[16], hq[8][3]); SBAR();
  MFX(ac2[0], wx[17], hq[8][0]); SBAR();
  GLDV(wf[15], wp0, lane16, -1024);
  MFX(ac2[1], wx[17], hq[8][1]); SBAR();
  asm volatile("s_waitcnt vmcnt(13)" :  :: "memory");
  MFX(ac2[2], wx[17], hq[8][2]); SBAR();
  DSWA(lr0, 11264, hq[11][0]);
  MFX(ac2[3], wx[17], hq[8][3]); SBAR();
  asm volatile("s_waitcnt vmcnt(12)" :  :: "memory");
  MFX(ac3[0], wx[18], hq[8][0]); SBAR();
  GLDV(wf[16], wp0, lane16, 0);
  MFX(ac3[1], wx[18], hq[8][1]); SBAR();
  wp1 = w_s + 208896; PINS(wp1);
  MFX(ac3[2], wx[18], hq[8][2]); SBAR();
  DSWA(lr0, 12288, hq[12][0]);
  MFX(ac3[3], wx[18], hq[8][3]); SBAR();
  asm volatile("s_waitcnt vmcnt(12)" :  :: "memory");
  asm volatile("s_waitcnt lgkmcnt(5)" :  :: "memory");
  MFX(ac0[0], wx[19], hq[9][0]); SBAR();
  GLDV(wf[17], wp0, lane16, 1024);
  MFX(ac0[1], wx[19], hq[9][1]); SBAR();
  DSWA(lr0, 13312, hq[13][0]);
  MFX(ac0[2], wx[19], hq[9][2]); SBAR();
  asm volatile("s_waitcnt vmcnt(12)" :  :: "memory");
  MFX(ac0[3], wx[19], hq[9][3]); SBAR();
  DSWA(lr0, 14336, hq[14][0]);
  MFX(ac1[0], wx[20], hq[9][0]); SBAR();
  GLDV(wf[0], wp0, lane16, 2048);
  MFX(ac1[1], wx[20], hq[9][1]); SBAR();
  MFX(ac1[2], wx[20], hq[9][2]); SBAR();
  MFX(ac1[3], wx[20], hq[9][3]); SBAR();
  MFX(ac2[0], wx[21], hq[9][0]); SBAR();
  GLDV(wf[1], wp0, lane16, 3072);
  MFX(ac2[1], wx[21], hq[9][1]); SBAR();
  MFX(ac2[2], wx[21], hq[9][2]); SBAR();
  MFX(ac2[3], wx[21], hq[9][3]); SBAR();
  MFX(ac3[0], wx[22], hq[9][0]); SBAR();
  GLDV(wf[2], wp1, lane16, -4096);
  MFX(ac3[1], wx[22], hq[9][1]); SBAR();
  MFX(ac3[2], wx[22], hq[9][2]); SBAR();
  MFX(ac3[3], wx[22], hq[9][3]); SBAR();
  asm volatile("s_waitcnt lgkmcnt(4)" :  :: "memory");
  MFX(ac0[0], wx[23], hq[10][0]); SBAR();
  GLDV(wf[3], wp1, lane16, -3072);
  MFX(ac0[1], wx[23], hq[10][1]); SBAR();
  MFX(ac0[2], wx[23], hq[10][2]); SBAR();
  MFX(ac0[3], wx[23], hq[10][3]); SBAR();
  MFX(ac1[0], wx[24], hq[10][0]); SBAR();
  GLDV(wf[4], wp1, lane16, -2048);
  MFX(ac1[1], wx[24], hq[10][1]); SBAR();
  MFX(ac1[2], wx[24], hq[10][2]); SBAR();
  asm volatile("s_waitcnt lgkmcnt(0)\n\ts_barrier" :  :: "memory");
  MFX(ac1[3], wx[24], hq[10][3]); SBAR();
  DSRA(hq[11][1], lr1, 11264);
  DSRA(hq[11][2], lr2, 11264);
  MFX(ac2[0], wx[25], hq[10][0]); SBAR();
  GLDV(wf[5], wp1, lane16, -1024);
  DSRA(hq[11][3], lr3, 11264);
  MFX(ac2[1], wx[25], hq[10][1]); SBAR();
  DSRA(hq[12][1], lr1, 12288);
  DSRA(hq[12][2], lr2, 12288);
  MFX(ac2[2], wx[25], hq[10][2]); SBAR();
  DSRA(hq[12][3], lr3, 12288);
  MFX(ac2[3], wx[25], hq[10][3]); SBAR();
  MFX(ac3[0], wx[26], hq[10][0]); SBAR();
  GLDV(wf[6], wp1, lane16, 0);
  MFX(ac3[1], wx[26], hq[10][1]); SBAR();
  wp0 = w_s + 217088; PINS(wp0);
  MFX(ac3[2], wx[26], hq[10][2]); SBAR();
  MFX(ac3[3], wx[26], hq[10][3]); SBAR();
  DSRA(hq[13][1], lr1, 13312);
  DSRA(hq[13][2], lr2, 13312);
  asm volatile("s_waitcnt lgkmcnt(5)" :  :: "memory");
  MFX(ac0[0], wx[27], hq[11][0]); SBAR();
  GLDV(wf[7], wp1, lane16, 1024);
  DSRA(hq[13][3], lr3, 13312);
  MFX(ac0[1], wx[27], hq[11][1]); SBAR();
  MFX(ac0[2], wx[27], hq[11][2]); SBAR();
  MFX(ac0[3], wx[27], hq[11][3]); SBAR();
  asm volatile("s_waitcnt vmcnt(16)" :  :: "memory");
  MF(ac1[0], wf[9], hq[11][0]); SBAR();
  GLDV(wf[8], wp1, lane16, 2048);
  MF(ac1[1], wf[9], hq[11][1]); SBAR();
  MF(ac1[2], wf[9], hq[11][2]); SBAR();
  MF(ac1[3], wf[9], hq[11][3]); SBAR();
  asm volatile("s_waitcnt vmcnt(16)" :  :: "memory");
  MF(ac2[0], wf[10], hq[11][0]); SBAR();
  GLDV(wf[9], wp1, lane16, 3072);
  MF(ac2[1], wf[10], hq[11][1]); SBAR();
  MF(ac2[2], wf[10], hq[11][2]); SBAR();
  MF(ac2[3], wf[10], hq[11][3]); SBAR();
  asm volatile("s_waitcnt vmcnt(16)" :  :: "memory");
  MF(ac3[0], wf[11], hq[11][0]); SBAR();
  GLDV(wf[10], wp0, lane16, -4096);
  MF(ac3[1], wf[11], hq[11][1]); SBAR();
  MF(ac3[2], wf[11], hq[11][2]); SBAR();
  MF(ac3[3], wf[11], hq[11][3]); SBAR();
  DSRA(hq[14][1], lr1, 14336);
  asm volatile("s_waitcnt vmcnt(16)" :  :: "memory");
  asm volatile("s_waitcnt lgkmcnt(4)" :  :: "memory");
  MF(ac0[0], wf[12], hq[12][0]); SBAR();
  GLDV(wf[11], wp0, lane16, -3072);
  DSRA(hq[14][2], lr2, 14336);
  MF(ac0[1], wf[12], hq[12][1]); SBAR();
  DSRA(hq[14][3], lr3, 14336);
  MF(ac0[2], wf[12], hq[12][2]); SBAR();
  MF(ac0[3], wf[12], hq[12][3]); SBAR();
  asm volatile("s_waitcnt vmcnt(16)" :  :: "memory");
  MF(ac1[0], wf[13], hq[12][0]); SBAR();
  GLDV(wf[12], wp0, lane16, -2048);
  MF(ac1[1], wf[13], hq[12][1]); SBAR();
  MF(ac1[2], wf[13], hq[12][2]); SBAR();
  MF(ac1[3], wf[13], hq[12][3]); SBAR();
  asm volatile("s_waitcnt vmcnt(16)" :  :: "memory");
  MF(ac2[0], wf[14], hq[12][0]); SBAR();
  GLDV(wf[13], wp0, lane16, -1024);
  MF(ac2[1], wf[14], hq[12][1]); SBAR();
  asm volatile("s_waitcnt vmcnt(25)" :  :: "memory");
  MF(ac2[2], wf[14], hq[12][2]); SBAR();
  DSWA(lr0, 15360, hq[15][0]);
  MF(ac2[3], wf[14], hq[12][3]); SBAR();
  asm volatile("s_waitcnt vmcnt(16)" :  :: "memory");
  asm volatile("s_waitcnt vmcnt(24)" :  :: "memory");
  MF(ac3[0], wf[15], hq[12][0]); SBAR();
  GLDV(wf[14], wp0, lane16, 0);
  MF(ac3[1], wf[15], hq[12][1]); SBAR();
  wp1 = w_s + 225280; PINS(wp1);
  MF(ac3[2], wf[15], hq[12][2]); SBAR();
  DSWV(lr0, 16384, hq[16][0]);
  MF(ac3[3], wf[15], hq[12][3]); SBAR();
  asm volatile("s_waitcnt vmcnt(16)" :  :: "memory");
  asm volatile("s_waitcnt vmcnt(24)" :  :: "memory");
  asm volatile("s_waitcnt lgkmcnt(5)" :  :: "memory");
  MF(ac0[0], wf[16], hq[13][0]); SBAR();
  GLDV(wf[15], wp0, lane16, 1024);
  MF(ac0[1], wf[16], hq[13][1]); SBAR();
  DSWV(lr0, 17408, hq[17][0]);
  MF(ac0[2], wf[16], hq[13][2]); SBAR();
  MF(ac0[3], wf[16], hq[13][3]); SBAR();
  asm volatile("s_waitcnt vmcnt(16)" :  :: "memory");
  MF(ac1[0], wf[17], hq[13][0]); SBAR();
  GLDV(wf[16], wp0, lane16, 2048);
  MF(ac1[1], wf[17], hq[13][1]); SBAR();
  MF(ac1[2], wf[17], hq[13][2]); SBAR();
  MF(ac1[3], wf[17], hq[13][3]); SBAR();
  asm volatile("s_waitcnt vmcnt(16)" :  :: "memory");
  MF(ac2[0], wf[0], hq[13][0]); SBAR();
  GLDV(wf[17], wp0, lane16, 3072);
  MF(ac2[1], wf[0], hq[13][1]); SBAR();
  MF(ac2[2], wf[0], hq[13][2]); SBAR();
  MF(ac2[3], wf[0], hq[13][3]); SBAR();
  asm volatile("s_waitcnt vmcnt(16)" :  :: "memory");
  MF(ac3[0], wf[1], hq[13][0]); SBAR();
  GLDV(wf[0], wp1, lane16, -4096);
  MF(ac3[1], wf[1], hq[13][1]); SBAR();
  MF(ac3[2], wf[1], hq[13][2]); SBAR();
  MF(ac3[3], wf[1], hq[13][3]); SBAR();
  asm volatile("s_waitcnt vmcnt(16)" :  :: "memory");
  asm volatile("s_waitcnt lgkmcnt(3)" :  :: "memory");
  MF(ac0[0], wf[2], hq[14][0]); SBAR();
  GLDV(wf[1], wp1, lane16, -3072);
  MF(ac0[1], wf[2], hq[14][1]); SBAR();
  MF(ac0[2], wf[2], hq[14][2]); SBAR();
  MF(ac0[3], wf[2], hq[14][3]); SBAR();
  asm volatile("s_waitcnt vmcnt(16)" :  :: "memory");
  MF(ac1[0], wf[3], hq[14][0]); SBAR();
  GLDV(wf[2], wp1, lane16, -2048);
  MF(ac1[1], wf[3], hq[14][1]); SBAR();
  MF(ac1[2], wf[3], hq[14][2]); SBAR();
  asm volatile("s_waitcnt lgkmcnt(0)\n\ts_barrier" :  :: "memory");
  MF(ac1[3], wf[3], hq[14][3]); SBAR();
  DSRA(hq[15][1], lr1, 15360);
  asm volatile("s_waitcnt vmcnt(16)" :  :: "memory");
  MF(ac2[0], wf[4], hq[14][0]); SBAR();
  GLDV(wf[3], wp1, lane16, -1024);
  DSRA(hq[15][2], lr2, 15360);
  MF(ac2[1], wf[4], hq[14][1]); SBAR();
  DSRA(hq[15][3], lr3, 15360);
  DSR(hq[16][1], lr1, 16384);
  MF(ac2[2], wf[4], hq[14][2]); SBAR();
  DSR(hq[16][2], lr2, 16384);
  DSR(hq[16][3], lr3, 16384);
  MF(ac2[3], wf[4], hq[14][3]); SBAR();
  asm volatile("s_waitcnt vmcnt(16)" :  :: "memory");
  MF(ac3[0], wf[5], hq[14][0]); SBAR();
  GLDV(wf[4], wp1, lane16, 0);
  MF(ac3[1], wf[5], hq[14][1]); SBAR();
  wp0 = w_s + 233472; PINS(wp0);
  MF(ac3[2], wf[5], hq[14][2]); SBAR();
  MF(ac3[3], wf[5], hq[14][3]); SBAR();
  DSR(hq[17][1], lr1, 17408);
  asm volatile("s_waitcnt vmcnt(16)" :  :: "memory");
  asm volatile("s_waitcnt lgkmcnt(4)" :  :: "memory");
  MF(ac0[0], wf[6], hq[15][0]); SBAR();
  GLDV(wf[5], wp1, lane16, 1024);
  DSR(hq[17][2], lr2, 17408);
  MF(ac0[1], wf[6], hq[15][1]); SBAR();
  DSR(hq[17][3], lr3, 17408);
  MF(ac0[2], wf[6], hq[15][2]); SBAR();
  MF(ac0[3], wf[6], hq[15][3]); SBAR();
  asm volatile("s_waitcnt vmcnt(16)" :  :: "memory");
  MF(ac1[0], wf[7], hq[15][0]); SBAR();
  GLDV(wf[6], wp1, lane16, 2048);
  MF(ac1[1], wf[7], hq[15][1]); SBAR();
  MF(ac1[2], wf[7], hq[15][2]); SBAR();
  MF(ac1[3], wf[7], hq[15][3]); SBAR();
  asm volatile("s_waitcnt vmcnt(16)" :  :: "memory");
  MF(ac2[0], wf[8], hq[15][0]); SBAR();
  GLDV(wf[7], wp1, lane16, 3072);
  MF(ac2[1], wf[8], hq[15][1]); SBAR();
  MF(ac2[2], wf[8], hq[15][2]); SBAR();
  MF(ac2[3], wf[8], hq[15][3]); SBAR();
  asm volatile("s_waitcnt vmcnt(16)" :  :: "memory");
  MF(ac3[0], wf[9], hq[15][0]); SBAR();
  GLDV(wf[8], wp0, lane16, -4096);
  MF(ac3[1], wf[9], hq[15][1]); SBAR();
  MF(ac3[2], wf[9], hq[15][2]); SBAR();
  MF(ac3[3], wf[9], hq[15][3]); SBAR();
  asm volatile("s_waitcnt vmcnt(16)" :  :: "memory");
  asm volatile("s_waitcnt lgkmcnt(3)" :  :: "memory");
  MFV(ac0[0], wf[10], hq[16][0]); SBAR();
  GLDV(wf[9], wp0, lane16, -3072);
  MFV(ac0[1], wf[10], hq[16][1]); SBAR();
  MFV(ac0[2], wf[10], hq[16][2]); SBAR();
  MFV(ac0[3], wf[10], hq[16][3]); SBAR();
  asm volatile("s_waitcnt vmcnt(16)" :  :: "memory");
  MFV(ac1[0], wf[11], hq[16][0]); SBAR();
  GLDV(wf[10], wp0, lane16, -2048);
  MFV(ac1[1], wf[11], hq[16][1]); SBAR();
  MFV(ac1[2], wf[11], hq[16][2]); SBAR();
  MFV(ac1[3], wf[11], hq[16][3]); SBAR();
  DSR(bb[8], b1_l, 0);
  asm volatile("s_waitcnt vmcnt(16)" :  :: "memory");
  MFV(ac2[0], wf[12], hq[16][0]); SBAR();
  GLDV(wf[11], wp0, lane16, -1024);
  MFV(ac2[1], wf[12], hq[16][1]); SBAR();
  MFV(ac2[2], wf[12], hq[16][2]); SBAR();
  MFV(ac2[3], wf[12], hq[16][3]); SBAR();
  asm volatile("s_waitcnt vmcnt(16)" :  :: "memory");
  MFV(ac3[0], wf[13], hq[16][0]); SBAR();
  GLDV(wf[12], wp0, lane16, 0);
  MFV(ac3[1], wf[13], hq[16][1]); SBAR();
  wp1 = w_s + 241664; PINS(wp1);
  MFV(ac3[2], wf[13], hq[16][2]); SBAR();
  MFV(ac3[3], wf[13], hq[16][3]); SBAR();
  asm volatile("s_waitcnt vmcnt(16)" :  :: "memory");
  asm volatile("s_waitcnt lgkmcnt(1)" :  :: "memory");
  MFV(ac0[0], wf[14], hq[17][0]); SBAR();
  GLDV(wf[13], wp0, lane16, 1024);
  MFV(ac0[1], wf[14], hq[17][1]); SBAR();
  MFV(ac0[2], wf[14], hq[17][2]); SBAR();
  MFV(ac0[3], wf[14], hq[17][3]); SBAR();
  asm volatile("s_waitcnt vmcnt(16)" :  :: "memory");
  MFV(ac1[0], wf[15], hq[17][0]); SBAR();
  GLDV(wf[14], wp0, lane16, 2048);
  MFV(ac1[1], wf[15], hq[17][1]); SBAR();
  MFV(ac1[2], wf[15], hq[17][2]); SBAR();
  MFV(ac1[3], wf[15], hq[17][3]); SBAR();
  asm volatile("s_waitcnt vmcnt(16)" :  :: "memory");
  MFV(ac2[0], wf[16], hq[17][0]); SBAR();
  GLDV(wf[15], wp0, lane16, 3072);
  MFV(ac2[1], wf[16], hq[17][1]); SBAR();
  MFV(ac2[2], wf[16], hq[17][2]); SBAR();
  MFV(ac2[3], wf[16], hq[17][3]); SBAR();
  asm volatile("s_waitcnt vmcnt(16)" :  :: "memory");
  MFV(ac3[0], wf[17], hq[17][0]); SBAR();
  GLDV(wf[16], wp1, lane16, -4096);
  MFV(ac3[1], wf[17], hq[17][1]); SBAR();
  MFV(ac3[2], wf[17], hq[17][2]); SBAR();
  asm volatile("s_barrier" :  :: "memory");
  MFV(ac3[3], wf[17], hq[17][3]); SBAR();
  asm volatile("s_waitcnt vmcnt(16)" :  :: "memory");
  asm volatile("s_waitcnt lgkmcnt(0)" :  :: "memory");
  MFC(ac4[0], wf[0], hq[0][0], bb[8]); SBAR();
  GLDV(wf[17], wp1, lane16, -3072);
  MFC(ac4[1], wf[0], hq[0][1], bb[8]); SBAR();
  MFC(ac4[2], wf[0], hq[0][2], bb[8]); SBAR();
  MFC(ac4[3], wf[0], hq[0][3], bb[8]); SBAR();
  asm volatile("s_waitcnt vmcnt(16)" :  :: "memory");
  MF(ac4[0], wf[1], hq[1][0]); SBAR();
  GLDV(wf[0], wp1, lane16, -2048);
  MF(ac4[1], wf[1], hq[1][1]); SBAR();
  MF(ac4[2], wf[1], hq[1][2]); SBAR();
  MF(ac4[3], wf[1], hq[1][3]); SBAR();
  tq[0] = ac0[0][0]; tq[0] *= __builtin_fabsf(tq[0]); PIN(tq[0]);
  asm volatile("s_waitcnt vmcnt(16)" :  :: "memory");
  MF(ac4[0], wf[2], hq[2][0]); SBAR();
  GLDV(wf[1], wp1, lane16, -1024);
  tq[1] = ac0[0][1]; tq[1] = __builtin_fmaf(tq[1], __builtin_fabsf(tq[1]), 0.f); PIN(tq[1]);
  MF(ac4[1], wf[2], hq[2][1]); SBAR();
  tq[2] = ac0[0][2]; tq[2] *= __builtin_fabsf(tq[2]); PIN(tq[2]);
  pk[0][0] = cvtpk(tq[0], tq[1]); PIN(pk[0][0]);
  MF(ac4[2], wf[2], hq[2][2]); SBAR();
  tq[3] = ac0[0][3]; tq[3] = __builtin_fmaf(tq[3], __builtin_fabsf(tq[3]), 0.f); PIN(tq[3]);
  pk[0][0] = relu2(pk[0][0]);
  MF(ac4[3], wf[2], hq[2][3]); SBAR();
  tq[0] = ac1[0][0]; tq[0] *= __builtin_fabsf(tq[0]); PIN(tq[0]);
  asm volatile("s_waitcnt vmcnt(16)" :  :: "memory");
  MF(ac4[0], wf[3], hq[3][0]); SBAR();
  GLDV(wf[2], wp1, lane16, 0);
  pk[0][1] = cvtpk(tq[2], tq[3]); PIN(pk[0][1]);
  MF(ac4[1], wf[3], hq[3][1]); SBAR();
  wp0 = w_s + 249856; PINS(wp0);
  MF(ac4[2], wf[3], hq[3][2]); SBAR();
  tq[1] = ac1[0][1]; tq[1] = __builtin_fmaf(tq[1], __builtin_fabsf(tq[1]), 0.f); PIN(tq[1]);
  pk[0][1] = relu2(pk[0][1]);
  MF(ac4[3], wf[3], hq[3][3]); SBAR();
  tq[2] = ac1[0][2]; tq[2] *= __builtin_fabsf(tq[2]); PIN(tq[2]);
  asm volatile("s_waitcnt vmcnt(16)" :  :: "memory");
  MF(ac4[0], wf[4], hq[4][0]); SBAR();
  GLDV(wf[3], wp1, lane16, 1024);
  pk[0][2] = cvtpk(tq[0], tq[1]); PIN(pk[0][2]);
  MF(ac4[1], wf[4], hq[4][1]); SBAR();
  tq[3] = ac1[0][3]; tq[3] = __builtin_fmaf(tq[3], __builtin_fabsf(tq[3]), 0.f); PIN(tq[3]);
  pk[0][2] = relu2(pk[0][2]);
  MF(ac4[2], wf[4], hq[4][2]); SBAR();
  tq[0] = ac0[1][0]; tq[0] *= __builtin_fabsf(tq[0]); PIN(tq[0]);
  pk[0][3] = cvtpk(tq[2], tq[3]); PIN(pk[0][3]);
  MF(ac4[3], wf[4], hq[4][3]); SBAR();
  tq[1] = ac0[1][1]; tq[1] = __builtin_fmaf(tq[1], __builtin_fabsf(tq[1]), 0.f); PIN(tq[1]);
  asm volatile("s_waitcnt vmcnt(16)" :  :: "memory");
  MF(ac4[0], wf[5], hq[5][0]); SBAR();
  GLDV(wf[4], wp1, lane16, 2048);
  pk[0][3] = relu2(pk[0][3]);
  MF(ac4[1], wf[5], hq[5][1]); SBAR();
  tq[2] = ac0[1][2]; tq[2] *= __builtin_fabsf(tq[2]); PIN(tq[2]);
  MF(ac4[2], wf[5], hq[5][2]); SBAR();
  DSW16(fw0, 0, (u32x4){pk[0][0], pk[0][1], pk[0][2], pk[0][3]});
  MF(ac4[3], wf[5], hq[5][3]); SBAR();
  pk[1][0] = cvtpk(tq[0], tq[1]); PIN(pk[1][0]);
  asm volatile("s_waitcnt vmcnt(16)" :  :: "memory");
  MF(ac4[0], wf[6], hq[6][0]); SBAR();
  GLDV(wf[5], wp1, lane16, 3072);
  tq[3] = ac0[1][3]; tq[3] = __builtin_fmaf(tq[3], __builtin_fabsf(tq[3]), 0.f); PIN(tq[3]);
  MF(ac4[1], wf[6], hq[6][1]); SBAR();
  pk[1][0] = relu2(pk[1][0]);
  tq[0] = ac1[1][0]; tq[0] *= __builtin_fabsf(tq[0]); PIN(tq[0]);
  MF(ac4[2], wf[6], hq[6][2]); SBAR();
  pk[1][1] = cvtpk(tq[2], tq[3]); PIN(pk[1][1]);
  tq[1] = ac1[1][1]; tq[1] = __builtin_fmaf(tq[1], __builtin_fabsf(tq[1]), 0.f); PIN(tq[1]);
  asm volatile(".Lk3_p4:\n\ts_cbranch_execz .Lk3_pr" ::: "memory");
  MF(ac4[3], wf[6], hq[6][3]); SBAR();
  pk[1][1] = relu2(pk[1][1]);
  asm volatile("s_waitcnt vmcnt(16)" :  :: "memory");
  MF(ac4[0], wf[7], hq[7][0]); SBAR();
  GLDV(wf[6], wp0, lane16, -4096);
  tq[2] = ac1[1][2]; tq[2] *= __builtin_fabsf(tq[2]); PIN(tq[2]);
  MF(ac4[1], wf[7], hq[7][1]); SBAR();
  pk[1][2] = cvtpk(tq[0], tq[1]); PIN(pk[1][2]);
  tq[3] = ac1[1][3]; tq[3] = __builtin_fmaf(tq[3], __builtin_fabsf(tq[3]), 0.f); PIN(tq[3]);
  MF(ac4[2], wf[7], hq[7][2]); SBAR();
  pk[1][2] = relu2(pk[1][2]);
  tq[0] = ac0[2][0]; tq[0] *= __builtin_fabsf(tq[0]); PIN(tq[0]);
  MF(ac4[3], wf[7], hq[7][3]); SBAR();
  pk[1][3] = cvtpk(tq[2], tq[3]); PIN(pk[1][3]);
  asm volatile("s_waitcnt vmcnt(16)" :  :: "memory");
  MF(ac4[0], wf[8], hq[8][0]); SBAR();
  GLDV(wf[7], wp0, lane16, -3072);
  tq[1] = ac0[2][1]; tq[1] = __builtin_fmaf(tq[1], __builtin_fabsf(tq[1]), 0.f); PIN(tq[1]);
  MF(ac4[1], wf[8], hq[8][1]); SBAR();
  pk[1][3] = relu2(pk[1][3]);
  tq[2] = ac0[2][2]; tq[2] *= __builtin_fabsf(tq[2]); PIN(tq[2]);
  MF(ac4[2], wf[8], hq[8][2]); SBAR();
  DSW16(fw1, 0, (u32x4){pk[1][0], pk[1][1], pk[1][2], pk[1][3]});
  MF(ac4[3], wf[8], hq[8][3]); SBAR();
  pk[0][0] = cvtpk(tq[0], tq[1]); PIN(pk[0][0]);
  asm volatile("s_waitcnt vmcnt(16)" :  :: "memory");
  MF(ac4[0], wf[9], hq[9][0]); SBAR();
  GLDV(wf[8], wp0, lane16, -2048);
  tq[3] = ac0[2][3]; tq[3] = __builtin_fmaf(tq[3], __builtin_fabsf(tq[3]), 0.f); PIN(tq[3]);
  MF(ac4[1], wf[9], hq[9][1]); SBAR();
  pk[0][0] = relu2(pk[0][0]);
  tq[0] = ac1[2][0]; tq[0] *= __builtin_fabsf(tq[0]); PIN(tq[0]);
  MF(ac4[2], wf[9], hq[9][2]); SBAR();
  pk[0][1] = cvtpk(tq[2], tq[3]); PIN(pk[0][1]);
  tq[1] = ac1[2][1]; tq[1] = __builtin_fmaf(tq[1], __builtin_fabsf(tq[1]), 0.f); PIN(tq[1]);
  MF(ac4[3], wf[9], hq[9][3]); SBAR();
  pk[0][1] = relu2(pk[0][1]);
  asm volatile("s_waitcnt vmcnt(16)" :  :: "memory");
  MF(ac4[0], wf[10], hq[10][0]); SBAR();
  GLDV(wf[9], wp0, lane16, -1024);
  tq[2] = ac1[2][2]; tq[2] *= __builtin_fabsf(tq[2]); PIN(tq[2]);
  MF(ac4[1], wf[10], hq[10][1]); SBAR();
  pk[0][2] = cvtpk(tq[0], tq[1]); PIN(pk[0][2]);
  tq[3] = ac1[2][3]; tq[3] = __builtin_fmaf(tq[3], __builtin_fabsf(tq[3]), 0.f); PIN(tq[3]);
  MF(ac4[2], wf[10], hq[10][2]); SBAR();
  pk[0][2] = relu2(pk[0][2]);
  tq[0] = ac0[3][0]; tq[0] *= __builtin_fabsf(tq[0]); PIN(tq[0]);
  MF(ac4[3], wf[10], hq[10][3]); SBAR();
  pk[0][3] = cvtpk(tq[2], tq[3]); PIN(pk[0][3]);
  asm volatile("s_waitcnt vmcnt(16)" :  :: "memory");
  MF(ac4[0], wf[11], hq[11][0]); SBAR();
  GLDV(wf[10], wp0, lane16, 0);
  tq[1] = ac0[3][1]; tq[1] = __builtin_fmaf(tq[1], __builtin_fabsf(tq[1]), 0.f); PIN(tq[1]);
  MF(ac4[1], wf[11], hq[11][1]); SBAR();
  wp1 = w_s + 258048; PINS(wp1);
  MF(ac4[2], wf[11], hq[11][2]); SBAR();
  pk[0][3] = relu2(pk[0][3]);
  tq[2] = ac0[3][2]; tq[2] *= __builtin_fabsf(tq[2]); PIN(tq[2]);
  MF(ac4[3], wf[11], hq[11][3]); SBAR();
  DSR(bb[4], b1_r, 256);
  asm volatile("s_waitcnt vmcnt(16)" :  :: "memory");
  MF(ac4[0], wf[12], hq[12][0]); SBAR();
  GLDV(wf[11], wp0, lane16, 1024);
  DSR(bb[5], b1_r, 320);
  MF(ac4[1], wf[12], hq[12][1]); SBAR();
  DSW16(fw2, 0, (u32x4){pk[0][0], pk[0][1], pk[0][2], pk[0][3]});
  MF(ac4[2], wf[12], hq[12][2]); SBAR();
  pk[1][0] = cvtpk(tq[0], tq[1]); PIN(pk[1][0]);
  tq[3] = ac0[3][3]; tq[3] = __builtin_fmaf(tq[3], __builtin_fabsf(tq[3]), 0.f); PIN(tq[3]);
  MF(ac4[3], wf[12], hq[12][3]); SBAR();
  pk[1][0] = relu2(pk[1][0]);
  asm volatile("s_waitcnt vmcnt(16)" :  :: "memory");
  MF(ac4[0], wf[13], hq[13][0]); SBAR();
  GLDV(wf[12], wp0, lane16, 2048);
  tq[0] = ac1[3][0]; tq[0] *= __builtin_fabsf(tq[0]); PIN(tq[0]);
  MF(ac4[1], wf[13], hq[13][1]); SBAR();
  pk[1][1] = cvtpk(tq[2], tq[3]); PIN(pk[1][1]);
  tq[1] = ac1[3][1]; tq[1] = __builtin_fmaf(tq[1], __builtin_fabsf(tq[1]), 0.f); PIN(tq[1]);
  MF(ac4[2], wf[13], hq[13][2]); SBAR();
  pk[1][1] = relu2(pk[1][1]);
  tq[2] = ac1[3][2]; tq[2] *= __builtin_fabsf(tq[2]); PIN(tq[2]);
  MF(ac4[3], wf[13], hq[13][3]); SBAR();
  pk[1][2] = cvtpk(tq[0], tq[1]); PIN(pk[1][2]);
  asm volatile("s_waitcnt vmcnt(16)" :  :: "memory");
  MF(ac4[0], wf[14], hq[14][0]); SBAR();
  GLDV(wf[13], wp0, lane16, 3072);
  tq[3] = ac1[3][3]; tq[3] = __builtin_fmaf(tq[3], __builtin_fabsf(tq[3]), 0.f); PIN(tq[3]);
  MF(ac4[1], wf[14], hq[14][1]); SBAR();
  pk[1][2] = relu2(pk[1][2]);
  pk[1][3] = cvtpk(tq[2], tq[3]); PIN(pk[1][3]);
  MF(ac4[2], wf[14], hq[14][2]); SBAR();
  pk[1][3] = relu2(pk[1][3]);
  MF(ac4[3], wf[14], hq[14][3]); SBAR();
  asm volatile("s_waitcnt vmcnt(16)" :  :: "memory");
  MF(ac4[0], wf[15], hq[15][0]); SBAR();
  GLDV(wf[14], wp1, lane16, -4096);
  MF(ac4[1], wf[15], hq[15][1]); SBAR();
  DSW16(fw3, 0, (u32x4){pk[1][0], pk[1][1], pk[1][2], pk[1][3]});
  MF(ac4[2], wf[15], hq[15][2]); SBAR();
  tq[0] = ac2[0][0]; tq[0] *= __builtin_fabsf(tq[0]); PIN(tq[0]);
  tq[1] = ac2[0][1]; tq[1] = __builtin_fmaf(tq[1], __builtin_fabsf(tq[1]), 0.f); PIN(tq[1]);
  MF(ac4[3], wf[15], hq[15][3]); SBAR();
  tq[2] = ac2[0][2]; tq[2] *= __builtin_fabsf(tq[2]); PIN(tq[2]);
  asm volatile("s_waitcnt vmcnt(16)" :  :: "memory");
  MFV(ac4[0], wf[16], hq[16][0]); SBAR();
  GLDV(wf[15], wp1, lane16, -3072);
  pk[0][0] = cvtpk(tq[0], tq[1]); PIN(pk[0][0]);
  MFV(ac4[1], wf[16], hq[16][1]); SBAR();
  tq[3] = ac2[0][3]; tq[3] = __builtin_fmaf(tq[3], __builtin_fabsf(tq[3]), 0.f); PIN(tq[3]);
  pk[0][0] = relu2(pk[0][0]);
  MFV(ac4[2], wf[16], hq[16][2]); SBAR();
  tq[0] = ac3[0][0]; tq[0] *= __builtin_fabsf(tq[0]); PIN(tq[0]);
  pk[0][1] = cvtpk(tq[2], tq[3]); PIN(pk[0][1]);
  MFV(ac4[3], wf[16], hq[16][3]); SBAR();
  tq[1] = ac3[0][1]; tq[1] = __builtin_fmaf(tq[1], __builtin_fabsf(tq[1]), 0.f); PIN(tq[1]);
  asm volatile("s_waitcnt vmcnt(16)" :  :: "memory");
  MFV(ac4[0], wf[17], hq[17][0]); SBAR();
  GLDV(wf[16], wp1, lane16, -2048);
  pk[0][1] = relu2(pk[0][1]);
  MFV(ac4[1], wf[17], hq[17][1]); SBAR();
  tq[2] = ac3[0][2]; tq[2] *= __builtin_fabsf(tq[2]); PIN(tq[2]);
  pk[0][2] = cvtpk(tq[0], tq[1]); PIN(pk[0][2]);
  MFV(ac4[2], wf[17], hq[17][2]); SBAR();
  tq[3] = ac3[0][3]; tq[3] = __builtin_fmaf(tq[3], __builtin_fabsf(tq[3]), 0.f); PIN(tq[3]);
  pk[0][2] = relu2(pk[0][2]);
  MFV(ac4[3], wf[17], hq[17][3]); SBAR();
  asm volatile("s_waitcnt vmcnt(16)" :  :: "memory");
  asm volatile("s_waitcnt lgkmcnt(2)" :  :: "memory");
  MFC(ac0[0], wf[0], hq[0][0], bb[4]); SBAR();
  GLDV(wf[17], wp1, lane16, -1024);
  tq[0] = ac2[1][0]; tq[0] *= __builtin_fabsf(tq[0]); PIN(tq[0]);
  MFC(ac0[1], wf[0], hq[0][1], bb[4]); SBAR();
  pk[0][3] = cvtpk(tq[2], tq[3]); PIN(pk[0][3]);
  tq[1] = ac2[1][1]; tq[1] = __builtin_fmaf(tq[1], __builtin_fabsf(tq[1]), 0.f); PIN(tq[1]);
  MFC(ac0[2], wf[0], hq[0][2], bb[4]); SBAR();
  pk[0][3] = relu2(pk[0][3]);
  tq[2] = ac2[1][2]; tq[2] *= __builtin_fabsf(tq[2]); PIN(tq[2]);
  MFC(ac0[3], wf[0], hq[0][3], bb[4]); SBAR();
  asm volatile("s_waitcnt vmcnt(16)" :  :: "memory");
  MFC(ac1[0], wf[1], hq[0][0], bb[5]); SBAR();
  GLDV(wf[0], wp1, lane16, 0);
  MFC(ac1[1], wf[1], hq[0][1], bb[5]); SBAR();
  wp0 = w_s + 266240; PINS(wp0);
  MFC(ac1[2], wf[1], hq[0][2], bb[5]); SBAR();
  DSW16(fw0, 16384, (u32x4){pk[0][0], pk[0][1], pk[0][2], pk[0][3]});
  MFC(ac1[3], wf[1], hq[0][3], bb[5]); SBAR();
  pk[1][0] = cvtpk(tq[0], tq[1]); PIN(pk[1][0]);
  asm volatile("s_waitcnt vmcnt(16)" :  :: "memory");
  MF(ac0[0], wf[2], hq[1][0]); SBAR();
  GLDV(wf[1], wp1, lane16, 1024);
  tq[3] = ac2[1][3]; tq[3] = __builtin_fmaf(tq[3], __builtin_fabsf(tq[3]), 0.f); PIN(tq[3]);
  MF(ac0[1], wf[2], hq[1][1]); SBAR();
  pk[1][0] = relu2(pk[1][0]);
  tq[0] = ac3[1][0]; tq[0] *= __builtin_fabsf(tq[0]); PIN(tq[0]);
  MF(ac0[2], wf[2], hq[1][2]); SBAR();
  pk[1][1] = cvtpk(tq[2], tq[3]); PIN(pk[1][1]);
  tq[1] = ac3[1][1]; tq[1] = __builtin_fmaf(tq[1], __builtin_fabsf(tq[1]), 0.f); PIN(tq[1]);
  MF(ac0[3], wf[2], hq[1][3]); SBAR();
  pk[1][1] = relu2(pk[1][1]);
  asm volatile("s_waitcnt vmcnt(16)" :  :: "memory");
  MF(ac1[0], wf[3], hq[1][0]); SBAR();
  GLDV(wf[2], wp1, lane16, 2048);
  tq[2] = ac3[1][2]; tq[2] *= __builtin_fabsf(tq[2]); PIN(tq[2]);
  MF(ac1[1], wf[3], hq[1][1]); SBAR();
  pk[1][2] = cvtpk(tq[0], tq[1]); PIN(pk[1][2]);
  tq[3] = ac3[1][3]; tq[3] = __builtin_fmaf(tq[3], __builtin_fabsf(tq[3]), 0.f); PIN(tq[3]);
  MF(ac1[2], wf[3], hq[1][2]); SBAR();
  pk[1][2] = relu2(pk[1][2]);
  tq[0] = ac2[2][0]; tq[0] *= __builtin_fabsf(tq[0]); PIN(tq[0]);
  MF(ac1[3], wf[3], hq[1][3]); SBAR();
  pk[1][3] = cvtpk(tq[2], tq[3]); PIN(pk[1][3]);
  asm volatile("s_waitcnt vmcnt(16)" :  :: "memory");
  MF(ac0[0], wf[4], hq[2][0]); SBAR();
  GLDV(wf[3], wp1, lane16, 3072);
  tq[1] = ac2[2][1]; tq[1] = __builtin_fmaf(tq[1], __builtin_fabsf(tq[1]), 0.f); PIN(tq[1]);
  MF(ac0[1], wf[4], hq[2][1]); SBAR();
  pk[1][3] = relu2(pk[1][3]);
  tq[2] = ac2[2][2]; tq[2] *= __builtin_fabsf(tq[2]); PIN(tq[2]);
  MF(ac0[2], wf[4], hq[2][2]); SBAR();
  DSW16(fw1, 16384, (u32x4){pk[1][0], pk[1][1], pk[1][2], pk[1][3]});
  MF(ac0[3], wf[4], hq[2][3]); SBAR();
  pk[0][0] = cvtpk(tq[0], tq[1]); PIN(pk[0][0]);
  asm volatile("s_waitcnt vmcnt(16)" :  :: "memory");
  MF(ac1[0], wf[5], hq[2][0]); SBAR();
  GLDV(wf[4], wp0, lane16, -4096);
  tq[3] = ac2[2][3]; tq[3] = __builtin_fmaf(tq[3], __builtin_fabsf(tq[3]), 0.f); PIN(tq[3]);
  MF(ac1[1], wf[5], hq[2][1]); SBAR();
  pk[0][0] = relu2(pk[0][0]);
  tq[0] = ac3[2][0]; tq[0] *= __builtin_fabsf(tq[0]); PIN(tq[0]);
  MF(ac1[2], wf[5], hq[2][2]); SBAR();
  pk[0][1] = cvtpk(tq[2], tq[3]); PIN(pk[0][1]);
  tq[1] = ac3[2][1]; tq[1] = __builtin_fmaf(tq[1], __builtin_fabsf(tq[1]), 0.f); PIN(tq[1]);
  MF(ac1[3], wf[5], hq[2][3]); SBAR();
  pk[0][1] = relu2(pk[0][1]);
  asm volatile("s_waitcnt vmcnt(16)" :  :: "memory");
  MF(ac0[0], wf[6], hq[3][0]); SBAR();
  GLDV(wf[5], wp0, lane16, -3072);
  tq[2] = ac3[2][2]; tq[2] *= __builtin_fabsf(tq[2]); PIN(tq[2]);
  MF(ac0[1], wf[6], hq[3][1]); SBAR();
  pk[0][2] = cvtpk(tq[0], tq[1]); PIN(pk[0][2]);
  tq[3] = ac3[2][3]; tq[3] = __builtin_fmaf(tq[3], __builtin_fabsf(tq[3]), 0.f); PIN(tq[3]);
  MF(ac0[2], wf[6], hq[3][2]); SBAR();
  pk[0][2] = relu2(pk[0][2]);
  tq[0] = ac2[3][0]; tq[0] *= __builtin_fabsf(tq[0]); PIN(tq[0]);
  MF(ac0[3], wf[6], hq[3][3]); SBAR();
  pk[0][3] = cvtpk(tq[2], tq[3]); PIN(pk[0][3]);
  asm volatile("s_waitcnt vmcnt(16)" :  :: "memory");
  MF(ac1[0], wf[7], hq[3][0]); SBAR();
  GLDV(wf[6], wp0, lane16, -2048);
  tq[1] = ac2[3][1]; tq[1] = __builtin_fmaf(tq[1], __builtin_fabsf(tq[1]), 0.f); PIN(tq[1]);
  MF(ac1[1], wf[7], hq[3][1]); SBAR();
  pk[0][3] = relu2(pk[0][3]);
  tq[2] = ac2[3][2]; tq[2] *= __builtin_fabsf(tq[2]); PIN(tq[2]);
  MF(ac1[2], wf[7], hq[3][2]); SBAR();
  DSW16(fw2, 16384, (u32x4){pk[0][0], pk[0][1], pk[0][2], pk[0][3]});
  MF(ac1[3], wf[7], hq[3][3]); SBAR();
  pk[1][0] = cvtpk(tq[0], tq[1]); PIN(pk[1][0]);
  asm volatile("s_waitcnt vmcnt(16)" :  :: "memory");
  MF(ac0[0], wf[8], hq[4][0]); SBAR();
  GLDV(wf[7], wp0, lane16, -1024);
  tq[3] = ac2[3][3]; tq[3] = __builtin_fmaf(tq[3], __builtin_fabsf(tq[3]), 0.f); PIN(tq[3]);
  MF(ac0[1], wf[8], hq[4][1]); SBAR();
  pk[1][0] = relu2(pk[1][0]);
  tq[0] = ac3[3][0]; tq[0] *= __builtin_fabsf(tq[0]); PIN(tq[0]);
  MF(ac0[2], wf[8], hq[4][2]); SBAR();
  pk[1][1] = cvtpk(tq[2], tq[3]); PIN(pk[1][1]);
  tq[1] = ac3[3][1]; tq[1] = __builtin_fmaf(tq[1], __builtin_fabsf(tq[1]), 0.f); PIN(tq[1]);
  MF(ac0[3], wf[8], hq[4][3]); SBAR();
  pk[1][1] = relu2(pk[1][1]);
  asm volatile("s_waitcnt vmcnt(16)" :  :: "memory");
  MF(ac1[0], wf[9], hq[4][0]); SBAR();
  GLDV(wf[8], wp0, lane16, 0);
  tq[2] = ac3[3][2]; tq[2] *= __builtin_fabsf(tq[2]); PIN(tq[2]);
  MF(ac1[1], wf[9], hq[4][1]); SBAR();
  wp1 = w_s + 274432; PINS(wp1);
  MF(ac1[2], wf[9], hq[4][2]); SBAR();
  pk[1][2] = cvtpk(tq[0], tq[1]); PIN(pk[1][2]);
  tq[3] = ac3[3][3]; tq[3] = __builtin_fmaf(tq[3], __builtin_fabsf(tq[3]), 0.f); PIN(tq[3]);
  MF(ac1[3], wf[9], hq[4][3]); SBAR();
  pk[1][2] = relu2(pk[1][2]);
  asm volatile("s_waitcnt vmcnt(16)" :  :: "memory");
  MF(ac0[0], wf[10], hq[5][0]); SBAR();
  GLDV(wf[9], wp0, lane16, 1024);
  pk[1][3] = cvtpk(tq[2], tq[3]); PIN(pk[1][3]);
  MF(ac0[1], wf[10], hq[5][1]); SBAR();
  pk[1][3] = relu2(pk[1][3]);
  MF(ac0[2], wf[10], hq[5][2]); SBAR();
  DSW16(fw3, 16384, (u32x4){pk[1][0], pk[1][1], pk[1][2], pk[1][3]});
  MF(ac0[3], wf[10], hq[5][3]); SBAR();
  tq[0] = ac4[0][0]; tq[0] *= __builtin_fabsf(tq[0]); PIN(tq[0]);
  asm volatile("s_waitcnt vmcnt(16)" :  :: "memory");
  MF(ac1[0], wf[11], hq[5][0]); SBAR();
  GLDV(wf[10], wp0, lane16, 2048);
  tq[1] = ac4[0][1]; tq[1] = __builtin_fmaf(tq[1], __builtin_fabsf(tq[1]), 0.f); PIN(tq[1]);
  MF(ac1[1], wf[11], hq[5][1]); SBAR();
  tq[2] = ac4[0][2]; tq[2] *= __builtin_fabsf(tq[2]); PIN(tq[2]);
  pk[0][0] = cvtpk(tq[0], tq[1]); PIN(pk[0][0]);
  MF(ac1[2], wf[11], hq[5][2]); SBAR();
  tq[3] = ac4[0][3]; tq[3] = __builtin_fmaf(tq[3], __builtin_fabsf(tq[3]), 0.f); PIN(tq[3]);
  pk[0][0] = relu2(pk[0][0]);
  MF(ac1[3], wf[11], hq[5][3]); SBAR();
  tq[0] = ac4[1][0]; tq[0] *= __builtin_fabsf(tq[0]); PIN(tq[0]);
  asm volatile("s_waitcnt vmcnt(16)" :  :: "memory");
  MF(ac0[0], wf[12], hq[6][0]); SBAR();
  GLDV(wf[11], wp0, lane16, 3072);
  pk[0][1] = cvtpk(tq[2], tq[3]); PIN(pk[0][1]);
  MF(ac0[1], wf[12], hq[6][1]); SBAR();
  tq[1] = ac4[1][1]; tq[1] = __builtin_fmaf(tq[1], __builtin_fabsf(tq[1]), 0.f); PIN(tq[1]);
  pk[0][1] = relu2(pk[0][1]);
  MF(ac0[2], wf[12], hq[6][2]); SBAR();
  tq[2] = ac4[1][2]; tq[2] *= __builtin_fabsf(tq[2]); PIN(tq[2]);
  fwl = fw0 + fdl; PIN(fwl);
  MF(ac0[3], wf[12], hq[6][3]); SBAR();
  asm volatile("s_waitcnt vmcnt(16)" :  :: "memory");
  MF(ac1[0], wf[13], hq[6][0]); SBAR();
  GLDV(wf[12], wp1, lane16, -4096);
  MF(ac1[1], wf[13], hq[6][1]); SBAR();
  DSW8(fwl, 0, (u32x2){pk[0][0], pk[0][1]});
  MF(ac1[2], wf[13], hq[6][2]); SBAR();
  pk[1][0] = cvtpk(tq[0], tq[1]); PIN(pk[1][0]);
  tq[3] = ac4[1][3]; tq[3] = __builtin_fmaf(tq[3], __builtin_fabsf(tq[3]), 0.f); PIN(tq[3]);
  MF(ac1[3], wf[13], hq[6][3]); SBAR();
  pk[1][0] = relu2(pk[1][0]);
  asm volatile("s_waitcnt vmcnt(16)" :  :: "memory");
  MF(ac0[0], wf[14], hq[7][0]); SBAR();
  GLDV(wf[13], wp1, lane16, -3072);
  tq[0] = ac4[2][0]; tq[0] *= __builtin_fabsf(tq[0]); PIN(tq[0]);
  MF(ac0[1], wf[14], hq[7][1]); SBAR();
  pk[1][1] = cvtpk(tq[2], tq[3]); PIN(pk[1][1]);
  tq[1] = ac4[2][1]; tq[1] = __builtin_fmaf(tq[1], __builtin_fabsf(tq[1]), 0.f); PIN(tq[1]);
  MF(ac0[2], wf[14], hq[7][2]); SBAR();
  pk[1][1] = relu2(pk[1][1]);
  tq[2] = ac4[2][2]; tq[2] *= __builtin_fabsf(tq[2]); PIN(tq[2]);
  MF(ac0[3], wf[14], hq[7][3]); SBAR();
  fwl = fw1 + fdl; PIN(fwl);
  asm volatile("s_waitcnt vmcnt(16)" :  :: "memory");
  MF(ac1[0], wf[15], hq[7][0]); SBAR();
  GLDV(wf[14], wp1, lane16, -2048);
  MF(ac1[1], wf[15], hq[7][1]); SBAR();
  DSW8(fwl, 0, (u32x2){pk[1][0], pk[1][1]});
  MF(ac1[2], wf[15], hq[7][2]); SBAR();
  pk[0][0] = cvtpk(tq[0], tq[1]); PIN(pk[0][0]);
  tq[3] = ac4[2][3]; tq[3] = __builtin_fmaf(tq[3], __builtin_fabsf(tq[3]), 0.f); PIN(tq[3]);
  MF(ac1[3], wf[15], hq[7][3]); SBAR();
  pk[0][0] = relu2(pk[0][0]);
  asm volatile("s_waitcnt vmcnt(16)" :  :: "memory");
  MF(ac0[0], wf[16], hq[8][0]); SBAR();
  GLDV(wf[15], wp1, lane16, -1024);
  tq[0] = ac4[3][0]; tq[0] *= __builtin_fabsf(tq[0]); PIN(tq[0]);
  MF(ac0[1], wf[16], hq[8][1]); SBAR();
  pk[0][1] = cvtpk(tq[2], tq[3]); PIN(pk[0][1]);
  tq[1] = ac4[3][1]; tq[1] = __builtin_fmaf(tq[1], __builtin_fabsf(tq[1]), 0.f); PIN(tq[1]);
  MF(ac0[2], wf[16], hq[8][2]); SBAR();
  pk[0][1] = relu2(pk[0][1]);
  tq[2] = ac4[3][2]; tq[2] *= __builtin_fabsf(tq[2]); PIN(tq[2]);
  MF(ac0[3], wf[16], hq[8][3]); SBAR();
  fwl = fw2 + fdl; PIN(fwl);
  asm volatile("s_waitcnt vmcnt(16)" :  :: "memory");
  MF(ac1[0], wf[17], hq[8][0]); SBAR();
  GLDV(wf[16], wp1, lane16, 0);
  MF(ac1[1], wf[17], hq[8][1]); SBAR();
  wp0 = w_s + 282624; PINS(wp0);
  MF(ac1[2], wf[17], hq[8][2]); SBAR();
  DSW8(fwl, 0, (u32x2){pk[0][0], pk[0][1]});
  MF(ac1[3], wf[17], hq[8][3]); SBAR();
  pk[1][0] = cvtpk(tq[0], tq[1]); PIN(pk[1][0]);
  asm volatile("s_waitcnt vmcnt(16)" :  :: "memory");
  MF(ac0[0], wf[0], hq[9][0]); SBAR();
  GLDV(wf[17], wp1, lane16, 1024);
  tq[3] = ac4[3][3]; tq[3] = __builtin_fmaf(tq[3], __builtin_fabsf(tq[3]), 0.f); PIN(tq[3]);
  MF(ac0[1], wf[0], hq[9][1]); SBAR();
  pk[1][0] = relu2(pk[1][0]);
  pk[1][1] = cvtpk(tq[2], tq[3]); PIN(pk[1][1]);
  MF(ac0[2], wf[0], hq[9][2]); SBAR();
  pk[1][1] = relu2(pk[1][1]);
  fwl = fw3 + fdl; PIN(fwl);
  MF(ac0[3], wf[0], hq[9][3]); SBAR();
  asm volatile("s_waitcnt vmcnt(16)" :  :: "memory");
  MF(ac1[0], wf[1], hq[9][0]); SBAR();
  GLDV(wf[0], wp1, lane16, 2048);
  MF(ac1[1], wf[1], hq[9][1]); SBAR();
  DSW8(fwl, 0, (u32x2){pk[1][0], pk[1][1]});
  MF(ac1[2], wf[1], hq[9][2]); SBAR();
  MF(ac1[3], wf[1], hq[9][3]); SBAR();
  asm volatile("s_waitcnt vmcnt(16)" :  :: "memory");
  MF(ac0[0], wf[2], hq[10][0]); SBAR();
  GLDV(wf[1], wp1, lane16, 3072);
  MF(ac0[1], wf[2], hq[10][1]); SBAR();
  MF(ac0[2], wf[2], hq[10][2]); SBAR();
  MF(ac0[3], wf[2], hq[10][3]); SBAR();
  asm volatile("s_waitcnt vmcnt(16)" :  :: "memory");
  MF(ac1[0], wf[3], hq[10][0]); SBAR();
  GLDV(wf[2], wp0, lane16, -4096);
  MF(ac1[1], wf[3], hq[10][1]); SBAR();
  MF(ac1[2], wf[3], hq[10][2]); SBAR();
  MF(ac1[3], wf[3], hq[10][3]); SBAR();
  asm volatile("s_waitcnt vmcnt(16)" :  :: "memory");
  MF(ac0[0], wf[4], hq[11][0]); SBAR();
  GLDV(wf[3], wp0, lane16, -3072);
  MF(ac0[1], wf[4], hq[11][1]); SBAR();
  MF(ac0[2], wf[4], hq[11][2]); SBAR();
  MF(ac0[3], wf[4], hq[11][3]); SBAR();
  asm volatile("s_waitcnt vmcnt(16)" :  :: "memory");
  MF(ac1[0], wf[5], hq[11][0]); SBAR();
  GLDV(wf[4], wp0, lane16, -2048);
  MF(ac1[1], wf[5], hq[11][1]); SBAR();
  MF(ac1[2], wf[5], hq[11][2]); SBAR();
  MF(ac1[3], wf[5], hq[11][3]); SBAR();
  asm volatile("s_waitcnt vmcnt(16)" :  :: "memory");
  MF(ac0[0], wf[6], hq[12][0]); SBAR();
  GLDV(wf[5], wp0, lane16, -1024);
  MF(ac0[1], wf[6], hq[12][1]); SBAR();
  MF(ac0[2], wf[6], hq[12][2]); SBAR();
  MF(ac0[3], wf[6], hq[12][3]); SBAR();
  asm volatile("s_waitcnt vmcnt(16)" :  :: "memory");
  MF(ac1[0], wf[7], hq[12][0]); SBAR();
  GLDV(wf[6], wp0, lane16, 0);
  MF(ac1[1], wf[7], hq[12][1]); SBAR();
  wp1 = w_s + 290816; PINS(wp1);
  MF(ac1[2], wf[7], hq[12][2]); SBAR();
  MF(ac1[3], wf[7], hq[12][3]); SBAR();
  asm volatile("s_waitcnt vmcnt(16)" :  :: "memory");
  MF(ac0[0], wf[8], hq[13][0]); SBAR();
  GLDV(wf[7], wp0, lane16, 1024);
  MF(ac0[1], wf[8], hq[13][1]); SBAR();
  MF(ac0[2], wf[8], hq[13][2]); SBAR();
  MF(ac0[3], wf[8], hq[13][3]); SBAR();
  asm volatile("s_waitcnt vmcnt(16)" :  :: "memory");
  MF(ac1[0], wf[9], hq[13][0]); SBAR();
  GLDV(wf[8], wp0, lane16, 2048);
  MF(ac1[1], wf[9], hq[13][1]); SBAR();
  MF(ac1[2], wf[9], hq[13][2]); SBAR();
  MF(ac1[3], wf[9], hq[13][3]); SBAR();
  asm volatile("s_waitcnt vmcnt(16)" :  :: "memory");
  MF(ac0[0], wf[10], hq[14][0]); SBAR();
  GLDV(wf[9], wp0, lane16, 3072);
  MF(ac0[1], wf[10], hq[14][1]); SBAR();
  MF(ac0[2], wf[10], hq[14][2]); SBAR();
  MF(ac0[3], wf[10], hq[14][3]); SBAR();
  asm volatile("s_waitcnt vmcnt(16)" :  :: "memory");
  MF(ac1[0], wf[11], hq[14][0]); SBAR();
  GLDV(wf[10], wp1, lane16, -4096);
  MF(ac1[1], wf[11], hq[14][1]); SBAR();
  MF(ac1[2], wf[11], hq[14][2]); SBAR();
  MF(ac1[3], wf[11], hq[14][3]); SBAR();
  DSR(bb[6], b1_r, 384);
  asm volatile("s_waitcnt vmcnt(16)" :  :: "memory");
  MF(ac0[0], wf[12], hq[15][0]); SBAR();
  GLDV(wf[11], wp1, lane16, -3072);
  DSR(bb[7], b1_r, 448);
  MF(ac0[1], wf[12], hq[15][1]); SBAR();
  MF(ac0[2], wf[12], hq[15][2]); SBAR();
  MF(ac0[3], wf[12], hq[15][3]); SBAR();
  asm volatile("s_waitcnt vmcnt(16)" :  :: "memory");
  MF(ac1[0], wf[13], hq[15][0]); SBAR();
  GLDV(wf[12], wp1, lane16, -2048);
  MF(ac1[1], wf[13], hq[15][1]); SBAR();
  MF(ac1[2], wf[13], hq[15][2]); SBAR();
  MF(ac1[3], wf[13], hq[15][3]); SBAR();
  asm volatile("s_waitcnt vmcnt(16)" :  :: "memory");
  MFV(ac0[0], wf[14], hq[16][0]); SBAR();
  GLDV(wf[13], wp1, lane16, -1024);
  MFV(ac0[1], wf[14], hq[16][1]); SBAR();
  MFV(ac0[2], wf[14], hq[16][2]); SBAR();
  MFV(ac0[3], wf[14], hq[16][3]); SBAR();
  asm volatile("s_waitcnt vmcnt(16)" :  :: "memory");
  MFV(ac1[0], wf[15], hq[16][0]); SBAR();
  GLDV(wf[14], wp1, lane16, 0);
  MFV(ac1[1], wf[15], hq[16][1]); SBAR();
  wp0 = w_s + 299008; PINS(wp0);
  MFV(ac1[2], wf[15], hq[16][2]); SBAR();
  MFV(ac1[3], wf[15], hq[16][3]); SBAR();
  asm volatile("s_waitcnt vmcnt(16)" :  :: "memory");
  MFV(ac0[0], wf[16], hq[17][0]); SBAR();
  GLDV(wf[15], wp1, lane16, 1024);
  MFV(ac0[1], wf[16], hq[17][1]); SBAR();
  MFV(ac0[2], wf[16], hq[17][2]); SBAR();
  MFV(ac0[3], wf[16], hq[17][3]); SBAR();
  asm volatile("s_waitcnt vmcnt(16)" :  :: "memory");
  MFV(ac1[0], wf[17], hq[17][0]); SBAR();
  GLDV(wf[16], wp1, lane16, 2048);
  MFV(ac1[1], wf[17], hq[17][1]); SBAR();
  MFV(ac1[2], wf[17], hq[17][2]); SBAR();
  asm volatile("s_waitcnt lgkmcnt(2)\n\ts_barrier" :  :: "memory");
  MFV(ac1[3], wf[17], hq[17][3]); SBAR();
  asm volatile("s_waitcnt vmcnt(16)" :  :: "memory");
  asm volatile("s_waitcnt lgkmcnt(0)" :  :: "memory");
  MFC(ac2[0], wf[0], hq[0][0], bb[6]); SBAR();
  GLDV(wf[17], wp1, lane16, 3072);
  MFC(ac2[1], wf[0], hq[0][1], bb[6]); SBAR();
  MFC(ac2[2], wf[0], hq[0][2], bb[6]); SBAR();
  MFC(ac2[3], wf[0], hq[0][3], bb[6]); SBAR();
  asm volatile("s_waitcnt vmcnt(16)" :  :: "memory");
  MFC(ac3[0], wf[1], hq[0][0], bb[7]); SBAR();
  GLDV(wf[0], wp0, lane16, -4096);
  MFC(ac3[1], wf[1], hq[0][1], bb[7]); SBAR();
  MFC(ac3[2], wf[1], hq[0][2], bb[7]); SBAR();
  MFC(ac3[3], wf[1], hq[0][3], bb[7]); SBAR();
  tq[0] = ac0[0][0]; tq[0] *= __builtin_fabsf(tq[0]); PIN(tq[0]);
  asm volatile("s_waitcnt vmcnt(16)" :  :: "memory");
  MF(ac2[0], wf[2], hq[1][0]); SBAR();
  GLDV(wf[1], wp0, lane16, -3072);
  DSRTA(hq[0][0], fr0, 0);
  MF(ac2[1], wf[2], hq[1][1]); SBAR();
  DSRTA(hq[0][1], fr0, 1024);
  DSRTA(hq[0][2], fr0, 2048);
  MF(ac2[2], wf[2], hq[1][2]); SBAR();
  DSRTA(hq[0][3], fr0, 3072);
  tq[1] = ac0[0][1]; tq[1] = __builtin_fmaf(tq[1], __builtin_fabsf(tq[1]), 0.f); PIN(tq[1]);
  MF(ac2[3], wf[2], hq[1][3]); SBAR();
  tq[2] = ac0[0][2]; tq[2] *= __builtin_fabsf(tq[2]); PIN(tq[2]);
  asm volatile("s_waitcnt vmcnt(16)" :  :: "memory");
  MF(ac3[0], wf[3], hq[1][0]); SBAR();
  GLDV(wf[2], wp0, lane16, -2048);
  pk[0][0] = cvtpk(tq[0], tq[1]); PIN(pk[0][0]);
  MF(ac3[1], wf[3], hq[1][1]); SBAR();
  tq[3] = ac0[0][3]; tq[3] = __builtin_fmaf(tq[3], __builtin_fabsf(tq[3]), 0.f); PIN(tq[3]);
  pk[0][0] = relu2(pk[0][0]);
  MF(ac3[2], wf[3], hq[1][2]); SBAR();
  tq[0] = ac1[0][0]; tq[0] *= __builtin_fabsf(tq[0]); PIN(tq[0]);
  pk[0][1] = cvtpk(tq[2], tq[3]); PIN(pk[0][1]);
  MF(ac3[3], wf[3], hq[1][3]); SBAR();
  tq[1] = ac1[0][1]; tq[1] = __builtin_fmaf(tq[1], __builtin_fabsf(tq[1]), 0.f); PIN(tq[1]);
  asm volatile("s_waitcnt vmcnt(16)" :  :: "memory");
  MF(ac2[0], wf[4], hq[2][0]); SBAR();
  GLDV(wf[3], wp0, lane16, -1024);
  DSRTA(hq[1][0], fr0, 4096);
  MF(ac2[1], wf[4], hq[2][1]); SBAR();
  DSRTA(hq[1][1], fr0, 5120);
  DSRTA(hq[1][2], fr0, 6144);
  MF(ac2[2], wf[4], hq[2][2]); SBAR();
  DSRTA(hq[1][3], fr0, 7168);
  pk[0][1] = relu2(pk[0][1]);
  MF(ac2[3], wf[4], hq[2][3]); SBAR();
  tq[2] = ac1[0][2]; tq[2] *= __builtin_fabsf(tq[2]); PIN(tq[2]);
  asm volatile("s_waitcnt vmcnt(16)" :  :: "memory");
  MF(ac3[0], wf[5], hq[2][0]); SBAR();
  GLDV(wf[4], wp0, lane16, 0);
  pk[0][2] = cvtpk(tq[0], tq[1]); PIN(pk[0][2]);
  MF(ac3[1], wf[5], hq[2][1]); SBAR();
  wp1 = w_s + 307200; PINS(wp1);
  MF(ac3[2], wf[5], hq[2][2]); SBAR();
  tq[3] = ac1[0][3]; tq[3] = __builtin_fmaf(tq[3], __builtin_fabsf(tq[3]), 0.f); PIN(tq[3]);
  pk[0][2] = relu2(pk[0][2]);
  MF(ac3[3], wf[5], hq[2][3]); SBAR();
  tq[0] = ac0[1][0]; tq[0] *= __builtin_fabsf(tq[0]); PIN(tq[0]);
  asm volatile("s_waitcnt vmcnt(16)" :  :: "memory");
  MF(ac2[0], wf[6], hq[3][0]); SBAR();
  GLDV(wf[5], wp0, lane16, 1024);
  DSRTA(hq[2][0], fr0, 8192);
  MF(ac2[1], wf[6], hq[3][1]); SBAR();
  DSRTA(hq[2][1], fr0, 9216);
  DSRTA(hq[2][2], fr0, 10240);
  MF(ac2[2], wf[6], hq[3][2]); SBAR();
  DSRTA(hq[2][3], fr0, 11264);
  pk[0][3] = cvtpk(tq[2], tq[3]); PIN(pk[0][3]);
  MF(ac2[3], wf[6], hq[3][3]); SBAR();
  tq[1] = ac0[1][1]; tq[1] = __builtin_fmaf(tq[1], __builtin_fabsf(tq[1]), 0.f); PIN(tq[1]);
  asm volatile("s_waitcnt vmcnt(16)" :  :: "memory");
  MF(ac3[0], wf[7], hq[3][0]); SBAR();
  GLDV(wf[6], wp0, lane16, 2048);
  pk[0][3] = relu2(pk[0][3]);
  MF(ac3[1], wf[7], hq[3][1]); SBAR();
  tq[2] = ac0[1][2]; tq[2] *= __builtin_fabsf(tq[2]); PIN(tq[2]);
  MF(ac3[2], wf[7], hq[3][2]); SBAR();
  DSW16(fw0, 32768, (u32x4){pk[0][0], pk[0][1], pk[0][2], pk[0][3]});
  MF(ac3[3], wf[7], hq[3][3]); SBAR();
  pk[1][0] = cvtpk(tq[0], tq[1]); PIN(pk[1][0]);
  asm volatile("s_waitcnt vmcnt(16)" :  :: "memory");
  MF(ac2[0], wf[8], hq[4][0]); SBAR();
  GLDV(wf[7], wp0, lane16, 3072);
  DSRTA(hq[3][0], fr0, 12288);
  asm volatile(".Lk3_p5:\n\ts_cbranch_execz .Lk3_pr" ::: "memory");
  MF(ac2[1], wf[8], hq[4][1]); SBAR();
  DSRTA(hq[3][1], fr0, 13312);
  DSRTA(hq[3][2], fr0, 14336);
  MF(ac2[2], wf[8], hq[4][2]); SBAR();
  DSRTA(hq[3][3], fr0, 15360);
  tq[3] = ac0[1][3]; tq[3] = __builtin_fmaf(tq[3], __builtin_fabsf(tq[3]), 0.f); PIN(tq[3]);
  MF(ac2[3], wf[8], hq[4][3]); SBAR();
  pk[1][0] = relu2(pk[1][0]);
  asm volatile("s_waitcnt vmcnt(16)" :  :: "memory");
  MF(ac3[0], wf[9], hq[4][0]); SBAR();
  GLDV(wf[8], wp1, lane16, -4096);
  tq[0] = ac1[1][0]; tq[0] *= __builtin_fabsf(tq[0]); PIN(tq[0]);
  MF(ac3[1], wf[9], hq[4][1]); SBAR();
  pk[1][1] = cvtpk(tq[2], tq[3]); PIN(pk[1][1]);
  tq[1] = ac1[1][1]; tq[1] = __builtin_fmaf(tq[1], __builtin_fabsf(tq[1]), 0.f); PIN(tq[1]);
  MF(ac3[2], wf[9], hq[4][2]); SBAR();
  pk[1][1] = relu2(pk[1][1]);
  tq[2] = ac1[1][2]; tq[2] *= __builtin_fabsf(tq[2]); PIN(tq[2]);
  MF(ac3[3], wf[9], hq[4][3]); SBAR();
  pk[1][2] = cvtpk(tq[0], tq[1]); PIN(pk[1][2]);
  asm volatile("s_waitcnt vmcnt(16)" :  :: "memory");
  MF(ac2[0], wf[10], hq[5][0]); SBAR();
  GLDV(wf[9], wp1, lane16, -3072);
  DSRTA(hq[4][0], fr0, 16384);
  MF(ac2[1], wf[10], hq[5][1]); SBAR();
  DSRTA(hq[4][1], fr0, 17408);
  DSRTA(hq[4][2], fr0, 18432);
  MF(ac2[2], wf[10], hq[5][2]); SBAR();
  DSRTA(hq[4][3], fr0, 19456);
  tq[3] = ac1[1][3]; tq[3] = __builtin_fmaf(tq[3], __builtin_fabsf(tq[3]), 0.f); PIN(tq[3]);
  MF(ac2[3], wf[10], hq[5][3]); SBAR();
  pk[1][2] = relu2(pk[1][2]);
  asm volatile("s_waitcnt vmcnt(16)" :  :: "memory");
  MF(ac3[0], wf[11], hq[5][0]); SBAR();
  GLDV(wf[10], wp1, lane16, -2048);
  tq[0] = ac0[2][0]; tq[0] *= __builtin_fabsf(tq[0]); PIN(tq[0]);
  MF(ac3[1], wf[11], hq[5][1]); SBAR();
  pk[1][3] = cvtpk(tq[2], tq[3]); PIN(pk[1][3]);
  tq[1] = ac0[2][1]; tq[1] = __builtin_fmaf(tq[1], __builtin_fabsf(tq[1]), 0.f); PIN(tq[1]);
  MF(ac3[2], wf[11], hq[5][2]); SBAR();
  pk[1][3] = relu2(pk[1][3]);
  tq[2] = ac0[2][2]; tq[2] *= __builtin_fabsf(tq[2]); PIN(tq[2]);
  MF(ac3[3], wf[11], hq[5][3]); SBAR();
  asm volatile("s_waitcnt vmcnt(16)" :  :: "memory");
  MF(ac2[0], wf[12], hq[6][0]); SBAR();
  GLDV(wf[11], wp1, lane16, -1024);
  DSRTA(hq[5][0], fr0, 20480);
  MF(ac2[1], wf[12], hq[6][1]); SBAR();
  DSRTA(hq[5][1], fr0, 21504);
  DSRTA(hq[5][2], fr0, 22528);
  MF(ac2[2], wf[12], hq[6][2]); SBAR();
  DSRTA(hq[5][3], fr0, 23552);
  MF(ac2[3], wf[12], hq[6][3]); SBAR();
  asm volatile("s_waitcnt vmcnt(16)" :  :: "memory");
  MF(ac3[0], wf[13], hq[6][0]); SBAR();
  GLDV(wf[12], wp1, lane16, 0);
  MF(ac3[1], wf[13], hq[6][1]); SBAR();
  wp0 = w_s + 315392; PINS(wp0);
  MF(ac3[2], wf[13], hq[6][2]); SBAR();
  DSW16(fw1, 32768, (u32x4){pk[1][0], pk[1][1], pk[1][2], pk[1][3]});
  MF(ac3[3], wf[13], hq[6][3]); SBAR();
  pk[0][0] = cvtpk(tq[0], tq[1]); PIN(pk[0][0]);
  asm volatile("s_waitcnt vmcnt(16)" :  :: "memory");
  MF(ac2[0], wf[14], hq[7][0]); SBAR();
  GLDV(wf[13], wp1, lane16, 1024);
  DSRTA(hq[6][0], fr0, 24576);
  MF(ac2[1], wf[14], hq[7][1]); SBAR();
  DSRTA(hq[6][1], fr0, 25600);
  DSRTA(hq[6][2], fr0, 26624);
  MF(ac2[2], wf[14], hq[7][2]); SBAR();
  DSRTA(hq[6][3], fr0, 27648);
  tq[3] = ac0[2][3]; tq[3] = __builtin_fmaf(tq[3], __builtin_fabsf(tq[3]), 0.f); PIN(tq[3]);
  MF(ac2[3], wf[14], hq[7][3]); SBAR();
  pk[0][0] = relu2(pk[0][0]);
  asm volatile("s_waitcnt vmcnt(16)" :  :: "memory");
  MF(ac3[0], wf[15], hq[7][0]); SBAR();
  GLDV(wf[14], wp1, lane16, 2048);
  tq[0] = ac1[2][0]; tq[0] *= __builtin_fabsf(tq[0]); PIN(tq[0]);
  MF(ac3[1], wf[15], hq[7][1]); SBAR();
  pk[0][1] = cvtpk(tq[2], tq[3]); PIN(pk[0][1]);
  tq[1] = ac1[2][1]; tq[1] = __builtin_fmaf(tq[1], __builtin_fabsf(tq[1]), 0.f); PIN(tq[1]);
  MF(ac3[2], wf[15], hq[7][2]); SBAR();
  pk[0][1] = relu2(pk[0][1]);
  tq[2] = ac1[2][2]; tq[2] *= __builtin_fabsf(tq[2]); PIN(tq[2]);
  MF(ac3[3], wf[15], hq[7][3]); SBAR();
  pk[0][2] = cvtpk(tq[0], tq[1]); PIN(pk[0][2]);
  asm volatile("s_waitcnt vmcnt(16)" :  :: "memory");
  MF(ac2[0], wf[16], hq[8][0]); SBAR();
  GLDV(wf[15], wp1, lane16, 3072);
  DSRTA(hq[7][0], fr0, 28672);
  MF(ac2[1], wf[16], hq[8][1]); SBAR();
  DSRTA(hq[7][1], fr0, 29696);
  DSRTA(hq[7][2], fr0, 30720);
  MF(ac2[2], wf[16], hq[8][2]); SBAR();
  DSRTA(hq[7][3], fr0, 31744);
  tq[3] = ac1[2][3]; tq[3] = __builtin_fmaf(tq[3], __builtin_fabsf(tq[3]), 0.f); PIN(tq[3]);
  MF(ac2[3], wf[16], hq[8][3]); SBAR();
  pk[0][2] = relu2(pk[0][2]);
  asm volatile("s_waitcnt vmcnt(16)" :  :: "memory");
  MF(ac3[0], wf[17], hq[8][0]); SBAR();
  GLDV(wf[16], wp0, lane16, -4096);
  tq[0] = ac0[3][0]; tq[0] *= __builtin_fabsf(tq[0]); PIN(tq[0]);
  MF(ac3[1], wf[17], hq[8][1]); SBAR();
  pk[0][3] = cvtpk(tq[2], tq[3]); PIN(pk[0][3]);
  tq[1] = ac0[3][1]; tq[1] = __builtin_fmaf(tq[1], __builtin_fabsf(tq[1]), 0.f); PIN(tq[1]);
  MF(ac3[2], wf[17], hq[8][2]); SBAR();
  pk[0][3] = relu2(pk[0][3]);
  tq[2] = ac0[3][2]; tq[2] *= __builtin_fabsf(tq[2]); PIN(tq[2]);
  MF(ac3[3], wf[17], hq[8][3]); SBAR();
  asm volatile("s_waitcnt vmcnt(16)" :  :: "memory");
  MF(ac2[0], wf[0], hq[9][0]); SBAR();
  GLDV(wf[17], wp0, lane16, -3072);
  MF(ac2[1], wf[0], hq[9][1]); SBAR();
  DSW16(fw2, 32768, (u32x4){pk[0][0], pk[0][1], pk[0][2], pk[0][3]});
  MF(ac2[2], wf[0], hq[9][2]); SBAR();
  pk[1][0] = cvtpk(tq[0], tq[1]); PIN(pk[1][0]);
  tq[3] = ac0[3][3]; tq[3] = __builtin_fmaf(tq[3], __builtin_fabsf(tq[3]), 0.f); PIN(tq[3]);
  MF(ac2[3], wf[0], hq[9][3]); SBAR();
  pk[1][0] = relu2(pk[1][0]);
  asm volatile("s_waitcnt vmcnt(16)" :  :: "memory");
  MF(ac3[0], wf[1], hq[9][0]); SBAR();
  GLDV(wf[0], wp0, lane16, -2048);
  tq[0] = ac1[3][0]; tq[0] *= __builtin_fabsf(tq[0]); PIN(tq[0]);
  MF(ac3[1], wf[1], hq[9][1]); SBAR();
  pk[1][1] = cvtpk(tq[2], tq[3]); PIN(pk[1][1]);
  tq[1] = ac1[3][1]; tq[1] = __builtin_fmaf(tq[1], __builtin_fabsf(tq[1]), 0.f); PIN(tq[1]);
  MF(ac3[2], wf[1], hq[9][2]); SBAR();
  pk[1][1] = relu2(pk[1][1]);
  tq[2] = ac1[3][2]; tq[2] *= __builtin_fabsf(tq[2]); PIN(tq[2]);
  MF(ac3[3], wf[1], hq[9][3]); SBAR();
  pk[1][2] = cvtpk(tq[0], tq[1]); PIN(pk[1][2]);
  asm volatile("s_waitcnt vmcnt(16)" :  :: "memory");
  MF(ac2[0], wf[2], hq[10][0]); SBAR();
  GLDV(wf[1], wp0, lane16, -1024);
  tq[3] = ac1[3][3]; tq[3] = __builtin_fmaf(tq[3], __builtin_fabsf(tq[3]), 0.f); PIN(tq[3]);
  MF(ac2[1], wf[2], hq[10][1]); SBAR();
  pk[1][2] = relu2(pk[1][2]);
  pk[1][3] = cvtpk(tq[2], tq[3]); PIN(pk[1][3]);
  MF(ac2[2], wf[2], hq[10][2]); SBAR();
  pk[1][3] = relu2(pk[1][3]);
  MF(ac2[3], wf[2], hq[10][3]); SBAR();
  asm volatile("s_waitcnt vmcnt(16)" :  :: "memory");
  MF(ac3[0], wf[3], hq[10][0]); SBAR();
  GLDV(wf[2], wp0, lane16, 0);
  MF(ac3[1], wf[3], hq[10][1]); SBAR();
  wp1 = w_s + 323584; PINS(wp1);
  MF(ac3[2], wf[3], hq[10][2]); SBAR();
  DSW16(fw3, 32768, (u32x4){pk[1][0], pk[1][1], pk[1][2], pk[1][3]});
  MF(ac3[3], wf[3], hq[10][3]); SBAR();
  asm volatile("s_waitcnt vmcnt(16)" :  :: "memory");
  MF(ac2[0], wf[4], hq[11][0]); SBAR();
  GLDV(wf[3], wp0, lane16, 1024);
  MF(ac2[1], wf[4], hq[11][1]); SBAR();
  MF(ac2[2], wf[4], hq[11][2]); SBAR();
  MF(ac2[3], wf[4], hq[11][3]); SBAR();
  asm volatile("s_waitcnt vmcnt(16)" :  :: "memory");
  MF(ac3[0], wf[5], hq[11][0]); SBAR();
  GLDV(wf[4], wp0, lane16, 2048);
  MF(ac3[1], wf[5], hq[11][1]); SBAR();
  MF(ac3[2], wf[5], hq[11][2]); SBAR();
  MF(ac3[3], wf[5], hq[11][3]); SBAR();
  asm volatile("s_waitcnt vmcnt(16)" :  :: "memory");
  MF(ac2[0], wf[6], hq[12][0]); SBAR();
  GLDV(wf[5], wp0, lane16, 3072);
  MF(ac2[1], wf[6], hq[12][1]); SBAR();
  MF(ac2[2], wf[6], hq[12][2]); SBAR();
  MF(ac2[3], wf[6], hq[12][3]); SBAR();
  asm volatile("s_waitcnt vmcnt(16)" :  :: "memory");
  MF(ac3[0], wf[7], hq[12][0]); SBAR();
  GLDV(wf[6], wp1, lane16, -4096);
  MF(ac3[1], wf[7], hq[12][1]); SBAR();
  MF(ac3[2], wf[7], hq[12][2]); SBAR();
  asm volatile("s_waitcnt lgkmcnt(0)\n\ts_barrier" :  :: "memory");
  MF(ac3[3], wf[7], hq[12][3]); SBAR();
  DSRTA(hq[8][0], fr0, 32768);
  asm volatile("s_waitcnt vmcnt(16)" :  :: "memory");
  MF(ac2[0], wf[8], hq[13][0]); SBAR();
  GLDV(wf[7], wp1, lane16, -3072);
  DSRTA(hq[8][1], fr0, 33792);
  MF(ac2[1], wf[8], hq[13][1]); SBAR();
  DSRTA(hq[8][2], fr0, 34816);
  DSRTA(hq[8][3], fr0, 35840);
  MF(ac2[2], wf[8], hq[13][2]); SBAR();
  DSRTA(hq[9][0], fr0, 36864);
  DSRTA(hq[9][1], fr0, 37888);
  MF(ac2[3], wf[8], hq[13][3]); SBAR();
  DSRTA(hq[9][2], fr0, 38912);
  asm volatile("s_waitcnt vmcnt(16)" :  :: "memory");
  MF(ac3[0], wf[9], hq[13][0]); SBAR();
  GLDV(wf[8], wp1, lane16, -2048);
  DSRTA(hq[9][3], fr0, 39936);
  MF(ac3[1], wf[9], hq[13][1]); SBAR();
  DSRTA(hq[10][0], fr0, 40960);
  DSRTA(hq[10][1], fr0, 41984);
  MF(ac3[2], wf[9], hq[13][2]); SBAR();
  DSRTA(hq[10][2], fr0, 43008);
  DSRTA(hq[10][3], fr0, 44032);
  MF(ac3[3], wf[9], hq[13][3]); SBAR();
  DSRTA(hq[11][0], fr0, 45056);
  asm volatile("s_waitcnt vmcnt(16)" :  :: "memory");
  MF(ac2[0], wf[10], hq[14][0]); SBAR();
  GLDV(wf[9], wp1, lane16, -1024);
  DSRTA(hq[11][1], fr0, 46080);
  MF(ac2[1], wf[10], hq[14][1]); SBAR();
  DSRTA(hq[11][2], fr0, 47104);
  DSRTA(hq[11][3], fr0, 48128);
  MF(ac2[2], wf[10], hq[14][2]); SBAR();
  DSRTA(hq[12][0], fr0, 49152);
  DSRTA(hq[12][1], fr0, 50176);
  MF(ac2[3], wf[10], hq[14][3]); SBAR();
  DSRTA(hq[12][2], fr0, 51200);
  asm volatile("s_waitcnt vmcnt(16)" :  :: "memory");
  MF(ac3[0], wf[11], hq[14][0]); SBAR();
  GLDV(wf[10], wp1, lane16, 0);
  DSRTA(hq[12][3], fr0, 52224);
  MF(ac3[1], wf[11], hq[14][1]); SBAR();
  wp0 = w_s + 331776; PINS(wp0);
  MF(ac3[2], wf[11], hq[14][2]); SBAR();
  DSRTA(hq[13][0], fr0, 53248);
  DSRTA(hq[13][1], fr0, 54272);
  MF(ac3[3], wf[11], hq[14][3]); SBAR();
  DSRTA(hq[13][2], fr0, 55296);
  asm volatile("s_waitcnt vmcnt(16)" :  :: "memory");
  MF(ac2[0], wf[12], hq[15][0]); SBAR();
  GLDV(wf[11], wp1, lane16, 1024);
  DSRTA(hq[13][3], fr0, 56320);
  MF(ac2[1], wf[12], hq[15][1]); SBAR();
  MF(ac2[2], wf[12], hq[15][2]); SBAR();
  MF(ac2[3], wf[12], hq[15][3]); SBAR();
  asm volatile("s_waitcnt vmcnt(16)" :  :: "memory");
  MF(ac3[0], wf[13], hq[15][0]); SBAR();
  GLDV(wf[12], wp1, lane16, 2048);
  MF(ac3[1], wf[13], hq[15][1]); SBAR();
  MF(ac3[2], wf[13], hq[15][2]); SBAR();
  MF(ac3[3], wf[13], hq[15][3]); SBAR();
  asm volatile("s_waitcnt vmcnt(16)" :  :: "memory");
  MFV(ac2[0], wf[14], hq[16][0]); SBAR();
  GLDV(wf[13], wp1, lane16, 3072);
  MFV(ac2[1], wf[14], hq[16][1]); SBAR();
  MFV(ac2[2], wf[14], hq[16][2]); SBAR();
  MFV(ac2[3], wf[14], hq[16][3]); SBAR();
  asm volatile("s_waitcnt vmcnt(16)" :  :: "memory");
  MFV(ac3[0], wf[15], hq[16][0]); SBAR();
  GLDV(wf[14], wp0, lane16, -4096);
  MFV(ac3[1], wf[15], hq[16][1]); SBAR();
  MFV(ac3[2], wf[15], hq[16][2]); SBAR();
  MFV(ac3[3], wf[15], hq[16][3]); SBAR();
  asm volatile("s_waitcnt vmcnt(16)" :  :: "memory");
  MFV(ac2[0], wf[16], hq[17][0]); SBAR();
  GLDV(wf[15], wp0, lane16, -3072);
  MFV(ac2[1], wf[16], hq[17][1]); SBAR();
  MFV(ac2[2], wf[16], hq[17][2]); SBAR();
  MFV(ac2[3], wf[16], hq[17][3]); SBAR();
  asm volatile("s_waitcnt vmcnt(16)" :  :: "memory");
  MFV(ac3[0], wf[17], hq[17][0]); SBAR();
  GLDV(wf[16], wp0, lane16, -2048);
  MFV(ac3[1], wf[17], hq[17][1]); SBAR();
  MFV(ac3[2], wf[17], hq[17][2]); SBAR();
  MFV(ac3[3], wf[17], hq[17][3]); SBAR();
  asm volatile("s_waitcnt vmcnt(16)" :  :: "memory");
  asm volatile("s_waitcnt lgkmcnt(0)" :  :: "memory");
  MF0(ac0[0], wf[0], hq[0][0]); SBAR();
  GLDV(wf[17], wp0, lane16, -1024);
  MF0(ac0[1], wf[0], hq[0][1]); SBAR();
  MF0(ac0[2], wf[0], hq[0][2]); SBAR();
  MF0(ac0[3], wf[0], hq[0][3]); SBAR();
  asm volatile("s_waitcnt vmcnt(16)" :  :: "memory");
  MF0(ac1[0], wf[1], hq[0][0]); SBAR();
  GLDV(wf[0], wp0, lane16, 0);
  MF0(ac1[1], wf[1], hq[0][1]); SBAR();
  wp1 = w_s + 339968; PINS(wp1);
  MF0(ac1[2], wf[1], hq[0][2]); SBAR();
  MF0(ac1[3], wf[1], hq[0][3]); SBAR();
  tq[0] = ac2[0][0]; tq[0] *= __builtin_fabsf(tq[0]); PIN(tq[0]);
  asm volatile("s_waitcnt vmcnt(16)" :  :: "memory");
  MF(ac0[0], wf[2], hq[1][0]); SBAR();
  GLDV(wf[1], wp0, lane16, 1024);
  tq[1] = ac2[0][1]; tq[1] = __builtin_fmaf(tq[1], __builtin_fabsf(tq[1]), 0.f); PIN(tq[1]);
  MF(ac0[1], wf[2], hq[1][1]); SBAR();
  tq[2] = ac2[0][2]; tq[2] *= __builtin_fabsf(tq[2]); PIN(tq[2]);
  pk[0][0] = cvtpk(tq[0], tq[1]); PIN(pk[0][0]);
  MF(ac0[2], wf[2], hq[1][2]); SBAR();
  tq[3] = ac2[0][3]; tq[3] = __builtin_fmaf(tq[3], __builtin_fabsf(tq[3]), 0.f); PIN(tq[3]);
  pk[0][0] = relu2(pk[0][0]);
  MF(ac0[3], wf[2], hq[1][3]); SBAR();
  tq[0] = ac3[0][0]; tq[0] *= __builtin_fabsf(tq[0]); PIN(tq[0]);
  asm volatile("s_waitcnt vmcnt(16)" :  :: "memory");
  MF(ac1[0], wf[3], hq[1][0]); SBAR();
  GLDV(wf[2], wp0, lane16, 2048);
  pk[0][1] = cvtpk(tq[2], tq[3]); PIN(pk[0][1]);
  MF(ac1[1], wf[3], hq[1][1]); SBAR();
  tq[1] = ac3[0][1]; tq[1] = __builtin_fmaf(tq[1], __builtin_fabsf(tq[1]), 0.f); PIN(tq[1]);
  pk[0][1] = relu2(pk[0][1]);
  MF(ac1[2], wf[3], hq[1][2]); SBAR();
  tq[2] = ac3[0][2]; tq[2] *= __builtin_fabsf(tq[2]); PIN(tq[2]);
  pk[0][2] = cvtpk(tq[0], tq[1]); PIN(pk[0][2]);
  MF(ac1[3], wf[3], hq[1][3]); SBAR();
  tq[3] = ac3[0][3]; tq[3] = __builtin_fmaf(tq[3], __builtin_fabsf(tq[3]), 0.f); PIN(tq[3]);
  asm volatile("s_waitcnt vmcnt(16)" :  :: "memory");
  MF(ac0[0], wf[4], hq[2][0]); SBAR();
  GLDV(wf[3], wp0, lane16, 3072);
  pk[0][2] = relu2(pk[0][2]);
  MF(ac0[1], wf[4], hq[2][1]); SBAR();
  tq[0] = ac2[1][0]; tq[0] *= __builtin_fabsf(tq[0]); PIN(tq[0]);
  pk[0][3] = cvtpk(tq[2], tq[3]); PIN(pk[0][3]);
  MF(ac0[2], wf[4], hq[2][2]); SBAR();
  tq[1] = ac2[1][1]; tq[1] = __builtin_fmaf(tq[1], __builtin_fabsf(tq[1]), 0.f); PIN(tq[1]);
  pk[0][3] = relu2(pk[0][3]);
  MF(ac0[3], wf[4], hq[2][3]); SBAR();
  tq[2] = ac2[1][2]; tq[2] *= __builtin_fabsf(tq[2]); PIN(tq[2]);
  asm volatile("s_waitcnt vmcnt(16)" :  :: "memory");
  MF(ac1[0], wf[5], hq[2][0]); SBAR();
  GLDV(wf[4], wp1, lane16, -4096);
  MF(ac1[1], wf[5], hq[2][1]); SBAR();
  DSW16(fw0, 57344, (u32x4){pk[0][0], pk[0][1], pk[0][2], pk[0][3]});
  MF(ac1[2], wf[5], hq[2][2]); SBAR();
  pk[1][0] = cvtpk(tq[0], tq[1]); PIN(pk[1][0]);
  tq[3] = ac2[1][3]; tq[3] = __builtin_fmaf(tq[3], __builtin_fabsf(tq[3]), 0.f); PIN(tq[3]);
  MF(ac1[3], wf[5], hq[2][3]); SBAR();
  pk[1][0] = relu2(pk[1][0]);
  asm volatile("s_waitcnt vmcnt(16)" :  :: "memory");
  MF(ac0[0], wf[6], hq[3][0]); SBAR();
  GLDV(wf[5], wp1, lane16, -3072);
  tq[0] = ac3[1][0]; tq[0] *= __builtin_fabsf(tq[0]); PIN(tq[0]);
  MF(ac0[1], wf[6], hq[3][1]); SBAR();
  pk[1][1] = cvtpk(tq[2], tq[3]); PIN(pk[1][1]);
  tq[1] = ac3[1][1]; tq[1] = __builtin_fmaf(tq[1], __builtin_fabsf(tq[1]), 0.f); PIN(tq[1]);
  MF(ac0[2], wf[6], hq[3][2]); SBAR();
  pk[1][1] = relu2(pk[1][1]);
  tq[2] = ac3[1][2]; tq[2] *= __builtin_fabsf(tq[2]); PIN(tq[2]);
  MF(ac0[3], wf[6], hq[3][3]); SBAR();
  pk[1][2] = cvtpk(tq[0], tq[1]); PIN(pk[1][2]);
  asm volatile("s_waitcnt vmcnt(16)" :  :: "memory");
  MF(ac1[0], wf[7], hq[3][0]); SBAR();
  GLDV(wf[6], wp1, lane16, -2048);
  tq[3] = ac3[1][3]; tq[3] = __builtin_fmaf(tq[3], __builtin_fabsf(tq[3]), 0.f); PIN(tq[3]);
  MF(ac1[1], wf[7], hq[3][1]); SBAR();
  pk[1][2] = relu2(pk[1][2]);
  tq[0] = ac2[2][0]; tq[0] *= __builtin_fabsf(tq[0]); PIN(tq[0]);
  MF(ac1[2], wf[7], hq[3][2]); SBAR();
  pk[1][3] = cvtpk(tq[2], tq[3]); PIN(pk[1][3]);
  tq[1] = ac2[2][1]; tq[1] = __builtin_fmaf(tq[1], __builtin_fabsf(tq[1]), 0.f); PIN(tq[1]);
  MF(ac1[3], wf[7], hq[3][3]); SBAR();
  pk[1][3] = relu2(pk[1][3]);
  asm volatile("s_waitcnt vmcnt(16)" :  :: "memory");
  MF(ac0[0], wf[8], hq[4][0]); SBAR();
  GLDV(wf[7], wp1, lane16, -1024);
  tq[2] = ac2[2][2]; tq[2] *= __builtin_fabsf(tq[2]); PIN(tq[2]);
  MF(ac0[1], wf[8], hq[4][1]); SBAR();
  DSW16(fw1, 57344, (u32x4){pk[1][0], pk[1][1], pk[1][2], pk[1][3]});
  MF(ac0[2], wf[8], hq[4][2]); SBAR();
  pk[0][0] = cvtpk(tq[0], tq[1]); PIN(pk[0][0]);
  tq[3] = ac2[2][3]; tq[3] = __builtin_fmaf(tq[3], __builtin_fabsf(tq[3]), 0.f); PIN(tq[3]);
  MF(ac0[3], wf[8], hq[4][3]); SBAR();
  pk[0][0] = relu2(pk[0][0]);
  asm volatile("s_waitcnt vmcnt(16)" :  :: "memory");
  MF(ac1[0], wf[9], hq[4][0]); SBAR();
  GLDV(wf[8], wp1, lane16, 0);
  tq[0] = ac3[2][0]; tq[0] *= __builtin_fabsf(tq[0]); PIN(tq[0]);
  MF(ac1[1], wf[9], hq[4][1]); SBAR();
  wp0 = w_s + 348160; PINS(wp0);
  MF(ac1[2], wf[9], hq[4][2]); SBAR();
  pk[0][1] = cvtpk(tq[2], tq[3]); PIN(pk[0][1]);
  tq[1] = ac3[2][1]; tq[1] = __builtin_fmaf(tq[1], __builtin_fabsf(tq[1]), 0.f); PIN(tq[1]);
  MF(ac1[3], wf[9], hq[4][3]); SBAR();
  pk[0][1] = relu2(pk[0][1]);
  asm volatile("s_waitcnt vmcnt(16)" :  :: "memory");
  MF(ac0[0], wf[10], hq[5][0]); SBAR();
  GLDV(wf[9], wp1, lane16, 1024);
  tq[2] = ac3[2][2]; tq[2] *= __builtin_fabsf(tq[2]); PIN(tq[2]);
  MF(ac0[1], wf[10], hq[5][1]); SBAR();
  pk[0][2] = cvtpk(tq[0], tq[1]); PIN(pk[0][2]);
  tq[3] = ac3[2][3]; tq[3] = __builtin_fmaf(tq[3], __builtin_fabsf(tq[3]), 0.f); PIN(tq[3]);
  MF(ac0[2], wf[10], hq[5][2]); SBAR();
  pk[0][2] = relu2(pk[0][2]);
  tq[0] = ac2[3][0]; tq[0] *= __builtin_fabsf(tq[0]); PIN(tq[0]);
  MF(ac0[3], wf[10], hq[5][3]); SBAR();
  pk[0][3] = cvtpk(tq[2], tq[3]); PIN(pk[0][3]);
  asm volatile("s_waitcnt vmcnt(16)" :  :: "memory");
  MF(ac1[0], wf[11], hq[5][0]); SBAR();
  GLDV(wf[10], wp1, lane16, 2048);
  tq[1] = ac2[3][1]; tq[1] = __builtin_fmaf(tq[1], __builtin_fabsf(tq[1]), 0.f); PIN(tq[1]);
  MF(ac1[1], wf[11], hq[5][1]); SBAR();
  pk[0][3] = relu2(pk[0][3]);
  tq[2] = ac2[3][2]; tq[2] *= __builtin_fabsf(tq[2]); PIN(tq[2]);
  MF(ac1[2], wf[11], hq[5][2]); SBAR();
  DSW16(fw2, 57344, (u32x4){pk[0][0], pk[0][1], pk[0][2], pk[0][3]});
  MF(ac1[3], wf[11], hq[5][3]); SBAR();
  pk[1][0] = cvtpk(tq[0], tq[1]); PIN(pk[1][0]);
  asm volatile("s_waitcnt vmcnt(16)" :  :: "memory");
  MF(ac0[0], wf[12], hq[6][0]); SBAR();
  GLDV(wf[11], wp1, lane16, 3072);
  tq[3] = ac2[3][3]; tq[3] = __builtin_fmaf(tq[3], __builtin_fabsf(tq[3]), 0.f); PIN(tq[3]);
  MF(ac0[1], wf[12], hq[6][1]); SBAR();
  pk[1][0] = relu2(pk[1][0]);
  tq[0] = ac3[3][0]; tq[0] *= __builtin_fabsf(tq[0]); PIN(tq[0]);
  MF(ac0[2], wf[12], hq[6][2]); SBAR();
  pk[1][1] = cvtpk(tq[2], tq[3]); PIN(pk[1][1]);
  tq[1] = ac3[3][1]; tq[1] = __builtin_fmaf(tq[1], __builtin_fabsf(tq[1]), 0.f); PIN(tq[1]);
  MF(ac0[3], wf[12], hq[6][3]); SBAR();
  pk[1][1] = relu2(pk[1][1]);
  asm volatile("s_waitcnt vmcnt(16)" :  :: "memory");
  MF(ac1[0], wf[13], hq[6][0]); SBAR();
  GLDV(wf[12], wp0, lane16, -4096);
  tq[2] = ac3[3][2]; tq[2] *= __builtin_fabsf(tq[2]); PIN(tq[2]);
  MF(ac1[1], wf[13], hq[6][1]); SBAR();
  pk[1][2] = cvtpk(tq[0], tq[1]); PIN(pk[1][2]);
  tq[3] = ac3[3][3]; tq[3] = __builtin_fmaf(tq[3], __builtin_fabsf(tq[3]), 0.f); PIN(tq[3]);
  MF(ac1[2], wf[13], hq[6][2]); SBAR();
  pk[1][2] = relu2(pk[1][2]);
  pk[1][3] = cvtpk(tq[2], tq[3]); PIN(pk[1][3]);
  MF(ac1[3], wf[13], hq[6][3]); SBAR();
  pk[1][3] = relu2(pk[1][3]);
  asm volatile("s_waitcnt vmcnt(16)" :  :: "memory");
  MF(ac0[0], wf[14], hq[7][0]); SBAR();
  GLDV(wf[13], wp0, lane16, -3072);
  MF(ac0[1], wf[14], hq[7][1]); SBAR();
  DSW16(fw3, 57344, (u32x4){pk[1][0], pk[1][1], pk[1][2], pk[1][3]});
  MF(ac0[2], wf[14], hq[7][2]); SBAR();
  MF(ac0[3], wf[14], hq[7][3]); SBAR();
  asm volatile("s_waitcnt vmcnt(16)" :  :: "memory");
  MF(ac1[0], wf[15], hq[7][0]); SBAR();
  GLDV(wf[14], wp0, lane16, -2048);
  MF(ac1[1], wf[15], hq[7][1]); SBAR();
  MF(ac1[2], wf[15], hq[7][2]); SBAR();
  asm volatile("s_waitcnt lgkmcnt(0)\n\ts_barrier" :  :: "memory");
  MF(ac1[3], wf[15], hq[7][3]); SBAR();
  DSRTA(hq[14][0], fr0, 57344);
  asm volatile("s_waitcnt vmcnt(16)" :  :: "memory");
  MF(ac0[0], wf[16], hq[8][0]); SBAR();
  GLDV(wf[15], wp0, lane16, -1024);
  DSRTA(hq[14][1], fr0, 58368);
  MF(ac0[1], wf[16], hq[8][1]); SBAR();
  DSRTA(hq[14][2], fr0, 59392);
  DSRTA(hq[14][3], fr0, 60416);
  MF(ac0[2], wf[16], hq[8][2]); SBAR();
  DSRTA(hq[15][0], fr0, 61440);
  DSRTA(hq[15][1], fr0, 62464);
  MF(ac0[3], wf[16], hq[8][3]); SBAR();
  DSRTA(hq[15][2], fr0, 63488);
  asm volatile("s_waitcnt vmcnt(16)" :  :: "memory");
  MF(ac1[0], wf[17], hq[8][0]); SBAR();
  GLDV(wf[16], wp0, lane16, 0);
  DSRTA(hq[15][3], fr0, 64512);
  MF(ac1[1], wf[17], hq[8][1]); SBAR();
  wp1 = w_s + 356352; PINS(wp1);
  MF(ac1[2], wf[17], hq[8][2]); SBAR();
  DSRTV(hq[16][0], fr1, 0);
  DSRTV(hq[16][1], fr1, 1024);
  MF(ac1[3], wf[17], hq[8][3]); SBAR();
  DSRTV(hq[16][2], fr1, 2048);
  asm volatile("s_waitcnt vmcnt(16)" :  :: "memory");
  MF(ac0[0], wf[0], hq[9][0]); SBAR();
  GLDV(wf[17], wp0, lane16, 1024);
  DSRTV(hq[16][3], fr1, 3072);
  MF(ac0[1], wf[0], hq[9][1]); SBAR();
  DSRTV(hq[17][0], fr1, 4096);
  DSRTV(hq[17][1], fr1, 5120);
  MF(ac0[2], wf[0], hq[9][2]); SBAR();
  DSRTV(hq[17][2], fr1, 6144);
  DSRTV(hq[17][3], fr1, 7168);
  MF(ac0[3], wf[0], hq[9][3]); SBAR();
  asm volatile("s_waitcnt vmcnt(16)" :  :: "memory");
  MF(ac1[0], wf[1], hq[9][0]); SBAR();
  GLDV(wf[0], wp0, lane16, 2048);
  MF(ac1[1], wf[1], hq[9][1]); SBAR();
  MF(ac1[2], wf[1], hq[9][2]); SBAR();
  MF(ac1[3], wf[1], hq[9][3]); SBAR();
  asm volatile("s_waitcnt vmcnt(16)" :  :: "memory");
  MF(ac0[0], wf[2], hq[10][0]); SBAR();
  GLDV(wf[1], wp0, lane16, 3072);
  MF(ac0[1], wf[2], hq[10][1]); SBAR();
  MF(ac0[2], wf[2], hq[10][2]); SBAR();
  MF(ac0[3], wf[2], hq[10][3]); SBAR();
  asm volatile("s_waitcnt vmcnt(16)" :  :: "memory");
  MF(ac1[0], wf[3], hq[10][0]); SBAR();
  GLDV(wf[2], wp1, lane16, -4096);
  MF(ac1[1], wf[3], hq[10][1]); SBAR();
  MF(ac1[2], wf[3], hq[10][2]); SBAR();
  MF(ac1[3], wf[3], hq[10][3]); SBAR();
  asm volatile("s_waitcnt vmcnt(16)" :  :: "memory");
  MF(ac0[0], wf[4], hq[11][0]); SBAR();
  GLDV(wf[3], wp1, lane16, -3072);
  MF(ac0[1], wf[4], hq[11][1]); SBAR();
  MF(ac0[2], wf[4], hq[11][2]); SBAR();
  MF(ac0[3], wf[4], hq[11][3]); SBAR();
  asm volatile("s_waitcnt vmcnt(16)" :  :: "memory");
  MF(ac1[0], wf[5], hq[11][0]); SBAR();
  GLDV(wf[4], wp1, lane16, -2048);
  MF(ac1[1], wf[5], hq[11][1]); SBAR();
  MF(ac1[2], wf[5], hq[11][2]); SBAR();
  MF(ac1[3], wf[5], hq[11][3]); SBAR();
  asm volatile("s_waitcnt vmcnt(16)" :  :: "memory");
  MF(ac0[0], wf[6], hq[12][0]); SBAR();
  GLDV(wf[5], wp1, lane16, -1024);
  MF(ac0[1], wf[6], hq[12][1]); SBAR();
  MF(ac0[2], wf[6], hq[12][2]); SBAR();
  MF(ac0[3], wf[6], hq[12][3]); SBAR();
  asm volatile("s_waitcnt vmcnt(16)" :  :: "memory");
  MF(ac1[0], wf[7], hq[12][0]); SBAR();
  GLDV(wf[6], wp1, lane16, 0);
  MF(ac1[1], wf[7], hq[12][1]); SBAR();
  wp0 = w_s + 364544; PINS(wp0);
  MF(ac1[2], wf[7], hq[12][2]); SBAR();
  MF(ac1[3], wf[7], hq[12][3]); SBAR();
  asm volatile("s_waitcnt vmcnt(16)" :  :: "memory");
  MF(ac0[0], wf[8], hq[13][0]); SBAR();
  GLDV(wf[7], wp1, lane16, 1024);
  MF(ac0[1], wf[8], hq[13][1]); SBAR();
  MF(ac0[2], wf[8], hq[13][2]); SBAR();
  MF(ac0[3], wf[8], hq[13][3]); SBAR();
  asm volatile("s_waitcnt vmcnt(16)" :  :: "memory");
  MF(ac1[0], wf[9], hq[13][0]); SBAR();
  GLDV(wf[8], wp1, lane16, 2048);
  MF(ac1[1], wf[9], hq[13][1]); SBAR();
  MF(ac1[2], wf[9], hq[13][2]); SBAR();
  MF(ac1[3], wf[9], hq[13][3]); SBAR();
  asm volatile("s_waitcnt vmcnt(16)" :  :: "memory");
  asm volatile("s_waitcnt lgkmcnt(12)" :  :: "memory");
  MF(ac0[0], wf[10], hq[14][0]); SBAR();
  GLDV(wf[9], wp1, lane16, 3072);
  MF(ac0[1], wf[10], hq[14][1]); SBAR();
  MF(ac0[2], wf[10], hq[14][2]); SBAR();
  MF(ac0[3], wf[10], hq[14][3]); SBAR();
  asm volatile("s_waitcnt vmcnt(16)" :  :: "memory");
  MF(ac1[0], wf[11], hq[14][0]); SBAR();
  GLDV(wf[10], wp0, lane16, -4096);
  MF(ac1[1], wf[11], hq[14][1]); SBAR();
  MF(ac1[2], wf[11], hq[14][2]); SBAR();
  asm volatile(".Lk3_p6:\n\ts_cbranch_execz .Lk3_pr" ::: "memory");
  MF(ac1[3], wf[11], hq[14][3]); SBAR();
  asm volatile("s_waitcnt vmcnt(16)" :  :: "memory");
  asm volatile("s_waitcnt lgkmcnt(8)" :  :: "memory");
  MF(ac0[0], wf[12], hq[15][0]); SBAR();
  GLDV(wf[11], wp0, lane16, -3072);
  MF(ac0[1], wf[12], hq[15][1]); SBAR();
  MF(ac0[2], wf[12], hq[15][2]); SBAR();
  MF(ac0[3], wf[12], hq[15][3]); SBAR();
  asm volatile("s_waitcnt vmcnt(16)" :  :: "memory");
  MF(ac1[0], wf[13], hq[15][0]); SBAR();
  GLDV(wf[12], wp0, lane16, -2048);
  MF(ac1[1], wf[13], hq[15][1]); SBAR();
  MF(ac1[2], wf[13], hq[15][2]); SBAR();
  MF(ac1[3], wf[13], hq[15][3]); SBAR();
  asm volatile("s_waitcnt vmcnt(16)" :  :: "memory");
  asm volatile("s_waitcnt lgkmcnt(4)" :  :: "memory");
  MFV(ac0[0], wf[14], hq[16][0]); SBAR();
  GLDV(wf[13], wp0, lane16, -1024);
  MFV(ac0[1], wf[14], hq[16][1]); SBAR();
  MFV(ac0[2], wf[14], hq[16][2]); SBAR();
  MFV(ac0[3], wf[14], hq[16][3]); SBAR();
  asm volatile("s_waitcnt vmcnt(16)" :  :: "memory");
  MFV(ac1[0], wf[15], hq[16][0]); SBAR();
  GLDV(wf[14], wp0, lane16, 0);
  MFV(ac1[1], wf[15], hq[16][1]); SBAR();
  wp1 = w_s + 372736; PINS(wp1);
  MFV(ac1[2], wf[15], hq[16][2]); SBAR();
  MFV(ac1[3], wf[15], hq[16][3]); SBAR();
  asm volatile("s_waitcnt vmcnt(16)" :  :: "memory");
  asm volatile("s_waitcnt lgkmcnt(0)" :  :: "memory");
  MFV(ac0[0], wf[16], hq[17][0]); SBAR();
  GLDV(wf[15], wp0, lane16, 1024);
  MFV(ac0[1], wf[16], hq[17][1]); SBAR();
  MFV(ac0[2], wf[16], hq[17][2]); SBAR();
  MFV(ac0[3], wf[16], hq[17][3]); SBAR();
  asm volatile("s_waitcnt vmcnt(16)" :  :: "memory");
  MFV(ac1[0], wf[17], hq[17][0]); SBAR();
  GLDV(wf[16], wp0, lane16, 2048);
  MFV(ac1[1], wf[17], hq[17][1]); SBAR();
  MFV(ac1[2], wf[17], hq[17][2]); SBAR();
  MFV(ac1[3], wf[17], hq[17][3]); SBAR();
  asm volatile("s_waitcnt vmcnt(16)" :  :: "memory");
  MF0(ac2[0], wf[0], hq[0][0]); SBAR();
  GLDV(wf[17], wp0, lane16, 3072);
  MF0(ac2[1], wf[0], hq[0][1]); SBAR();
  sp = p2 + 0; PINS(sp);
  MF0(ac2[2], wf[0], hq[0][2]); SBAR();
  MF0(ac2[3], wf[0], hq[0][3]); SBAR();
  asm volatile("s_waitcnt vmcnt(16)" :  :: "memory");
  MF0(ac3[0], wf[1], hq[0][0]); SBAR();
  MF0(ac3[1], wf[1], hq[0][1]); SBAR();
  MF0(ac3[2], wf[1], hq[0][2]); SBAR();
  MF0(ac3[3], wf[1], hq[0][3]); SBAR();
  pk[0][0] = cvtpk(ac0[0][0], ac0[0][1]); PIN(pk[0][0]);
  asm volatile("s_waitcnt vmcnt(15)" :  :: "memory");
  MF(ac2[0], wf[2], hq[1][0]); SBAR();
  pk[0][1] = cvtpk(ac0[0][2], ac0[0][3]); PIN(pk[0][1]);
  pk[0][2] = cvtpk(ac1[0][0], ac1[0][1]); PIN(pk[0][2]);
  MF(ac2[1], wf[2], hq[1][1]); SBAR();
  pk[0][3] = cvtpk(ac1[0][2], ac1[0][3]); PIN(pk[0][3]);
  MF(ac2[2], wf[2], hq[1][2]); SBAR();
  st16n<0>(sp, lane16, (u32x4){pk[0][0], pk[0][1], pk[0][2], pk[0][3]});
  MF(ac2[3], wf[2], hq[1][3]); SBAR();
  asm volatile("s_waitcnt vmcnt(15)" :  :: "memory");
  MF(ac3[0], wf[3], hq[1][0]); SBAR();
  MF(ac3[1], wf[3], hq[1][1]); SBAR();
  pk[1][0] = cvtpk(ac0[1][0], ac0[1][1]); PIN(pk[1][0]);
  pk[1][1] = cvtpk(ac0[1][2], ac0[1][3]); PIN(pk[1][1]);
  MF(ac3[2], wf[3], hq[1][2]); SBAR();
  pk[1][2] = cvtpk(ac1[1][0], ac1[1][1]); PIN(pk[1][2]);
  pk[1][3] = cvtpk(ac1[1][2], ac1[1][3]); PIN(pk[1][3]);
  MF(ac3[3], wf[3], hq[1][3]); SBAR();
  st16n<1024>(sp, lane16, (u32x4){pk[1][0], pk[1][1], pk[1][2], pk[1][3]});
  asm volatile("s_waitcnt vmcnt(15)" :  :: "memory");
  MF(ac2[0], wf[4], hq[2][0]); SBAR();
  MF(ac2[1], wf[4], hq[2][1]); SBAR();
  MF(ac2[2], wf[4], hq[2][2]); SBAR();
  pk[0][0] = cvtpk(ac0[2][0], ac0[2][1]); PIN(pk[0][0]);
  pk[0][1] = cvtpk(ac0[2][2], ac0[2][3]); PIN(pk[0][1]);
  MF(ac2[3], wf[4], hq[2][3]); SBAR();
  pk[0][2] = cvtpk(ac1[2][0], ac1[2][1]); PIN(pk[0][2]);
  asm volatile("s_waitcnt vmcnt(14)" :  :: "memory");
  MF(ac3[0], wf[5], hq[2][0]); SBAR();
  pk[0][3] = cvtpk(ac1[2][2], ac1[2][3]); PIN(pk[0][3]);
  MF(ac3[1], wf[5], hq[2][1]); SBAR();
  wp0 = w_s + 380928; PINS(wp0);
  MF(ac3[2], wf[5], hq[2][2]); SBAR();
  st16n<2048>(sp, lane16, (u32x4){pk[0][0], pk[0][1], pk[0][2], pk[0][3]});
  MF(ac3[3], wf[5], hq[2][3]); SBAR();
  asm volatile("s_waitcnt vmcnt(14)" :  :: "memory");
  MF(ac2[0], wf[6], hq[3][0]); SBAR();
  MF(ac2[1], wf[6], hq[3][1]); SBAR();
  pk[1][0] = cvtpk(ac0[3][0], ac0[3][1]); PIN(pk[1][0]);
  pk[1][1] = cvtpk(ac0[3][2], ac0[3][3]); PIN(pk[1][1]);
  MF(ac2[2], wf[6], hq[3][2]); SBAR();
  pk[1][2] = cvtpk(ac1[3][0], ac1[3][1]); PIN(pk[1][2]);
  pk[1][3] = cvtpk(ac1[3][2], ac1[3][3]); PIN(pk[1][3]);
  MF(ac2[3], wf[6], hq[3][3]); SBAR();
  st16n<3072>(sp, lane16, (u32x4){pk[1][0], pk[1][1], pk[1][2], pk[1][3]});
  asm volatile("s_waitcnt vmcnt(14)" :  :: "memory");
  MF(ac3[0], wf[7], hq[3][0]); SBAR();
  MF(ac3[1], wf[7], hq[3][1]); SBAR();
  MF(ac3[2], wf[7], hq[3][2]); SBAR();
  MF(ac3[3], wf[7], hq[3][3]); SBAR();
  asm volatile("s_waitcnt vmcnt(13)" :  :: "memory");
  MF(ac2[0], wf[8], hq[4][0]); SBAR();
  GLDV(wf[7], wp1, lane16, 3072);
  MF(ac2[1], wf[8], hq[4][1]); SBAR();
  MF(ac2[2], wf[8], hq[4][2]); SBAR();
  MF(ac2[3], wf[8], hq[4][3]); SBAR();
  asm volatile("s_waitcnt vmcnt(13)" :  :: "memory");
  MF(ac3[0], wf[9], hq[4][0]); SBAR();
  GLDV(wf[8], wp0, lane16, -4096);
  MF(ac3[1], wf[9], hq[4][1]); SBAR();
  MF(ac3[2], wf[9], hq[4][2]); SBAR();
  MF(ac3[3], wf[9], hq[4][3]); SBAR();
  asm volatile("s_waitcnt vmcnt(13)" :  :: "memory");
  MF(ac2[0], wf[10], hq[5][0]); SBAR();
  GLDV(wf[9], wp0, lane16, -3072);
  MF(ac2[1], wf[10], hq[5][1]); SBAR();
  MF(ac2[2], wf[10], hq[5][2]); SBAR();
  MF(ac2[3], wf[10], hq[5][3]); SBAR();
  asm volatile("s_waitcnt vmcnt(13)" :  :: "memory");
  MF(ac3[0], wf[11], hq[5][0]); SBAR();
  GLDV(wf[10], wp0, lane16, -2048);
  MF(ac3[1], wf[11], hq[5][1]); SBAR();
  MF(ac3[2], wf[11], hq[5][2]); SBAR();
  MF(ac3[3], wf[11], hq[5][3]); SBAR();
  asm volatile("s_waitcnt vmcnt(13)" :  :: "memory");
  MF(ac2[0], wf[12], hq[6][0]); SBAR();
  GLDV(wf[11], wp0, lane16, -1024);
  MF(ac2[1], wf[12], hq[6][1]); SBAR();
  MF(ac2[2], wf[12], hq[6][2]); SBAR();
  MF(ac2[3], wf[12], hq[6][3]); SBAR();
  asm volatile("s_waitcnt vmcnt(13)" :  :: "memory");
  MF(ac3[0], wf[13], hq[6][0]); SBAR();
  GLDV(wf[12], wp0, lane16, 0);
  MF(ac3[1], wf[13], hq[6][1]); SBAR();
  wp1 = w_s + 389120; PINS(wp1);
  MF(ac3[2], wf[13], hq[6][2]); SBAR();
  MF(ac3[3], wf[13], hq[6][3]); SBAR();
  asm volatile("s_waitcnt vmcnt(13)" :  :: "memory");
  MF(ac2[0], wf[14], hq[7][0]); SBAR();
  GLDV(wf[13], wp0, lane16, 1024);
  MF(ac2[1], wf[14], hq[7][1]); SBAR();
  MF(ac2[2], wf[14], hq[7][2]); SBAR();
  DSR(wf[0], sr0, 0);
  MF(ac2[3], wf[14], hq[7][3]); SBAR();
  asm volatile("s_waitcnt vmcnt(13)" :  :: "memory");
  MF(ac3[0], wf[15], hq[7][0]); SBAR();
  GLDV(wf[14], wp0, lane16, 2048);
  MF(ac3[1], wf[15], hq[7][1]); SBAR();
  MF(ac3[2], wf[15], hq[7][2]); SBAR();
  DSR(wf[1], sr0, 1024);
  MF(ac3[3], wf[15], hq[7][3]); SBAR();
  asm volatile("s_waitcnt vmcnt(13)" :  :: "memory");
  MF(ac2[0], wf[16], hq[8][0]); SBAR();
  GLDV(wf[15], wp0, lane16, 3072);
  MF(ac2[1], wf[16], hq[8][1]); SBAR();
  MF(ac2[2], wf[16], hq[8][2]); SBAR();
  DSR(wf[2], sr0, 2048);
  MF(ac2[3], wf[16], hq[8][3]); SBAR();
  asm volatile("s_waitcnt vmcnt(13)" :  :: "memory");
  MF(ac3[0], wf[17], hq[8][0]); SBAR();
  GLDV(wf[16], wp1, lane16, -4096);
  MF(ac3[1], wf[17], hq[8][1]); SBAR();
  MF(ac3[2], wf[17], hq[8][2]); SBAR();
  DSR(wf[3], sr0, 3072);
  MF(ac3[3], wf[17], hq[8][3]); SBAR();
  asm volatile("s_waitcnt lgkmcnt(3)" :  :: "memory");
  MF(ac2[0], wf[0], hq[9][0]); SBAR();
  GLDV(wf[17], wp1, lane16, -3072);
  MF(ac2[1], wf[0], hq[9][1]); SBAR();
  MF(ac2[2], wf[0], hq[9][2]); SBAR();
  DSR(wf[4], sr0, 16384);
  MF(ac2[3], wf[0], hq[9][3]); SBAR();
  asm volatile("s_waitcnt lgkmcnt(3)" :  :: "memory");
  MF(ac3[0], wf[1], hq[9][0]); SBAR();
  GLDV(wf[0], wp1, lane16, -2048);
  MF(ac3[1], wf[1], hq[9][1]); SBAR();
  MF(ac3[2], wf[1], hq[9][2]); SBAR();
  DSR(wf[5], sr0, 17408);
  MF(ac3[3], wf[1], hq[9][3]); SBAR();
  asm volatile("s_waitcnt lgkmcnt(3)" :  :: "memory");
  MF(ac2[0], wf[2], hq[10][0]); SBAR();
  GLDV(wf[1], wp1, lane16, -1024);
  MF(ac2[1], wf[2], hq[10][1]); SBAR();
  MF(ac2[2], wf[2], hq[10][2]); SBAR();
  DSR(wf[6], sr0, 18432);
  MF(ac2[3], wf[2], hq[10][3]); SBAR();
  asm volatile("s_waitcnt lgkmcnt(3)" :  :: "memory");
  MF(ac3[0], wf[3], hq[10][0]); SBAR();
  GLDV(wf[2], wp1, lane16, 0);
  MF(ac3[1], wf[3], hq[10][1]); SBAR();
  wp0 = w_s + 397312; PINS(wp0);
  MF(ac3[2], wf[3], hq[10][2]); SBAR();
  MF(ac3[3], wf[3], hq[10][3]); SBAR();
  asm volatile("s_waitcnt lgkmcnt(2)" :  :: "memory");
  MF(ac2[0], wf[4], hq[11][0]); SBAR();
  GLDV(wf[3], wp1, lane16, 1024);
  MF(ac2[1], wf[4], hq[11][1]); SBAR();
  MF(ac2[2], wf[4], hq[11][2]); SBAR();
  MF(ac2[3], wf[4], hq[11][3]); SBAR();
  asm volatile("s_waitcnt lgkmcnt(1)" :  :: "memory");
  MF(ac3[0], wf[5], hq[11][0]); SBAR();
  GLDV(wf[4], wp1, lane16, 2048);
  MF(ac3[1], wf[5], hq[11][1]); SBAR();
  MF(ac3[2], wf[5], hq[11][2]); SBAR();
  MF(ac3[3], wf[5], hq[11][3]); SBAR();
  asm volatile("s_waitcnt lgkmcnt(0)" :  :: "memory");
  MF(ac2[0], wf[6], hq[12][0]); SBAR();
  GLDV(wf[5], wp1, lane16, 3072);
  MF(ac2[1], wf[6], hq[12][1]); SBAR();
  MF(ac2[2], wf[6], hq[12][2]); SBAR();
  MF(ac2[3], wf[6], hq[12][3]); SBAR();
  asm volatile("s_waitcnt vmcnt(16)" :  :: "memory");
  MF(ac3[0], wf[7], hq[12][0]); SBAR();
  GLDV(wf[6], wp0, lane16, -4096);
  MF(ac3[1], wf[7], hq[12][1]); SBAR();
  MF(ac3[2], wf[7], hq[12][2]); SBAR();
  MF(ac3[3], wf[7], hq[12][3]); SBAR();
  asm volatile("s_waitcnt vmcnt(16)" :  :: "memory");
  MF(ac2[0], wf[8], hq[13][0]); SBAR();
  GLDV(wf[7], wp0, lane16, -3072);
  MF(ac2[1], wf[8], hq[13][1]); SBAR();
  MF(ac2[2], wf[8], hq[13][2]); SBAR();
  MF(ac2[3], wf[8], hq[13][3]); SBAR();
  asm volatile("s_waitcnt vmcnt(16)" :  :: "memory");
  MF(ac3[0], wf[9], hq[13][0]); SBAR();
  GLDV(wf[8], wp0, lane16, -2048);
  MF(ac3[1], wf[9], hq[13][1]); SBAR();
  MF(ac3[2], wf[9], hq[13][2]); SBAR();
  MF(ac3[3], wf[9], hq[13][3]); SBAR();
  asm volatile("s_waitcnt vmcnt(16)" :  :: "memory");
  MF(ac2[0], wf[10], hq[14][0]); SBAR();
  GLDV(wf[9], wp0, lane16, -1024);
  MF(ac2[1], wf[10], hq[14][1]); SBAR();
  MF(ac2[2], wf[10], hq[14][2]); SBAR();
  MF(ac2[3], wf[10], hq[14][3]); SBAR();
  asm volatile("s_waitcnt vmcnt(16)" :  :: "memory");
  MF(ac3[0], wf[11], hq[14][0]); SBAR();
  GLDV(wf[10], wp0, lane16, 0);
  MF(ac3[1], wf[11], hq[14][1]); SBAR();
  wp1 = w_s + 405504; PINS(wp1);
  MF(ac3[2], wf[11], hq[14][2]); SBAR();
  MF(ac3[3], wf[11], hq[14][3]); SBAR();
  asm volatile("s_waitcnt vmcnt(16)" :  :: "memory");
  MF(ac2[0], wf[12], hq[15][0]); SBAR();
  GLDV(wf[11], wp0, lane16, 1024);
  MF(ac2[1], wf[12], hq[15][1]); SBAR();
  MF(ac2[2], wf[12], hq[15][2]); SBAR();
  MF(ac2[3], wf[12], hq[15][3]); SBAR();
  asm volatile("s_waitcnt vmcnt(16)" :  :: "memory");
  MF(ac3[0], wf[13], hq[15][0]); SBAR();
  GLDV(wf[12], wp0, lane16, 2048);
  MF(ac3[1], wf[13], hq[15][1]); SBAR();
  MF(ac3[2], wf[13], hq[15][2]); SBAR();
  MF(ac3[3], wf[13], hq[15][3]); SBAR();
  asm volatile("s_waitcnt vmcnt(16)" :  :: "memory");
  MFV(ac2[0], wf[14], hq[16][0]); SBAR();
  GLDV(wf[13], wp0, lane16, 3072);
  MFV(ac2[1], wf[14], hq[16][1]); SBAR();
  MFV(ac2[2], wf[14], hq[16][2]); SBAR();
  MFV(ac2[3], wf[14], hq[16][3]); SBAR();
  asm volatile("s_waitcnt vmcnt(16)" :  :: "memory");
  MFV(ac3[0], wf[15], hq[16][0]); SBAR();
  GLDV(wf[14], wp1, lane16, -4096);
  MFV(ac3[1], wf[15], hq[16][1]); SBAR();
  MFV(ac3[2], wf[15], hq[16][2]); SBAR();
  MFV(ac3[3], wf[15], hq[16][3]); SBAR();
  asm volatile("s_waitcnt vmcnt(16)" :  :: "memory");
  MFV(ac2[0], wf[16], hq[17][0]); SBAR();
  GLDV(wf[15], wp1, lane16, -3072);
  MFV(ac2[1], wf[16], hq[17][1]); SBAR();
  MFV(ac2[2], wf[16], hq[17][2]); SBAR();
  MFV(ac2[3], wf[16], hq[17][3]); SBAR();
  asm volatile("s_waitcnt vmcnt(16)" :  :: "memory");
  MFV(ac3[0], wf[17], hq[17][0]); SBAR();
  GLDV(wf[16], wp1, lane16, -2048);
  MFV(ac3[1], wf[17], hq[17][1]); SBAR();
  MFV(ac3[2], wf[17], hq[17][2]); SBAR();
  MFV(ac3[3], wf[17], hq[17][3]); SBAR();
  asm volatile("s_waitcnt vmcnt(16)" :  :: "memory");
  MF0(ac0[0], wf[0], hq[0][0]); SBAR();
  GLDV(wf[17], wp1, lane16, -1024);
  MF0(ac0[1], wf[0], hq[0][1]); SBAR();
  sp = p2 + 8192; PINS(sp);
  MF0(ac0[2], wf[0], hq[0][2]); SBAR();
  MF0(ac0[3], wf[0], hq[0][3]); SBAR();
  asm volatile("s_waitcnt vmcnt(16)" :  :: "memory");
  MF0(ac1[0], wf[1], hq[0][0]); SBAR();
  MF0(ac1[1], wf[1], hq[0][1]); SBAR();
  wp0 = w_s + 413696; PINS(wp0);
  MF0(ac1[2], wf[1], hq[0][2]); SBAR();
  MF0(ac1[3], wf[1], hq[0][3]); SBAR();
  pk[0][0] = cvtpk(ac2[0][0], ac2[0][1]); PIN(pk[0][0]);
  asm volatile("s_waitcnt vmcnt(15)" :  :: "memory");
  MF(ac0[0], wf[2], hq[1][0]); SBAR();
  pk[0][1] = cvtpk(ac2[0][2], ac2[0][3]); PIN(pk[0][1]);
  pk[0][2] = cvtpk(ac3[0][0], ac3[0][1]); PIN(pk[0][2]);
  MF(ac0[1], wf[2], hq[1][1]); SBAR();
  pk[0][3] = cvtpk(ac3[0][2], ac3[0][3]); PIN(pk[0][3]);
  MF(ac0[2], wf[2], hq[1][2]); SBAR();
  st16n<0>(sp, lane16, (u32x4){pk[0][0], pk[0][1], pk[0][2], pk[0][3]});
  MF(ac0[3], wf[2], hq[1][3]); SBAR();
  asm volatile("s_waitcnt vmcnt(15)" :  :: "memory");
  MF(ac1[0], wf[3], hq[1][0]); SBAR();
  MF(ac1[1], wf[3], hq[1][1]); SBAR();
  pk[1][0] = cvtpk(ac2[1][0], ac2[1][1]); PIN(pk[1][0]);
  pk[1][1] = cvtpk(ac2[1][2], ac2[1][3]); PIN(pk[1][1]);
  MF(ac1[2], wf[3], hq[1][2]); SBAR();
  pk[1][2] = cvtpk(ac3[1][0], ac3[1][1]); PIN(pk[1][2]);
  pk[1][3] = cvtpk(ac3[1][2], ac3[1][3]); PIN(pk[1][3]);
  MF(ac1[3], wf[3], hq[1][3]); SBAR();
  st16n<1024>(sp, lane16, (u32x4){pk[1][0], pk[1][1], pk[1][2], pk[1][3]});
  asm volatile("s_waitcnt vmcnt(15)" :  :: "memory");
  MF(ac0[0], wf[4], hq[2][0]); SBAR();
  MF(ac0[1], wf[4], hq[2][1]); SBAR();
  MF(ac0[2], wf[4], hq[2][2]); SBAR();
  pk[0][0] = cvtpk(ac2[2][0], ac2[2][1]); PIN(pk[0][0]);
  pk[0][1] = cvtpk(ac2[2][2], ac2[2][3]); PIN(pk[0][1]);
  MF(ac0[3], wf[4], hq[2][3]); SBAR();
  pk[0][2] = cvtpk(ac3[2][0], ac3[2][1]); PIN(pk[0][2]);
  asm volatile("s_waitcnt vmcnt(14)" :  :: "memory");
  MF(ac1[0], wf[5], hq[2][0]); SBAR();
  pk[0][3] = cvtpk(ac3[2][2], ac3[2][3]); PIN(pk[0][3]);
  MF(ac1[1], wf[5], hq[2][1]); SBAR();
  st16n<2048>(sp, lane16, (u32x4){pk[0][0], pk[0][1], pk[0][2], pk[0][3]});
  MF(ac1[2], wf[5], hq[2][2]); SBAR();
  MF(ac1[3], wf[5], hq[2][3]); SBAR();
  asm volatile("s_waitcnt vmcnt(14)" :  :: "memory");
  MF(ac0[0], wf[6], hq[3][0]); SBAR();
  pk[1][0] = cvtpk(ac2[3][0], ac2[3][1]); PIN(pk[1][0]);
  pk[1][1] = cvtpk(ac2[3][2], ac2[3][3]); PIN(pk[1][1]);
  MF(ac0[1], wf[6], hq[3][1]); SBAR();
  pk[1][2] = cvtpk(ac3[3][0], ac3[3][1]); PIN(pk[1][2]);
  pk[1][3] = cvtpk(ac3[3][2], ac3[3][3]); PIN(pk[1][3]);
  MF(ac0[2], wf[6], hq[3][2]); SBAR();
  st16n<3072>(sp, lane16, (u32x4){pk[1][0], pk[1][1], pk[1][2], pk[1][3]});
  MF(ac0[3], wf[6], hq[3][3]); SBAR();
  asm volatile("s_waitcnt vmcnt(14)" :  :: "memory");
  MF(ac1[0], wf[7], hq[3][0]); SBAR();
  MF(ac1[1], wf[7], hq[3][1]); SBAR();
  MF(ac1[2], wf[7], hq[3][2]); SBAR();
  MF(ac1[3], wf[7], hq[3][3]); SBAR();
  asm volatile("s_waitcnt vmcnt(13)" :  :: "memory");
  MF(ac0[0], wf[8], hq[4][0]); SBAR();
  GLDV(wf[7], wp0, lane16, -1024);
  MF(ac0[1], wf[8], hq[4][1]); SBAR();
  MF(ac0[2], wf[8], hq[4][2]); SBAR();
  MF(ac0[3], wf[8], hq[4][3]); SBAR();
  asm volatile("s_waitcnt vmcnt(13)" :  :: "memory");
  MF(ac1[0], wf[9], hq[4][0]); SBAR();
  GLDV(wf[8], wp0, lane16, 0);
  MF(ac1[1], wf[9], hq[4][1]); SBAR();
  wp1 = w_s + 421888; PINS(wp1);
  MF(ac1[2], wf[9], hq[4][2]); SBAR();
  MF(ac1[3], wf[9], hq[4][3]); SBAR();
  asm volatile("s_waitcnt vmcnt(13)" :  :: "memory");
  MF(ac0[0], wf[10], hq[5][0]); SBAR();
  GLDV(wf[9], wp0, lane16, 1024);
  MF(ac0[1], wf[10], hq[5][1]); SBAR();
  MF(ac0[2], wf[10], hq[5][2]); SBAR();
  MF(ac0[3], wf[10], hq[5][3]); SBAR();
  asm volatile("s_waitcnt vmcnt(13)" :  :: "memory");
  MF(ac1[0], wf[11], hq[5][0]); SBAR();
  GLDV(wf[10], wp0, lane16, 2048);
  MF(ac1[1], wf[11], hq[5][1]); SBAR();
  MF(ac1[2], wf[11], hq[5][2]); SBAR();
  MF(ac1[3], wf[11], hq[5][3]); SBAR();
  asm volatile("s_waitcnt vmcnt(13)" :  :: "memory");
  MF(ac0[0], wf[12], hq[6][0]); SBAR();
  GLDV(wf[11], wp0, lane16, 3072);
  MF(ac0[1], wf[12], hq[6][1]); SBAR();
  MF(ac0[2], wf[12], hq[6][2]); SBAR();
  MF(ac0[3], wf[12], hq[6][3]); SBAR();
  asm volatile("s_waitcnt vmcnt(13)" :  :: "memory");
  MF(ac1[0], wf[13], hq[6][0]); SBAR();
  GLDV(wf[12], wp1, lane16, -4096);
  MF(ac1[1], wf[13], hq[6][1]); SBAR();
  MF(ac1[2], wf[13], hq[6][2]); SBAR();
  MF(ac1[3], wf[13], hq[6][3]); SBAR();
  asm volatile("s_waitcnt vmcnt(13)" :  :: "memory");
  MF(ac0[0], wf[14], hq[7][0]); SBAR();
  GLDV(wf[13], wp1, lane16, -3072);
  MF(ac0[1], wf[14], hq[7][1]); SBAR();
  MF(ac0[2], wf[14], hq[7][2]); SBAR();
  DSR(wf[0], sr0, 19456);
  MF(ac0[3], wf[14], hq[7][3]); SBAR();
  asm volatile("s_waitcnt vmcnt(13)" :  :: "memory");
  MF(ac1[0], wf[15], hq[7][0]); SBAR();
  GLDV(wf[14], wp1, lane16, -2048);
  MF(ac1[1], wf[15], hq[7][1]); SBAR();
  MF(ac1[2], wf[15], hq[7][2]); SBAR();
  DSR(wf[1], sr0, 32768);
  MF(ac1[3], wf[15], hq[7][3]); SBAR();
  asm volatile("s_waitcnt vmcnt(13)" :  :: "memory");
  MF(ac0[0], wf[16], hq[8][0]); SBAR();
  GLDV(wf[15], wp1, lane16, -1024);
  MF(ac0[1], wf[16], hq[8][1]); SBAR();
  MF(ac0[2], wf[16], hq[8][2]); SBAR();
  DSR(wf[2], sr0, 33792);
  MF(ac0[3], wf[16], hq[8][3]); SBAR();
  asm volatile("s_waitcnt vmcnt(13)" :  :: "memory");
  MF(ac1[0], wf[17], hq[8][0]); SBAR();
  GLDV(wf[16], wp1, lane16, 0);
  MF(ac1[1], wf[17], hq[8][1]); SBAR();
  wp0 = w_s + 430080; PINS(wp0);
  MF(ac1[2], wf[17], hq[8][2]); SBAR();
  DSR(wf[3], sr0, 34816);
  MF(ac1[3], wf[17], hq[8][3]); SBAR();
  asm volatile("s_waitcnt lgkmcnt(3)" :  :: "memory");
  MF(ac0[0], wf[0], hq[9][0]); SBAR();
  GLDV(wf[17], wp1, lane16, 1024);
  MF(ac0[1], wf[0], hq[9][1]); SBAR();
  MF(ac0[2], wf[0], hq[9][2]); SBAR();
  DSR(wf[4], sr0, 35840);
  MF(ac0[3], wf[0], hq[9][3]); SBAR();
  asm volatile("s_waitcnt lgkmcnt(3)" :  :: "memory");
  MF(ac1[0], wf[1], hq[9][0]); SBAR();
  GLDV(wf[0], wp1, lane16, 2048);
  MF(ac1[1], wf[1], hq[9][1]); SBAR();
  MF(ac1[2], wf[1], hq[9][2]); SBAR();
  DSR(wf[5], sr0, 49152);
  MF(ac1[3], wf[1], hq[9][3]); SBAR();
  asm volatile("s_waitcnt lgkmcnt(3)" :  :: "memory");
  MF(ac0[0], wf[2], hq[10][0]); SBAR();
  GLDV(wf[1], wp1, lane16, 3072);
  MF(ac0[1], wf[2], hq[10][1]); SBAR();
  MF(ac0[2], wf[2], hq[10][2]); SBAR();
  DSR(wf[6], sr0, 50176);
  MF(ac0[3], wf[2], hq[10][3]); SBAR();
  asm volatile("s_waitcnt lgkmcnt(3)" :  :: "memory");
  MF(ac1[0], wf[3], hq[10][0]); SBAR();
  GLDV(wf[2], wp0, lane16, -4096);
  MF(ac1[1], wf[3], hq[10][1]); SBAR();
  MF(ac1[2], wf[3], hq[10][2]); SBAR();
  MF(ac1[3], wf[3], hq[10][3]); SBAR();
  asm volatile("s_waitcnt lgkmcnt(2)" :  :: "memory");
  MF(ac0[0], wf[4], hq[11][0]); SBAR();
  GLDV(wf[3], wp0, lane16, -3072);
  MF(ac0[1], wf[4], hq[11][1]); SBAR();
  MF(ac0[2], wf[4], hq[11][2]); SBAR();
  MF(ac0[3], wf[4], hq[11][3]); SBAR();
  asm volatile("s_waitcnt lgkmcnt(1)" :  :: "memory");
  MF(ac1[0], wf[5], hq[11][0]); SBAR();
  GLDV(wf[4], wp0, lane16, -2048);
  MF(ac1[1], wf[5], hq[11][1]); SBAR();
  MF(ac1[2], wf[5], hq[11][2]); SBAR();
  MF(ac1[3], wf[5], hq[11][3]); SBAR();
  asm volatile("s_waitcnt lgkmcnt(0)" :  :: "memory");
  MF(ac0[0], wf[6], hq[12][0]); SBAR();
  GLDV(wf[5], wp0, lane16, -1024);
  MF(ac0[1], wf[6], hq[12][1]); SBAR();
  MF(ac0[2], wf[6], hq[12][2]); SBAR();
  MF(ac0[3], wf[6], hq[12][3]); SBAR();
  asm volatile("s_waitcnt vmcnt(16)" :  :: "memory");
  MF(ac1[0], wf[7], hq[12][0]); SBAR();
  GLDV(wf[6], wp0, lane16, 0);
  MF(ac1[1], wf[7], hq[12][1]); SBAR();
  wp1 = w_s + 438272; PINS(wp1);
  MF(ac1[2], wf[7], hq[12][2]); SBAR();
  MF(ac1[3], wf[7], hq[12][3]); SBAR();
  asm volatile("s_waitcnt vmcnt(16)" :  :: "memory");
  MF(ac0[0], wf[8], hq[13][0]); SBAR();
  GLDV(wf[7], wp0, lane16, 1024);
  MF(ac0[1], wf[8], hq[13][1]); SBAR();
  MF(ac0[2], wf[8], hq[13][2]); SBAR();
  MF(ac0[3], wf[8], hq[13][3]); SBAR();
  asm volatile("s_waitcnt vmcnt(16)" :  :: "memory");
  MF(ac1[0], wf[9], hq[13][0]); SBAR();
  GLDV(wf[8], wp0, lane16, 2048);
  MF(ac1[1], wf[9], hq[13][1]); SBAR();
  MF(ac1[2], wf[9], hq[13][2]); SBAR();
  asm volatile("s_waitcnt vmcnt(20)\n\ts_barrier" :  :: "memory");
  MF(ac1[3], wf[9], hq[13][3]); SBAR();
  asm volatile("s_waitcnt vmcnt(16)" :  :: "memory");
  MF(ac0[0], wf[10], hq[14][0]); SBAR();
  GLDV(wf[9], wp0, lane16, 3072);
  MF(ac0[1], wf[10], hq[14][1]); SBAR();
  MF(ac0[2], wf[10], hq[14][2]); SBAR();
  PUBLISH(C_F + team * 3, fo);
  MF(ac0[3], wf[10], hq[14][3]); SBAR();
  asm volatile("s_waitcnt vmcnt(16)" :  :: "memory");
  MF(ac1[0], wf[11], hq[14][0]); SBAR();
  GLDV(wf[10], wp1, lane16, -4096);
  MF(ac1[1], wf[11], hq[14][1]); SBAR();
  MF(ac1[2], wf[11], hq[14][2]); SBAR();
  MF(ac1[3], wf[11], hq[14][3]); SBAR();
  asm volatile("s_waitcnt vmcnt(16)" :  :: "memory");
  MF(ac0[0], wf[12], hq[15][0]); SBAR();
  GLDV(wf[11], wp1, lane16, -3072);
  MF(ac0[1], wf[12], hq[15][1]); SBAR();
  MF(ac0[2], wf[12], hq[15][2]); SBAR();
  MF(ac0[3], wf[12], hq[15][3]); SBAR();
  asm volatile("s_waitcnt vmcnt(16)" :  :: "memory");
  MF(ac1[0], wf[13], hq[15][0]); SBAR();
  GLDV(wf[12], wp1, lane16, -2048);
  MF(ac1[1], wf[13], hq[15][1]); SBAR();
  MF(ac1[2], wf[13], hq[15][2]); SBAR();
  MF(ac1[3], wf[13], hq[15][3]); SBAR();
  asm volatile("s_waitcnt vmcnt(16)" :  :: "memory");
  MFV(ac0[0], wf[14], hq[16][0]); SBAR();
  GLDV(wf[13], wp1, lane16, -1024);
  MFV(ac0[1], wf[14], hq[16][1]); SBAR();
  MFV(ac0[2], wf[14], hq[16][2]); SBAR();
  MFV(ac0[3], wf[14], hq[16][3]); SBAR();
  asm volatile("s_waitcnt vmcnt(16)" :  :: "memory");
  MFV(ac1[0], wf[15], hq[16][0]); SBAR();
  GLDV(wf[14], wp1, lane16, 0);
  MFV(ac1[1], wf[15], hq[16][1]); SBAR();
  wp0 = w_s + 446464; PINS(wp0);
  MFV(ac1[2], wf[15], hq[16][2]); SBAR();
  MFV(ac1[3], wf[15], hq[16][3]); SBAR();
  asm volatile("s_waitcnt vmcnt(16)" :  :: "memory");
  MFV(ac0[0], wf[16], hq[17][0]); SBAR();
  GLDV(wf[15], wp1, lane16, 1024);
  MFV(ac0[1], wf[16], hq[17][1]); SBAR();
  MFV(ac0[2], wf[16], hq[17][2]); SBAR();
  MFV(ac0[3], wf[16], hq[17][3]); SBAR();
  asm volatile("s_waitcnt vmcnt(16)" :  :: "memory");
  MFV(ac1[0], wf[17], hq[17][0]); SBAR();
  GLDV(wf[16], wp1, lane16, 2048);
  MFV(ac1[1], wf[17], hq[17][1]); SBAR();
  MFV(ac1[2], wf[17], hq[17][2]); SBAR();
  MFV(ac1[3], wf[17], hq[17][3]); SBAR();
  asm volatile("s_waitcnt vmcnt(16)" :  :: "memory");
  MF0(ac2[0], wf[0], hq[0][0]); SBAR();
  GLDV(wf[17], wp1, lane16, 3072);
  MF0(ac2[1], wf[0], hq[0][1]); SBAR();
  sp = p2 + 16384; PINS(sp);
  MF0(ac2[2], wf[0], hq[0][2]); SBAR();
  MF0(ac2[3], wf[0], hq[0][3]); SBAR();
  asm volatile("s_waitcnt vmcnt(16)" :  :: "memory");
  asm volatile(".Lk3_p7:\n\ts_cbranch_execz .Lk3_pr" ::: "memory");
  MF0(ac3[0], wf[1], hq[0][0]); SBAR();
  MF0(ac3[1], wf[1], hq[0][1]); SBAR();
  MF0(ac3[2], wf[1], hq[0][2]); SBAR();
  MF0(ac3[3], wf[1], hq[0][3]); SBAR();
  pk[0][0] = cvtpk(ac0[0][0], ac0[0][1]); PIN(pk[0][0]);
  asm volatile("s_waitcnt vmcnt(15)" :  :: "memory");
  MF(ac2[0], wf[2], hq[1][0]); SBAR();
  pk[0][1] = cvtpk(ac0[0][2], ac0[0][3]); PIN(pk[0][1]);
  pk[0][2] = cvtpk(ac1[0][0], ac1[0][1]); PIN(pk[0][2]);
  MF(ac2[1], wf[2], hq[1][1]); SBAR();
  pk[0][3] = cvtpk(ac1[0][2], ac1[0][3]); PIN(pk[0][3]);
  MF(ac2[2], wf[2], hq[1][2]); SBAR();
  st16n<0>(sp, lane16, (u32x4){pk[0][0], pk[0][1], pk[0][2], pk[0][3]});
  MF(ac2[3], wf[2], hq[1][3]); SBAR();
  asm volatile("s_waitcnt vmcnt(15)" :  :: "memory");
  MF(ac3[0], wf[3], hq[1][0]); SBAR();
  MF(ac3[1], wf[3], hq[1][1]); SBAR();
  pk[1][0] = cvtpk(ac0[1][0], ac0[1][1]); PIN(pk[1][0]);
  pk[1][1] = cvtpk(ac0[1][2], ac0[1][3]); PIN(pk[1][1]);
  MF(ac3[2], wf[3], hq[1][2]); SBAR();
  pk[1][2] = cvtpk(ac1[1][0], ac1[1][1]); PIN(pk[1][2]);
  pk[1][3] = cvtpk(ac1[1][2], ac1[1][3]); PIN(pk[1][3]);
  MF(ac3[3], wf[3], hq[1][3]); SBAR();
  st16n<1024>(sp, lane16, (u32x4){pk[1][0], pk[1][1], pk[1][2], pk[1][3]});
  asm volatile("s_waitcnt vmcnt(15)" :  :: "memory");
  MF(ac2[0], wf[4], hq[2][0]); SBAR();
  MF(ac2[1], wf[4], hq[2][1]); SBAR();
  MF(ac2[2], wf[4], hq[2][2]); SBAR();
  pk[0][0] = cvtpk(ac0[2][0], ac0[2][1]); PIN(pk[0][0]);
  pk[0][1] = cvtpk(ac0[2][2], ac0[2][3]); PIN(pk[0][1]);
  MF(ac2[3], wf[4], hq[2][3]); SBAR();
  pk[0][2] = cvtpk(ac1[2][0], ac1[2][1]); PIN(pk[0][2]);
  asm volatile("s_waitcnt vmcnt(14)" :  :: "memory");
  MF(ac3[0], wf[5], hq[2][0]); SBAR();
  pk[0][3] = cvtpk(ac1[2][2], ac1[2][3]); PIN(pk[0][3]);
  MF(ac3[1], wf[5], hq[2][1]); SBAR();
  wp1 = w_s + 454656; PINS(wp1);
  MF(ac3[2], wf[5], hq[2][2]); SBAR();
  st16n<2048>(sp, lane16, (u32x4){pk[0][0], pk[0][1], pk[0][2], pk[0][3]});
  MF(ac3[3], wf[5], hq[2][3]); SBAR();
  asm volatile("s_waitcnt vmcnt(14)" :  :: "memory");
  MF(ac2[0], wf[6], hq[3][0]); SBAR();
  MF(ac2[1], wf[6], hq[3][1]); SBAR();
  pk[1][0] = cvtpk(ac0[3][0], ac0[3][1]); PIN(pk[1][0]);
  pk[1][1] = cvtpk(ac0[3][2], ac0[3][3]); PIN(pk[1][1]);
  MF(ac2[2], wf[6], hq[3][2]); SBAR();
  pk[1][2] = cvtpk(ac1[3][0], ac1[3][1]); PIN(pk[1][2]);
  pk[1][3] = cvtpk(ac1[3][2], ac1[3][3]); PIN(pk[1][3]);
  MF(ac2[3], wf[6], hq[3][3]); SBAR();
  st16n<3072>(sp, lane16, (u32x4){pk[1][0], pk[1][1], pk[1][2], pk[1][3]});
  asm volatile("s_waitcnt vmcnt(14)" :  :: "memory");
  MF(ac3[0], wf[7], hq[3][0]); SBAR();
  GLDV(wf[6], wp0, lane16, 2048);
  MF(ac3[1], wf[7], hq[3][1]); SBAR();
  MF(ac3[2], wf[7], hq[3][2]); SBAR();
  MF(ac3[3], wf[7], hq[3][3]); SBAR();
  asm volatile("s_waitcnt vmcnt(14)" :  :: "memory");
  MF(ac2[0], wf[8], hq[4][0]); SBAR();
  GLDV(wf[7], wp0, lane16, 3072);
  MF(ac2[1], wf[8], hq[4][1]); SBAR();
  MF(ac2[2], wf[8], hq[4][2]); SBAR();
  MF(ac2[3], wf[8], hq[4][3]); SBAR();
  asm volatile("s_waitcnt vmcnt(14)" :  :: "memory");
  MF(ac3[0], wf[9], hq[4][0]); SBAR();
  GLDV(wf[8], wp1, lane16, -4096);
  MF(ac3[1], wf[9], hq[4][1]); SBAR();
  MF(ac3[2], wf[9], hq[4][2]); SBAR();
  MF(ac3[3], wf[9], hq[4][3]); SBAR();
  asm volatile("s_waitcnt vmcnt(14)" :  :: "memory");
  MF(ac2[0], wf[10], hq[5][0]); SBAR();
  GLDV(wf[9], wp1, lane16, -3072);
  MF(ac2[1], wf[10], hq[5][1]); SBAR();
  MF(ac2[2], wf[10], hq[5][2]); SBAR();
  MF(ac2[3], wf[10], hq[5][3]); SBAR();
  asm volatile("s_waitcnt vmcnt(14)" :  :: "memory");
  MF(ac3[0], wf[11], hq[5][0]); SBAR();
  GLDV(wf[10], wp1, lane16, -2048);
  MF(ac3[1], wf[11], hq[5][1]); SBAR();
  MF(ac3[2], wf[11], hq[5][2]); SBAR();
  MF(ac3[3], wf[11], hq[5][3]); SBAR();
  asm volatile("s_waitcnt vmcnt(14)" :  :: "memory");
  MF(ac2[0], wf[12], hq[6][0]); SBAR();
  GLDV(wf[11], wp1, lane16, -1024);
  MF(ac2[1], wf[12], hq[6][1]); SBAR();
  MF(ac2[2], wf[12], hq[6][2]); SBAR();
  MF(ac2[3], wf[12], hq[6][3]); SBAR();
  asm volatile("s_waitcnt vmcnt(14)" :  :: "memory");
  MF(ac3[0], wf[13], hq[6][0]); SBAR();
  GLDV(wf[12], wp1, lane16, 0);
  MF(ac3[1], wf[13], hq[6][1]); SBAR();
  wp0 = w_s + 462848; PINS(wp0);
  MF(ac3[2], wf[13], hq[6][2]); SBAR();
  MF(ac3[3], wf[13], hq[6][3]); SBAR();
  asm volatile("s_waitcnt vmcnt(14)" :  :: "memory");
  MF(ac2[0], wf[14], hq[7][0]); SBAR();
  GLDV(wf[13], wp1, lane16, 1024);
  MF(ac2[1], wf[14], hq[7][1]); SBAR();
  MF(ac2[2], wf[14], hq[7][2]); SBAR();
  DSR(wf[0], sr0, 51200);
  MF(ac2[3], wf[14], hq[7][3]); SBAR();
  asm volatile("s_waitcnt vmcnt(14)" :  :: "memory");
  MF(ac3[0], wf[15], hq[7][0]); SBAR();
  GLDV(wf[14], wp1, lane16, 2048);
  MF(ac3[1], wf[15], hq[7][1]); SBAR();
  MF(ac3[2], wf[15], hq[7][2]); SBAR();
  DSR(wf[1], sr0, 52224);
  MF(ac3[3], wf[15], hq[7][3]); SBAR();
  asm volatile("s_waitcnt vmcnt(14)" :  :: "memory");
  MF(ac2[0], wf[16], hq[8][0]); SBAR();
  GLDV(wf[15], wp1, lane16, 3072);
  MF(ac2[1], wf[16], hq[8][1]); SBAR();
  MF(ac2[2], wf[16], hq[8][2]); SBAR();
  DSR(wf[2], sr1, 0);
  MF(ac2[3], wf[16], hq[8][3]); SBAR();
  asm volatile("s_waitcnt vmcnt(14)" :  :: "memory");
  MF(ac3[0], wf[17], hq[8][0]); SBAR();
  GLDV(wf[16], wp0, lane16, -4096);
  MF(ac3[1], wf[17], hq[8][1]); SBAR();
  MF(ac3[2], wf[17], hq[8][2]); SBAR();
  DSR(wf[3], sr1, 1024);
  MF(ac3[3], wf[17], hq[8][3]); SBAR();
  asm volatile("s_waitcnt lgkmcnt(3)" :  :: "memory");
  MF(ac2[0], wf[0], hq[9][0]); SBAR();
  GLDV(wf[17], wp0, lane16, -3072);
  MF(ac2[1], wf[0], hq[9][1]); SBAR();
  MF(ac2[2], wf[0], hq[9][2]); SBAR();
  DSR(wf[4], sr1, 2048);
  poll_issue(pv, CNT(C_F + rteam * 3), fo);
  MF(ac2[3], wf[0], hq[9][3]); SBAR();
  asm volatile("s_waitcnt lgkmcnt(3)" :  :: "memory");
  MF(ac3[0], wf[1], hq[9][0]); SBAR();
  GLDV(wf[0], wp0, lane16, -2048);
  MF(ac3[1], wf[1], hq[9][1]); SBAR();
  MF(ac3[2], wf[1], hq[9][2]); SBAR();
  DSR(wf[5], sr1, 3072);
  MF(ac3[3], wf[1], hq[9][3]); SBAR();
  asm volatile("s_waitcnt lgkmcnt(3)" :  :: "memory");
  MF(ac2[0], wf[2], hq[10][0]); SBAR();
  GLDV(wf[1], wp0, lane16, -1024);
  MF(ac2[1], wf[2], hq[10][1]); SBAR();
  MF(ac2[2], wf[2], hq[10][2]); SBAR();
  MF(ac2[3], wf[2], hq[10][3]); SBAR();
  asm volatile("s_waitcnt lgkmcnt(2)" :  :: "memory");
  MF(ac3[0], wf[3], hq[10][0]); SBAR();
  GLDV(wf[2], wp0, lane16, 0);
  MF(ac3[1], wf[3], hq[10][1]); SBAR();
  wp1 = w_s + 471040; PINS(wp1);
  MF(ac3[2], wf[3], hq[10][2]); SBAR();
  MF(ac3[3], wf[3], hq[10][3]); SBAR();
  asm volatile("s_waitcnt lgkmcnt(1)" :  :: "memory");
  MF(ac2[0], wf[4], hq[11][0]); SBAR();
  GLDV(wf[3], wp0, lane16, 1024);
  MF(ac2[1], wf[4], hq[11][1]); SBAR();
  R_XLOAD(5);
  MF(ac2[2], wf[4], hq[11][2]); SBAR();
  MF(ac2[3], wf[4], hq[11][3]); SBAR();
  asm volatile("s_waitcnt lgkmcnt(0)" :  :: "memory");
  MF(ac3[0], wf[5], hq[11][0]); SBAR();
  GLDV(wf[4], wp0, lane16, 2048);
  MF(ac3[1], wf[5], hq[11][1]); SBAR();
  R_XLOAD(6);
  MF(ac3[2], wf[5], hq[11][2]); SBAR();
  MF(ac3[3], wf[5], hq[11][3]); SBAR();
  asm volatile("s_waitcnt vmcnt(19)" :  :: "memory");
  MF(ac2[0], wf[6], hq[12][0]); SBAR();
  GLDV(wf[5], wp0, lane16, 3072);
  MF(ac2[1], wf[6], hq[12][1]); SBAR();
  R_XLOAD(7);
  MF(ac2[2], wf[6], hq[12][2]); SBAR();
  MF(ac2[3], wf[6], hq[12][3]); SBAR();
  asm volatile("s_waitcnt vmcnt(20)" :  :: "memory");
  MF(ac3[0], wf[7], hq[12][0]); SBAR();
  GLDV(wf[6], wp1, lane16, -4096);
  MF(ac3[1], wf[7], hq[12][1]); SBAR();
  R_XLOAD(8);
  MF(ac3[2], wf[7], hq[12][2]); SBAR();
  MF(ac3[3], wf[7], hq[12][3]); SBAR();
  asm volatile("s_waitcnt vmcnt(21)" :  :: "memory");
  MF(ac2[0], wf[8], hq[13][0]); SBAR();
  GLDV(wf[7], wp1, lane16, -3072);
  MF(ac2[1], wf[8], hq[13][1]); SBAR();
  R_XLOAD(9);
  MF(ac2[2], wf[8], hq[13][2]); SBAR();
  MF(ac2[3], wf[8], hq[13][3]); SBAR();
  asm volatile("s_waitcnt vmcnt(22)" :  :: "memory");
  MF(ac3[0], wf[9], hq[13][0]); SBAR();
  GLDV(wf[8], wp1, lane16, -2048);
  MF(ac3[1], wf[9], hq[13][1]); SBAR();
  MF(ac3[2], wf[9], hq[13][2]); SBAR();
  MF(ac3[3], wf[9], hq[13][3]); SBAR();
  asm volatile("s_waitcnt vmcnt(22)" :  :: "memory");
  MF(ac2[0], wf[10], hq[14][0]); SBAR();
  GLDV(wf[9], wp1, lane16, -1024);
  MF(ac2[1], wf[10], hq[14][1]); SBAR();
  asm volatile("s_waitcnt vmcnt(15)" : "+v"(pv) :: "memory");
  if (__builtin_expect(__builtin_amdgcn_ballot_w64(pv == 0u) != 0ull, 0)) spin_until(CNT(C_F + rteam * 3), fo);
  MF(ac2[2], wf[10], hq[14][2]); SBAR();
  gld_sc1<0>(rp[0][0], ws + 0, ro2);
  MF(ac2[3], wf[10], hq[14][3]); SBAR();
  asm volatile("s_waitcnt vmcnt(23)" :  :: "memory");
  MF(ac3[0], wf[11], hq[14][0]); SBAR();
  GLDV(wf[10], wp1, lane16, 0);
  MF(ac3[1], wf[11], hq[14][1]); SBAR();
  wp0 = w_s + 479232; PINS(wp0);
  MF(ac3[2], wf[11], hq[14][2]); SBAR();
  gld_sc1<0>(rp[0][1], ws + 73728, ro2);
  MF(ac3[3], wf[11], hq[14][3]); SBAR();
  asm volatile("s_waitcnt vmcnt(24)" :  :: "memory");
  MF(ac2[0], wf[12], hq[15][0]); SBAR();
  GLDV(wf[11], wp1, lane16, 1024);
  MF(ac2[1], wf[12], hq[15][1]); SBAR();
  MF(ac2[2], wf[12], hq[15][2]); SBAR();
  gld_sc1<0>(rp[5][0], ws + 40960, ro2);
  MF(ac2[3], wf[12], hq[15][3]); SBAR();
  asm volatile("s_waitcnt vmcnt(25)" :  :: "memory");
  MF(ac3[0], wf[13], hq[15][0]); SBAR();
  GLDV(wf[12], wp1, lane16, 2048);
  MF(ac3[1], wf[13], hq[15][1]); SBAR();
  MF(ac3[2], wf[13], hq[15][2]); SBAR();
  asm volatile("s_waitcnt vmcnt(34)\n\ts_barrier" :  :: "memory");
  MF(ac3[3], wf[13], hq[15][3]); SBAR();
  asm volatile("s_waitcnt vmcnt(25)" :  :: "memory");
  MFV(ac2[0], wf[14], hq[16][0]); SBAR();
  GLDV(wf[13], wp1, lane16, 3072);
  MFV(ac2[1], wf[14], hq[16][1]); SBAR();
  gld_sc1<0>(rp[5][1], ws + 114688, ro2);
  MFV(ac2[2], wf[14], hq[16][2]); SBAR();
  PUBLISH(C_F + team * 3 + 1, fo);
  MFV(ac2[3], wf[14], hq[16][3]); SBAR();
  asm volatile("s_waitcnt vmcnt(26)" :  :: "memory");
  MFV(ac3[0], wf[15], hq[16][0]); SBAR();
  GLDV(wf[14], wp0, lane16, -4096);
  MFV(ac3[1], wf[15], hq[16][1]); SBAR();
  gld_sc1<0>(rp[1][0], ws + 8192, ro2);
  MFV(ac3[2], wf[15], hq[16][2]); SBAR();
  MFV(ac3[3], wf[15], hq[16][3]); SBAR();
  asm volatile("s_waitcnt vmcnt(27)" :  :: "memory");
  MFV(ac2[0], wf[16], hq[17][0]); SBAR();
  GLDV(wf[15], wp0, lane16, -3072);
  MFV(ac2[1], wf[16], hq[17][1]); SBAR();
  gld_sc1<0>(rp[1][1], ws + 81920, ro2);
  MFV(ac2[2], wf[16], hq[17][2]); SBAR();
  MFV(ac2[3], wf[16], hq[17][3]); SBAR();
  asm volatile("s_waitcnt vmcnt(28)" :  :: "memory");
  MFV(ac3[0], wf[17], hq[17][0]); SBAR();
  GLDV(wf[16], wp0, lane16, -2048);
  MFV(ac3[1], wf[17], hq[17][1]); SBAR();
  gld_sc1<0>(rp[6][0], ws + 49152, ro2);
  MFV(ac3[2], wf[17], hq[17][2]); SBAR();
  MFV(ac3[3], wf[17], hq[17][3]); SBAR();
  asm volatile("s_waitcnt vmcnt(28)" :  :: "memory");
  MF0(ac4[0], wf[0], hq[0][0]); SBAR();
  GLDV(wf[17], wp0, lane16, -1024);
  MF0(ac4[1], wf[0], hq[0][1]); SBAR();
  sp = p2 + 24576; PINS(sp);
  MF0(ac4[2], wf[0], hq[0][2]); SBAR();
  gld_sc1<0>(rp[6][1], ws + 122880, ro2);
  MF0(ac4[3], wf[0], hq[0][3]); SBAR();
  asm volatile("s_waitcnt vmcnt(29)" :  :: "memory");
  MF(ac4[0], wf[1], hq[1][0]); SBAR();
  MF(ac4[1], wf[1], hq[1][1]); SBAR();
  MF(ac4[2], wf[1], hq[1][2]); SBAR();
  MF(ac4[3], wf[1], hq[1][3]); SBAR();
  pk[0][0] = cvtpk(ac2[0][0], ac2[0][1]); PIN(pk[0][0]);
  asm volatile("s_waitcnt vmcnt(28)" :  :: "memory");
  MF(ac4[0], wf[2], hq[2][0]); SBAR();
  pk[0][1] = cvtpk(ac2[0][2], ac2[0][3]); PIN(pk[0][1]);
  pk[0][2] = cvtpk(ac3[0][0], ac3[0][1]); PIN(pk[0][2]);
  MF(ac4[1], wf[2], hq[2][1]); SBAR();
  pk[0][3] = cvtpk(ac3[0][2], ac3[0][3]); PIN(pk[0][3]);
  MF(ac4[2], wf[2], hq[2][2]); SBAR();
  st16n<0>(sp, lane16, (u32x4){pk[0][0], pk[0][1], pk[0][2], pk[0][3]});
  MF(ac4[3], wf[2], hq[2][3]); SBAR();
  asm volatile("s_waitcnt vmcnt(28)" :  :: "memory");
  MF(ac4[0], wf[3], hq[3][0]); SBAR();
  MF(ac4[1], wf[3], hq[3][1]); SBAR();
  MF(ac4[2], wf[3], hq[3][2]); SBAR();
  MF(ac4[3], wf[3], hq[3][3]); SBAR();
  asm volatile("s_waitcnt vmcnt(26)" :  :: "memory");
  MF(ac4[0], wf[4], hq[4][0]); SBAR();
  MF(ac4[1], wf[4], hq[4][1]); SBAR();
  MF(ac4[2], wf[4], hq[4][2]); SBAR();
  pk[1][0] = cvtpk(ac2[1][0], ac2[1][1]); PIN(pk[1][0]);
  pk[1][1] = cvtpk(ac2[1][2], ac2[1][3]); PIN(pk[1][1]);
  MF(ac4[3], wf[4], hq[4][3]); SBAR();
  pk[1][2] = cvtpk(ac3[1][0], ac3[1][1]); PIN(pk[1][2]);
  asm volatile("s_waitcnt vmcnt(24)" :  :: "memory");
  MF(ac4[0], wf[5], hq[5][0]); SBAR();
  pk[1][3] = cvtpk(ac3[1][2], ac3[1][3]); PIN(pk[1][3]);
  MF(ac4[1], wf[5], hq[5][1]); SBAR();
  st16n<1024>(sp, lane16, (u32x4){pk[1][0], pk[1][1], pk[1][2], pk[1][3]});
  MF(ac4[2], wf[5], hq[5][2]); SBAR();
  MF(ac4[3], wf[5], hq[5][3]); SBAR();
  asm volatile("s_waitcnt vmcnt(23)" :  :: "memory");
  MF(ac4[0], wf[6], hq[6][0]); SBAR();
  MF(ac4[1], wf[6], hq[6][1]); SBAR();
  MF(ac4[2], wf[6], hq[6][2]); SBAR();
  MF(ac4[3], wf[6], hq[6][3]); SBAR();
  asm volatile("s_waitcnt vmcnt(21)" :  :: "memory");
  MF(ac4[0], wf[7], hq[7][0]); SBAR();
  MF(ac4[1], wf[7], hq[7][1]); SBAR();
  pk[0][0] = cvtpk(ac2[2][0], ac2[2][1]); PIN(pk[0][0]);
  pk[0][1] = cvtpk(ac2[2][2], ac2[2][3]); PIN(pk[0][1]);
  MF(ac4[2], wf[7], hq[7][2]); SBAR();
  pk[0][2] = cvtpk(ac3[2][0], ac3[2][1]); PIN(pk[0][2]);
  pk[0][3] = cvtpk(ac3[2][2], ac3[2][3]); PIN(pk[0][3]);
  MF(ac4[3], wf[7], hq[7][3]); SBAR();
  st16n<2048>(sp, lane16, (u32x4){pk[0][0], pk[0][1], pk[0][2], pk[0][3]});
  asm volatile("s_waitcnt vmcnt(20)" :  :: "memory");
  MF(ac4[0], wf[8], hq[8][0]); SBAR();
  MF(ac4[1], wf[8], hq[8][1]); SBAR();
  MF(ac4[2], wf[8], hq[8][2]); SBAR();
  MF(ac4[3], wf[8], hq[8][3]); SBAR();
  asm volatile("s_waitcnt vmcnt(19)" :  :: "memory");
  MF(ac4[0], wf[9], hq[9][0]); SBAR();
  MF(ac4[1], wf[9], hq[9][1]); SBAR();
  MF(ac4[2], wf[9], hq[9][2]); SBAR();
  MF(ac4[3], wf[9], hq[9][3]); SBAR();
  pk[1][0] = cvtpk(ac2[3][0], ac2[3][1]); PIN(pk[1][0]);
  asm volatile("s_waitcnt vmcnt(17)" :  :: "memory");
  MF(ac4[0], wf[10], hq[10][0]); SBAR();
  pk[1][1] = cvtpk(ac2[3][2], ac2[3][3]); PIN(pk[1][1]);
  pk[1][2] = cvtpk(ac3[3][0], ac3[3][1]); PIN(pk[1][2]);
  MF(ac4[1], wf[10], hq[10][1]); SBAR();
  pk[1][3] = cvtpk(ac3[3][2], ac3[3][3]); PIN(pk[1][3]);
  MF(ac4[2], wf[10], hq[10][2]); SBAR();
  st16n<3072>(sp, lane16, (u32x4){pk[1][0], pk[1][1], pk[1][2], pk[1][3]});
  MF(ac4[3], wf[10], hq[10][3]); SBAR();
  asm volatile("s_waitcnt vmcnt(16)" :  :: "memory");
  MF(ac4[0], wf[11], hq[11][0]); SBAR();
  sp = p2_l; PINS(sp);
  MF(ac4[1], wf[11], hq[11][1]); SBAR();
  MF(ac4[2], wf[11], hq[11][2]); SBAR();
  MF(ac4[3], wf[11], hq[11][3]); SBAR();
  asm volatile("s_waitcnt vmcnt(14)" :  :: "memory");
  MF(ac4[0], wf[12], hq[12][0]); SBAR();
  MF(ac4[1], wf[12], hq[12][1]); SBAR();
  MF(ac4[2], wf[12], hq[12][2]); SBAR();
  MF(ac4[3], wf[12], hq[12][3]); SBAR();
  asm volatile("s_waitcnt vmcnt(13)" :  :: "memory");
  MF(ac4[0], wf[13], hq[13][0]); SBAR();
  MF(ac4[1], wf[13], hq[13][1]); SBAR();
  MF(ac4[2], wf[13], hq[13][2]); SBAR();
  MF(ac4[3], wf[13], hq[13][3]); SBAR();
  asm volatile("s_waitcnt vmcnt(11)" :  :: "memory");
  MF(ac4[0], wf[14], hq[14][0]); SBAR();
  MF(ac4[1], wf[14], hq[14][1]); SBAR();
  MF(ac4[2], wf[14], hq[14][2]); SBAR();
  MF(ac4[3], wf[14], hq[14][3]); SBAR();
  asm volatile("s_waitcnt vmcnt(9)" :  :: "memory");
  MF(ac4[0], wf[15], hq[15][0]); SBAR();
  MF(ac4[1], wf[15], hq[15][1]); SBAR();
  MF(ac4[2], wf[15], hq[15][2]); SBAR();
  MF(ac4[3], wf[15], hq[15][3]); SBAR();
  asm volatile("s_waitcnt vmcnt(7)" :  :: "memory");
  MFV(ac4[0], wf[16], hq[16][0]); SBAR();
  MFV(ac4[1], wf[16], hq[16][1]); SBAR();
  MFV(ac4[2], wf[16], hq[16][2]); SBAR();
  MFV(ac4[3], wf[16], hq[16][3]); SBAR();
  asm volatile("s_waitcnt vmcnt(5)" :  :: "memory");
  MFV(ac4[0], wf[17], hq[17][0]); SBAR();
  MFV(ac4[1], wf[17], hq[17][1]); SBAR();
  MFV(ac4[2], wf[17], hq[17][2]); SBAR();
  poll_issue(pv, CNT(C_F + rteam * 3 + 1), fo);
  MFV(ac4[3], wf[17], hq[17][3]); SBAR();
  asm volatile("s_nop 7" ::: "memory");
  pk[0][0] = cvtpk(ac4[0][0], ac4[0][1]); PIN(pk[0][0]);
  pk[0][1] = cvtpk(ac4[0][2], ac4[0][3]); PIN(pk[0][1]);
  pk[1][0] = cvtpk(ac4[1][0], ac4[1][1]); PIN(pk[1][0]);
  pk[1][1] = cvtpk(ac4[1][2], ac4[1][3]); PIN(pk[1][1]);
  st16o<0>(sp, lane16, (u32x4){pk[0][0], pk[0][1], pk[1][0], pk[1][1]});
  pk[0][0] = cvtpk(ac4[2][0], ac4[2][1]); PIN(pk[0][0]);
  pk[0][1] = cvtpk(ac4[2][2], ac4[2][3]); PIN(pk[0][1]);
  pk[1][0] = cvtpk(ac4[3][0], ac4[3][1]); PIN(pk[1][0]);
  pk[1][1] = cvtpk(ac4[3][2], ac4[3][3]); PIN(pk[1][1]);
  st16o<1024>(sp, lane16, (u32x4){pk[0][0], pk[0][1], pk[1][0], pk[1][1]});
  asm volatile("s_waitcnt vmcnt(0)\n\ts_barrier" :  :: "memory");
  PUBLISH(C_F + team * 3 + 1 + 1, fo);
  asm volatile("s_waitcnt vmcnt(2)" : "+v"(pv) :: "memory");
  if (__builtin_expect(__builtin_amdgcn_ballot_w64(pv == 0u) != 0ull, 0)) spin_until(CNT(C_F + rteam * 3 + 1), fo);
  gld_sc1<0>(rp[2][0], ws + 16384, ro2);
  gld_sc1<0>(rp[2][1], ws + 90112, ro2);
  gld_sc1<0>(rp[7][0], ws + 57344, ro2);
  gld_sc1<0>(rp[7][1], ws + 131072, ro2);
  R_ADDB();
  asm volatile("s_nop 1" : "+v"(h4[0]), "+v"(h4[1]), "+v"(h4[2]), "+v"(h4[3]), "+v"(h4[4]));
  asm volatile("s_waitcnt vmcnt(24)" : "+v"(rp[0][0]), "+v"(rp[0][1]) :: "memory");
  MFV(h4[0], se0, rp[0][0]);
  MFV(h4[0], se0, rp[0][1]);
  asm volatile("s_waitcnt vmcnt(19)" : "+v"(rp[5][0]), "+v"(rp[5][1]) :: "memory");
  MFV(h4[2], se1, rp[5][0]);
  MFV(h4[2], se1, rp[5][1]);
  asm volatile("s_waitcnt vmcnt(15)" : "+v"(rp[1][0]), "+v"(rp[1][1]) :: "memory");
  MFV(h4[0], se1, rp[1][0]);
  MFV(h4[0], se1, rp[1][1]);
  asm volatile("s_waitcnt vmcnt(11)" : "+v"(rp[6][0]), "+v"(rp[6][1]) :: "memory");
  MFV(h4[3], se0, rp[6][0]);
  MFV(h4[3], se0, rp[6][1]);
  asm volatile("s_waitcnt vmcnt(2)" : "+v"(rp[2][0]), "+v"(rp[2][1]) :: "memory");
  MFV(h4[1], se0, rp[2][0]);
  MFV(h4[1], se0, rp[2][1]);
  asm volatile("s_waitcnt vmcnt(0)" : "+v"(rp[7][0]), "+v"(rp[7][1]) :: "memory");
  MFV(h4[3], se1, rp[7][0]);
  MFV(h4[3], se1, rp[7][1]);
  spin_until(CNT(C_F + rteam * 3 + 1 + 1), fo);
  gld_sc1<0>(rp[3][0], ws + 24576, ro2);
  gld_sc1<0>(rp[3][1], ws + 98304, ro2);
  gld_sc1<0>(rp[8][0], ws + 65536, ro2);
  gld_sc1<0>(rp[8][1], ws + 139264, ro2);
  gld2x8(rp[4][0], ws + 32768, ro2l);
  gld2x8(rp[4][1], ws + 106496, ro2l);
  asm volatile("s_waitcnt vmcnt(6)" : "+v"(rp[3][0]), "+v"(rp[3][1]) :: "memory");
  MFV(h4[1], se1, rp[3][0]);
  MFV(h4[1], se1, rp[3][1]);
  asm volatile("s_waitcnt vmcnt(4)" : "+v"(rp[8][0]), "+v"(rp[8][1]) :: "memory");
  MFV(h4[4], se0, rp[8][0]);
  MFV(h4[4], se0, rp[8][1]);
  asm volatile("s_waitcnt vmcnt(0)" : "+v"(rp[4][0]), "+v"(rp[4][1]) :: "memory");
  MFV(h4[2], se0, rp[4][0]);
  MFV(h4[2], se0, rp[4][1]);
  asm volatile("s_nop 7" : "+v"(h4[0]), "+v"(h4[1]), "+v"(h4[2]), "+v"(h4[3]), "+v"(h4[4]));
  R_OUT();
  asm volatile(".Lk3_end:" ::: "memory");
  }
}

extern "C" void kernel_launch(void* const* d_in, const int* in_sizes, int n_in, void* d_out, int out_size, void* d_ws, size_t ws_size,
                              hipStream_t stream) {
  const u16* x = (const u16*)d_in[0];   const u16* ckv = (const u16*)d_in[1]; const u16* wq = (const u16*)d_in[2];
  const u16* wuk = (const u16*)d_in[3]; const u16* wuv = (const u16*)d_in[4]; const u16* wo = (const u16*)d_in[5];
  const u16* w1 = (const u16*)d_in[7];  const u16* b1 = (const u16*)d_in[8];  const u16* w2 = (const u16*)d_in[9];
  const u16* b2 = (const u16*)d_in[10];
  char* ws = (char*)d_ws;
  (void)hipFuncSetAttribute((const void*)k1_kvproj, hipFuncAttributeMaxDynamicSharedMemorySize, K1_LDS);
  (void)hipFuncSetAttribute((const void*)k2_rest, hipFuncAttributeMaxDynamicSharedMemorySize, K2_LDS);
  k1_kvproj<<<256, 256, K1_LDS, stream>>>(x, ckv, wuk, wuv, wq, wo, w1, w2, ws);
  k2_rest<<<256, 256, K2_LDS, stream>>>(wq, ws, x, b1, b2, (u16*)d_out);
}
```
